```python
import torch

def _softmax(x, axis=-1):
    return torch.softmax(x, dim=axis)

def _gelu(x):
    return torch.nn.functional.gelu(x, approximate='tanh')

def _silu(x):
    return torch.nn.functional.silu(x)

def _sigmoid(x):
    return torch.sigmoid(x)

def _layernorm(x, axis=-1, eps=1e-05):
    m = x.mean(dim=axis, keepdim=True)
    v = x.var(dim=axis, keepdim=True, unbiased=False)
    return (x - m) / torch.sqrt(v + eps)

def _scatter_add(tgt, idx, src, axis=0):
    return tgt.clone().index_add_(axis, idx, src)

def _segment_sum(src, seg, n):
    out = torch.zeros((n,) + src.shape[1:], dtype=src.dtype)
    return out.index_add_(0, seg, src)

def _pad(x, pw):
    flat = tuple((v for pair in reversed(pw) for v in pair))
    return torch.nn.functional.pad(x, flat)
kernel_name = 'adaLN_cond__ssm_conv_s4d__glu_gated_geglu__prompt'

def setup_inputs():

    _w = lambda a: (lambda t: t.to(torch.bfloat16) if t.is_floating_point() else t)(torch.as_tensor(a))

    def _A_setup():
        Bsz, L, D, N = (1, 2048, 512, 128)
        return {'u': _w((lambda *s: torch.randn(*s, dtype=torch.bfloat16) * 0.05)(Bsz, L, D)), 'log_A_real': _w((lambda *s: torch.randn(*s, dtype=torch.bfloat16) * 3.2)(D, N) * 0.5), 'A_imag': _w((lambda *s: torch.randn(*s, dtype=torch.bfloat16) * 0.05)(D, N) * 3.14), 'C_real': _w((lambda *s: torch.randn(*s, dtype=torch.bfloat16) * 0.4)(D, N) / N ** 0.5), 'C_imag': _w((lambda *s: torch.randn(*s, dtype=torch.bfloat16) * 0.4)(D, N) / N ** 0.5), 'log_dt': _w((lambda *s: torch.randn(*s, dtype=torch.bfloat16) * 0.05)(D) * 0.5 - 4.0), 'D_skip': _w((lambda *s: torch.randn(*s, dtype=torch.bfloat16) * 0.1)(D))}
    _A_in = _A_setup()

    def _B_setup():
        B, S, Dm, Df = (1, 2048, 512, 1376)
        return {'x': _w((lambda *s: torch.randn(*s, dtype=torch.bfloat16) * 0.05)(B, S, Dm)), 'w_gate': _w((lambda *s: torch.randn(*s, dtype=torch.bfloat16) * 0.8)(Dm, Df) * Dm ** (-0.5)), 'w_up': _w((lambda *s: torch.randn(*s, dtype=torch.bfloat16) * 0.8)(Dm, Df) * Dm ** (-0.5)), 'w_down': _w((lambda *s: torch.randn(*s, dtype=torch.bfloat16) * 0.8)(Df, Dm) * Df ** (-0.5))}
    _B_in = _B_setup()
    return {'A_u': _A_in['u'], 'A_log_A_real': _A_in['log_A_real'], 'A_A_imag': _A_in['A_imag'], 'A_C_real': _A_in['C_real'], 'A_C_imag': _A_in['C_imag'], 'A_log_dt': _A_in['log_dt'], 'A_D_skip': _A_in['D_skip'], 'B_x': _B_in['x'], 'B_w_gate': _B_in['w_gate'], 'B_w_up': _B_in['w_up'], 'B_w_down': _B_in['w_down'], 'c': _w((lambda *s: torch.randn(*s, dtype=torch.bfloat16) * 0.2)(1, 512) * 8.0), 'w_mod': _w((lambda *s: torch.randn(*s, dtype=torch.bfloat16) * 0.2)(512, 6 * 512) * 512 ** (-0.5) * 8.0)}

def reference(A_u, A_log_A_real, A_A_imag, A_C_real, A_C_imag, A_log_dt, A_D_skip, B_x, B_w_gate, B_w_up, B_w_down, c, w_mod):

    def _A_ref(u, log_A_real, A_imag, C_real, C_imag, log_dt, D_skip):
        L = u.shape[1]
        dt = torch.exp(log_dt)
        A = -torch.exp(log_A_real) + 1j * A_imag
        C = (C_real + 1j * C_imag) * dt[:, None]
        pos = (lambda x, dt: x.to(dt))(torch.arange(L), log_dt.dtype)
        P = torch.exp((A * dt[:, None])[:, :, None] * pos[None, None, :])
        K = 2.0 * torch.einsum('dn,dnl->dl', C, P).real
        L2 = 2 * L
        u_t = (lambda x, *d: x.permute(*d))(u, 0, 2, 1)
        Kf = (lambda x, *a, **k: torch.fft.rfft(x.float(), *a, **k))(K, L2)
        Uf = (lambda x, *a, **k: torch.fft.rfft(x.float(), *a, **k))(u_t, L2)
        y = (lambda x, *a, **k: torch.fft.irfft(x, *a, **k).to(torch.bfloat16))(Uf * Kf[None, :, :], L2)[:, :, :L]
        y = (lambda x, *d: x.permute(*d))(y, 0, 2, 1)
        return y + u * D_skip[None, None, :]

    def _B_ref(x, w_gate, w_up, w_down):
        g = torch.einsum('bsd,df->bsf', x, w_gate)
        u = torch.einsum('bsd,df->bsf', x, w_up)
        h = _gelu(g) * u
        return torch.einsum('bsf,fd->bsd', h, w_down)
    x = A_u
    mod = (_silu(c) @ w_mod).reshape(c.shape[0], 6, 1, -1)
    sa, ca, ga, sb, cb, gb = (mod[:, 0].clone(), mod[:, 1].clone(), mod[:, 2].clone(), mod[:, 3].clone(), mod[:, 4].clone(), mod[:, 5].clone())
    h = _layernorm(x, axis=-1) * (1 + ca) + sa
    x = x + ga * _A_ref(h, A_log_A_real, A_A_imag, A_C_real, A_C_imag, A_log_dt, A_D_skip)
    h = _layernorm(x, axis=-1) * (1 + cb) + sb
    x = x + gb * _B_ref(h, B_w_gate, B_w_up, B_w_down)
    return x
```

```cpp
#include <hip/hip_runtime.h>
#include <hip/hip_bf16.h>
#include <stdint.h>

typedef unsigned short u16;
constexpr int L = 2048, D = 512, N = 128, DF = 1376;

__device__ __forceinline__ float b2f(u16 v) { return __uint_as_float(((unsigned)v) << 16); }
__device__ __forceinline__ u16 f2b(float f) { __hip_bfloat16 h = __float2bfloat16(f); return *(u16*)&h; }
__device__ __forceinline__ float rb(float f) { return b2f(f2b(f)); }

__global__ void k_mod(const u16* c, const u16* w, float* mod) {
  int j = blockIdx.x * blockDim.x + threadIdx.x;
  float acc = 0.f;
  for (int k = 0; k < D; ++k) {
    float cv = b2f(c[k]);
    float s = rb(cv / (1.f + expf(-cv)));
    acc += s * b2f(w[k * 6 * D + j]);
  }
  mod[j] = rb(acc);
}

__global__ void k_ln(const u16* x, const float* shift, const float* scale, u16* h) {
  __shared__ float red[256];
  int r = blockIdx.x, t = threadIdx.x;
  float a = b2f(x[r * D + t]), b = b2f(x[r * D + t + 256]);
  red[t] = a + b; __syncthreads();
  for (int s = 128; s > 0; s >>= 1) { if (t < s) red[t] += red[t + s]; __syncthreads(); }
  float mean = red[0] / D; __syncthreads();
  red[t] = (a - mean) * (a - mean) + (b - mean) * (b - mean); __syncthreads();
  for (int s = 128; s > 0; s >>= 1) { if (t < s) red[t] += red[t + s]; __syncthreads(); }
  float var = rb(red[0] / D);
  float m = rb(mean);
  float sd = rb(sqrtf(rb(var + 1e-5f)));
  float xa = rb(rb(a - m) / sd), xb = rb(rb(b - m) / sd);
  h[r * D + t] = f2b(rb(xa * rb(1.f + scale[t])) + shift[t]);
  h[r * D + t + 256] = f2b(rb(xb * rb(1.f + scale[t + 256])) + shift[t + 256]);
}

__global__ void k_kgen(const u16* lar, const u16* aim, const u16* cre, const u16* cim, const u16* ldt, float* K) {
  int d = blockIdx.y; int l = blockIdx.x * blockDim.x + threadIdx.x;
  float dt = rb(expf(b2f(ldt[d])));
  float p = rb((float)l);
  float acc = 0.f;
  for (int n = 0; n < N; ++n) {
    float zr = -rb(expf(b2f(lar[d * N + n]))) * dt;
    float zi = b2f(aim[d * N + n]) * dt;
    float cr = b2f(cre[d * N + n]) * dt, ci = b2f(cim[d * N + n]) * dt;
    float e = expf(zr * p); float s, co; sincosf(zi * p, &s, &co);
    acc += cr * e * co - ci * e * s;
  }
  K[d * L + l] = 2.f * acc;
}

__global__ void k_conv(const u16* h, const float* K, const u16* x, const u16* dskip, const float* ga, u16* x1) {
  int d = blockIdx.x * blockDim.x + threadIdx.x; int l = blockIdx.y;
  float acc = 0.f;
  for (int j = 0; j <= l; ++j) acc += K[d * L + j] * b2f(h[(l - j) * D + d]);
  float y = rb(acc);
  float t = rb(y + rb(b2f(h[l * D + d]) * b2f(dskip[d])));
  x1[l * D + d] = f2b(b2f(x[l * D + d]) + rb(ga[d] * t));
}

__global__ void k_g1(const u16* h, const u16* wg, const u16* wu, u16* hh) {
  int f = blockIdx.x * blockDim.x + threadIdx.x; int l = blockIdx.y;
  if (f >= DF) return;
  float g = 0.f, u = 0.f;
  for (int k = 0; k < D; ++k) { float a = b2f(h[l * D + k]); g += a * b2f(wg[k * DF + f]); u += a * b2f(wu[k * DF + f]); }
  g = rb(g); u = rb(u);
  float ge = rb(0.5f * g * (1.f + tanhf(0.7978845608028654f * (g + 0.044715f * g * g * g))));
  hh[l * DF + f] = f2b(ge * u);
}

__global__ void k_g2(const u16* hh, const u16* wd, const u16* x1, const float* gb, u16* out) {
  int d = blockIdx.x * blockDim.x + threadIdx.x; int l = blockIdx.y;
  float acc = 0.f;
  for (int k = 0; k < DF; ++k) acc += b2f(hh[l * DF + k]) * b2f(wd[k * D + d]);
  out[l * D + d] = f2b(b2f(x1[l * D + d]) + rb(gb[d] * rb(acc)));
}

extern "C" void kernel_launch(void* const* d_in, const int* in_sizes, int n_in,
                              void* d_out, int out_size, void* d_ws, size_t ws_size,
                              hipStream_t stream) {
  const u16* x = (const u16*)d_in[0];
  const u16* lar = (const u16*)d_in[1];
  const u16* aim = (const u16*)d_in[2];
  const u16* cre = (const u16*)d_in[3];
  const u16* cim = (const u16*)d_in[4];
  const u16* ldt = (const u16*)d_in[5];
  const u16* dsk = (const u16*)d_in[6];
  const u16* wg = (const u16*)d_in[8];
  const u16* wu = (const u16*)d_in[9];
  const u16* wd = (const u16*)d_in[10];
  const u16* c = (const u16*)d_in[11];
  const u16* wm = (const u16*)d_in[12];
  char* ws = (char*)d_ws;
  float* mod = (float*)ws;
  float* K = (float*)(ws + (1 << 16));
  u16* h = (u16*)(ws + (8 << 20));
  u16* x1 = (u16*)(ws + (12 << 20));
  u16* h2 = (u16*)(ws + (16 << 20));
  u16* hh = (u16*)(ws + (20 << 20));
  k_mod<<<12, 256, 0, stream>>>(c, wm, mod);
  k_ln<<<L, 256, 0, stream>>>(x, mod, mod + D, h);
  k_kgen<<<dim3(L / 256, D), 256, 0, stream>>>(lar, aim, cre, cim, ldt, K);
  k_conv<<<dim3(D / 256, L), 256, 0, stream>>>(h, K, x, dsk, mod + 2 * D, x1);
  k_ln<<<L, 256, 0, stream>>>(x1, mod + 3 * D, mod + 4 * D, h2);
  k_g1<<<dim3((DF + 255) / 256, L), 256, 0, stream>>>(h2, wg, wu, hh);
  k_g2<<<dim3(D / 256, L), 256, 0, stream>>>(hh, wd, x1, mod + 5 * D, (u16*)d_out);
}
```

```cpp
#include <hip/hip_runtime.h>
#include <hip/hip_bf16.h>
#include <stdint.h>

typedef unsigned short u16;
typedef unsigned u32;
typedef float f2 __attribute__((ext_vector_type(2)));
typedef float f4 __attribute__((ext_vector_type(4)));
typedef float f16v __attribute__((ext_vector_type(16)));
typedef u32 u2 __attribute__((ext_vector_type(2)));
typedef u32 u4 __attribute__((ext_vector_type(4)));
typedef u4 u4un __attribute__((aligned(2)));
typedef short s4 __attribute__((ext_vector_type(4)));
typedef __bf16 bf2 __attribute__((ext_vector_type(2)));
typedef __bf16 bf8 __attribute__((ext_vector_type(8)));
typedef _Float16 h2 __attribute__((ext_vector_type(2)));
typedef _Float16 h8 __attribute__((ext_vector_type(8)));
#define LAS __attribute__((address_space(3)))
#define DI __device__ __forceinline__

constexpr int L = 2048, D = 512, NM = 128, DF = 1376, NU = 86;
constexpr int KXS = 2056;

constexpr size_t WS_MOD = 0, WS_STAT = 16 << 10, WS_CSB = 64 << 10, WS_XT = 1 << 20, WS_KX = 4 << 20, WS_X1T = 8 << 20,
                 WS_WT = 12 << 20, WS_WDT = 16 << 20, WS_HH = 20 << 20;

DI float blo(u32 v) { return __uint_as_float(v << 16); }
DI float bhi(u32 v) { return __uint_as_float(v & 0xffff0000u); }
DI float b2f(u16 v) { return __uint_as_float(((u32)v) << 16); }
DI u32 pk(float a, float b) { return __builtin_bit_cast(u32, __builtin_convertvector((f2){a, b}, bf2)); }
DI float rb(float a) { return blo(pk(a, a)); }
DI u2 trd(const char* p) { return __builtin_bit_cast(u2, __builtin_amdgcn_ds_read_tr16_b64_v4i16((LAS s4*)p)); }
DI u4 cat(u2 a, u2 b) { return (u4){a.x, a.y, b.x, b.y}; }
DI float dot2(u32 a, u32 b, float c) { return __builtin_amdgcn_fdot2_f32_bf16(__builtin_bit_cast(bf2, a), __builtin_bit_cast(bf2, b), c, false); }
DI f4 mfma16(u4 a, u4 b, f4 c) { return __builtin_amdgcn_mfma_f32_16x16x32_bf16(__builtin_bit_cast(bf8, a), __builtin_bit_cast(bf8, b), c, 0, 0, 0); }
DI f4 mfma16h(u4 a, u4 b, f4 c) { return __builtin_amdgcn_mfma_f32_16x16x32_f16(__builtin_bit_cast(h8, a), __builtin_bit_cast(h8, b), c, 0, 0, 0); }
DI f16v mfma32(u4 a, u4 b, f16v c) { return __builtin_amdgcn_mfma_f32_32x32x16_bf16(__builtin_bit_cast(bf8, a), __builtin_bit_cast(bf8, b), c, 0, 0, 0); }
DI f2 cmul(f2 a, f2 b) {
  f2 t, r;
  asm("v_pk_mul_f32 %0, %1, %2 op_sel_hi:[0,1]" : "=v"(t) : "v"(a), "v"(b));
  asm("v_pk_fma_f32 %0, %1, %2, %3 op_sel:[1,1,0] op_sel_hi:[1,0,1] neg_lo:[0,1,0]" : "=v"(r) : "v"(a), "v"(b), "v"(t));
  return r;
}
template <int CTRL> DI float dppf(float v) { return __int_as_float(__builtin_amdgcn_update_dpp(0, __float_as_int(v), CTRL, 0xf, 0xf, true)); }
DI float swap32(float v) {
  auto r = __builtin_amdgcn_permlane32_swap(__float_as_uint(v), __float_as_uint(v), false, false);
  return __uint_as_float((threadIdx.x & 32) ? r[0] : r[1]);
}
DI f2 ln_consts(float s1, float s2) {
  const float mean = s1 * (1.f / D);
  const float var = fmaxf(s2 * (1.f / D) - mean * mean, 0.f);
  const float sd = rb(sqrtf(rb(rb(var) + 1e-5f)));
  const float a = 1.f / sd;
  return (f2){a, -rb(mean) * a};
}

constexpr int KG_ROW = 544, KG_CH = 57 * KG_ROW, KG_T = 2 * KG_CH, KG_TS = 4160, K1_LDS = KG_T + 2 * KG_TS;

DI f2 cexp_pow2(float a, float fh, float fl, float p) {
  const float e = __builtin_amdgcn_exp2f(a * p);
  const float r = __builtin_amdgcn_fractf(fh * p) + fl * p;
  return (f2){e * __builtin_amdgcn_cosf(r), e * __builtin_amdgcn_sinf(r)};
}

DI void k1_kgen(char* sm, int b, const u16* lar, const u16* aim, const u16* cre, const u16* cim, const u16* ldt, const u16* dsk, u16* kx) {
  const int t = threadIdx.x, w = t >> 6, lane = t & 63, ch = w >> 1, half = w & 1;
  const int d0 = 64 * (b & 7) + 2 * (b >> 3), d = d0 + ch, n = 64 * half + lane;
  const float v_lar = b2f(lar[d * NM + n]), v_aim = b2f(aim[d * NM + n]);
  const u32 cr = cre[d * NM + n], ci = cim[d * NM + n], cr2 = cre[d * NM + (n ^ 64)], ci2 = cim[d * NM + (n ^ 64)];
  const float dt = rb(expf(b2f(ldt[d])));
  const float ds = b2f(dsk[d]);
  int mx = (int)max(max(cr & 0x7fffu, ci & 0x7fffu), max(cr2 & 0x7fffu, ci2 & 0x7fffu));
  mx = max(mx, __builtin_amdgcn_update_dpp(0, mx, 0x111, 0xf, 0xf, true));
  mx = max(mx, __builtin_amdgcn_update_dpp(0, mx, 0x112, 0xf, 0xf, true));
  mx = max(mx, __builtin_amdgcn_update_dpp(0, mx, 0x114, 0xf, 0xf, true));
  mx = max(mx, __builtin_amdgcn_update_dpp(0, mx, 0x118, 0xf, 0xf, true));
  mx = max(max(__builtin_amdgcn_readlane(mx, 15), __builtin_amdgcn_readlane(mx, 31)), max(__builtin_amdgcn_readlane(mx, 47), __builtin_amdgcn_readlane(mx, 63)));
  const int ex = min(max(mx >> 7, 1), 253);
  const float cs = __int_as_float((254 - ex) << 23), kf = 2.f * dt * __int_as_float(ex << 23);
  const float zr = -rb(expf(v_lar)) * dt, zi = v_aim * dt;
  const float a = zr * 1.44269504088896341f;
  const float fh = zi * 0.159154937f, fl = fmaf(zi, 0.159154937f, -fh) + zi * 6.42063833e-9f;
  char* base = sm + ch * KG_CH + n * 4;
  auto put_lo = [&](int row, f2 v) { *(u32*)(base + row * KG_ROW) = __builtin_bit_cast(u32, __builtin_convertvector((f2){v.x, -v.y}, h2)); };
  auto put_hi = [&](int row, f2 v) { *(u32*)(base + (16 + row) * KG_ROW) = __builtin_bit_cast(u32, __builtin_convertvector(v, h2)); };
  const f2 e1 = cexp_pow2(a, fh, fl, 1.f), e8 = cexp_pow2(a, fh, fl, 8.f), e128 = cexp_pow2(a, fh, fl, 128.f), e1024 = cexp_pow2(a, fh, fl, 1024.f);
  {
    f2 lo[8];
    const f2 e16 = cmul(e8, e8), e32 = cmul(e16, e16), e64 = cmul(e32, e32);
    lo[0] = (f2){b2f(cr) * cs, b2f(ci) * cs};
    lo[1] = cmul(lo[0], e8);
    lo[2] = cmul(lo[0], e16); lo[3] = cmul(lo[1], e16);
#pragma unroll
    for (int i = 0; i < 4; ++i) lo[4 + i] = cmul(lo[i], e32);
#pragma unroll
    for (int i = 0; i < 8; ++i) { put_lo(i, lo[i]); put_lo(8 + i, cmul(lo[i], e64)); }
  }
  {
    f2 e[8], m[8];
    e[0] = (f2){1.f, 0.f}; e[1] = e1; e[2] = cmul(e1, e1); e[3] = cmul(e[2], e1); e[4] = cmul(e[2], e[2]);
    e[5] = cmul(e[4], e1); e[6] = cmul(e[4], e[2]); e[7] = cmul(e[4], e[3]);
    m[1] = e128; m[2] = cmul(e128, e128); m[3] = cmul(m[2], e128); m[4] = cmul(m[2], m[2]);
    m[5] = cmul(m[4], e128); m[6] = cmul(m[4], m[2]); m[7] = cmul(m[4], m[3]);
#pragma unroll
    for (int i = 0; i < 8; ++i) put_hi(i, e[i]);
    put_hi(8, e128);
#pragma unroll
    for (int i = 1; i < 8; ++i) put_hi(8 + i, cmul(e128, e[i]));
#pragma unroll
    for (int tt = 0; tt < 2; ++tt) {
      put_hi(16 + 4 * tt, m[2 + tt]);
#pragma unroll
      for (int r = 1; r < 4; ++r) put_hi(16 + 4 * tt + r, cmul(m[2 + tt], e[2 * r]));
    }
#pragma unroll
    for (int tt = 0; tt < 4; ++tt) { put_hi(24 + 2 * tt, m[4 + tt]); put_hi(25 + 2 * tt, cmul(m[4 + tt], e[4])); }
    put_hi(32, e1024);
#pragma unroll
    for (int tt = 1; tt < 8; ++tt) put_hi(32 + tt, cmul(e1024, m[tt]));
    put_hi(40, cmul(e1024, e1024));
  }
  __syncthreads();
  {
    const int c = lane & 15, g = lane >> 4;
    const char* fb = sm + ch * KG_CH + c * KG_ROW + g * 16;
    u16* T = (u16*)(sm + KG_T + ch * KG_TS);
    f4 acc0 = {0.f, 0.f, 0.f, 0.f}, acc1 = acc0;
    const int tile = half ? 1 : 0;
#pragma unroll
    for (int s = 0; s < 8; ++s) {
      const u4 bfr = *(const u4*)(fb + s * 64);
      acc0 = mfma16h(*(const u4*)(fb + (16 + 16 * tile) * KG_ROW + s * 64), bfr, acc0);
      if (!half) acc1 = mfma16h(*(const u4*)(fb + 48 * KG_ROW + s * 64), bfr, acc1);
    }
    acc0 *= kf; acc1 *= kf;
    if (!half) {
      if (lane == 0) acc0[0] += ds;
      *(u2*)(T + 8 * c + 4 * (g & 1) + 128 * (g >> 1)) = (u2){pk(acc0[0], acc0[1]), pk(acc0[2], acc0[3])};
      if (g < 2) {
#pragma unroll
        for (int e = 0; e < 4; ++e) T[1024 + 128 * (4 * g + e) + 8 * c] = (u16)pk(acc1[e], 0.f);
      } else if (lane == 32) T[2048] = (u16)pk(acc1[0], 0.f);
    } else {
#pragma unroll
      for (int e = 0; e < 4; ++e) {
        const int p = g < 2 ? 256 + 128 * g + 2 * e : 512 + 128 * (2 * (g - 2) + (e >> 1)) + 4 * (e & 1);
        T[p + 8 * c] = (u16)pk(acc0[e], 0.f);
      }
    }
  }
  __syncthreads();
#pragma unroll
  for (int c2 = 0; c2 < 2; ++c2) {
    const u16* T = (const u16*)(sm + KG_T + c2 * KG_TS);
    u4 v = *(const u4*)(T + 8 * t);
    const u32 e8v = T[8 * t + 8];
    const u32 e0 = v.x & 0xffffu, e2 = v.y & 0xffffu, e4 = v.z & 0xffffu, e6 = v.w & 0xffffu;
    if (t >= 128) {
      const u32 m4 = (t & 1) ? e8v : e0;
      v = (u4){e0 | (e0 << 16), e0 | (e0 << 16), m4 | (e8v << 16), e8v | (e8v << 16)};
    } else if (t >= 64) {
      v = (u4){e0 | (e0 << 16), e0 | (e4 << 16), e4 | (e4 << 16), e8v | (e8v << 16)};
    } else if (t >= 32) {
      v = (u4){e0 | (e0 << 16), e2 | (e4 << 16), e4 | (e4 << 16), e6 | (e8v << 16)};
    }
    u16* dst = kx + (d0 + c2) * KXS;
    *(u4*)(dst + 8 + 8 * t) = v;
    if (t == 0) *(u4*)dst = (u4){0u, 0u, 0u, 0u};
  }
}

DI void k1_xt(char* sm, int b, const u16* x, u16* xt) {
  const int t = threadIdx.x, w = t >> 6, lane = t & 63;
  const int c0 = 64 * (b & 7), r0 = 256 * (b >> 3);
  u4 v[8];
#pragma unroll
  for (int i = 0; i < 8; ++i) v[i] = *(const u4*)(x + (r0 + 32 * i + (t >> 3)) * D + c0 + 8 * (t & 7));
#pragma unroll
  for (int i = 0; i < 8; ++i) *(u4*)(sm + (32 * i + (t >> 3)) * 144 + 16 * (t & 7)) = v[i];
  __syncthreads();
  const int g = lane >> 4, q = (lane & 15) >> 2, p = lane & 3;
#pragma unroll
  for (int i = 0; i < 8; ++i) {
    const char* a = sm + (32 * i + 8 * g + q) * 144 + 32 * w + 8 * p;
    *(u4*)(xt + (c0 + 16 * w + (lane & 15)) * L + r0 + 32 * i + 8 * g) = cat(trd(a), trd(a + 4 * 144));
  }
}

DI void k1_stats(int b, const u16* x, f2* stat) {
  const int t = threadIdx.x, row = 32 * b + (t >> 3);
  u4 v[8];
#pragma unroll
  for (int i = 0; i < 8; ++i) v[i] = *(const u4*)(x + row * D + 64 * i + 8 * (t & 7));
  float s1 = 0.f;
#pragma unroll
  for (int i = 0; i < 8; ++i)
#pragma unroll
    for (int j = 0; j < 4; ++j) s1 = dot2(v[i][j], 0x3f803f80u, s1);
  s1 += dppf<0xb1>(s1); s1 += dppf<0x4e>(s1); s1 += dppf<0x141>(s1);
  const float mean = s1 * (1.f / D);
  float s2 = 0.f;
#pragma unroll
  for (int i = 0; i < 8; ++i)
#pragma unroll
    for (int j = 0; j < 4; ++j) { const float lo = blo(v[i][j]) - mean, hi = bhi(v[i][j]) - mean; s2 = fmaf(lo, lo, s2); s2 = fmaf(hi, hi, s2); }
  s2 += dppf<0xb1>(s2); s2 += dppf<0x4e>(s2); s2 += dppf<0x141>(s2);
  const float sd = rb(sqrtf(rb(rb(s2 * (1.f / D)) + 1e-5f)));
  const float a = 1.f / sd;
  if ((t & 7) == 0) stat[row] = (f2){a, -rb(mean) * a};
}

DI void k1_mod(char* sm, int b, const u16* c, const u16* wm, float* mod) {
  const int t = threadIdx.x, w = t >> 6, lane = t & 63, rg = t >> 3;
  u4 v[16];
#pragma unroll
  for (int i = 0; i < 16; ++i) v[i] = *(const u4*)(wm + (rg + 32 * i) * (6 * D) + 64 * b + 8 * (t & 7));
  float acc[8] = {0.f, 0.f, 0.f, 0.f, 0.f, 0.f, 0.f, 0.f};
#pragma unroll
  for (int i = 0; i < 16; ++i) {
    const float cv = b2f(c[rg + 32 * i]);
    const float s = rb(cv / (1.f + __expf(-cv)));
#pragma unroll
    for (int j = 0; j < 4; ++j) { acc[2 * j] = fmaf(s, blo(v[i][j]), acc[2 * j]); acc[2 * j + 1] = fmaf(s, bhi(v[i][j]), acc[2 * j + 1]); }
  }
  float* red = (float*)sm;
  *(f4*)(red + rg * 68 + 8 * (t & 7)) = (f4){acc[0], acc[1], acc[2], acc[3]};
  *(f4*)(red + rg * 68 + 8 * (t & 7) + 4) = (f4){acc[4], acc[5], acc[6], acc[7]};
  __syncthreads();
  float s = 0.f;
#pragma unroll
  for (int i = 0; i < 8; ++i) s += red[(8 * w + i) * 68 + lane];
  float* red2 = red + 32 * 68;
  red2[t] = s;
  __syncthreads();
  if (t < 64) mod[64 * b + t] = rb(red2[t] + red2[t + 64] + red2[t + 128] + red2[t + 192]);
}

__global__ __launch_bounds__(256) void k1_prep(const u16* x, const u16* lar, const u16* aim, const u16* cre, const u16* cim, const u16* ldt,
                                               const u16* dsk, const u16* c, const u16* wm, char* ws) {
  __shared__ __attribute__((aligned(16))) char sm[K1_LDS];
  const int b = blockIdx.x;
  if (b < 256) k1_kgen(sm, b, lar, aim, cre, cim, ldt, dsk, (u16*)(ws + WS_KX));
  else if (b < 320) k1_xt(sm, b - 256, x, (u16*)(ws + WS_XT));
  else if (b < 384) k1_stats(b - 320, x, (f2*)(ws + WS_STAT));
  else k1_mod(sm, b - 384, c, wm, (float*)(ws + WS_MOD));
}

constexpr int CV_KC = 4128, CV_H = 8 * CV_KC, CV_HP = 1152, CV_EX = CV_H + 8 * CV_HP, K2_LDS = CV_EX + 16384;

DI void k2_conv(char* sm, int b, char* ws) {
  const int t = threadIdx.x, w = t >> 6, lane = t & 63, ch = w >> 1, half = w & 1;
  const int d0 = 64 * (b & 7) + 2 * (b >> 3), d = d0 + ch;
  const u16* xt = (const u16*)(ws + WS_XT);
  const u16* kx = (const u16*)(ws + WS_KX);
  const float* mod = (const float*)(ws + WS_MOD);
  const int c = lane & 15, g = lane >> 4;
  u4 xv[2], st[4], kv[8];
  u2 xr[4];
#pragma unroll
  for (int i = 0; i < 2; ++i) xv[i] = *(const u4*)(xt + (d0 + i) * L + 8 * t);
#pragma unroll
  for (int i = 0; i < 4; ++i) st[i] = *(const u4*)(ws + WS_STAT + 64 * t + 16 * i);
#pragma unroll
  for (int i = 0; i < 8; ++i) kv[i] = *(const u4un*)(kx + (d0 + (i >> 2)) * KXS + 8 + 8 * t - (i & 3));
#pragma unroll
  for (int i = 0; i < 4; ++i) xr[i] = *(const u2*)(xt + d * L + 256 * (4 * half + i) + 16 * c + 4 * g);
  const float ga = mod[2 * D + d];
  if (t < 16) *(u4*)(sm + (t >> 1) * CV_KC + 16 * (t & 1)) = (u4){0u, 0u, 0u, 0u};
  else if (t >= 64 && t < 128) *(u4*)(sm + CV_H + ((t - 64) >> 3) * CV_HP + 16 * (t & 7)) = (u4){0u, 0u, 0u, 0u};
#pragma unroll
  for (int i = 0; i < 2; ++i) {
    const float c1 = rb(1.f + mod[D + d0 + i]), sa = mod[d0 + i];
    float h[8];
#pragma unroll
    for (int j = 0; j < 8; ++j) {
      const float xx = (j & 1) ? bhi(xv[i][j >> 1]) : blo(xv[i][j >> 1]);
      h[j] = fmaf(fmaf(xx, __uint_as_float(st[j >> 1][2 * (j & 1)]), __uint_as_float(st[j >> 1][2 * (j & 1) + 1])), c1, sa);
    }
#pragma unroll
    for (int r = 0; r < 4; ++r) *(u32*)(sm + CV_H + (4 * i + r) * CV_HP + 128 + 4 * t) = pk(h[r], h[r + 4]);
  }
#pragma unroll
  for (int i = 0; i < 8; ++i) *(u4*)(sm + i * CV_KC + 32 + 16 * t) = kv[i];
  __syncthreads();
  f4 acc[8];
#pragma unroll
  for (int i = 0; i < 8; ++i) acc[i] = (f4){0.f, 0.f, 0.f, 0.f};
  const char* ka = sm + (4 * ch + 2 * half) * CV_KC + 32 + 8 * (8 * g + (lane & 3) - ((lane & 15) >> 2));
  const char* hb = sm + CV_H + (4 * ch + 2 * half) * CV_HP + 128 + 8 * c - 16 * g;
#pragma unroll
  for (int grp = 0; grp < 4; ++grp) {
    const int rho = grp >> 1, par = grp & 1;
    u4 A[8], B[8];
#pragma unroll
    for (int i = 0; i < 8; ++i) {
      const char* pa = ka + rho * CV_KC + 256 * (2 * i + par);
      const char* pb = hb + rho * CV_HP + 64 * (2 * i - par);
      A[i] = cat(trd(pa), trd(pa + 32));
      B[i] = cat(*(const u2*)pb, *(const u2*)(pb - 8));
    }
#pragma unroll
    for (int i = 0; i < 8; ++i)
#pragma unroll
      for (int tt = i; tt < 8; ++tt) acc[tt] = mfma16(A[i], B[tt - i], acc[tt]);
  }
  f4* ex = (f4*)(sm + CV_EX);
  if (__builtin_amdgcn_readfirstlane(half)) {
#pragma unroll
    for (int i = 0; i < 4; ++i) { ex[(w * 4 + i) * 64 + lane] = acc[i]; acc[i] = acc[4 + i]; }
  } else {
#pragma unroll
    for (int i = 0; i < 4; ++i) ex[(w * 4 + i) * 64 + lane] = acc[4 + i];
  }
  __syncthreads();
  u16* x1t = (u16*)(ws + WS_X1T);
#pragma unroll
  for (int i = 0; i < 4; ++i) {
    const f4 y = acc[i] + ex[((w ^ 1) * 4 + i) * 64 + lane];
    const u2 o = {pk(fmaf(ga, y[0], blo(xr[i].x)), fmaf(ga, y[1], bhi(xr[i].x))), pk(fmaf(ga, y[2], blo(xr[i].y)), fmaf(ga, y[3], bhi(xr[i].y)))};
    *(u2*)(x1t + d * L + 256 * (4 * half + i) + 16 * c + 4 * g) = o;
  }
}

DI void k2_wprep(char* sm, int b, const u16* wg, const u16* wu, char* ws) {
  if (b >= 86) return;
  const int t = threadIdx.x, w = t >> 6, lane = t & 63;
  const int up = b >> 1, kh = b & 1;
  const float* mod = (const float*)(ws + WS_MOD);
  u4 v[8];
#pragma unroll
  for (int i = 0; i < 8; ++i) v[i] = *(const u4*)(((i & 1) ? wu : wg) + (256 * kh + 64 * (i >> 1) + (t >> 2)) * DF + 32 * up + 8 * (t & 3));
  u32* tab = (u32*)(sm + 256 * 144);
  { const int k = 256 * kh + t; tab[t] = pk(1.f + mod[4 * D + k], mod[3 * D + k]); }
#pragma unroll
  for (int i = 0; i < 8; ++i) *(u4*)(sm + (64 * (i >> 1) + (t >> 2)) * 144 + 64 * (i & 1) + 16 * (t & 3)) = v[i];
  __syncthreads();
  const int ul = w & 1, kq = w >> 1, hh = lane >> 5, mat = (lane >> 4) & 1, q = (lane & 15) >> 2, p = lane & 3;
  float cs = 0.f, bias = 0.f;
  u16* wt = (u16*)(ws + WS_WT);
#pragma unroll
  for (int s = 0; s < 8; ++s) {
    const int k0 = 128 * kq + 16 * s + 8 * hh;
    const char* a = sm + (k0 + q) * 144 + 64 * mat + 32 * ul + 8 * p;
    const u4 raw = cat(trd(a), trd(a + 4 * 144));
    const u4 t0 = *(const u4*)(tab + k0), t1 = *(const u4*)(tab + k0 + 4);
    u4 o;
#pragma unroll
    for (int j = 0; j < 4; ++j) {
      const u32 ta = j < 2 ? t0[2 * j] : t1[2 * j - 4], tb = j < 2 ? t0[2 * j + 1] : t1[2 * j - 3];
      o[j] = pk(blo(raw[j]) * blo(ta), bhi(raw[j]) * blo(tb));
      bias = fmaf(blo(raw[j]), bhi(ta), bias); bias = fmaf(bhi(raw[j]), bhi(tb), bias);
      cs = dot2(o[j], 0x3f803f80u, cs);
    }
    *(u4*)(wt + (((2 * up + ul) * 32 + 16 * kh + 8 * kq + s) * 64 + lane) * 8) = o;
  }
  float* red = (float*)(sm + 256 * 144 + 1024);
  red[t] = cs; red[256 + t] = bias;
  __syncthreads();
  if (t < 128) {
    const int which = t >> 6, u = (t >> 5) & 1, row = t & 31;
    const float* r = red + 256 * which + 64 * u + row;
    ((float*)(ws + WS_CSB))[(kh * 2 + which) * (NU * 32) + (2 * up + u) * 32 + row] = (r[0] + r[32]) + (r[128] + r[160]);
  }
}

DI void k2_dprep(char* sm, int b, const u16* wd, char* ws) {
  if (b >= 43) return;
  const int t = threadIdx.x, w = t >> 6, lane = t & 63;
  u4 v[8];
#pragma unroll
  for (int i = 0; i < 8; ++i) v[i] = *(const u4*)(wd + (32 * b + 4 * i + w) * D + 8 * lane);
#pragma unroll
  for (int i = 0; i < 8; ++i) *(u4*)(sm + (4 * i + w) * 1040 + 16 * lane) = v[i];
  __syncthreads();
  const int hh = lane >> 5, blk = (lane >> 4) & 1, q = (lane & 15) >> 2, p = lane & 3;
  u16* wdt = (u16*)(ws + WS_WDT);
#pragma unroll
  for (int i = 0; i < 8; ++i) {
    const int dt = 4 * w + (i >> 1), s = i & 1;
    const char* a = sm + (16 * s + 4 * hh + q) * 1040 + 64 * dt + 32 * blk + 8 * p;
    *(u4*)(wdt + ((dt * NU + 2 * b + s) * 64 + lane) * 8) = cat(trd(a), trd(a + 8 * 1040));
  }
}

__global__ __launch_bounds__(256, 2) void k2_conv_prep(const u16* wg, const u16* wu, const u16* wd, char* ws) {
  __shared__ __attribute__((aligned(16))) char sm[K2_LDS];
  const int b = blockIdx.x;
  if (b < 256) k2_conv(sm, b, ws);
  else if (b < 344) k2_wprep(sm, b - 256, wg, wu, ws);
  else k2_dprep(sm, b - 344, wd, ws);
}

constexpr int G1_TAB = 131072, G1_ST = G1_TAB + 6 * 64 * 4, K3_LDS = G1_ST + 128 * 8;

DI void g1_epilogue(const f16v& acc, f2 st, const float* tab, int hh, u4* dst, bool ok) {
  u4 o;
#pragma unroll
  for (int h = 0; h < 2; ++h) {
    const f4 cg = *(const f4*)(tab + 8 * h + 4 * hh), cu = *(const f4*)(tab + 16 + 8 * h + 4 * hh);
    const f4 bg = *(const f4*)(tab + 32 + 8 * h + 4 * hh), bu = *(const f4*)(tab + 48 + 8 * h + 4 * hh);
    float r[4];
#pragma unroll
    for (int e = 0; e < 4; ++e) {
      const float gv = fmaf(st.x, acc[4 * h + e], fmaf(st.y, cg[e], bg[e]));
      const float uv = fmaf(st.x, acc[8 + 4 * h + e], fmaf(st.y, cu[e], bu[e]));
      const float z = gv * fmaf(gv * gv, -0.10294324f, -2.30220819f);
      r[e] = gv * uv * __builtin_amdgcn_rcpf(1.f + __builtin_amdgcn_exp2f(z));
    }
    o[2 * h] = pk(r[0], r[1]); o[2 * h + 1] = pk(r[2], r[3]);
  }
  if (ok) *dst = o;
}

__global__ __launch_bounds__(256, 1) void k3_gemm1(char* ws) {
  extern __shared__ __attribute__((aligned(16))) char sm[];
  const int b = blockIdx.x, j = b >> 3;
  if (j >= 30) return;
  const int rt = 2 * (b & 7) + (j & 1), cg = j >> 1;
  const int t = threadIdx.x, w = __builtin_amdgcn_readfirstlane(t >> 6), lane = t & 63, hh = lane >> 5;
  const int la = (w >> 1) * 3 + 2 * (w & 1), lb = (w >> 1) * 3 + 1;
  const int ua = min(6 * cg + la, NU - 1), ub = min(6 * cg + lb, NU - 1);
  const u16* x1t = (const u16*)(ws + WS_X1T);
  const u4* wa = (const u4*)(ws + WS_WT) + ua * 32 * 64 + lane;
  const u4* wb = (const u4*)(ws + WS_WT) + ub * 32 * 64 + lane;
  u4 xs[32], A[32], Bw[32];
#pragma unroll
  for (int i = 0; i < 32; ++i) {
    xs[i] = *(const u4*)(x1t + (16 * i + (t >> 4)) * L + 128 * rt + 8 * (t & 15));
    A[i] = wa[i * 64];
  }
  __builtin_amdgcn_sched_barrier(0);
  float* tab = (float*)(sm + G1_TAB);
  for (int i = t; i < 384; i += 256) {
    const int u = min(6 * cg + (i >> 6), NU - 1), which = (i >> 5) & 1, row = i & 31;
    const float* p = (const float*)(ws + WS_CSB) + which * (NU * 32) + u * 32 + row;
    tab[i] = p[0] + p[2 * NU * 32];
  }
  const int blk = (lane >> 4) & 1, q = (lane & 15) >> 2, p = lane & 3;
  const int wsw = (((t >> 4) & 3) << 2) | ((t >> 6) & 3);
  char* wr = sm + (t >> 4) * 256 + 16 * ((t & 15) ^ wsw);
  const char* rd[4][2];
#pragma unroll
  for (int c = 0; c < 4; ++c)
#pragma unroll
    for (int tt = 0; tt < 2; ++tt)
      rd[c][tt] = sm + 256 * (8 * hh + 4 * tt + q) + 16 * ((4 * c + 2 * blk + (p >> 1)) ^ ((q << 2) | (2 * hh + tt))) + 8 * (p & 1);
  f16v acc[4], acb[2];
#pragma unroll
  for (int c = 0; c < 4; ++c)
#pragma unroll
    for (int e = 0; e < 16; ++e) acc[c][e] = 0.f;
#pragma unroll
  for (int c = 0; c < 2; ++c)
#pragma unroll
    for (int e = 0; e < 16; ++e) acb[c][e] = 0.f;
  float s1 = 0.f, s2 = 0.f;
#pragma unroll
  for (int ck = 0; ck < 4; ++ck) {
#pragma unroll
    for (int i = 0; i < 8; ++i) *(u4*)(wr + (8 * ck + i) * 4096) = xs[8 * ck + i];
    __syncthreads();
#pragma unroll
    for (int i = 0; i < 8; ++i) {
      const int s = 8 * ck + i;
      Bw[s] = wb[s * 64];
#pragma unroll
      for (int c = 0; c < 4; ++c) {
        const u4 f = cat(trd(rd[c][0] + s * 4096), trd(rd[c][1] + s * 4096));
        acc[c] = mfma32(A[s], f, acc[c]);
        if (c == w) {
#pragma unroll
          for (int e = 0; e < 4; ++e) { s1 = dot2(f[e], 0x3f803f80u, s1); s2 = dot2(f[e], f[e], s2); }
        }
      }
    }
  }
  s1 += swap32(s1); s2 += swap32(s2);
  f2* stl = (f2*)(sm + G1_ST);
  if (lane < 32) stl[32 * w + lane] = ln_consts(s1, s2);
  __syncthreads();
  u4* hhp = (u4*)(ws + WS_HH);
  const int cb = 2 * (w & 1);
#pragma unroll
  for (int s = 0; s < 32; ++s) {
#pragma unroll
    for (int c = 0; c < 2; ++c) {
      const char* r0 = sm + ((rd[c][0] - sm) ^ (128 * (w & 1)));
      const char* r1 = sm + ((rd[c][1] - sm) ^ (128 * (w & 1)));
      acb[c] = mfma32(Bw[s], cat(trd(r0 + s * 4096), trd(r1 + s * 4096)), acb[c]);
    }
    if ((s & 7) == 7) {
      const int c = s >> 3;
      g1_epilogue(acc[c], stl[32 * c + (lane & 31)], tab + 64 * la, hh, hhp + ((4 * rt + c) * NU + ua) * 64 + lane, 6 * cg + la < NU);
    }
  }
#pragma unroll
  for (int c = 0; c < 2; ++c)
    g1_epilogue(acb[c], stl[32 * (cb + c) + (lane & 31)], tab + 64 * lb, hh, hhp + ((4 * rt + cb + c) * NU + ub) * 64 + lane, 6 * cg + lb < NU);
}

constexpr int G2_P = 64 * 272, G2_X = 4 * G2_P, K4_LDS = G2_X + 64 * 144;

__global__ __launch_bounds__(256, 1) void k4_gemm2(char* ws, u16* out) {
  extern __shared__ __attribute__((aligned(16))) char sm[];
  const int b = blockIdx.x, j = b >> 3;
  const int lt = 4 * (b & 7) + (j & 3), dt = j >> 2;
  const int t = threadIdx.x, w = __builtin_amdgcn_readfirstlane(t >> 6), lane = t & 63, hh = lane >> 5;
  const int s0 = w < 2 ? 22 * w : 44 + 21 * (w - 2);
  const u4* wp = (const u4*)(ws + WS_WDT) + (2 * dt * NU + s0) * 64 + lane;
  const u4* hp = (const u4*)(ws + WS_HH) + (2 * lt * NU + s0) * 64 + lane;
  const u16* x1t = (const u16*)(ws + WS_X1T);
  u4 xv[2];
#pragma unroll
  for (int i = 0; i < 2; ++i) xv[i] = *(const u4*)(x1t + (64 * dt + 32 * i + (t >> 3)) * L + 64 * lt + 8 * (t & 7));
  const int g = lane >> 4;
  const float* gbp = (const float*)(ws + WS_MOD) + 5 * D + 64 * dt + 8 * g;
  f4 gb[4];
#pragma unroll
  for (int i = 0; i < 4; ++i) gb[i] = *(const f4*)(gbp + 32 * (i >> 1) + 4 * (i & 1));
  u4 A[22][2], B[22][2];
#pragma unroll
  for (int s = 0; s < 22; ++s) {
    const int so = (s == 21 && w >= 2) ? 20 : s;
#pragma unroll
    for (int i = 0; i < 2; ++i) { A[s][i] = wp[(i * NU + so) * 64]; B[s][i] = hp[(i * NU + so) * 64]; }
  }
  __builtin_amdgcn_sched_barrier(0);
#pragma unroll
  for (int i = 0; i < 2; ++i) *(u4*)(sm + G2_X + (32 * i + (t >> 3)) * 144 + 16 * (t & 7)) = xv[i];
  f16v acc[2][2];
#pragma unroll
  for (int i = 0; i < 4; ++i)
#pragma unroll
    for (int e = 0; e < 16; ++e) acc[i >> 1][i & 1][e] = 0.f;
#pragma unroll
  for (int s = 0; s < 22; ++s) {
    if (s == 21 && w >= 2) break;
#pragma unroll
    for (int i = 0; i < 4; ++i) acc[i >> 1][i & 1] = mfma32(A[s][i >> 1], B[s][i & 1], acc[i >> 1][i & 1]);
  }
#pragma unroll
  for (int i = 0; i < 4; ++i)
#pragma unroll
    for (int e = 0; e < 4; ++e) {
      const f16v& a = acc[i >> 1][i & 1];
      *(f4*)(sm + w * G2_P + (32 * (i & 1) + (lane & 31)) * 272 + (32 * (i >> 1) + 8 * e + 4 * hh) * 4) = (f4){a[4 * e], a[4 * e + 1], a[4 * e + 2], a[4 * e + 3]};
    }
  __syncthreads();
  const int l = 16 * w + (lane & 15), q = (lane & 15) >> 2, p = lane & 3;
#pragma unroll
  for (int it = 0; it < 2; ++it) {
    const char* pp = sm + l * 272 + (32 * it + 8 * g) * 4;
    f4 y0 = *(const f4*)pp, y1 = *(const f4*)(pp + 16);
#pragma unroll
    for (int i = 1; i < 4; ++i) { y0 += *(const f4*)(pp + i * G2_P); y1 += *(const f4*)(pp + i * G2_P + 16); }
    const char* xa = sm + G2_X + (32 * it + 8 * g + q) * 144 + 32 * w + 8 * p;
    const u2 x0 = trd(xa), x1 = trd(xa + 4 * 144);
    const f4 g0 = gb[2 * it], g1 = gb[2 * it + 1];
    const u4 o = {pk(fmaf(g0[0], y0[0], blo(x0.x)), fmaf(g0[1], y0[1], bhi(x0.x))), pk(fmaf(g0[2], y0[2], blo(x0.y)), fmaf(g0[3], y0[3], bhi(x0.y))),
                  pk(fmaf(g1[0], y1[0], blo(x1.x)), fmaf(g1[1], y1[1], bhi(x1.x))), pk(fmaf(g1[2], y1[2], blo(x1.y)), fmaf(g1[3], y1[3], bhi(x1.y)))};
    *(u4*)(out + (64 * lt + l) * D + 64 * dt + 32 * it + 8 * g) = o;
  }
}

extern "C" void kernel_launch(void* const* d_in, const int* in_sizes, int n_in, void* d_out, int out_size, void* d_ws, size_t ws_size,
                              hipStream_t stream) {
  const u16* x = (const u16*)d_in[0];
  char* ws = (char*)d_ws;
  (void)hipFuncSetAttribute((const void*)k3_gemm1, hipFuncAttributeMaxDynamicSharedMemorySize, K3_LDS);
  (void)hipFuncSetAttribute((const void*)k4_gemm2, hipFuncAttributeMaxDynamicSharedMemorySize, K4_LDS);
  k1_prep<<<432, 256, 0, stream>>>(x, (const u16*)d_in[1], (const u16*)d_in[2], (const u16*)d_in[3], (const u16*)d_in[4], (const u16*)d_in[5],
                                   (const u16*)d_in[6], (const u16*)d_in[11], (const u16*)d_in[12], ws);
  k2_conv_prep<<<392, 256, 0, stream>>>((const u16*)d_in[8], (const u16*)d_in[9], (const u16*)d_in[10], ws);
  k3_gemm1<<<256, 256, K3_LDS, stream>>>(ws);
  k4_gemm2<<<256, 256, K4_LDS, stream>>>(ws, (u16*)d_out);
}
```

```cpp
#include <hip/hip_runtime.h>
#include <hip/hip_bf16.h>
#include <stdint.h>
#include <type_traits>

typedef unsigned short u16;
typedef unsigned u32;
typedef float f2 __attribute__((ext_vector_type(2)));
typedef float f4 __attribute__((ext_vector_type(4)));
typedef float f16v __attribute__((ext_vector_type(16)));
typedef u32 u2 __attribute__((ext_vector_type(2)));
typedef u32 u4 __attribute__((ext_vector_type(4)));
typedef u4 u4a8 __attribute__((aligned(8)));
typedef short s4 __attribute__((ext_vector_type(4)));
typedef __bf16 bf2 __attribute__((ext_vector_type(2)));
typedef __bf16 bf8 __attribute__((ext_vector_type(8)));
typedef _Float16 h2 __attribute__((ext_vector_type(2)));
typedef _Float16 h8 __attribute__((ext_vector_type(8)));
#define LAS __attribute__((address_space(3)))
#define DI __device__ __forceinline__

constexpr int L = 2048, D = 512, NM = 128, DF = 1376, NU = 86;
constexpr int KXS = 2056;

constexpr size_t WS_MOD = 0, WS_MEAN = 16 << 10, WS_SD = 20 << 10, WS_ST2 = 32 << 10, WS_CSB = 64 << 10, WS_XT = 1 << 20, WS_KX = 4 << 20, WS_X1T = 8 << 20,
                 WS_WT = 12 << 20, WS_WDT = 16 << 20, WS_HH = 20 << 20;

DI float blo(u32 v) { return __uint_as_float(v << 16); }
DI float bhi(u32 v) { return __uint_as_float(v & 0xffff0000u); }
DI float b2f(u16 v) { return __uint_as_float(((u32)v) << 16); }
DI u32 pk(float a, float b) { return __builtin_bit_cast(u32, __builtin_convertvector((f2){a, b}, bf2)); }
DI u32 pkh(f2 v) { return __builtin_bit_cast(u32, __builtin_convertvector(v, h2)); }
DI float rb(float a) { return blo(pk(a, a)); }
DI u2 trd(const char* p) { return __builtin_bit_cast(u2, __builtin_amdgcn_ds_read_tr16_b64_v4i16((LAS s4*)p)); }
DI u2 trd(u32 a) { return __builtin_bit_cast(u2, __builtin_amdgcn_ds_read_tr16_b64_v4i16((LAS s4*)(size_t)a)); }
DI u4 cat(u2 a, u2 b) { return (u4){a.x, a.y, b.x, b.y}; }
DI float dot2(u32 a, u32 b, float c) { return __builtin_amdgcn_fdot2_f32_bf16(__builtin_bit_cast(bf2, a), __builtin_bit_cast(bf2, b), c, false); }
DI f4 mfma16(u4 a, u4 b, f4 c) { return __builtin_amdgcn_mfma_f32_16x16x32_bf16(__builtin_bit_cast(bf8, a), __builtin_bit_cast(bf8, b), c, 0, 0, 0); }
DI f4 mfma16h(u4 a, u4 b, f4 c) { return __builtin_amdgcn_mfma_f32_16x16x32_f16(__builtin_bit_cast(h8, a), __builtin_bit_cast(h8, b), c, 0, 0, 0); }
DI f16v mfma32(u4 a, u4 b, f16v c) { return __builtin_amdgcn_mfma_f32_32x32x16_bf16(__builtin_bit_cast(bf8, a), __builtin_bit_cast(bf8, b), c, 0, 0, 0); }
#define CM_MUL(d, a, b) "v_pk_mul_f32 " d ", " a ", " b " op_sel_hi:[0,1]\n\t"
#define CM_FMA(d, a, b) "v_pk_fma_f32 " d ", " a ", " b ", " d " op_sel:[1,1,0] op_sel_hi:[1,0,1] neg_lo:[0,1,0]\n\t"
DI void cmul2(f2& r0, f2 a0, f2 b0, f2& r1, f2 a1, f2 b1) {
  asm(CM_MUL("%0", "%2", "%3") CM_MUL("%1", "%4", "%5") CM_FMA("%0", "%2", "%3") CM_FMA("%1", "%4", "%5")
      : "=&v"(r0), "=&v"(r1) : "v"(a0), "v"(b0), "v"(a1), "v"(b1));
}
DI void cmul3(f2& r0, f2 a0, f2 b0, f2& r1, f2 a1, f2 b1, f2& r2, f2 a2, f2 b2) {
  asm(CM_MUL("%0", "%3", "%4") CM_MUL("%1", "%5", "%6") CM_MUL("%2", "%7", "%8") CM_FMA("%0", "%3", "%4") CM_FMA("%1", "%5", "%6") CM_FMA("%2", "%7", "%8")
      : "=&v"(r0), "=&v"(r1), "=&v"(r2) : "v"(a0), "v"(b0), "v"(a1), "v"(b1), "v"(a2), "v"(b2));
}
DI void cmul4(f2& r0, f2& r1, f2& r2, f2& r3, f2 a0, f2 a1, f2 a2, f2 a3, f2 b) {
  asm(CM_MUL("%0", "%4", "%8") CM_MUL("%1", "%5", "%8") CM_MUL("%2", "%6", "%8") CM_MUL("%3", "%7", "%8")
      CM_FMA("%0", "%4", "%8") CM_FMA("%1", "%5", "%8") CM_FMA("%2", "%6", "%8") CM_FMA("%3", "%7", "%8")
      : "=&v"(r0), "=&v"(r1), "=&v"(r2), "=&v"(r3) : "v"(a0), "v"(a1), "v"(a2), "v"(a3), "v"(b));
}
DI float exp_acc(float x) {
  const float t = x * 1.44269502f, r = fmaf(x, 1.44269502f, -t) + x * 1.92596299e-8f, e = __builtin_amdgcn_exp2f(t);
  return fmaf(e, r * 0.693147182f, e);
}
template <int CTRL> DI float dppf(float v) { return __int_as_float(__builtin_amdgcn_update_dpp(0, __float_as_int(v), CTRL, 0xf, 0xf, true)); }
DI float sum32(float v) {
  auto r = __builtin_amdgcn_permlane32_swap(__float_as_uint(v), __float_as_uint(v), false, false);
  return __uint_as_float(r[0]) + __uint_as_float(r[1]);
}
DI f2 ln_stats(float s1, float s2) {
  const float mean = s1 * (1.f / D);
  const float var = fmaxf(fmaf(-mean, mean, s2 * (1.f / D)), 0.f);
  return (f2){rb(mean), rb(__builtin_amdgcn_sqrtf(rb(rb(var) + 1e-5f)))};
}
typedef __amdgpu_buffer_rsrc_t rsrc_t;
DI rsrc_t mkrsrc(const void* p, u32 bytes) { return __builtin_amdgcn_make_buffer_rsrc((void*)p, 0, bytes, 0x00020000); }
DI u32 ldsaddr(const char* p) { return (u32)(size_t)(LAS const char*)p; }
template <int OFF> DI void glds16(u32 m0v, u32 voff, rsrc_t rs, u32 soff) {
  asm volatile("s_mov_b32 m0, %0\n\ts_nop 0\n\tbuffer_load_dwordx4 %1, %2, %3 offen offset:%4 lds" :: "s"(m0v), "v"(voff), "s"(rs), "s"(soff), "n"(OFF) : "memory");
}
DI void glds16x4(u32 m0v, u32 voff, rsrc_t rs, u32 soff) {
  asm volatile("s_mov_b32 m0, %0\n\ts_nop 0\n\tbuffer_load_dwordx4 %1, %2, %3 offen lds\n\tbuffer_load_dwordx4 %1, %2, %3 offen offset:1024 lds\n\t"
               "buffer_load_dwordx4 %1, %2, %3 offen offset:2048 lds\n\tbuffer_load_dwordx4 %1, %2, %3 offen offset:3072 lds"
               :: "s"(m0v), "v"(voff), "s"(rs), "s"(soff) : "memory");
}
template <int OFF> DI void bld16(u4& d, u32 voff, rsrc_t rs, u32 soff) {
  asm volatile("buffer_load_dwordx4 %0, %1, %2, %3 offen offset:%4" : "=v"(d) : "v"(voff), "s"(rs), "s"(soff), "n"(OFF));
}
template <int N> DI void vmwait() { asm volatile("s_waitcnt vmcnt(%0)" :: "n"(N) : "memory"); }
template <int N> DI void vmwait(u4& d) { asm volatile("s_waitcnt vmcnt(%1)" : "+v"(d) : "n"(N)); }
DI void bar() { asm volatile("s_waitcnt lgkmcnt(0)\n\ts_barrier" ::: "memory"); }
#define SB() __builtin_amdgcn_sched_barrier(0)
#define SGB(m, n) __builtin_amdgcn_sched_group_barrier(m, n, 0)
template <int I, int N, class F> DI void sfor(F&& f) { if constexpr (I < N) { f(std::integral_constant<int, I>{}); sfor<I + 1, N>(f); } }

constexpr int KG_ROW = 544, KG_CH = 57 * KG_ROW, KG_T = 2 * KG_CH, KG_TS = 4160, K1_LDS = KG_T + 2 * KG_TS;

DI f2 cexp_pow2(float a, float fh, float fl, float p) {
  const float e = __builtin_amdgcn_exp2f(a * p);
  const float r = fmaf(fl, p, __builtin_amdgcn_fractf(fh * p));
  return (f2){e * __builtin_amdgcn_cosf(r), e * __builtin_amdgcn_sinf(r)};
}

DI void k1_kgen(char* sm, int b, const u16* lar, const u16* aim, const u16* cre, const u16* cim, const u16* ldt, const u16* dsk, u16* kx) {
  const int t = threadIdx.x, w = __builtin_amdgcn_readfirstlane(t >> 6), lane = t & 63, ch = w >> 1, half = w & 1;
  const int d0 = 64 * (b & 7) + 2 * (b >> 3), d = d0 + ch, n = 64 * half + lane;
  const u32 v_lar = lar[d * NM + n], v_aim = aim[d * NM + n];
  const u32 cr = cre[d * NM + n], ci = cim[d * NM + n], cr2 = cre[d * NM + (n ^ 64)], ci2 = cim[d * NM + (n ^ 64)];
  const float dt = rb(exp_acc(b2f(ldt[d])));
  const float ds = b2f(dsk[d]);
  const float zr = -rb(exp_acc(blo(v_lar))) * dt, zi = blo(v_aim) * dt;
  const float a = zr * 1.44269502f;
  const float fh = zi * 0.159154937f, fl = fmaf(zi, 0.159154937f, -fh) + zi * 6.42063833e-9f;
  const f2 e1 = cexp_pow2(a, fh, fl, 1.f), e8 = cexp_pow2(a, -fh, -fl, 8.f), e128 = cexp_pow2(a, fh, fl, 128.f), e1024 = cexp_pow2(a, fh, fl, 1024.f);
  int mx = (int)max(max(cr & 0x7fffu, ci & 0x7fffu), max(cr2 & 0x7fffu, ci2 & 0x7fffu));
  mx = max(mx, __builtin_amdgcn_update_dpp(0, mx, 0x111, 0xf, 0xf, true));
  mx = max(mx, __builtin_amdgcn_update_dpp(0, mx, 0x112, 0xf, 0xf, true));
  mx = max(mx, __builtin_amdgcn_update_dpp(0, mx, 0x114, 0xf, 0xf, true));
  mx = max(mx, __builtin_amdgcn_update_dpp(0, mx, 0x118, 0xf, 0xf, true));
  mx = max(max(__builtin_amdgcn_readlane(mx, 15), __builtin_amdgcn_readlane(mx, 31)), max(__builtin_amdgcn_readlane(mx, 47), __builtin_amdgcn_readlane(mx, 63)));
  const int ex = min(max(mx >> 7, 1), 253);
  const float cs = __int_as_float((254 - ex) << 23), kf = 2.f * dt * __int_as_float(ex << 23);
  if (t < 2) *(u4*)(kx + (d0 + t) * KXS) = (u4){0u, 0u, 0u, 0u};
  constexpr u32 KEEP = 0x03020100u, LL = 0x01000100u, LH = 0x05040100u, HH = 0x05040504u;
  const u32 sel0 = t < 32 ? KEEP : LL, sel1 = t < 32 ? KEEP : t < 128 ? LH : LL;
  const u32 sel2 = t < 32 ? KEEP : t < 128 ? LL : (t & 1) ? HH : LH, sel3 = t < 32 ? KEEP : t < 64 ? LH : HH;
  char* base = sm + ch * KG_CH + n * 4;
  auto put = [&](int row, f2 v) { *(u32*)(base + row * KG_ROW) = pkh(v); };
  f2 lo[8], e[8], m[8], x0, x1, x2, x3, e16, e32, e64;
  lo[0] = (f2){blo(cr) * cs, -blo(ci) * cs};
  e[1] = e1; m[1] = e128;
  put(0, lo[0]); put(16, (f2){1.f, 0.f}); put(17, e1); put(24, e128); put(48, e1024);
  cmul3(e16, e8, e8, e[2], e1, e1, m[2], e128, e128);
  cmul2(lo[1], lo[0], e8, x0, e1024, e1024);
  put(18, e[2]); put(32, m[2]); put(1, lo[1]); put(56, x0);
  cmul3(e32, e16, e16, e[4], e[2], e[2], m[4], m[2], m[2]);
  cmul2(e[3], e[2], e1, m[3], m[2], e128);
  cmul2(lo[2], lo[0], e16, lo[3], lo[1], e16);
  put(20, e[4]); put(40, m[4]); put(19, e[3]); put(36, m[3]); put(2, lo[2]); put(3, lo[3]);
  cmul4(e[5], e[6], e[7], x0, e1, e[2], e[3], m[4], e[4]);
  put(21, e[5]); put(22, e[6]); put(23, e[7]); put(41, x0);
  cmul3(m[5], m[4], e128, m[6], m[4], m[2], m[7], m[4], m[3]);
  put(42, m[5]); put(44, m[6]); put(46, m[7]);
  cmul4(lo[4], lo[5], lo[6], lo[7], lo[0], lo[1], lo[2], lo[3], e32);
  put(4, lo[4]); put(5, lo[5]); put(6, lo[6]); put(7, lo[7]);
  cmul2(e64, e32, e32, x0, e128, e1);
  put(25, x0);
  cmul3(x0, m[5], e[4], x1, m[6], e[4], x2, m[7], e[4]);
  put(43, x0); put(45, x1); put(47, x2);
  cmul4(x0, x1, x2, x3, lo[0], lo[1], lo[2], lo[3], e64);
  put(8, x0); put(9, x1); put(10, x2); put(11, x3);
  cmul4(x0, x1, x2, x3, lo[4], lo[5], lo[6], lo[7], e64);
  put(12, x0); put(13, x1); put(14, x2); put(15, x3);
  cmul4(x0, x1, x2, x3, e[2], e[3], e[4], e[5], e128);
  put(26, x0); put(27, x1); put(28, x2); put(29, x3);
  cmul2(x0, e[6], e128, x1, e[7], e128);
  put(30, x0); put(31, x1);
  cmul3(x0, e[2], m[2], x1, e[4], m[2], x2, e[6], m[2]);
  put(33, x0); put(34, x1); put(35, x2);
  cmul3(x0, e[2], m[3], x1, e[4], m[3], x2, e[6], m[3]);
  put(37, x0); put(38, x1); put(39, x2);
  cmul4(x0, x1, x2, x3, m[1], m[2], m[3], m[4], e1024);
  put(49, x0); put(50, x1); put(51, x2); put(52, x3);
  cmul3(x0, m[5], e1024, x1, m[6], e1024, x2, m[7], e1024);
  put(53, x0); put(54, x1); put(55, x2);
  __syncthreads();
  {
    const int c = lane & 15, g = lane >> 4;
    const char* fb = sm + ch * KG_CH + c * KG_ROW + g * 16;
    u16* T = (u16*)(sm + KG_T + ch * KG_TS);
    f4 acc0 = {0.f, 0.f, 0.f, 0.f}, acc1 = acc0;
    u4 B[8], A[8];
#pragma unroll
    for (int s = 0; s < 8; ++s) { B[s] = *(const u4*)(fb + s * 64); A[s] = *(const u4*)(fb + (16 + 16 * half) * KG_ROW + s * 64); }
    if (!half) {
      u4 A2[8];
#pragma unroll
      for (int s = 0; s < 8; ++s) A2[s] = *(const u4*)(fb + 48 * KG_ROW + s * 64);
      SB();
#pragma unroll
      for (int s = 0; s < 8; ++s) acc0 = mfma16h(A[s], B[s], acc0);
#pragma unroll
      for (int s = 0; s < 8; ++s) acc1 = mfma16h(A2[s], B[s], acc1);
      acc0 *= kf; acc1 *= kf;
      if (lane == 0) acc0[0] += ds;
      *(u2*)(T + 8 * c + 4 * (g & 1) + 128 * (g >> 1)) = (u2){pk(acc0[0], acc0[1]), pk(acc0[2], acc0[3])};
      if (g < 2) {
#pragma unroll
        for (int e = 0; e < 4; ++e) T[1024 + 128 * (4 * g + e) + 8 * c] = (u16)pk(acc1[e], 0.f);
      } else if (lane == 32) T[2048] = (u16)pk(acc1[0], 0.f);
    } else {
      SB();
#pragma unroll
      for (int s = 0; s < 8; ++s) acc0 = mfma16h(A[s], B[s], acc0);
      acc0 *= kf;
#pragma unroll
      for (int e = 0; e < 4; ++e) {
        const int p = g < 2 ? 256 + 128 * g + 2 * e : 512 + 128 * (2 * (g - 2) + (e >> 1)) + 4 * (e & 1);
        T[p + 8 * c] = (u16)pk(acc0[e], 0.f);
      }
    }
  }
  __syncthreads();
  u4 v[2];
  u32 e8v[2];
#pragma unroll
  for (int c2 = 0; c2 < 2; ++c2) {
    const u16* T = (const u16*)(sm + KG_T + c2 * KG_TS);
    v[c2] = *(const u4*)(T + 8 * t);
    e8v[c2] = T[8 * t + 8];
  }
#pragma unroll
  for (int c2 = 0; c2 < 2; ++c2) {
    const u4 o = {__builtin_amdgcn_perm(v[c2].x, v[c2].x, sel0), __builtin_amdgcn_perm(v[c2].z, t < 64 ? v[c2].y : v[c2].x, sel1),
                  __builtin_amdgcn_perm(e8v[c2], t < 128 ? v[c2].z : v[c2].x, sel2), __builtin_amdgcn_perm(e8v[c2], v[c2].w, sel3)};
    *(u4*)(kx + (d0 + c2) * KXS + 8 + 8 * t) = o;
  }
}

DI void k1_xt(char* sm, int b, const u16* x, u16* xt) {
  const int t = threadIdx.x, w = t >> 6, lane = t & 63;
  const int c0 = 64 * (b & 7), r0 = 128 * (b >> 3);
  u4 v[4];
#pragma unroll
  for (int i = 0; i < 4; ++i) v[i] = *(const u4*)(x + (r0 + 32 * i + (t >> 3)) * D + c0 + 8 * (t & 7));
#pragma unroll
  for (int i = 0; i < 4; ++i) *(u4*)(sm + (32 * i + (t >> 3)) * 144 + 16 * (t & 7)) = v[i];
  __syncthreads();
  const int g = lane >> 4, q = (lane & 15) >> 2, p = lane & 3;
#pragma unroll
  for (int i = 0; i < 4; ++i) {
    const char* a = sm + (32 * i + 8 * g + q) * 144 + 32 * w + 8 * p;
    *(u4*)(xt + (c0 + 16 * w + (lane & 15)) * L + r0 + 32 * i + 8 * g) = cat(trd(a), trd(a + 4 * 144));
  }
}

DI void k1_stats(int b, const u16* x, char* ws) {
  const int t = threadIdx.x, row = 32 * b + (t >> 3);
  u4 v[8];
#pragma unroll
  for (int i = 0; i < 8; ++i) v[i] = *(const u4*)(x + row * D + 64 * i + 8 * (t & 7));
  float s1 = 0.f, s2 = 0.f, s3 = 0.f, s4 = 0.f;
#pragma unroll
  for (int i = 0; i < 8; ++i)
#pragma unroll
    for (int j = 0; j < 4; j += 2) {
      s1 = dot2(v[i][j], 0x3f803f80u, s1); s2 = dot2(v[i][j], v[i][j], s2);
      s3 = dot2(v[i][j + 1], 0x3f803f80u, s3); s4 = dot2(v[i][j + 1], v[i][j + 1], s4);
    }
  s1 += s3; s2 += s4;
  s1 += dppf<0xb1>(s1); s2 += dppf<0xb1>(s2); s1 += dppf<0x4e>(s1); s2 += dppf<0x4e>(s2); s1 += dppf<0x141>(s1); s2 += dppf<0x141>(s2);
  const f2 st = ln_stats(s1, s2);
  if ((t & 7) == 0) {
    ((u16*)(ws + WS_MEAN))[row] = (u16)(__float_as_uint(st.x) >> 16);
    ((u16*)(ws + WS_SD))[row] = (u16)(__float_as_uint(st.y) >> 16);
    ((u32*)(ws + WS_ST2))[row] = 0u;
  }
}

DI void k1_mod(char* sm, int b, const u16* c, const u16* wm, float* mod) {
  const int t = threadIdx.x, rg = t >> 3;
  u4 v[16];
  const u32 cc = ((const u32*)c)[t];
#pragma unroll
  for (int i = 0; i < 16; ++i) v[i] = *(const u4*)(wm + (16 * rg + i) * (6 * D) + 64 * b + 8 * (t & 7));
  u32* sl = (u32*)(sm + 32 * 68 * 4);
  {
    const float c0 = blo(cc), c1 = bhi(cc);
    sl[t] = pk(c0 / (1.f + __expf(-c0)), c1 / (1.f + __expf(-c1)));
  }
  __syncthreads();
  const u4 sa = *(const u4*)(sl + 8 * rg), sb = *(const u4*)(sl + 8 * rg + 4);
  float acc[8] = {0.f, 0.f, 0.f, 0.f, 0.f, 0.f, 0.f, 0.f};
#pragma unroll
  for (int i = 0; i < 8; ++i) {
    const u32 sp = i < 4 ? sa[i & 3] : sb[i & 3];
#pragma unroll
    for (int j = 0; j < 4; ++j) {
      acc[2 * j] = dot2(__builtin_amdgcn_perm(v[2 * i + 1][j], v[2 * i][j], 0x05040100u), sp, acc[2 * j]);
      acc[2 * j + 1] = dot2(__builtin_amdgcn_perm(v[2 * i + 1][j], v[2 * i][j], 0x07060302u), sp, acc[2 * j + 1]);
    }
  }
  float* red = (float*)sm;
  *(f4*)(red + rg * 68 + 8 * (t & 7)) = (f4){acc[0], acc[1], acc[2], acc[3]};
  *(f4*)(red + rg * 68 + 8 * (t & 7) + 4) = (f4){acc[4], acc[5], acc[6], acc[7]};
  __syncthreads();
  float s = 0.f;
#pragma unroll
  for (int i = 0; i < 8; ++i) s += red[((t & 3) + 4 * i) * 68 + (t >> 2)];
  s += dppf<0xb1>(s); s += dppf<0x4e>(s);
  if ((t & 3) == 0) mod[64 * b + (t >> 2)] = rb(s);
}

__global__ __launch_bounds__(256, 2) void k1_prep(const u16* x, const u16* lar, const u16* aim, const u16* cre, const u16* cim, const u16* ldt,
                                                  const u16* dsk, const u16* c, const u16* wm, char* ws) {
  __shared__ __attribute__((aligned(16))) char sm[K1_LDS];
  const int b = blockIdx.x;
  if (b < 256) k1_kgen(sm, b, lar, aim, cre, cim, ldt, dsk, (u16*)(ws + WS_KX));
  else if (b < 384) k1_xt(sm, b - 256, x, (u16*)(ws + WS_XT));
  else if (b < 448) k1_stats(b - 384, x, ws);
  else k1_mod(sm, b - 448, c, wm, (float*)(ws + WS_MOD));
}

constexpr int CV_KC = 4128, CV_H = 8 * CV_KC, CV_HP = 1152, CV_X = CV_H + 8 * CV_HP, CV_EX = CV_X + 8192, K2_LDS = CV_EX + 16384;

DI void k2_conv(char* sm, int b, char* ws) {
  const int t = threadIdx.x, w = __builtin_amdgcn_readfirstlane(t >> 6), lane = t & 63, ch = w >> 1, half = w & 1;
  const int d0 = 64 * (b & 7) + 2 * (b >> 3), d = d0 + ch;
  const u16* xt = (const u16*)(ws + WS_XT);
  const u16* kx = (const u16*)(ws + WS_KX);
  const float* mod = (const float*)(ws + WS_MOD);
  const int c = lane & 15, g = lane >> 4;
  u4 xv[2], kv[2];
  u2 kp[2];
  const u4 mv = *(const u4*)(ws + WS_MEAN + 16 * t), sv = *(const u4*)(ws + WS_SD + 16 * t);
#pragma unroll
  for (int i = 0; i < 2; ++i) xv[i] = *(const u4*)(xt + (d0 + i) * L + 8 * t);
#pragma unroll
  for (int i = 0; i < 2; ++i) { kv[i] = *(const u4*)(kx + (d0 + i) * KXS + 8 + 8 * t); kp[i] = *(const u2*)(kx + (d0 + i) * KXS + 4 + 8 * t); }
  const float ga = mod[2 * D + d];
  const f2 ca = *(const f2*)(mod + D + d0), sa = *(const f2*)(mod + d0);
  SB();
  if (t < 16) *(u4*)(sm + (t >> 1) * CV_KC + 16 * (t & 1)) = (u4){0u, 0u, 0u, 0u};
  if (w == 1) *(u4*)(sm + CV_H + (lane >> 3) * CV_HP + 16 * (t & 7)) = (u4){0u, 0u, 0u, 0u};
  float rs[8];
#pragma unroll
  for (int j = 0; j < 8; ++j) rs[j] = __builtin_amdgcn_rcpf((j & 1) ? bhi(sv[j >> 1]) : blo(sv[j >> 1]));
#pragma unroll
  for (int i = 0; i < 2; ++i) {
    const float c1 = rb(1.f + ca[i]);
    float h[8];
#pragma unroll
    for (int j = 0; j < 8; ++j) {
      const float xx = (j & 1) ? bhi(xv[i][j >> 1]) : blo(xv[i][j >> 1]);
      const float mm = (j & 1) ? bhi(mv[j >> 1]) : blo(mv[j >> 1]);
      h[j] = fmaf((xx - mm) * rs[j], c1, sa[i]);
    }
#pragma unroll
    for (int r = 0; r < 4; ++r) *(u32*)(sm + CV_H + (4 * i + r) * CV_HP + 128 + 4 * t) = pk(h[r], h[r + 4]);
    *(u4*)(sm + CV_X + 4096 * i + 16 * t) = xv[i];
  }
#pragma unroll
  for (int i = 0; i < 2; ++i) {
    const u4 v = kv[i];
    const u32 a0 = __builtin_amdgcn_alignbit(kp[i].y, kp[i].x, 16), a1 = __builtin_amdgcn_alignbit(v.x, kp[i].y, 16), a2 = __builtin_amdgcn_alignbit(v.y, v.x, 16),
              a3 = __builtin_amdgcn_alignbit(v.z, v.y, 16), a4 = __builtin_amdgcn_alignbit(v.w, v.z, 16);
    char* kd = sm + 4 * i * CV_KC + 32 + 16 * t;
    *(u4*)kd = v;
    *(u4*)(kd + CV_KC) = (u4){a1, a2, a3, a4};
    *(u4*)(kd + 2 * CV_KC) = (u4){kp[i].y, v.x, v.y, v.z};
    *(u4*)(kd + 3 * CV_KC) = (u4){a0, a1, a2, a3};
  }
  __syncthreads();
  f4 acc[8];
#pragma unroll
  for (int i = 0; i < 8; ++i) acc[i] = (f4){0.f, 0.f, 0.f, 0.f};
  const char* ka = sm + (4 * ch + 2 * half) * CV_KC + 32 + 8 * (8 * g + (lane & 3) - ((lane & 15) >> 2));
  const char* hb = sm + CV_H + (4 * ch + 2 * half) * CV_HP + 128 + 8 * c - 16 * g - 8;
  u4 A[2][8], B[2][8];
  auto ldgrp = [&](int grp, int f) {
    const int rho = grp >> 1, par = grp & 1, i = f & 7;
    if (f < 8) {
      const char* pa = ka + rho * CV_KC + 256 * (2 * i + par);
      A[grp & 1][i] = cat(trd(pa + 32), trd(pa));
    } else {
      B[grp & 1][i] = *(const u4a8*)(hb + rho * CV_HP + 64 * (2 * i - par));
    }
  };
  constexpr int ord[36] = {6, 7, 4, 5, 14, 15, 12, 2, 22, 3, 23, 20, 13, 30, 31, 28, 1, 21, 38, 0, 39, 29, 10, 46, 47, 11, 18, 37, 55, 54, 19, 9, 63, 45, 36, 27};
  constexpr int lord[16] = {0, 14, 15, 12, 13, 1, 11, 10, 2, 3, 9, 4, 8, 5, 6, 7};
#pragma unroll
  for (int i = 0; i < 16; ++i) ldgrp(0, lord[i]);
  SB();
#pragma unroll
  for (int grp = 0; grp < 4; ++grp) {
    if (grp < 3) {
#pragma unroll
      for (int i = 0; i < 16; ++i) ldgrp(grp + 1, lord[i]);
    }
#pragma unroll
    for (int m = 0; m < 36; ++m) {
      const int i = ord[m] >> 3, tt = ord[m] & 7;
      acc[tt] = mfma16(A[grp & 1][i], B[grp & 1][tt - i], acc[tt]);
    }
    if (grp < 3) {
#pragma unroll
      for (int i = 0; i < 24; ++i) { SGB(0x8, 1); SGB(0x100, 1); }
      SGB(0x8, 12);
    }
    SB();
  }
  f4* ex = (f4*)(sm + CV_EX);
  if (half) {
#pragma unroll
    for (int i = 0; i < 4; ++i) { ex[(w * 4 + i) * 64 + lane] = acc[i]; acc[i] = acc[4 + i]; }
  } else {
#pragma unroll
    for (int i = 0; i < 4; ++i) ex[(w * 4 + i) * 64 + lane] = acc[4 + i];
  }
  __syncthreads();
  u16* x1t = (u16*)(ws + WS_X1T);
#pragma unroll
  for (int i = 0; i < 4; ++i) {
    const int l = 256 * (4 * half + i) + 16 * c + 4 * g;
    const f4 y = acc[i] + ex[((w ^ 1) * 4 + i) * 64 + lane];
    const u2 xr = *(const u2*)(sm + CV_X + 4096 * ch + 2 * l);
    const u2 o = {pk(fmaf(ga, y[0], blo(xr.x)), fmaf(ga, y[1], bhi(xr.x))), pk(fmaf(ga, y[2], blo(xr.y)), fmaf(ga, y[3], bhi(xr.y)))};
    *(u2*)(x1t + d * L + l) = o;
  }
}

DI void k2_wprep(char* sm, int b, const u16* wg, const u16* wu, char* ws) {
  if (b >= 86) return;
  const int t = threadIdx.x, w = t >> 6, lane = t & 63;
  const int up = b >> 1, kh = b & 1;
  const float* mod = (const float*)(ws + WS_MOD);
  u4 v[8];
  const float cbv = mod[4 * D + 256 * kh + t], sbv = mod[3 * D + 256 * kh + t];
#pragma unroll
  for (int i = 0; i < 8; ++i) v[i] = *(const u4*)(((i & 1) ? wu : wg) + (256 * kh + 64 * (i >> 1) + (t >> 2)) * DF + 32 * up + 8 * (t & 3));
  SB();
  float* tc = (float*)(sm + 256 * 144);
  u16* tv = (u16*)(sm + 256 * 144 + 1024);
  const float c1 = rb(1.f + cbv);
  tc[t] = c1;
  tv[t] = (u16)(__float_as_uint(c1) >> 16);
  tv[256 + t] = (u16)(__float_as_uint(sbv) >> 16);
#pragma unroll
  for (int i = 0; i < 8; ++i) *(u4*)(sm + (64 * (i >> 1) + (t >> 2)) * 144 + 64 * (i & 1) + 16 * (t & 3)) = v[i];
  __syncthreads();
  const int ul = w & 1, kq = w >> 1, hh = lane >> 5, mat = (lane >> 4) & 1, q = (lane & 15) >> 2, p = lane & 3;
  f16v sums;
#pragma unroll
  for (int e = 0; e < 16; ++e) sums[e] = 0.f;
  u16* wt = (u16*)(ws + WS_WT);
#pragma unroll
  for (int s = 0; s < 8; ++s) {
    const int k0 = 128 * kq + 16 * s + 8 * hh;
    const char* a = sm + (k0 + q) * 144 + 64 * mat + 32 * ul + 8 * p;
    const u4 raw = cat(trd(a), trd(a + 4 * 144));
    const f4 t0 = *(const f4*)(tc + k0), t1 = *(const f4*)(tc + k0 + 4);
    sums = mfma32(*(const u4*)(tv + 256 * (lane & 1) + k0), raw, sums);
    u4 o;
#pragma unroll
    for (int j = 0; j < 4; ++j) {
      float lo = blo(raw[j]) * (j < 2 ? t0[2 * j] : t1[2 * j - 4]), hi = bhi(raw[j]) * (j < 2 ? t0[2 * j + 1] : t1[2 * j - 3]);
      asm("" : "+v"(lo));
      o[j] = pk(lo, hi);
    }
    *(u4*)(wt + (((2 * up + ul) * 32 + 16 * kh + 8 * kq + s) * 64 + lane) * 8) = o;
  }
  float* red = (float*)(sm + 256 * 144 + 2048);
  if (lane < 32) { red[64 * w + lane] = sums[0]; red[64 * w + 32 + lane] = sums[1]; }
  __syncthreads();
  if (t < 128) {
    const float v = red[t] + red[128 + t], hi = rb(v);
    ((u32*)(ws + WS_CSB))[((2 * up + (t >> 6)) * 64 + (t & 63)) * 2 + kh] = pk(hi, v - hi);
  }
}

DI void k2_dprep(char* sm, int b, const u16* wd, char* ws) {
  if (b >= 43) return;
  const int t = threadIdx.x, w = t >> 6, lane = t & 63;
  u4 v[8];
#pragma unroll
  for (int i = 0; i < 8; ++i) v[i] = *(const u4*)(wd + (32 * b + 4 * i + w) * D + 8 * lane);
#pragma unroll
  for (int i = 0; i < 8; ++i) *(u4*)(sm + (4 * i + w) * 1040 + 16 * lane) = v[i];
  __syncthreads();
  const int hh = lane >> 5, blk = (lane >> 4) & 1, q = (lane & 15) >> 2, p = lane & 3;
  u16* wdt = (u16*)(ws + WS_WDT);
#pragma unroll
  for (int i = 0; i < 8; ++i) {
    const int dt = 4 * w + (i >> 1), s = i & 1;
    const char* a = sm + (16 * s + 4 * hh + q) * 1040 + 64 * dt + 32 * blk + 8 * p;
    *(u4*)(wdt + ((dt * NU + 2 * b + s) * 64 + lane) * 8) = cat(trd(a), trd(a + 8 * 1040));
  }
}

__global__ __launch_bounds__(256, 2) void k2_conv_prep(const u16* wg, const u16* wu, const u16* wd, char* ws) {
  __shared__ __attribute__((aligned(16))) char sm[K2_LDS];
  const int b = blockIdx.x;
  if (b < 256) k2_conv(sm, b, ws);
  else if (b < 344) k2_wprep(sm, b - 256, wg, wu, ws);
  else k2_dprep(sm, b - 344, wd, ws);
}

constexpr int K3_LDS = 131072;
constexpr int G1_WIN = 8;
constexpr int G1_W0 = 5;
constexpr int G1_X0 = 10;
constexpr int G1_NSINGLE = 3;
constexpr int g1_nw(int s) { return G1_W0 + s < 32 ? G1_W0 + s : 32; }

template <int K> DI u32 g1_pair(const f16v& acc, float rs, float sd) {
  const f2 g = (f2){acc[2 * K], acc[2 * K + 1]} * rs;
  const f2 z = g * (g * g * -0.10294324f + -2.30220819f);
  const f2 dn = (f2){__builtin_amdgcn_exp2f(z.x), __builtin_amdgcn_exp2f(z.y)} * sd + sd;
  const f2 r = g * (f2){acc[8 + 2 * K], acc[9 + 2 * K]} * (f2){__builtin_amdgcn_rcpf(dn.x), __builtin_amdgcn_rcpf(dn.y)};
  return pk(r.x, r.y);
}
DI u4 g1_tile(const f16v& acc, float rs, float sd) { return (u4){g1_pair<0>(acc, rs, sd), g1_pair<1>(acc, rs, sd), g1_pair<2>(acc, rs, sd), g1_pair<3>(acc, rs, sd)}; }
#define R8(op) op(0) op(1) op(2) op(3) op(4) op(5) op(6) op(7)
#define OUT8(c, a, o) c(a[o]), c(a[o + 1]), c(a[o + 2]), c(a[o + 3]), c(a[o + 4]), c(a[o + 5]), c(a[o + 6]), c(a[o + 7])
DI u4 g1_tile_single(const f16v& acc, float rs, float sd, float c0) {
  float g[8], q[8];
#define Q0(i) "v_mul_f32 %1" #i ", %18, %2" #i "\n\t"
#define Q1(i) "v_mul_f32 %" #i ", %1" #i ", %1" #i "\n\t"
#define Q2(i) "v_fmamk_f32 %" #i ", %" #i ", 0xbdd2d3e8, %19\n\t"
#define Q3(i) "v_mul_f32 %" #i ", %1" #i ", %" #i "\n\t"
#define Q4(i) "v_exp_f32 %" #i ", %" #i "\n\t"
  float d0, d1;
  asm("s_nop 0\n\t" R8(Q0) R8(Q1) R8(Q2) R8(Q3) R8(Q4)
      : OUT8("=&v", q, 0), "=v"(d0), "=v"(d1), OUT8("=&v", g, 0) : "v"(rs), "v"(c0), OUT8("v", acc, 0));
#define Q5(i) "v_mul_f32 %1" #i ", %1" #i ", %2" #i "\n\t"
#define Q6(i) "v_fma_f32 %" #i ", %" #i ", %18, %18\n\t"
#define Q7(i) "v_rcp_f32 %" #i ", %" #i "\n\t"
#define Q8(i) "v_mul_f32 %" #i ", %" #i ", %1" #i "\n\t"
  asm(R8(Q5) R8(Q6) R8(Q7) R8(Q8) : OUT8("+v", q, 0), "=v"(d0), "=v"(d1), OUT8("+v", g, 0) : "v"(sd), "v"(sd), OUT8("v", acc, 8));
  return (u4){pk(q[0], q[1]), pk(q[2], q[3]), pk(q[4], q[5]), pk(q[6], q[7])};
}
DI void st2_request(u32& d, u32 voff, rsrc_t rs, u32 soff) { asm volatile("buffer_load_dword %0, %1, %2, %3 offen sc1" : "=v"(d) : "v"(voff), "s"(rs), "s"(soff)); }
template <int N> DI void st2_wait(u32 (&v)[N], const u32* words) {
#pragma unroll
  for (int c = 0; c < N; ++c) asm volatile("s_waitcnt vmcnt(0)" : "+v"(v[c]));
#pragma unroll 1
  for (int spin = 0; spin < (1 << 16); ++spin) {
    bool ok = true;
#pragma unroll
    for (int c = 0; c < N; ++c) ok = ok && v[c] != 0u;
    if (__all(ok)) break;
    __builtin_amdgcn_s_sleep(2);
#pragma unroll
    for (int c = 0; c < N; ++c) v[c] = __hip_atomic_load(words + 32 * c, __ATOMIC_RELAXED, __HIP_MEMORY_SCOPE_AGENT);
  }
}
DI f16v g1_shift(u2 ex, u32 sv, int hh, f16v acc) {
  const u32 bv = hh ? __builtin_amdgcn_perm(sv, sv, 0x03020302u) : (__builtin_amdgcn_perm(sv, sv, 0x01000100u) ^ 0x80008000u);
  return __builtin_amdgcn_mfma_f32_32x32x8bf16_1k(__builtin_bit_cast(s4, ex), __builtin_bit_cast(s4, (u2){bv, bv}), acc, 0, 0, 0);
}

__global__ __launch_bounds__(512, 2) void k3_gemm1(char* ws) {
  extern __shared__ __attribute__((aligned(16))) char sm[];
  const int b = blockIdx.x, j = b >> 3;
#define G1_MAP 1
  const int rt = G1_MAP == 0 ? 2 * (b & 7) + (j & 1) : G1_MAP == 1 ? 4 * ((b & 7) >> 1) + (j & 3) : 8 * ((b & 7) >> 2) + (j & 7);
  const int cg = (G1_MAP == 0 ? (j >> 1) : G1_MAP == 1 ? 2 * (j >> 2) + (b & 1) : 4 * (j >> 3) + (b & 3)) - 1;
  const int t = threadIdx.x, w8 = __builtin_amdgcn_readfirstlane(t >> 6), w = w8 & 3, lane = t & 63, hh = lane >> 5;
  const rsrc_t rw = mkrsrc(ws + WS_WT, NU * 32768), rs2 = mkrsrc(ws + WS_ST2, L * 4);
  const u32 lb0 = ldsaddr(sm), vw = lane * 16;
  const int blk = (lane >> 4) & 1, q = (lane & 15) >> 2, p = lane & 3;
  auto raddr = [&](int tile, int tt) -> u32 {
    return lb0 + 256 * (8 * hh + 4 * tt + q) + 8 * (p & 1) + 16 * ((4 * tile + 2 * blk + (p >> 1)) ^ ((q << 2) | (2 * hh + tt)));
  };
  u32* st2 = (u32*)(ws + WS_ST2) + 128 * rt + (lane & 31);
  const int u = 6 * cg + (w >> 1) * 3 + (w8 < 4 ? 2 * (w & 1) : 1);
  u4* ho = (u4*)(ws + WS_HH) + (4 * rt * NU + u) * 64 + lane;
  if (w8 < 4) {
    if (cg < 0) {
      u32 rs0 = raddr(w, 0), rs1 = raddr(w, 1);
      float s1a = 0.f, s1b = 0.f, s2a = 0.f, s2b = 0.f;
      u2 f[2][8];
      auto rd = [&](int k) {
        asm volatile("s_barrier" ::: "memory");
#pragma unroll
        for (int i = 0; i < 4; ++i) { f[k][2 * i] = trd(rs0 + i * 4096); f[k][2 * i + 1] = trd(rs1 + i * 4096); }
        rs0 += 16384; rs1 += 16384;
      };
      auto add = [&](int k) {
#pragma unroll
        for (int i = 0; i < 8; ++i) {
          s1a = dot2(f[k][i].x, 0x3f803f80u, s1a); s1b = dot2(f[k][i].y, 0x3f803f80u, s1b);
          s2a = dot2(f[k][i].x, f[k][i].x, s2a); s2b = dot2(f[k][i].y, f[k][i].y, s2b);
        }
      };
      rd(0);
#pragma unroll 1
      for (int c = 0; c < 3; ++c) { rd(1); SB(); add(0); SB(); rd(0); SB(); add(1); SB(); }
      rd(1); SB(); add(0); add(1);
      const f2 ms = ln_stats(sum32(s1a + s1b), sum32(s2a + s2b));
      if (lane < 32) __hip_atomic_store(st2 + 32 * w, (__float_as_uint(ms.x) >> 16) | __float_as_uint(ms.y), __ATOMIC_RELAXED, __HIP_MEMORY_SCOPE_AGENT);
    } else if (u >= NU) {
#pragma unroll 1
      for (int c = 0; c < 8; ++c) asm volatile("s_barrier" ::: "memory");
    } else {
      u4 W[G1_WIN], F[2][4];
      auto ldw = [&](auto g) { bld16<1024 * (g & 3)>(W[g % G1_WIN], vw, rw, u * 32768 + (g & 28) * 1024); };
      asm volatile("s_nop 4");
      sfor<0, G1_W0>(ldw);
      u32 ra[4][2], sv[4];
#pragma unroll
      for (int c = 0; c < 4; ++c) { ra[c][0] = raddr(c, 0); ra[c][1] = raddr(c, 1); }
      f16v acc[4];
#pragma unroll
      for (int c = 0; c < 4; ++c)
#pragma unroll
        for (int e = 0; e < 16; ++e) acc[c][e] = 0.f;
      u2 ex;
      asm volatile("s_barrier" ::: "memory");
#pragma unroll
      for (int c = 0; c < 4; ++c) F[0][c] = cat(trd(ra[c][0]), trd(ra[c][1]));
      sfor<0, 32>([&](auto s) {
        vmwait<g1_nw(s) - s - 1 + (s > 28 ? 5 : 0)>(W[s % G1_WIN]);
        if constexpr ((s & 3) == 3 && s < 31) asm volatile("s_barrier" ::: "memory");
        sfor<0, 4>([&](auto c) {
          SB();
          acc[c] = mfma32(W[s % G1_WIN], F[s & 1][c], acc[c]);
          if constexpr (s == 15) { ra[c][0] += 65536; ra[c][1] += 65536; asm("" : "+v"(ra[c][0]), "+v"(ra[c][1])); }
          if constexpr (s < 31) F[(s + 1) & 1][c] = cat(trd(ra[c][0] + ((s + 1) & 15) * 4096), trd(ra[c][1] + ((s + 1) & 15) * 4096));
          SGB(0x8, 1); SGB(0x100, 2);
          SB();
          if constexpr (c == 0 && g1_nw(s) < g1_nw(s + 1)) ldw(std::integral_constant<int, g1_nw(s)>{});
          if constexpr (s == 28 && c == 0) asm volatile("global_load_dwordx2 %0, %1, %2" : "=v"(ex) : "v"(lane * 8), "s"(ws + WS_CSB + u * 512));
          if constexpr (s == 28) st2_request(sv[c], 4 * (lane & 31), rs2, 512 * rt + 128 * c);
        });
      });
      st2_wait(sv, st2);
      asm volatile("" : "+v"(ex));
#pragma unroll
      for (int c = 0; c < 4; ++c) acc[c] = g1_shift(ex, sv[c], hh, acc[c]);
      asm volatile("s_nop 7\n\ts_nop 7\n\ts_nop 7" : "+v"(acc[0]), "+v"(acc[1]), "+v"(acc[2]), "+v"(acc[3]));
#pragma unroll
      for (int c = 0; c < 4; ++c) {
        SB();
        const float sd = bhi(sv[c]), rs = __builtin_amdgcn_rcpf(sd);
        const u4 o = c < G1_NSINGLE ? g1_tile_single(acc[c], rs, sd, -2.30220819f) : g1_tile(acc[c], rs, sd);
        ho[c * (NU * 64)] = o;
      }
    }
  } else {
    const rsrc_t rx = mkrsrc(ws + WS_X1T, D * L * 2);
    const u32 vx = (4 * w + (lane >> 4)) * 4096 + 256 * rt + 16 * ((lane & 15) ^ (((lane >> 4) << 2) | w));
    u4 W[32];
    auto req = [&](auto n) {
      if constexpr (n < 32) glds16<0>(lb0 + n * 4096 + 1024 * w, vx, rx, n * 65536);
      else bld16<1024 * (n & 3)>(W[n - 32], vw, rw, u * 32768 + (n & 28) * 1024);
    };
    asm volatile("s_nop 4");
    if (cg < 0) {
      sfor<0, 16>(req);
      vmwait<12>(); asm volatile("s_barrier" ::: "memory");
      sfor<16, 24>(req);
      vmwait<16>(); asm volatile("s_barrier" ::: "memory");
      sfor<24, 32>(req);
      sfor<2, 8>([&](auto c) { vmwait<28 - 4 * c>(); asm volatile("s_barrier" ::: "memory"); });
      return;
    }
    sfor<0, G1_X0>(req);
    vmwait<G1_X0 - 4>();
    asm volatile("s_barrier" ::: "memory");
    sfor<0, 5>([&](auto c) {
      sfor<G1_X0 + 4 * c, G1_X0 + 4 * c + 4>(req);
      vmwait<G1_X0 - 4>();
      asm volatile("s_barrier" ::: "memory");
    });
    sfor<G1_X0 + 20, 32>(req);
    if (u >= NU) {
      vmwait<4>(); asm volatile("s_barrier" ::: "memory");
      vmwait<0>(); asm volatile("s_barrier" ::: "memory");
      return;
    }
    sfor<32, G1_X0 + 24>(req);
    vmwait<G1_X0 - 4>(); asm volatile("s_barrier" ::: "memory");
    sfor<G1_X0 + 24, G1_X0 + 28>(req);
    vmwait<G1_X0 - 4>(); asm volatile("s_barrier" ::: "memory");
    sfor<G1_X0 + 28, 64>(req);
    u32 rb2[2][2], sv[2];
    u2 ex;
    asm volatile("global_load_dwordx2 %0, %1, %2" : "=v"(ex) : "v"(lane * 8), "s"(ws + WS_CSB + u * 512));
#pragma unroll
    for (int c = 0; c < 2; ++c) {
      rb2[c][0] = raddr(2 * (w & 1) + c, 0); rb2[c][1] = raddr(2 * (w & 1) + c, 1);
      st2_request(sv[c], 4 * (lane & 31), rs2, 512 * rt + 128 * (2 * (w & 1) + c));
    }
    u4 F[2][2];
#pragma unroll
    for (int c = 0; c < 2; ++c) F[0][c] = cat(trd(rb2[c][0]), trd(rb2[c][1]));
    f16v acc[2];
#pragma unroll
    for (int c = 0; c < 2; ++c)
#pragma unroll
      for (int e = 0; e < 16; ++e) acc[c][e] = 0.f;
    sfor<0, 32>([&](auto s) {
      vmwait<34 - s>(W[s]);
      sfor<0, 2>([&](auto c) {
        SB();
        acc[c] = mfma32(W[s], F[s & 1][c], acc[c]);
        if constexpr (s == 15) { rb2[c][0] += 65536; rb2[c][1] += 65536; asm("" : "+v"(rb2[c][0]), "+v"(rb2[c][1])); }
        if constexpr (s < 31) F[(s + 1) & 1][c] = cat(trd(rb2[c][0] + ((s + 1) & 15) * 4096), trd(rb2[c][1] + ((s + 1) & 15) * 4096));
        SGB(0x8, 1); SGB(0x100, 2);
        SB();
      });
    });
    st2_wait(sv, st2 + 64 * (w & 1));
    asm volatile("" : "+v"(ex));
#pragma unroll
    for (int c = 0; c < 2; ++c) acc[c] = g1_shift(ex, sv[c], hh, acc[c]);
#pragma unroll
    for (int c = 0; c < 2; ++c) {
      const float sd = bhi(sv[c]);
      ho[(2 * (w & 1) + c) * (NU * 64)] = g1_tile(acc[c], __builtin_amdgcn_rcpf(sd), sd);
    }
  }
}

constexpr int G2_DEPTH = 4, G2_NS = G2_DEPTH + 1, G2_X = G2_NS * 16384, K4_LDS = G2_X + 8192;

__global__ __launch_bounds__(512, 2) void k4_gemm2(char* ws, u16* out) {
  extern __shared__ __attribute__((aligned(16))) char sm[];
  const int b = blockIdx.x, j = b >> 3;
  const int lt = 4 * (b & 7) + (j & 3), dt = j >> 2;
  const int t = threadIdx.x, w8 = __builtin_amdgcn_readfirstlane(t >> 6), w = w8 & 3, lane = t & 63, hh = lane >> 5;
  if (w8 >= 4) {
    const rsrc_t rs = mkrsrc(w < 2 ? ws + WS_WDT + (2 * dt + w) * (NU * 1024) : ws + WS_HH + (2 * lt + w - 2) * (NU * 1024), NU * 1024);
    const rsrc_t rx = mkrsrc(ws + WS_X1T, D * L * 2);
    const u32 lb0 = ldsaddr(sm) + 4096 * w, vw = lane * 16;
    asm volatile("s_nop 4");
#pragma unroll
    for (int i = 0; i < G2_DEPTH; ++i) glds16x4(lb0 + i * 16384, vw, rs, i * 4096);
    const u32 vx = (64 * dt + 16 * w + (lane >> 3)) * 4096 + 128 * lt + 16 * (lane & 7);
    glds16<0>(ldsaddr(sm) + G2_X + 2048 * w, vx, rx, 0);
    glds16<0>(ldsaddr(sm) + G2_X + 2048 * w + 1024, vx, rx, 8 * 4096);
    vmwait<4 * (G2_DEPTH - 1) + 2>();
    asm volatile("s_barrier" ::: "memory");
    int fill = G2_DEPTH;
#pragma unroll 1
    for (int i = 0; i < 22 - G2_DEPTH; ++i) {
      glds16x4(lb0 + fill * 16384, vw, rs, (i + G2_DEPTH) * 4096);
      fill = fill == G2_NS - 1 ? 0 : fill + 1;
      vmwait<4 * (G2_DEPTH - 1)>();
      asm volatile("s_barrier" ::: "memory");
    }
    sfor<0, G2_DEPTH - 1>([&](auto i) {
      vmwait<4 * (G2_DEPTH - 2 - i)>();
      asm volatile("s_barrier" ::: "memory");
    });
    return;
  }
  const int wd = w >> 1, wl = w & 1;
  const float* gbp = (const float*)(ws + WS_MOD) + 5 * D + 64 * dt + 32 * wd + 4 * hh;
  f4 gb[4];
#pragma unroll
  for (int i = 0; i < 4; ++i) gb[i] = *(const f4*)(gbp + 8 * i);
  f16v acc;
#pragma unroll
  for (int e = 0; e < 16; ++e) acc[e] = 0.f;
  const char* pa = sm + 4096 * wd + 16 * lane;
  const char* pb = sm + 4096 * (2 + wl) + 16 * lane;
  u4 A[2][4], B[2][4];
  int slot = 0;
  auto stage = [&](int cur, auto first) {
    bar();
#pragma unroll
    for (int k = 0; k < 4; ++k) { A[cur][k] = *(const u4*)(pa + slot * 16384 + k * 1024); B[cur][k] = *(const u4*)(pb + slot * 16384 + k * 1024); }
    if constexpr (!first) {
#pragma unroll
      for (int k = 0; k < 4; ++k) acc = mfma32(A[cur ^ 1][k], B[cur ^ 1][k], acc);
#pragma unroll
      for (int k = 0; k < 4; ++k) { SGB(0x100, 2); SGB(0x8, 1); }
    }
    SB();
    slot = slot == G2_NS - 1 ? 0 : slot + 1;
  };
  stage(0, std::true_type{});
  stage(1, std::false_type{});
#pragma unroll 1
  for (int i = 2; i < 22; i += 2) { stage(0, std::false_type{}); stage(1, std::false_type{}); }
  acc = mfma32(A[1][0], B[1][0], acc);
  acc = mfma32(A[1][1], B[1][1], acc);
  const int q = (lane & 15) >> 2, p = lane & 3;
  const char* xa = sm + G2_X + (32 * wd + 4 * hh + q) * 128 + 64 * wl + 32 * ((lane >> 4) & 1) + 8 * p;
  u2 o[4];
#pragma unroll
  for (int e = 0; e < 4; ++e) {
    const u2 xr = trd(xa + 8 * 128 * e);
    o[e] = (u2){pk(fmaf(gb[e][0], acc[4 * e], blo(xr.x)), fmaf(gb[e][1], acc[4 * e + 1], bhi(xr.x))),
                pk(fmaf(gb[e][2], acc[4 * e + 2], blo(xr.y)), fmaf(gb[e][3], acc[4 * e + 3], bhi(xr.y)))};
  }
  u16* op = out + (64 * lt + 32 * wl + (lane & 31)) * D + 64 * dt + 32 * wd + 8 * hh;
#pragma unroll
  for (int e = 0; e < 4; e += 2) {
    const auto r0 = __builtin_amdgcn_permlane32_swap(o[e].x, o[e + 1].x, false, false);
    const auto r1 = __builtin_amdgcn_permlane32_swap(o[e].y, o[e + 1].y, false, false);
    *(u4*)(op + 8 * e) = (u4){r0[0], r1[0], r0[1], r1[1]};
  }
}

extern "C" void kernel_launch(void* const* d_in, const int* in_sizes, int n_in, void* d_out, int out_size, void* d_ws, size_t ws_size,
                              hipStream_t stream) {
  const u16* x = (const u16*)d_in[0];
  char* ws = (char*)d_ws;
  (void)hipFuncSetAttribute((const void*)k3_gemm1, hipFuncAttributeMaxDynamicSharedMemorySize, K3_LDS);
  (void)hipFuncSetAttribute((const void*)k4_gemm2, hipFuncAttributeMaxDynamicSharedMemorySize, K4_LDS);
  k1_prep<<<496, 256, 0, stream>>>(x, (const u16*)d_in[1], (const u16*)d_in[2], (const u16*)d_in[3], (const u16*)d_in[4], (const u16*)d_in[5],
                                   (const u16*)d_in[6], (const u16*)d_in[11], (const u16*)d_in[12], ws);
  k2_conv_prep<<<392, 256, 0, stream>>>((const u16*)d_in[8], (const u16*)d_in[9], (const u16*)d_in[10], ws);
  k3_gemm1<<<256, 512, K3_LDS, stream>>>(ws);
  k4_gemm2<<<256, 512, K4_LDS, stream>>>(ws, (u16*)d_out);
}
```

```cpp
#include <hip/hip_runtime.h>
#include <hip/hip_bf16.h>
#include <stdint.h>
#include <type_traits>

typedef unsigned short u16;
typedef unsigned u32;
typedef float f2 __attribute__((ext_vector_type(2)));
typedef float f4 __attribute__((ext_vector_type(4)));
typedef float f16v __attribute__((ext_vector_type(16)));
typedef u32 u2 __attribute__((ext_vector_type(2)));
typedef u32 u4 __attribute__((ext_vector_type(4)));
typedef u4 u4a8 __attribute__((aligned(8)));
typedef short s4 __attribute__((ext_vector_type(4)));
typedef __bf16 bf2 __attribute__((ext_vector_type(2)));
typedef __bf16 bf8 __attribute__((ext_vector_type(8)));
typedef _Float16 h2 __attribute__((ext_vector_type(2)));
typedef _Float16 h8 __attribute__((ext_vector_type(8)));
#define LAS __attribute__((address_space(3)))
#define DI __device__ __forceinline__

constexpr int L = 2048, D = 512, NM = 128, DF = 1376, NU = 86;
constexpr int KXS = 2056;

constexpr size_t WS_MOD = 0, WS_MEAN = 16 << 10, WS_SD = 20 << 10, WS_CV = 24 << 10, WS_INV = 26 << 10, WS_ST2 = 32 << 10, WS_CNT = 40 << 10,
                 WS_EX = 64 << 10, WS_XT = 1 << 20, WS_KX = 4 << 20, WS_X1T = 8 << 20, WS_HH = 20 << 20;

DI float blo(u32 v) { return __uint_as_float(v << 16); }
DI float bhi(u32 v) { return __uint_as_float(v & 0xffff0000u); }
DI float b2f(u16 v) { return __uint_as_float(((u32)v) << 16); }
DI u32 pk(float a, float b) { return __builtin_bit_cast(u32, __builtin_convertvector((f2){a, b}, bf2)); }
DI u32 pkh(f2 v) { return __builtin_bit_cast(u32, __builtin_convertvector(v, h2)); }
DI float rb(float a) { return blo(pk(a, a)); }
DI u2 trd(const char* p) { return __builtin_bit_cast(u2, __builtin_amdgcn_ds_read_tr16_b64_v4i16((LAS s4*)p)); }
DI u2 trd(u32 a) { return __builtin_bit_cast(u2, __builtin_amdgcn_ds_read_tr16_b64_v4i16((LAS s4*)(size_t)a)); }
DI u4 cat(u2 a, u2 b) { return (u4){a.x, a.y, b.x, b.y}; }
DI float dot2(u32 a, u32 b, float c) { return __builtin_amdgcn_fdot2_f32_bf16(__builtin_bit_cast(bf2, a), __builtin_bit_cast(bf2, b), c, false); }
DI f4 mfma16(u4 a, u4 b, f4 c) { return __builtin_amdgcn_mfma_f32_16x16x32_bf16(__builtin_bit_cast(bf8, a), __builtin_bit_cast(bf8, b), c, 0, 0, 0); }
DI f4 mfma16h(u4 a, u4 b, f4 c) { return __builtin_amdgcn_mfma_f32_16x16x32_f16(__builtin_bit_cast(h8, a), __builtin_bit_cast(h8, b), c, 0, 0, 0); }
DI f16v mfma32(u4 a, u4 b, f16v c) { return __builtin_amdgcn_mfma_f32_32x32x16_bf16(__builtin_bit_cast(bf8, a), __builtin_bit_cast(bf8, b), c, 0, 0, 0); }
#define CM_MUL(d, a, b) "v_pk_mul_f32 " d ", " a ", " b " op_sel_hi:[0,1]\n\t"
#define CM_FMA(d, a, b) "v_pk_fma_f32 " d ", " a ", " b ", " d " op_sel:[1,1,0] op_sel_hi:[1,0,1] neg_lo:[0,1,0]\n\t"
DI void cmul2(f2& r0, f2 a0, f2 b0, f2& r1, f2 a1, f2 b1) {
  asm(CM_MUL("%0", "%2", "%3") CM_MUL("%1", "%4", "%5") CM_FMA("%0", "%2", "%3") CM_FMA("%1", "%4", "%5")
      : "=&v"(r0), "=&v"(r1) : "v"(a0), "v"(b0), "v"(a1), "v"(b1));
}
DI void cmul3(f2& r0, f2 a0, f2 b0, f2& r1, f2 a1, f2 b1, f2& r2, f2 a2, f2 b2) {
  asm(CM_MUL("%0", "%3", "%4") CM_MUL("%1", "%5", "%6") CM_MUL("%2", "%7", "%8") CM_FMA("%0", "%3", "%4") CM_FMA("%1", "%5", "%6") CM_FMA("%2", "%7", "%8")
      : "=&v"(r0), "=&v"(r1), "=&v"(r2) : "v"(a0), "v"(b0), "v"(a1), "v"(b1), "v"(a2), "v"(b2));
}
DI void cmul4(f2& r0, f2& r1, f2& r2, f2& r3, f2 a0, f2 a1, f2 a2, f2 a3, f2 b) {
  asm(CM_MUL("%0", "%4", "%8") CM_MUL("%1", "%5", "%8") CM_MUL("%2", "%6", "%8") CM_MUL("%3", "%7", "%8")
      CM_FMA("%0", "%4", "%8") CM_FMA("%1", "%5", "%8") CM_FMA("%2", "%6", "%8") CM_FMA("%3", "%7", "%8")
      : "=&v"(r0), "=&v"(r1), "=&v"(r2), "=&v"(r3) : "v"(a0), "v"(a1), "v"(a2), "v"(a3), "v"(b));
}
DI float exp_acc(float x) {
  const float t = x * 1.44269502f, r = fmaf(x, 1.44269502f, -t) + x * 1.92596299e-8f, e = __builtin_amdgcn_exp2f(t);
  return fmaf(e, r * 0.693147182f, e);
}
template <int CTRL> DI float dppf(float v) { return __int_as_float(__builtin_amdgcn_update_dpp(0, __float_as_int(v), CTRL, 0xf, 0xf, true)); }
DI float sum32(float v) {
  auto r = __builtin_amdgcn_permlane32_swap(__float_as_uint(v), __float_as_uint(v), false, false);
  return __uint_as_float(r[0]) + __uint_as_float(r[1]);
}
DI f2 ln_stats(float s1, float s2) {
  const float mean = s1 * (1.f / D);
  const float var = fmaxf(fmaf(-mean, mean, s2 * (1.f / D)), 0.f);
  return (f2){rb(mean), rb(__builtin_amdgcn_sqrtf(rb(rb(var) + 1e-5f)))};
}
typedef __amdgpu_buffer_rsrc_t rsrc_t;
DI rsrc_t mkrsrc(const void* p, u32 bytes) { return __builtin_amdgcn_make_buffer_rsrc((void*)p, 0, bytes, 0x00020000); }
DI u32 ldsaddr(const char* p) { return (u32)(size_t)(LAS const char*)p; }
template <int OFF> DI void glds16(u32 m0v, u32 voff, rsrc_t rs, u32 soff) {
  asm volatile("s_mov_b32 m0, %0\n\ts_nop 0\n\tbuffer_load_dwordx4 %1, %2, %3 offen offset:%4 lds" :: "s"(m0v), "v"(voff), "s"(rs), "s"(soff), "n"(OFF) : "memory");
}
DI void glds16x4(u32 m0v, u32 voff, rsrc_t rs, u32 soff) {
  asm volatile("s_mov_b32 m0, %0\n\ts_nop 0\n\tbuffer_load_dwordx4 %1, %2, %3 offen lds\n\tbuffer_load_dwordx4 %1, %2, %3 offen offset:1024 lds\n\t"
               "buffer_load_dwordx4 %1, %2, %3 offen offset:2048 lds\n\tbuffer_load_dwordx4 %1, %2, %3 offen offset:3072 lds"
               :: "s"(m0v), "v"(voff), "s"(rs), "s"(soff) : "memory");
}
template <int OFF> DI void bld16(u4& d, u32 voff, rsrc_t rs, u32 soff) {
  asm volatile("buffer_load_dwordx4 %0, %1, %2, %3 offen offset:%4" : "=v"(d) : "v"(voff), "s"(rs), "s"(soff), "n"(OFF));
}
template <int OFF> DI void bld16_sc1(u4& d, u32 voff, rsrc_t rs, u32 soff) {
  asm volatile("buffer_load_dwordx4 %0, %1, %2, %3 offen offset:%4 sc1" : "=v"(d) : "v"(voff), "s"(rs), "s"(soff), "n"(OFF));
}
template <int OFF, class T> DI void gld16(T& d, const void* p) { asm volatile("global_load_dwordx4 %0, %1, off offset:%2" : "=v"(d) : "v"(p), "n"(OFF)); }
DI void pin(u32& v) { asm("" : "+v"(v)); }
DI void rawbar() { asm volatile("s_barrier" ::: "memory"); }
template <int N> DI void vmwait() { asm volatile("s_waitcnt vmcnt(%0)" :: "n"(N) : "memory"); }
template <int N> DI void vmwait(u4& d) { asm volatile("s_waitcnt vmcnt(%1)" : "+v"(d) : "n"(N)); }
DI void bar() { asm volatile("s_waitcnt lgkmcnt(0)\n\ts_barrier" ::: "memory"); }
#define SB() __builtin_amdgcn_sched_barrier(0)
#define SGB(m, n) __builtin_amdgcn_sched_group_barrier(m, n, 0)
template <int I, int N, class F> DI void sfor(F&& f) { if constexpr (I < N) { f(std::integral_constant<int, I>{}); sfor<I + 1, N>(f); } }

constexpr int KG_ROW = 544, KG_CH = 57 * KG_ROW, KG_T = 2 * KG_CH, KG_TS = 4160, K1_LDS = KG_T + 2 * KG_TS;

DI f2 cexp_pow2(float a, float fh, float fl, float p) {
  const float e = __builtin_amdgcn_exp2f(a * p);
  const float r = fmaf(fl, p, __builtin_amdgcn_fractf(fh * p));
  return (f2){e * __builtin_amdgcn_cosf(r), e * __builtin_amdgcn_sinf(r)};
}

DI void k1_kgen(char* sm, int b, const u16* lar, const u16* aim, const u16* cre, const u16* cim, const u16* ldt, const u16* dsk, u16* kx) {
  const int t = threadIdx.x, w = __builtin_amdgcn_readfirstlane(t >> 6), lane = t & 63, ch = w >> 1, half = w & 1;
  const int d0 = 64 * (b & 7) + 2 * (b >> 3), d = d0 + ch, n = 64 * half + lane;
  const u32 v_lar = lar[d * NM + n], v_aim = aim[d * NM + n];
  const u32 cr = cre[d * NM + n], ci = cim[d * NM + n], cr2 = cre[d * NM + (n ^ 64)], ci2 = cim[d * NM + (n ^ 64)];
  const float dt = rb(exp_acc(b2f(ldt[d])));
  const float ds = b2f(dsk[d]);
  const float zr = -rb(exp_acc(blo(v_lar))) * dt, zi = blo(v_aim) * dt;
  const float a = zr * 1.44269502f;
  const float fh = zi * 0.159154937f, fl = fmaf(zi, 0.159154937f, -fh) + zi * 6.42063833e-9f;
  const f2 e1 = cexp_pow2(a, fh, fl, 1.f), e8 = cexp_pow2(a, -fh, -fl, 8.f), e128 = cexp_pow2(a, fh, fl, 128.f), e1024 = cexp_pow2(a, fh, fl, 1024.f);
  int mx = (int)max(max(cr & 0x7fffu, ci & 0x7fffu), max(cr2 & 0x7fffu, ci2 & 0x7fffu));
  mx = max(mx, __builtin_amdgcn_update_dpp(0, mx, 0x111, 0xf, 0xf, true));
  mx = max(mx, __builtin_amdgcn_update_dpp(0, mx, 0x112, 0xf, 0xf, true));
  mx = max(mx, __builtin_amdgcn_update_dpp(0, mx, 0x114, 0xf, 0xf, true));
  mx = max(mx, __builtin_amdgcn_update_dpp(0, mx, 0x118, 0xf, 0xf, true));
  mx = max(max(__builtin_amdgcn_readlane(mx, 15), __builtin_amdgcn_readlane(mx, 31)), max(__builtin_amdgcn_readlane(mx, 47), __builtin_amdgcn_readlane(mx, 63)));
  const int ex = min(max(mx >> 7, 1), 253);
  const float cs = __int_as_float((254 - ex) << 23), kf = 2.f * dt * __int_as_float(ex << 23);
  if (t < 2) *(u4*)(kx + (d0 + t) * KXS) = (u4){0u, 0u, 0u, 0u};
  constexpr u32 KEEP = 0x03020100u, LL = 0x01000100u, LH = 0x05040100u, HH = 0x05040504u;
  const u32 sel0 = t < 32 ? KEEP : LL, sel1 = t < 32 ? KEEP : t < 128 ? LH : LL;
  const u32 sel2 = t < 32 ? KEEP : t < 128 ? LL : (t & 1) ? HH : LH, sel3 = t < 32 ? KEEP : t < 64 ? LH : HH;
  char* base = sm + ch * KG_CH + n * 4;
  auto put = [&](int row, f2 v) { *(u32*)(base + row * KG_ROW) = pkh(v); };
  f2 lo[8], e[8], m[8], x0, x1, x2, x3, e16, e32, e64;
  lo[0] = (f2){blo(cr) * cs, -blo(ci) * cs};
  e[1] = e1; m[1] = e128;
  put(0, lo[0]); put(16, (f2){1.f, 0.f}); put(17, e1); put(24, e128); put(48, e1024);
  cmul3(e16, e8, e8, e[2], e1, e1, m[2], e128, e128);
  cmul2(lo[1], lo[0], e8, x0, e1024, e1024);
  put(18, e[2]); put(32, m[2]); put(1, lo[1]); put(56, x0);
  cmul3(e32, e16, e16, e[4], e[2], e[2], m[4], m[2], m[2]);
  cmul2(e[3], e[2], e1, m[3], m[2], e128);
  cmul2(lo[2], lo[0], e16, lo[3], lo[1], e16);
  put(20, e[4]); put(40, m[4]); put(19, e[3]); put(36, m[3]); put(2, lo[2]); put(3, lo[3]);
  cmul4(e[5], e[6], e[7], x0, e1, e[2], e[3], m[4], e[4]);
  put(21, e[5]); put(22, e[6]); put(23, e[7]); put(41, x0);
  cmul3(m[5], m[4], e128, m[6], m[4], m[2], m[7], m[4], m[3]);
  put(42, m[5]); put(44, m[6]); put(46, m[7]);
  cmul4(lo[4], lo[5], lo[6], lo[7], lo[0], lo[1], lo[2], lo[3], e32);
  put(4, lo[4]); put(5, lo[5]); put(6, lo[6]); put(7, lo[7]);
  cmul2(e64, e32, e32, x0, e128, e1);
  put(25, x0);
  cmul3(x0, m[5], e[4], x1, m[6], e[4], x2, m[7], e[4]);
  put(43, x0); put(45, x1); put(47, x2);
  cmul4(x0, x1, x2, x3, lo[0], lo[1], lo[2], lo[3], e64);
  put(8, x0); put(9, x1); put(10, x2); put(11, x3);
  cmul4(x0, x1, x2, x3, lo[4], lo[5], lo[6], lo[7], e64);
  put(12, x0); put(13, x1); put(14, x2); put(15, x3);
  cmul4(x0, x1, x2, x3, e[2], e[3], e[4], e[5], e128);
  put(26, x0); put(27, x1); put(28, x2); put(29, x3);
  cmul2(x0, e[6], e128, x1, e[7], e128);
  put(30, x0); put(31, x1);
  cmul3(x0, e[2], m[2], x1, e[4], m[2], x2, e[6], m[2]);
  put(33, x0); put(34, x1); put(35, x2);
  cmul3(x0, e[2], m[3], x1, e[4], m[3], x2, e[6], m[3]);
  put(37, x0); put(38, x1); put(39, x2);
  cmul4(x0, x1, x2, x3, m[1], m[2], m[3], m[4], e1024);
  put(49, x0); put(50, x1); put(51, x2); put(52, x3);
  cmul3(x0, m[5], e1024, x1, m[6], e1024, x2, m[7], e1024);
  put(53, x0); put(54, x1); put(55, x2);
  __syncthreads();
  {
    const int c = lane & 15, g = lane >> 4;
    const char* fb = sm + ch * KG_CH + c * KG_ROW + g * 16;
    u16* T = (u16*)(sm + KG_T + ch * KG_TS);
    f4 acc0 = {0.f, 0.f, 0.f, 0.f}, acc1 = acc0;
    u4 B[8], A[8];
#pragma unroll
    for (int s = 0; s < 8; ++s) { B[s] = *(const u4*)(fb + s * 64); A[s] = *(const u4*)(fb + (16 + 16 * half) * KG_ROW + s * 64); }
    if (!half) {
      u4 A2[8];
#pragma unroll
      for (int s = 0; s < 8; ++s) A2[s] = *(const u4*)(fb + 48 * KG_ROW + s * 64);
      SB();
#pragma unroll
      for (int s = 0; s < 8; ++s) acc0 = mfma16h(A[s], B[s], acc0);
#pragma unroll
      for (int s = 0; s < 8; ++s) acc1 = mfma16h(A2[s], B[s], acc1);
      acc0 *= kf; acc1 *= kf;
      if (lane == 0) acc0[0] += ds;
      *(u2*)(T + 8 * c + 4 * (g & 1) + 128 * (g >> 1)) = (u2){pk(acc0[0], acc0[1]), pk(acc0[2], acc0[3])};
      if (g < 2) {
#pragma unroll
        for (int e = 0; e < 4; ++e) T[1024 + 128 * (4 * g + e) + 8 * c] = (u16)pk(acc1[e], 0.f);
      } else if (lane == 32) T[2048] = (u16)pk(acc1[0], 0.f);
    } else {
      SB();
#pragma unroll
      for (int s = 0; s < 8; ++s) acc0 = mfma16h(A[s], B[s], acc0);
      acc0 *= kf;
#pragma unroll
      for (int e = 0; e < 4; ++e) {
        const int p = g < 2 ? 256 + 128 * g + 2 * e : 512 + 128 * (2 * (g - 2) + (e >> 1)) + 4 * (e & 1);
        T[p + 8 * c] = (u16)pk(acc0[e], 0.f);
      }
    }
  }
  __syncthreads();
  u4 v[2];
  u32 e8v[2];
#pragma unroll
  for (int c2 = 0; c2 < 2; ++c2) {
    const u16* T = (const u16*)(sm + KG_T + c2 * KG_TS);
    v[c2] = *(const u4*)(T + 8 * t);
    e8v[c2] = T[8 * t + 8];
  }
#pragma unroll
  for (int c2 = 0; c2 < 2; ++c2) {
    const u4 o = {__builtin_amdgcn_perm(v[c2].x, v[c2].x, sel0), __builtin_amdgcn_perm(v[c2].z, t < 64 ? v[c2].y : v[c2].x, sel1),
                  __builtin_amdgcn_perm(e8v[c2], t < 128 ? v[c2].z : v[c2].x, sel2), __builtin_amdgcn_perm(e8v[c2], v[c2].w, sel3)};
    *(u4*)(kx + (d0 + c2) * KXS + 8 + 8 * t) = o;
  }
}

DI void k1_xt(char* sm, int b, const u16* x, u16* xt) {
  const int t = threadIdx.x, w = t >> 6, lane = t & 63;
  const int c0 = 64 * (b & 7), r0 = 128 * (b >> 3);
  u4 v[4];
#pragma unroll
  for (int i = 0; i < 4; ++i) v[i] = *(const u4*)(x + (r0 + 32 * i + (t >> 3)) * D + c0 + 8 * (t & 7));
#pragma unroll
  for (int i = 0; i < 4; ++i) *(u4*)(sm + (32 * i + (t >> 3)) * 144 + 16 * (t & 7)) = v[i];
  __syncthreads();
  const int g = lane >> 4, q = (lane & 15) >> 2, p = lane & 3;
#pragma unroll
  for (int i = 0; i < 4; ++i) {
    const char* a = sm + (32 * i + 8 * g + q) * 144 + 32 * w + 8 * p;
    *(u4*)(xt + (c0 + 16 * w + (lane & 15)) * L + r0 + 32 * i + 8 * g) = cat(trd(a), trd(a + 4 * 144));
  }
}

DI void k1_stats(int b, const u16* x, char* ws) {
  const int t = threadIdx.x, row = 32 * b + (t >> 3);
  u4 v[8];
#pragma unroll
  for (int i = 0; i < 8; ++i) v[i] = *(const u4*)(x + row * D + 64 * i + 8 * (t & 7));
  float s1 = 0.f, s2 = 0.f, s3 = 0.f, s4 = 0.f;
#pragma unroll
  for (int i = 0; i < 8; ++i)
#pragma unroll
    for (int j = 0; j < 4; j += 2) {
      s1 = dot2(v[i][j], 0x3f803f80u, s1); s2 = dot2(v[i][j], v[i][j], s2);
      s3 = dot2(v[i][j + 1], 0x3f803f80u, s3); s4 = dot2(v[i][j + 1], v[i][j + 1], s4);
    }
  s1 += s3; s2 += s4;
  s1 += dppf<0xb1>(s1); s2 += dppf<0xb1>(s2); s1 += dppf<0x4e>(s1); s2 += dppf<0x4e>(s2); s1 += dppf<0x141>(s1); s2 += dppf<0x141>(s2);
  const f2 st = ln_stats(s1, s2);
  if ((t & 7) == 0) {
    ((u16*)(ws + WS_MEAN))[row] = (u16)(__float_as_uint(st.x) >> 16);
    ((u16*)(ws + WS_SD))[row] = (u16)(__float_as_uint(st.y) >> 16);
    ((u32*)(ws + WS_ST2))[row] = 0u;
  }
  if (t == 0 && b < 16) ((u32*)(ws + WS_CNT))[64 * b] = 0u;
}

DI void k1_mod(char* sm, int b, const u16* c, const u16* wm, char* ws) {
  const int t = threadIdx.x, rg = t >> 3;
  u4 v[16];
  const u32 cc = ((const u32*)c)[t];
#pragma unroll
  for (int i = 0; i < 16; ++i) v[i] = *(const u4*)(wm + (16 * rg + i) * (6 * D) + 64 * b + 8 * (t & 7));
  u32* sl = (u32*)(sm + 32 * 68 * 4);
  {
    const float c0 = blo(cc), c1 = bhi(cc);
    sl[t] = pk(c0 / (1.f + __expf(-c0)), c1 / (1.f + __expf(-c1)));
  }
  __syncthreads();
  const u4 sa = *(const u4*)(sl + 8 * rg), sb = *(const u4*)(sl + 8 * rg + 4);
  float acc[8] = {0.f, 0.f, 0.f, 0.f, 0.f, 0.f, 0.f, 0.f};
#pragma unroll
  for (int i = 0; i < 8; ++i) {
    const u32 sp = i < 4 ? sa[i & 3] : sb[i & 3];
#pragma unroll
    for (int j = 0; j < 4; ++j) {
      acc[2 * j] = dot2(__builtin_amdgcn_perm(v[2 * i + 1][j], v[2 * i][j], 0x05040100u), sp, acc[2 * j]);
      acc[2 * j + 1] = dot2(__builtin_amdgcn_perm(v[2 * i + 1][j], v[2 * i][j], 0x07060302u), sp, acc[2 * j + 1]);
    }
  }
  float* red = (float*)sm;
  *(f4*)(red + rg * 68 + 8 * (t & 7)) = (f4){acc[0], acc[1], acc[2], acc[3]};
  *(f4*)(red + rg * 68 + 8 * (t & 7) + 4) = (f4){acc[4], acc[5], acc[6], acc[7]};
  __syncthreads();
  float s = 0.f;
#pragma unroll
  for (int i = 0; i < 8; ++i) s += red[((t & 3) + 4 * i) * 68 + (t >> 2)];
  s += dppf<0xb1>(s); s += dppf<0x4e>(s);
  if ((t & 3) == 0) {
    const int j = 64 * b + (t >> 2), part = b >> 3;
    float v = rb(s);
    if (part == 1 || part == 4) v = rb(1.f + v);
    if (part == 4) {
      v = v == 0.f ? 0x1p-60f : v;
      ((float*)(ws + WS_INV))[j - 4 * D] = 1.f / v;
    }
    if (part == 3 || part == 4) ((u16*)(ws + WS_CV))[j - 3 * D] = (u16)(__float_as_uint(v) >> 16);
    ((float*)(ws + WS_MOD))[j] = v;
  }
}

__global__ __launch_bounds__(256, 2) void k1_prep(const u16* x, const u16* lar, const u16* aim, const u16* cre, const u16* cim, const u16* ldt,
                                                  const u16* dsk, const u16* c, const u16* wm, char* ws) {
  __shared__ __attribute__((aligned(16))) char sm[K1_LDS];
  const int b = blockIdx.x;
  if (b < 256) k1_kgen(sm, b, lar, aim, cre, cim, ldt, dsk, (u16*)(ws + WS_KX));
  else if (b < 384) k1_xt(sm, b - 256, x, (u16*)(ws + WS_XT));
  else if (b < 448) k1_stats(b - 384, x, ws);
  else k1_mod(sm, b - 448, c, wm, ws);
}

constexpr int CV_KC = 4128, CV_H = 8 * CV_KC, CV_HP = 1152, CV_X = CV_H + 8 * CV_HP, CV_EX = CV_X + 8192, CV_W = CV_EX + 16384,
              CV_FL = CV_W + 16384, K2_LDS = CV_FL + 16;

DI void k2_conv(char* sm, int b, char* ws) {
  const int t = threadIdx.x, w = __builtin_amdgcn_readfirstlane(t >> 6), lane = t & 63, ch = w >> 1, half = w & 1;
  const int d0 = 64 * (b & 7) + 2 * (b >> 3), d = d0 + ch;
  const u16* xt = (const u16*)(ws + WS_XT);
  const float* mod = (const float*)(ws + WS_MOD);
  const int c = lane & 15, g = lane >> 4;
  u4 xv[2];
  const u4 mv = *(const u4*)(ws + WS_MEAN + 16 * t), sv = *(const u4*)(ws + WS_SD + 16 * t);
#pragma unroll
  for (int i = 0; i < 2; ++i) xv[i] = *(const u4*)(xt + (d0 + i) * L + 8 * t);
  const float ga = mod[2 * D + d], c2 = mod[4 * D + d];
  const f2 c1 = *(const f2*)(mod + D + d0), sa = *(const f2*)(mod + d0);
  SB();
  float rs[8];
#pragma unroll
  for (int j = 0; j < 8; ++j) rs[j] = __builtin_amdgcn_rcpf((j & 1) ? bhi(sv[j >> 1]) : blo(sv[j >> 1]));
#pragma unroll
  for (int i = 0; i < 2; ++i) {
    float h[8];
#pragma unroll
    for (int j = 0; j < 8; ++j) {
      const float xx = (j & 1) ? bhi(xv[i][j >> 1]) : blo(xv[i][j >> 1]);
      const float mm = (j & 1) ? bhi(mv[j >> 1]) : blo(mv[j >> 1]);
      h[j] = fmaf((xx - mm) * rs[j], c1[i], sa[i]);
    }
#pragma unroll
    for (int r = 0; r < 4; ++r) *(u32*)(sm + CV_H + (4 * i + r) * CV_HP + 128 + 4 * t) = pk(h[r], h[r + 4]);
    *(u4*)(sm + CV_X + 4096 * i + 16 * t) = xv[i];
  }
  f4 acc[8];
#pragma unroll
  for (int i = 0; i < 8; ++i) acc[i] = (f4){0.f, 0.f, 0.f, 0.f};
  u32 ka = ldsaddr(sm) + (4 * ch + 2 * half) * CV_KC + 32 + 8 * (8 * g + (lane & 3) - ((lane & 15) >> 2));
  u32 hb = ldsaddr(sm) + CV_H + (4 * ch + 2 * half) * CV_HP + 128 + 8 * c - 16 * g - 8 - 64;
  bar();
  {
    u32 cnt;
    asm volatile(
      "ds_read_b64_tr_b16 v[64:65], %8 offset:32\n"
      "ds_read_b64_tr_b16 v[66:67], %8 offset:0\n"
      "ds_read_b64 v[120:121], %9 offset:832\n"
      "ds_read_b64 v[122:123], %9 offset:840\n"
      "ds_read_b64 v[124:125], %9 offset:960\n"
      "ds_read_b64 v[126:127], %9 offset:968\n"
      "ds_read_b64 v[112:113], %9 offset:576\n"
      "ds_read_b64 v[114:115], %9 offset:584\n"
      "ds_read_b64 v[116:117], %9 offset:704\n"
      "ds_read_b64 v[118:119], %9 offset:712\n"
      "ds_read_b64_tr_b16 v[68:69], %8 offset:544\n"
      "ds_read_b64_tr_b16 v[70:71], %8 offset:512\n"
      "ds_read_b64 v[108:109], %9 offset:448\n"
      "ds_read_b64 v[110:111], %9 offset:456\n"
      "ds_read_b64 v[104:105], %9 offset:320\n"
      "ds_read_b64 v[106:107], %9 offset:328\n"
      "ds_read_b64_tr_b16 v[72:73], %8 offset:1056\n"
      "ds_read_b64_tr_b16 v[74:75], %8 offset:1024\n"
      "ds_read_b64_tr_b16 v[76:77], %8 offset:1568\n"
      "ds_read_b64_tr_b16 v[78:79], %8 offset:1536\n"
      "ds_read_b64 v[100:101], %9 offset:192\n"
      "ds_read_b64 v[102:103], %9 offset:200\n"
      "ds_read_b64_tr_b16 v[80:81], %8 offset:2080\n"
      "ds_read_b64_tr_b16 v[82:83], %8 offset:2048\n"
      "ds_read_b64 v[96:97], %9 offset:64\n"
      "ds_read_b64 v[98:99], %9 offset:72\n"
      "ds_read_b64_tr_b16 v[84:85], %8 offset:2592\n"
      "ds_read_b64_tr_b16 v[86:87], %8 offset:2560\n"
      "ds_read_b64_tr_b16 v[88:89], %8 offset:3104\n"
      "ds_read_b64_tr_b16 v[90:91], %8 offset:3072\n"
      "ds_read_b64_tr_b16 v[92:93], %8 offset:3616\n"
      "ds_read_b64_tr_b16 v[94:95], %8 offset:3584\n"
      "s_mov_b32 %10, 2\n"
      "s_waitcnt lgkmcnt(0)\n"
      "Lconv%=:\n"
      "v_mfma_f32_16x16x32_bf16 %6, v[64:67], v[120:123], %6\n"
      "ds_read_b64_tr_b16 v[128:129], %8 offset:288\n"
      "v_mfma_f32_16x16x32_bf16 %7, v[64:67], v[124:127], %7\n"
      "ds_read_b64_tr_b16 v[130:131], %8 offset:256\n"
      "v_mfma_f32_16x16x32_bf16 %4, v[64:67], v[112:115], %4\n"
      "ds_read_b64 v[184:185], %9 offset:768\n"
      "v_mfma_f32_16x16x32_bf16 %5, v[64:67], v[116:119], %5\n"
      "ds_read_b64 v[186:187], %9 offset:776\n"
      "v_mfma_f32_16x16x32_bf16 %6, v[68:71], v[116:119], %6\n"
      "ds_read_b64 v[188:189], %9 offset:896\n"
      "v_mfma_f32_16x16x32_bf16 %7, v[68:71], v[120:123], %7\n"
      "ds_read_b64 v[190:191], %9 offset:904\n"
      "v_mfma_f32_16x16x32_bf16 %4, v[68:71], v[108:111], %4\n"
      "ds_read_b64 v[176:177], %9 offset:512\n"
      "v_mfma_f32_16x16x32_bf16 %2, v[64:67], v[104:107], %2\n"
      "ds_read_b64 v[178:179], %9 offset:520\n"
      "v_mfma_f32_16x16x32_bf16 %6, v[72:75], v[112:115], %6\n"
      "ds_read_b64 v[180:181], %9 offset:640\n"
      "v_mfma_f32_16x16x32_bf16 %3, v[64:67], v[108:111], %3\n"
      "ds_read_b64 v[182:183], %9 offset:648\n"
      "v_mfma_f32_16x16x32_bf16 %7, v[72:75], v[116:119], %7\n"
      "ds_read_b64_tr_b16 v[132:133], %8 offset:800\n"
      "v_mfma_f32_16x16x32_bf16 %4, v[72:75], v[104:107], %4\n"
      "ds_read_b64_tr_b16 v[134:135], %8 offset:768\n"
      "v_mfma_f32_16x16x32_bf16 %5, v[68:71], v[112:115], %5\n"
      "ds_read_b64 v[172:173], %9 offset:384\n"
      "v_mfma_f32_16x16x32_bf16 %6, v[76:79], v[108:111], %6\n"
      "ds_read_b64 v[174:175], %9 offset:392\n"
      "v_mfma_f32_16x16x32_bf16 %7, v[76:79], v[112:115], %7\n"
      "ds_read_b64 v[168:169], %9 offset:256\n"
      "v_mfma_f32_16x16x32_bf16 %4, v[76:79], v[100:103], %4\n"
      "ds_read_b64 v[170:171], %9 offset:264\n"
      "v_mfma_f32_16x16x32_bf16 %1, v[64:67], v[100:103], %1\n"
      "ds_read_b64_tr_b16 v[136:137], %8 offset:1312\n"
      "v_mfma_f32_16x16x32_bf16 %5, v[72:75], v[108:111], %5\n"
      "ds_read_b64_tr_b16 v[138:139], %8 offset:1280\n"
      "v_mfma_f32_16x16x32_bf16 %6, v[80:83], v[104:107], %6\n"
      "ds_read_b64_tr_b16 v[140:141], %8 offset:1824\n"
      "v_mfma_f32_16x16x32_bf16 %0, v[64:67], v[96:99], %0\n"
      "ds_read_b64_tr_b16 v[142:143], %8 offset:1792\n"
      "v_mfma_f32_16x16x32_bf16 %7, v[80:83], v[108:111], %7\n"
      "ds_read_b64 v[164:165], %9 offset:128\n"
      "v_mfma_f32_16x16x32_bf16 %5, v[76:79], v[104:107], %5\n"
      "ds_read_b64 v[166:167], %9 offset:136\n"
      "v_mfma_f32_16x16x32_bf16 %2, v[68:71], v[100:103], %2\n"
      "ds_read_b64_tr_b16 v[144:145], %8 offset:2336\n"
      "v_mfma_f32_16x16x32_bf16 %6, v[84:87], v[100:103], %6\n"
      "ds_read_b64_tr_b16 v[146:147], %8 offset:2304\n"
      "v_mfma_f32_16x16x32_bf16 %7, v[84:87], v[104:107], %7\n"
      "ds_read_b64 v[160:161], %9 offset:0\n"
      "v_mfma_f32_16x16x32_bf16 %3, v[68:71], v[104:107], %3\n"
      "ds_read_b64 v[162:163], %9 offset:8\n"
      "v_mfma_f32_16x16x32_bf16 %2, v[72:75], v[96:99], %2\n"
      "ds_read_b64_tr_b16 v[148:149], %8 offset:2848\n"
      "v_mfma_f32_16x16x32_bf16 %5, v[80:83], v[100:103], %5\n"
      "ds_read_b64_tr_b16 v[150:151], %8 offset:2816\n"
      "v_mfma_f32_16x16x32_bf16 %7, v[88:91], v[100:103], %7\n"
      "ds_read_b64_tr_b16 v[152:153], %8 offset:3360\n"
      "v_mfma_f32_16x16x32_bf16 %6, v[88:91], v[96:99], %6\n"
      "ds_read_b64_tr_b16 v[154:155], %8 offset:3328\n"
      "v_mfma_f32_16x16x32_bf16 %3, v[72:75], v[100:103], %3\n"
      "ds_read_b64_tr_b16 v[156:157], %8 offset:3872\n"
      "v_mfma_f32_16x16x32_bf16 %1, v[68:71], v[96:99], %1\n"
      "ds_read_b64_tr_b16 v[158:159], %8 offset:3840\n"
      "v_mfma_f32_16x16x32_bf16 %7, v[92:95], v[96:99], %7\n"
      "v_mfma_f32_16x16x32_bf16 %5, v[84:87], v[96:99], %5\n"
      "v_mfma_f32_16x16x32_bf16 %4, v[80:83], v[96:99], %4\n"
      "v_mfma_f32_16x16x32_bf16 %3, v[76:79], v[96:99], %3\n"
      "s_waitcnt lgkmcnt(0)\n"
      "v_add_u32 %8, 4128, %8\n"
      "v_add_u32 %9, 1152, %9\n"
      "v_mfma_f32_16x16x32_bf16 %6, v[128:131], v[184:187], %6\n"
      "ds_read_b64_tr_b16 v[64:65], %8 offset:32\n"
      "v_mfma_f32_16x16x32_bf16 %7, v[128:131], v[188:191], %7\n"
      "ds_read_b64_tr_b16 v[66:67], %8 offset:0\n"
      "v_mfma_f32_16x16x32_bf16 %4, v[128:131], v[176:179], %4\n"
      "ds_read_b64 v[120:121], %9 offset:832\n"
      "v_mfma_f32_16x16x32_bf16 %5, v[128:131], v[180:183], %5\n"
      "ds_read_b64 v[122:123], %9 offset:840\n"
      "v_mfma_f32_16x16x32_bf16 %6, v[132:135], v[180:183], %6\n"
      "ds_read_b64 v[124:125], %9 offset:960\n"
      "v_mfma_f32_16x16x32_bf16 %7, v[132:135], v[184:187], %7\n"
      "ds_read_b64 v[126:127], %9 offset:968\n"
      "v_mfma_f32_16x16x32_bf16 %4, v[132:135], v[172:175], %4\n"
      "ds_read_b64 v[112:113], %9 offset:576\n"
      "v_mfma_f32_16x16x32_bf16 %2, v[128:131], v[168:171], %2\n"
      "ds_read_b64 v[114:115], %9 offset:584\n"
      "v_mfma_f32_16x16x32_bf16 %6, v[136:139], v[176:179], %6\n"
      "ds_read_b64 v[116:117], %9 offset:704\n"
      "v_mfma_f32_16x16x32_bf16 %3, v[128:131], v[172:175], %3\n"
      "ds_read_b64 v[118:119], %9 offset:712\n"
      "v_mfma_f32_16x16x32_bf16 %7, v[136:139], v[180:183], %7\n"
      "ds_read_b64_tr_b16 v[68:69], %8 offset:544\n"
      "v_mfma_f32_16x16x32_bf16 %4, v[136:139], v[168:171], %4\n"
      "ds_read_b64_tr_b16 v[70:71], %8 offset:512\n"
      "v_mfma_f32_16x16x32_bf16 %5, v[132:135], v[176:179], %5\n"
      "ds_read_b64 v[108:109], %9 offset:448\n"
      "v_mfma_f32_16x16x32_bf16 %6, v[140:143], v[172:175], %6\n"
      "ds_read_b64 v[110:111], %9 offset:456\n"
      "v_mfma_f32_16x16x32_bf16 %7, v[140:143], v[176:179], %7\n"
      "ds_read_b64 v[104:105], %9 offset:320\n"
      "v_mfma_f32_16x16x32_bf16 %4, v[140:143], v[164:167], %4\n"
      "ds_read_b64 v[106:107], %9 offset:328\n"
      "v_mfma_f32_16x16x32_bf16 %1, v[128:131], v[164:167], %1\n"
      "ds_read_b64_tr_b16 v[72:73], %8 offset:1056\n"
      "v_mfma_f32_16x16x32_bf16 %5, v[136:139], v[172:175], %5\n"
      "ds_read_b64_tr_b16 v[74:75], %8 offset:1024\n"
      "v_mfma_f32_16x16x32_bf16 %6, v[144:147], v[168:171], %6\n"
      "ds_read_b64_tr_b16 v[76:77], %8 offset:1568\n"
      "v_mfma_f32_16x16x32_bf16 %0, v[128:131], v[160:163], %0\n"
      "ds_read_b64_tr_b16 v[78:79], %8 offset:1536\n"
      "v_mfma_f32_16x16x32_bf16 %7, v[144:147], v[172:175], %7\n"
      "ds_read_b64 v[100:101], %9 offset:192\n"
      "v_mfma_f32_16x16x32_bf16 %5, v[140:143], v[168:171], %5\n"
      "ds_read_b64 v[102:103], %9 offset:200\n"
      "v_mfma_f32_16x16x32_bf16 %2, v[132:135], v[164:167], %2\n"
      "ds_read_b64_tr_b16 v[80:81], %8 offset:2080\n"
      "v_mfma_f32_16x16x32_bf16 %6, v[148:151], v[164:167], %6\n"
      "ds_read_b64_tr_b16 v[82:83], %8 offset:2048\n"
      "v_mfma_f32_16x16x32_bf16 %7, v[148:151], v[168:171], %7\n"
      "ds_read_b64 v[96:97], %9 offset:64\n"
      "v_mfma_f32_16x16x32_bf16 %3, v[132:135], v[168:171], %3\n"
      "ds_read_b64 v[98:99], %9 offset:72\n"
      "v_mfma_f32_16x16x32_bf16 %2, v[136:139], v[160:163], %2\n"
      "ds_read_b64_tr_b16 v[84:85], %8 offset:2592\n"
      "v_mfma_f32_16x16x32_bf16 %5, v[144:147], v[164:167], %5\n"
      "ds_read_b64_tr_b16 v[86:87], %8 offset:2560\n"
      "v_mfma_f32_16x16x32_bf16 %7, v[152:155], v[164:167], %7\n"
      "ds_read_b64_tr_b16 v[88:89], %8 offset:3104\n"
      "v_mfma_f32_16x16x32_bf16 %6, v[152:155], v[160:163], %6\n"
      "ds_read_b64_tr_b16 v[90:91], %8 offset:3072\n"
      "v_mfma_f32_16x16x32_bf16 %3, v[136:139], v[164:167], %3\n"
      "ds_read_b64_tr_b16 v[92:93], %8 offset:3616\n"
      "v_mfma_f32_16x16x32_bf16 %1, v[132:135], v[160:163], %1\n"
      "ds_read_b64_tr_b16 v[94:95], %8 offset:3584\n"
      "v_mfma_f32_16x16x32_bf16 %7, v[156:159], v[160:163], %7\n"
      "v_mfma_f32_16x16x32_bf16 %5, v[148:151], v[160:163], %5\n"
      "v_mfma_f32_16x16x32_bf16 %4, v[144:147], v[160:163], %4\n"
      "v_mfma_f32_16x16x32_bf16 %3, v[140:143], v[160:163], %3\n"
      "s_waitcnt lgkmcnt(0)\n"
      "s_sub_u32 %10, %10, 1\n"
      "s_cmp_lg_u32 %10, 0\n"
      "s_cbranch_scc1 Lconv%=\n"
      "s_nop 7\n"
      : "+v"(acc[0]), "+v"(acc[1]), "+v"(acc[2]), "+v"(acc[3]), "+v"(acc[4]), "+v"(acc[5]), "+v"(acc[6]), "+v"(acc[7]), "+v"(ka), "+v"(hb), "=&s"(cnt)
      :: "memory", "scc", "v64", "v65", "v66", "v67", "v68", "v69", "v70", "v71", "v72", "v73", "v74", "v75", "v76", "v77", "v78", "v79", "v80", "v81", "v82", "v83", "v84", "v85", "v86", "v87", "v88", "v89", "v90", "v91", "v92", "v93", "v94", "v95", "v96", "v97", "v98", "v99", "v100", "v101", "v102", "v103", "v104", "v105", "v106", "v107", "v108", "v109", "v110", "v111", "v112", "v113", "v114", "v115", "v116", "v117", "v118", "v119", "v120", "v121", "v122", "v123", "v124", "v125", "v126", "v127", "v128", "v129", "v130", "v131", "v132", "v133", "v134", "v135", "v136", "v137", "v138", "v139", "v140", "v141", "v142", "v143", "v144", "v145", "v146", "v147", "v148", "v149", "v150", "v151", "v152", "v153", "v154", "v155", "v156", "v157", "v158", "v159", "v160", "v161", "v162", "v163", "v164", "v165", "v166", "v167", "v168", "v169", "v170", "v171", "v172", "v173", "v174", "v175", "v176", "v177", "v178", "v179", "v180", "v181", "v182", "v183", "v184", "v185", "v186", "v187", "v188", "v189", "v190", "v191");
  }
  f4* ex = (f4*)(sm + CV_EX);
  if (half) {
#pragma unroll
    for (int i = 0; i < 4; ++i) { ex[(w * 4 + i) * 64 + lane] = acc[i]; acc[i] = acc[4 + i]; }
  } else {
#pragma unroll
    for (int i = 0; i < 4; ++i) ex[(w * 4 + i) * 64 + lane] = acc[4 + i];
  }
  volatile LAS u32* fl = (volatile LAS u32*)(size_t)(ldsaddr(sm) + CV_FL);
  asm volatile("s_waitcnt lgkmcnt(0)" ::: "memory");
  fl[w] = 1u;
  u2 xr[4];
#pragma unroll
  for (int i = 0; i < 4; ++i) xr[i] = *(const u2*)(sm + CV_X + 4096 * ch + 2 * (256 * (4 * half + i) + 16 * c + 4 * g));
  while (fl[w ^ 1] == 0u) {}
  asm volatile("" ::: "memory");
  u16* x1t = (u16*)(ws + WS_X1T);
  const float gc = ga * c2;
#pragma unroll
  for (int i = 0; i < 4; ++i) {
    const f4 y = (acc[i] + ex[((w ^ 1) * 4 + i) * 64 + lane]) * gc + (f4){blo(xr[i].x), bhi(xr[i].x), blo(xr[i].y), bhi(xr[i].y)} * c2;
    *(u2*)(x1t + d * L + 256 * (4 * half + i) + 16 * c + 4 * g) = (u2){pk(y[0], y[1]), pk(y[2], y[3])};
  }
}

DI void k2_help(char* sm, int b, const u16* wg, const u16* wu, char* ws) {
  const int t = threadIdx.x - 256, w = __builtin_amdgcn_readfirstlane(t >> 6), lane = t & 63;
  const int d0 = 64 * (b & 7) + 2 * (b >> 3);
  const u16* kx = (const u16*)(ws + WS_KX);
  u4 kv[2];
  u2 kp[2];
#pragma unroll
  for (int i = 0; i < 2; ++i) { kv[i] = *(const u4*)(kx + (d0 + i) * KXS + 8 + 8 * t); kp[i] = *(const u2*)(kx + (d0 + i) * KXS + 4 + 8 * t); }
  SB();
  if (t < 16) *(u4*)(sm + (t >> 1) * CV_KC + 16 * (t & 1)) = (u4){0u, 0u, 0u, 0u};
  if (w == 1) *(u4*)(sm + CV_H + (lane >> 3) * CV_HP + 16 * (t & 7)) = (u4){0u, 0u, 0u, 0u};
  if (t == 128) *(u4*)(sm + CV_FL) = (u4){0u, 0u, 0u, 0u};
#pragma unroll
  for (int i = 0; i < 2; ++i) {
    const u4 v = kv[i];
    const u32 a0 = __builtin_amdgcn_alignbit(kp[i].y, kp[i].x, 16), a1 = __builtin_amdgcn_alignbit(v.x, kp[i].y, 16), a2 = __builtin_amdgcn_alignbit(v.y, v.x, 16),
              a3 = __builtin_amdgcn_alignbit(v.z, v.y, 16), a4 = __builtin_amdgcn_alignbit(v.w, v.z, 16);
    char* kd = sm + 4 * i * CV_KC + 32 + 16 * t;
    *(u4*)kd = v;
    *(u4*)(kd + CV_KC) = (u4){a1, a2, a3, a4};
    *(u4*)(kd + 2 * CV_KC) = (u4){kp[i].y, v.x, v.y, v.z};
    *(u4*)(kd + 3 * CV_KC) = (u4){a0, a1, a2, a3};
  }
  bar();
  const int task = b + 256 * w, kq = task & 3, blk = task >> 2, mat = blk >= 43, jb = blk - 43 * mat;
  const bool work = task < 344;
  const int hh = lane >> 5;
  u4 vec[8];
  if (work) {
    const rsrc_t rw = mkrsrc(mat ? wu : wg, D * DF * 2);
    const u32 vo = (128 * kq + (lane >> 2)) * (DF * 2) + 64 * jb + 16 * (lane & 3);
    asm volatile("s_nop 4");
#pragma unroll
    for (int i = 0; i < 8; ++i) glds16<0>(ldsaddr(sm) + CV_W + 8192 * w + 1024 * i, vo, rw, i * 16 * DF * 2);
    const char* cv = ws + WS_CV + ((lane & 1) ? 0 : 1024) + 256 * kq + 16 * hh;
    sfor<0, 8>([&](auto i) { gld16<32 * i>(vec[i], cv); });
  }
  u32 dummy[2];
  {
    const int row = 32 * (b >> 3) + 8 * w + (lane >> 3);
    const char* base = (const char*)(row >= D ? wu : wg) + (row & (D - 1)) * (DF * 2);
#pragma unroll
    for (int i = 0; i < 2; ++i) {
      const int off = min((b & 1) * 1344 + 128 * ((lane & 7) + 8 * i), (b & 1) ? DF * 2 - 4 : 1340);
      asm volatile("global_load_dword %0, %1, off" : "=v"(dummy[i]) : "v"(base + off));
    }
  }
  if (work) {
    asm volatile("s_waitcnt vmcnt(2)" : "+v"(vec[0]), "+v"(vec[1]), "+v"(vec[2]), "+v"(vec[3]), "+v"(vec[4]), "+v"(vec[5]), "+v"(vec[6]), "+v"(vec[7]) :: "memory");
    const u32 ra = ldsaddr(sm) + CV_W + 8192 * w + 64 * (8 * hh + ((lane & 15) >> 2)) + 32 * ((lane >> 4) & 1) + 8 * (lane & 3);
    f16v sums;
#pragma unroll
    for (int e = 0; e < 16; ++e) sums[e] = 0.f;
#pragma unroll
    for (int i = 0; i < 8; ++i) sums = mfma32(vec[i], cat(trd(ra + 1024 * i), trd(ra + 1024 * i + 256)), sums);
    if (lane < 32) {
      u32* exf = (u32*)(ws + WS_EX) + ((2 * jb + (lane >> 4)) * 64 + 16 * mat + (lane & 15)) * 4 + kq;
      const float h0 = rb(sums[0]), h1 = rb(sums[1]);
      exf[0] = pk(h0, sums[0] - h0);
      exf[128] = pk(h1, sums[1] - h1);
    }
  }
  asm volatile("s_waitcnt vmcnt(0)" : "+v"(dummy[0]), "+v"(dummy[1]) :: "memory");
}

__global__ __launch_bounds__(512, 2) void k2_conv_prep(const u16* wg, const u16* wu, char* ws) {
  extern __shared__ __attribute__((aligned(16))) char sm[];
  const int b = blockIdx.x;
  if (__builtin_amdgcn_readfirstlane(threadIdx.x) < 256) k2_conv(sm, b, ws);
  else k2_help(sm, b, wg, wu, ws);
}

constexpr int G1_LEAD = 3, G1_NS = 5, G1_SLOT = 20480;
constexpr int G2_RES = 19, G2_FL = 4 * G2_RES * 2048, G1_SV = G2_FL + 64, K3_LDS = G1_SV + 512;
constexpr int G2_LEAD = 6;
constexpr int HH_T = NU * 1024;

template <int K> DI u32 g1_pair(const f16v& acc, float rs, float sd) {
  const f2 g = (f2){acc[2 * K], acc[2 * K + 1]} * rs;
  const f2 z = g * (g * g * -0.10294324f + -2.30220819f);
  const f2 dn = (f2){__builtin_amdgcn_exp2f(z.x), __builtin_amdgcn_exp2f(z.y)} * sd + sd;
  const f2 r = g * (f2){acc[8 + 2 * K], acc[9 + 2 * K]} * (f2){__builtin_amdgcn_rcpf(dn.x), __builtin_amdgcn_rcpf(dn.y)};
  return pk(r.x, r.y);
}
DI u4 g1_tile(const f16v& acc, float rs, float sd) { return (u4){g1_pair<0>(acc, rs, sd), g1_pair<1>(acc, rs, sd), g1_pair<2>(acc, rs, sd), g1_pair<3>(acc, rs, sd)}; }

DI void g1_stats(char* sm, int rt, char* ws) {
  const int t = threadIdx.x;
  float* red = (float*)sm;
  if (t < 512) {
    const u16* xp = (const u16*)(ws + WS_X1T) + (t >> 4) * L + 128 * rt + 8 * (t & 15);
    u4 v[16];
    float iv[16];
#pragma unroll
    for (int i = 0; i < 16; ++i) { v[i] = *(const u4*)(xp + 32 * i * L); iv[i] = ((const float*)(ws + WS_INV))[(t >> 4) + 32 * i]; }
    f2 s1[4], s2[4];
#pragma unroll
    for (int j = 0; j < 4; ++j) s1[j] = s2[j] = (f2){0.f, 0.f};
#pragma unroll
    for (int i = 0; i < 16; ++i)
#pragma unroll
      for (int j = 0; j < 4; ++j) { const f2 x = (f2){blo(v[i][j]), bhi(v[i][j])} * iv[i]; s1[j] += x; s2[j] += x * x; }
#pragma unroll
    for (int j = 0; j < 4; ++j) *(f4*)(red + (t >> 4) * 260 + 16 * (t & 15) + 4 * j) = (f4){s1[j].x, s2[j].x, s1[j].y, s2[j].y};
  }
  __syncthreads();
  if (t < 512) {
    f2 p = {0.f, 0.f};
#pragma unroll
    for (int i = 0; i < 8; ++i) p += *(const f2*)(red + (8 * (t & 3) + i) * 260 + 2 * (t >> 2));
    p.x += dppf<0xb1>(p.x); p.y += dppf<0xb1>(p.y); p.x += dppf<0x4e>(p.x); p.y += dppf<0x4e>(p.y);
    const f2 ms = ln_stats(p.x, p.y);
    if ((t & 3) == 0)
      __hip_atomic_store((u32*)(ws + WS_ST2) + 128 * rt + (t >> 2), (__float_as_uint(ms.x) >> 16) | __float_as_uint(ms.y), __ATOMIC_RELAXED, __HIP_MEMORY_SCOPE_AGENT);
  }
  __syncthreads();
}

__global__ __launch_bounds__(768, 3) void k3_mlp(const u16* wg, const u16* wu, const u16* wd, char* ws, u16* out) {
  extern __shared__ __attribute__((aligned(16))) char sm[];
  const int b = blockIdx.x, j = b >> 3;
  const int t = threadIdx.x, w12 = __builtin_amdgcn_readfirstlane(t >> 6), w = w12 & 3, lane = t & 63, hh = lane >> 5;
  const u32 lb0 = ldsaddr(sm);
  const int q = (lane & 15) >> 2, p = lane & 3, g = lane >> 4;
  const int lt = 4 * (b & 7) + (j & 3), dt = j >> 2;
  auto kfirst = [&](int kq) { const u32 k = 22 * kq - (kq == 3) + 3 * dt; return min(k, k - NU); };
  auto knext = [](u32 k) { return min(k + 1, k + 1 - NU); };
  const rsrc_t rd = mkrsrc(wd, DF * D * 2);
  const u32 vd = (lane >> 3) * 1024 + 128 * dt + 16 * ((lane & 7) ^ (4 * ((lane >> 4) & 1)));
  volatile LAS u32* fl = (volatile LAS u32*)(size_t)(lb0 + G2_FL);
  auto fill2 = [&](int i0, int n, int slot0) {
    u32 kd = kfirst(w) + i0;
    kd = min(kd, kd - NU);
#pragma unroll 1
    for (int i = 0; i < n; ++i) {
      glds16<0>(lb0 + (G2_RES * w + slot0 + i) * 2048, vd, rd, kd * 16384);
      glds16<0>(lb0 + (G2_RES * w + slot0 + i) * 2048 + 1024, vd, rd, kd * 16384 + 8192);
      kd = knext(kd);
    }
  };
  {
    const int rt = 4 * ((b & 7) >> 1) + (j & 3), cg = (j >> 2) + 8 * (b & 1) - 1;
    if (t == 0) fl[0] = 0u;
    if (cg < 0) {
      g1_stats(sm, rt, ws);
    } else if (w12 >= 8) {
      const rsrc_t rx = mkrsrc(ws + WS_X1T, D * L * 2), rg = mkrsrc(wg, D * DF * 2), ru = mkrsrc(wu, D * DF * 2);
      const u32 vx = (4 * w + g) * 4096 + 256 * rt + 16 * ((lane & 15) ^ ((g << 2) | w));
      u32 vw[3];
#pragma unroll
      for (int i = 0; i < 3; ++i) {
        const int pc = (3 * (w & 1) + i) * 64 + lane, row = pc / 12, chk = pc - 12 * row;
        vw[i] = row * (DF * 2) + min(192 * cg + 16 * (chk ^ (w & 2)), DF * 2 - 16);
      }
      const rsrc_t rwm = w < 2 ? rg : ru;
      auto stage = [&](int i, int slot) {
        const u32 m = lb0 + slot * G1_SLOT;
        glds16<0>(m + 1024 * w, vx, rx, i * 131072);
        glds16<0>(m + 4096 + 1024 * w, vx, rx, i * 131072 + 65536);
#pragma unroll
        for (int k = 0; k < 3; ++k) glds16<0>(m + 8192 + 3072 * w + 1024 * k, vw[k], rwm, i * (32 * DF * 2));
      };
      asm volatile("s_nop 4");
      static_assert(G1_LEAD + 2 <= G1_NS, "ring too short");
#pragma unroll 1
      for (int i = 0; i < G1_LEAD - 1; ++i) stage(i, i);
      int slot = G1_LEAD - 1;
#pragma unroll 1
      for (int i = 0; i < 17 - G1_LEAD; ++i) {
        stage(i + G1_LEAD - 1, slot);
        slot = slot == G1_NS - 1 ? 0 : slot + 1;
        vmwait<5 * (G1_LEAD - 1)>();
        rawbar();
      }
      const u32* st2 = (const u32*)(ws + WS_ST2) + 128 * rt + 32 * w + (lane & 31);
      u32 v;
      asm volatile("global_load_dword %0, %1, off sc1" : "=v"(v) : "v"(st2));
      sfor<17 - G1_LEAD, 16>([&](auto i) { vmwait<5 * (15 - i) + 1>(); rawbar(); });
      asm volatile("s_waitcnt vmcnt(0)" : "+v"(v));
#pragma unroll 1
      for (int spin = 0; spin < (1 << 16) && !__all(v != 0u); ++spin) {
        __builtin_amdgcn_s_sleep(2);
        v = __hip_atomic_load(st2, __ATOMIC_RELAXED, __HIP_MEMORY_SCOPE_AGENT);
      }
      ((u32*)(sm + G1_SV))[32 * w + (lane & 31)] = v;
      bar();
      bar();
    } else if (w12 >= 4) {
      const char* pf = (const char*)wd + j * (43 * 1024) + w * (43 * 256);
#pragma unroll 1
      for (int i = 0; i < 18; ++i) {
        rawbar();
      }
    } else {
      const int wl = w & 1, wu3 = 3 * (w >> 1), u0 = 6 * cg + wu3;
      u32 xa[2][2], wa[3];
#pragma unroll
      for (int c = 0; c < 2; ++c)
#pragma unroll
        for (int tt = 0; tt < 2; ++tt)
          xa[c][tt] = lb0 + 256 * (8 * hh + 4 * tt + q) + 8 * (p & 1) + 16 * ((4 * (2 * wl + c) + 2 * (g & 1) + (p >> 1)) ^ ((q << 2) | (2 * hh + tt)));
#pragma unroll
      for (int v = 0; v < 3; ++v) wa[v] = lb0 + 8192 + 6144 * (g & 1) + 192 * (8 * hh + q) + 16 * ((2 * (wu3 + v) + (p >> 1)) ^ (2 * (g & 1))) + 8 * (p & 1);
      u4 ex[3];
#pragma unroll
      for (int v = 0; v < 3; ++v) ex[v] = *(const u4*)(ws + WS_EX + min(u0 + v, NU - 1) * 1024 + lane * 16);
      f16v acc[6];
#pragma unroll
      for (int i = 0; i < 6; ++i)
#pragma unroll
        for (int e = 0; e < 16; ++e) acc[i][e] = 0.f;
#pragma unroll
      for (int i = 0; i < 6; ++i) asm volatile("" : "+v"(acc[i]));
      rawbar();
      {
        u32 c0, c1, c2;
        asm volatile(
          "ds_read_b64_tr_b16 v[112:113], %6 offset:0\n"
          "ds_read_b64_tr_b16 v[114:115], %7 offset:0\n"
          "ds_read_b64_tr_b16 v[120:121], %10 offset:0\n"
          "ds_read_b64_tr_b16 v[122:123], %10 offset:768\n"
          "ds_read_b64_tr_b16 v[116:117], %8 offset:0\n"
          "ds_read_b64_tr_b16 v[118:119], %9 offset:0\n"
          "ds_read_b64_tr_b16 v[124:125], %11 offset:0\n"
          "ds_read_b64_tr_b16 v[126:127], %11 offset:768\n"
          "ds_read_b64_tr_b16 v[128:129], %12 offset:0\n"
          "ds_read_b64_tr_b16 v[130:131], %12 offset:768\n"
          "s_mov_b32 %13, 16\n"
          "s_mov_b32 %14, 0\n"
          "Lg1%=:\n"
          "s_add_u32 %14, %14, 1\n"
          "s_cmp_eq_u32 %14, 5\n"
          "s_cselect_b32 %15, -81920, %16\n"
          "s_cselect_b32 %14, 0, %14\n"
          "s_waitcnt lgkmcnt(6)\n"
          "v_mfma_f32_32x32x16_bf16 %0, v[120:123], v[112:115], %0\n"
          "ds_read_b64_tr_b16 v[132:133], %6 offset:4096\n"
          "ds_read_b64_tr_b16 v[134:135], %7 offset:4096\n"
          "s_waitcnt lgkmcnt(6)\n"
          "v_mfma_f32_32x32x16_bf16 %1, v[120:123], v[116:119], %1\n"
          "ds_read_b64_tr_b16 v[140:141], %10 offset:3072\n"
          "ds_read_b64_tr_b16 v[142:143], %10 offset:3840\n"
          "s_waitcnt lgkmcnt(6)\n"
          "v_mfma_f32_32x32x16_bf16 %2, v[124:127], v[112:115], %2\n"
          "ds_read_b64_tr_b16 v[136:137], %8 offset:4096\n"
          "ds_read_b64_tr_b16 v[138:139], %9 offset:4096\n"
          "v_mfma_f32_32x32x16_bf16 %3, v[124:127], v[116:119], %3\n"
          "ds_read_b64_tr_b16 v[144:145], %11 offset:3072\n"
          "ds_read_b64_tr_b16 v[146:147], %11 offset:3840\n"
          "s_waitcnt lgkmcnt(8)\n"
          "v_mfma_f32_32x32x16_bf16 %4, v[128:131], v[112:115], %4\n"
          "ds_read_b64_tr_b16 v[148:149], %12 offset:3072\n"
          "ds_read_b64_tr_b16 v[150:151], %12 offset:3840\n"
          "v_add_u32 %6, %15, %6\n"
          "v_add_u32 %7, %15, %7\n"
          "v_add_u32 %8, %15, %8\n"
          "v_add_u32 %9, %15, %9\n"
          "v_mfma_f32_32x32x16_bf16 %5, v[128:131], v[116:119], %5\n"
          "v_add_u32 %10, %15, %10\n"
          "v_add_u32 %11, %15, %11\n"
          "v_add_u32 %12, %15, %12\n"
          "s_barrier\n"
          "s_waitcnt lgkmcnt(6)\n"
          "v_mfma_f32_32x32x16_bf16 %0, v[140:143], v[132:135], %0\n"
          "ds_read_b64_tr_b16 v[112:113], %6 offset:0\n"
          "ds_read_b64_tr_b16 v[114:115], %7 offset:0\n"
          "s_waitcnt lgkmcnt(6)\n"
          "v_mfma_f32_32x32x16_bf16 %1, v[140:143], v[136:139], %1\n"
          "ds_read_b64_tr_b16 v[120:121], %10 offset:0\n"
          "ds_read_b64_tr_b16 v[122:123], %10 offset:768\n"
          "s_waitcnt lgkmcnt(6)\n"
          "v_mfma_f32_32x32x16_bf16 %2, v[144:147], v[132:135], %2\n"
          "ds_read_b64_tr_b16 v[116:117], %8 offset:0\n"
          "ds_read_b64_tr_b16 v[118:119], %9 offset:0\n"
          "v_mfma_f32_32x32x16_bf16 %3, v[144:147], v[136:139], %3\n"
          "ds_read_b64_tr_b16 v[124:125], %11 offset:0\n"
          "ds_read_b64_tr_b16 v[126:127], %11 offset:768\n"
          "s_waitcnt lgkmcnt(8)\n"
          "v_mfma_f32_32x32x16_bf16 %4, v[148:151], v[132:135], %4\n"
          "ds_read_b64_tr_b16 v[128:129], %12 offset:0\n"
          "ds_read_b64_tr_b16 v[130:131], %12 offset:768\n"
          "s_sub_u32 %13, %13, 1\n"
          "s_cmp_lg_u32 %13, 0\n"
          "v_mfma_f32_32x32x16_bf16 %5, v[148:151], v[136:139], %5\n"
          "s_cbranch_scc1 Lg1%=\n"
          "s_waitcnt lgkmcnt(0)\n"
          : "+v"(acc[0]), "+v"(acc[1]), "+v"(acc[2]), "+v"(acc[3]), "+v"(acc[4]), "+v"(acc[5]), "+v"(xa[0][0]), "+v"(xa[0][1]), "+v"(xa[1][0]), "+v"(xa[1][1]),
            "+v"(wa[0]), "+v"(wa[1]), "+v"(wa[2]), "=&s"(c0), "=&s"(c1), "=&s"(c2)
          : "s"(G1_SLOT) : "memory", "scc", "v112", "v113", "v114", "v115", "v116", "v117", "v118", "v119", "v120", "v121", "v122", "v123", "v124", "v125", "v126", "v127", "v128", "v129", "v130", "v131", "v132", "v133", "v134", "v135", "v136", "v137", "v138", "v139", "v140", "v141", "v142", "v143", "v144", "v145", "v146", "v147", "v148", "v149", "v150", "v151");
      }
      u32 sv[2];
#pragma unroll
      for (int c = 0; c < 2; ++c) sv[c] = ((const u32*)(sm + G1_SV))[64 * wl + 32 * c + (lane & 31)];
      bar();
#pragma unroll
      for (int c = 0; c < 2; ++c) {
        const u32 bv = hh ? __builtin_amdgcn_perm(sv[c], sv[c], 0x03020302u) : (__builtin_amdgcn_perm(sv[c], sv[c], 0x01000100u) ^ 0x80008000u);
#pragma unroll
        for (int v = 0; v < 3; ++v) acc[2 * v + c] = mfma32(ex[v], (u4){bv, bv, bv, bv}, acc[2 * v + c]);
      }
      char* hp = ws + WS_HH + (4 * rt + 2 * wl) * HH_T + u0 * 1024 + 16 * lane;
#pragma unroll
      for (int v = 0; v < 3; ++v) {
        if (u0 + v < NU) {
#pragma unroll
          for (int c = 0; c < 2; ++c) {
            const float sd = bhi(sv[c]);
            const u4 o = g1_tile(acc[2 * v + c], __builtin_amdgcn_rcpf(sd), sd);
            char* dst = hp + c * HH_T + v * 1024;
            asm volatile("global_store_dwordx4 %0, %1, off sc1\n\ts_nop 1" :: "v"(dst), "v"(o) : "memory");
          }
        }
      }
      {
        asm volatile("s_waitcnt vmcnt(0)" ::: "memory");
        if (lane == 0 && __hip_atomic_fetch_add((LAS u32*)fl, 1u, __ATOMIC_RELAXED, __HIP_MEMORY_SCOPE_WORKGROUP) == 3u)
          __hip_atomic_fetch_add((u32*)(ws + WS_CNT) + 64 * rt, 1u, __ATOMIC_RELAXED, __HIP_MEMORY_SCOPE_AGENT);
      }
    }
  }
  if (w12 >= 8) {
    asm volatile("s_nop 4");
    fill2(G2_RES, 3, 0);
    fill2(3, G2_RES - 3, 3);
    vmwait<2 * (G2_RES - 3)>();
    rawbar();
    rawbar();
    fill2(0, 3, 0);
    vmwait<0>();
    rawbar();
    rawbar();
    rawbar();
  } else {
    const int wl = w12 & 1, kq = w12 >> 1, dc = 64 * dt + 16 * kq + 4 * hh;
    f4 gb[2], xr[2];
#pragma unroll
    for (int e = 0; e < 2; ++e) {
      gb[e] = *(const f4*)((const float*)(ws + WS_MOD) + 5 * D + dc + 8 * e);
      xr[e] = *(const f4*)((const float*)(ws + WS_INV) + dc + 8 * e);
#pragma unroll
      for (int i = 0; i < 4; ++i) xr[e][i] *= b2f(((const u16*)(ws + WS_X1T))[(dc + 8 * e + i) * L + 64 * lt + 32 * wl + (lane & 31)]);
    }
    u32 pa[2];
#pragma unroll
    for (int e = 0; e < 2; ++e)
      pa[e] = lb0 + G2_RES * 2048 * kq + 128 * (4 * hh + q) + 16 * ((((4 * e + 2 * (g & 1)) ^ (2 * kq)) + (p >> 1)) ^ (4 * (q >> 1))) + 8 * (p & 1);
    f16v acc[2];
#pragma unroll
    for (int e = 0; e < 16; ++e) acc[0][e] = acc[1][e] = 0.f;
    asm volatile("" : "+v"(xr[0]), "+v"(xr[1]), "+v"(gb[0]), "+v"(gb[1]), "+v"(acc[0]), "+v"(acc[1]));
    rawbar();
    u4 A[2][2], AR[3][2];
#pragma unroll
    for (int i = 0; i < 6; ++i) AR[i >> 1][i & 1] = cat(trd(pa[i & 1] + (i >> 1) * 2048), trd(pa[i & 1] + (i >> 1) * 2048 + 1024));
    asm volatile("s_waitcnt lgkmcnt(0)\n\ts_barrier" : "+v"(AR[0][0]), "+v"(AR[0][1]), "+v"(AR[1][0]), "+v"(AR[1][1]), "+v"(AR[2][0]), "+v"(AR[2][1]) :: "memory");
    if (w12 == 0) {
      const u32* cnt = (const u32*)(ws + WS_CNT) + 64 * (lt >> 1);
#pragma unroll 1
      for (int spin = 0; spin < (1 << 18); ++spin)
        if (__hip_atomic_load(cnt, __ATOMIC_RELAXED, __HIP_MEMORY_SCOPE_AGENT) >= 15u) break;
    }
    rawbar();
    const rsrc_t rh = mkrsrc(ws + WS_HH + (2 * lt + wl) * HH_T, HH_T);
    constexpr int LEAD = G2_LEAD, NH = LEAD + 2;
    u4 H[NH];
    u32 kh = kfirst(kq);
    auto ldh = [&](auto i) { if constexpr (i < 22) { bld16_sc1<0>(H[i % NH], lane * 16, rh, kh * 1024); kh = knext(kh); } };
    sfor<0, LEAD>(ldh);
    auto rda = [&](auto i) {
#pragma unroll
      for (int e = 0; e < 2; ++e) A[i & 1][e] = cat(trd(pa[e] + i * 2048), trd(pa[e] + i * 2048 + 1024));
    };
    rda(std::integral_constant<int, 0>{});
    sfor<0, 22>([&](auto i) {
      vmwait<(i < 22 - LEAD ? LEAD - 1 : 21 - i)>(H[i % NH]);
      SB();
      if constexpr (i + 1 < G2_RES) rda(std::integral_constant<int, i + 1>{});
      if (i < 21 || kq < 2) {
#pragma unroll
        for (int e = 0; e < 2; ++e) acc[e] = mfma32(i < G2_RES ? A[i & 1][e] : AR[i < G2_RES ? 0 : i - G2_RES][e], H[i % NH], acc[e]);
      }
      if constexpr (i + 1 < G2_RES) { SGB(0x8, 1); SGB(0x100, 2); SGB(0x8, 1); SGB(0x100, 2); }
      SB();
      ldh(std::integral_constant<int, i + LEAD>{});
      if constexpr (i == G2_RES - 1) rawbar();
    });
    f4* red = (f4*)sm + wl * 1536 + lane;
#pragma unroll
    for (int k = 1; k < 4; ++k)
#pragma unroll
      for (int e = 0; e < 2; ++e) {
        const f16v& a = acc[k >> 1];
        const int r = 8 * (k & 1) + 4 * e;
        red[(kq ^ k) * 384 + (k - 1) * 128 + 64 * e] = (f4){a[r], a[r + 1], a[r + 2], a[r + 3]};
      }
    bar();
    u2 o[2];
#pragma unroll
    for (int e = 0; e < 2; ++e) {
      const f4* rp = red + kq * 384 + 64 * e;
      const f4 y = ((f4){acc[0][4 * e], acc[0][4 * e + 1], acc[0][4 * e + 2], acc[0][4 * e + 3]} + rp[0] + rp[128] + rp[256]) * gb[e] + xr[e];
      o[e] = (u2){pk(y[0], y[1]), pk(y[2], y[3])};
    }
    const auto r0 = __builtin_amdgcn_permlane32_swap(o[0].x, o[1].x, false, false);
    const auto r1 = __builtin_amdgcn_permlane32_swap(o[0].y, o[1].y, false, false);
    *(u4*)(out + (64 * lt + 32 * wl + (lane & 31)) * D + 64 * dt + 16 * kq + 8 * hh) = (u4){r0[0], r1[0], r0[1], r1[1]};
  }
}

extern "C" void kernel_launch(void* const* d_in, const int* in_sizes, int n_in, void* d_out, int out_size, void* d_ws, size_t ws_size,
                              hipStream_t stream) {
  const u16* x = (const u16*)d_in[0];
  const u16 *wg = (const u16*)d_in[8], *wu = (const u16*)d_in[9], *wd = (const u16*)d_in[10];
  char* ws = (char*)d_ws;
  (void)hipFuncSetAttribute((const void*)k2_conv_prep, hipFuncAttributeMaxDynamicSharedMemorySize, K2_LDS);
  (void)hipFuncSetAttribute((const void*)k3_mlp, hipFuncAttributeMaxDynamicSharedMemorySize, K3_LDS);
  k1_prep<<<496, 256, 0, stream>>>(x, (const u16*)d_in[1], (const u16*)d_in[2], (const u16*)d_in[3], (const u16*)d_in[4], (const u16*)d_in[5],
                                   (const u16*)d_in[6], (const u16*)d_in[11], (const u16*)d_in[12], ws);
  k2_conv_prep<<<256, 512, K2_LDS, stream>>>(wg, wu, ws);
  k3_mlp<<<256, 768, K3_LDS, stream>>>(wg, wu, wd, ws, (u16*)d_out);
}
```

```cpp
#include <hip/hip_runtime.h>
#include <hip/hip_bf16.h>
#include <stdint.h>
#include <type_traits>

typedef unsigned short u16;
typedef unsigned u32;
typedef float f2 __attribute__((ext_vector_type(2)));
typedef float f4 __attribute__((ext_vector_type(4)));
typedef float f16v __attribute__((ext_vector_type(16)));
typedef u32 u2 __attribute__((ext_vector_type(2)));
typedef u32 u4 __attribute__((ext_vector_type(4)));
typedef u4 u4a8 __attribute__((aligned(8)));
typedef short s4 __attribute__((ext_vector_type(4)));
typedef __bf16 bf2 __attribute__((ext_vector_type(2)));
typedef __bf16 bf8 __attribute__((ext_vector_type(8)));
typedef _Float16 h2 __attribute__((ext_vector_type(2)));
typedef _Float16 h8 __attribute__((ext_vector_type(8)));
#define LAS __attribute__((address_space(3)))
#define DI __device__ __forceinline__

constexpr int L = 2048, D = 512, NM = 128, DF = 1376, NU = 86;
constexpr int KXS = 2056;

constexpr size_t WS_MOD = 0, WS_MEAN = 16 << 10, WS_SD = 20 << 10, WS_CV = 24 << 10, WS_INV = 26 << 10, WS_ST2 = 32 << 10, WS_CNT = 40 << 10,
                 WS_EX = 64 << 10, WS_XT = 1 << 20, WS_KX = 4 << 20, WS_X1T = 8 << 20, WS_HH = 20 << 20;

template <class T> DI T ldnt(const T* p) { return __builtin_nontemporal_load(p); }
DI float blo(u32 v) { return __uint_as_float(v << 16); }
DI float bhi(u32 v) { return __uint_as_float(v & 0xffff0000u); }
DI float b2f(u16 v) { return __uint_as_float(((u32)v) << 16); }
DI u32 pk(float a, float b) { return __builtin_bit_cast(u32, __builtin_convertvector((f2){a, b}, bf2)); }
DI u32 pkh(f2 v) { return __builtin_bit_cast(u32, __builtin_convertvector(v, h2)); }
DI float rb(float a) { return blo(pk(a, a)); }
DI u2 trd(const char* p) { return __builtin_bit_cast(u2, __builtin_amdgcn_ds_read_tr16_b64_v4i16((LAS s4*)p)); }
DI u2 trd(u32 a) { return __builtin_bit_cast(u2, __builtin_amdgcn_ds_read_tr16_b64_v4i16((LAS s4*)(size_t)a)); }
DI u4 cat(u2 a, u2 b) { return (u4){a.x, a.y, b.x, b.y}; }
DI float dot2(u32 a, u32 b, float c) { return __builtin_amdgcn_fdot2_f32_bf16(__builtin_bit_cast(bf2, a), __builtin_bit_cast(bf2, b), c, false); }
DI f4 mfma16(u4 a, u4 b, f4 c) { return __builtin_amdgcn_mfma_f32_16x16x32_bf16(__builtin_bit_cast(bf8, a), __builtin_bit_cast(bf8, b), c, 0, 0, 0); }
DI f4 mfma16h(u4 a, u4 b, f4 c) { return __builtin_amdgcn_mfma_f32_16x16x32_f16(__builtin_bit_cast(h8, a), __builtin_bit_cast(h8, b), c, 0, 0, 0); }
DI f16v mfma32(u4 a, u4 b, f16v c) { return __builtin_amdgcn_mfma_f32_32x32x16_bf16(__builtin_bit_cast(bf8, a), __builtin_bit_cast(bf8, b), c, 0, 0, 0); }
#define CM_MUL(d, a, b) "v_pk_mul_f32 " d ", " a ", " b " op_sel_hi:[0,1]\n\t"
#define CM_FMA(d, a, b) "v_pk_fma_f32 " d ", " a ", " b ", " d " op_sel:[1,1,0] op_sel_hi:[1,0,1] neg_lo:[0,1,0]\n\t"
DI void cmul2(f2& r0, f2 a0, f2 b0, f2& r1, f2 a1, f2 b1) {
  asm(CM_MUL("%0", "%2", "%3") CM_MUL("%1", "%4", "%5") CM_FMA("%0", "%2", "%3") CM_FMA("%1", "%4", "%5")
      : "=&v"(r0), "=&v"(r1) : "v"(a0), "v"(b0), "v"(a1), "v"(b1));
}
DI void cmul3(f2& r0, f2 a0, f2 b0, f2& r1, f2 a1, f2 b1, f2& r2, f2 a2, f2 b2) {
  asm(CM_MUL("%0", "%3", "%4") CM_MUL("%1", "%5", "%6") CM_MUL("%2", "%7", "%8") CM_FMA("%0", "%3", "%4") CM_FMA("%1", "%5", "%6") CM_FMA("%2", "%7", "%8")
      : "=&v"(r0), "=&v"(r1), "=&v"(r2) : "v"(a0), "v"(b0), "v"(a1), "v"(b1), "v"(a2), "v"(b2));
}
DI void cmul4(f2& r0, f2& r1, f2& r2, f2& r3, f2 a0, f2 a1, f2 a2, f2 a3, f2 b) {
  asm(CM_MUL("%0", "%4", "%8") CM_MUL("%1", "%5", "%8") CM_MUL("%2", "%6", "%8") CM_MUL("%3", "%7", "%8")
      CM_FMA("%0", "%4", "%8") CM_FMA("%1", "%5", "%8") CM_FMA("%2", "%6", "%8") CM_FMA("%3", "%7", "%8")
      : "=&v"(r0), "=&v"(r1), "=&v"(r2), "=&v"(r3) : "v"(a0), "v"(a1), "v"(a2), "v"(a3), "v"(b));
}
DI float exp_acc(float x) {
  const float t = x * 1.44269502f, r = fmaf(x, 1.44269502f, -t) + x * 1.92596299e-8f, e = __builtin_amdgcn_exp2f(t);
  return fmaf(e, r * 0.693147182f, e);
}
template <int CTRL> DI float dppf(float v) { return __int_as_float(__builtin_amdgcn_update_dpp(0, __float_as_int(v), CTRL, 0xf, 0xf, true)); }
DI float sum32(float v) {
  auto r = __builtin_amdgcn_permlane32_swap(__float_as_uint(v), __float_as_uint(v), false, false);
  return __uint_as_float(r[0]) + __uint_as_float(r[1]);
}
DI f2 ln_stats(float s1, float s2) {
  const float mean = s1 * (1.f / D);
  const float var = fmaxf(fmaf(-mean, mean, s2 * (1.f / D)), 0.f);
  return (f2){rb(mean), rb(__builtin_amdgcn_sqrtf(rb(rb(var) + 1e-5f)))};
}
typedef __amdgpu_buffer_rsrc_t rsrc_t;
DI rsrc_t mkrsrc(const void* p, u32 bytes) { return __builtin_amdgcn_make_buffer_rsrc((void*)p, 0, bytes, 0x00020000); }
DI u32 ldsaddr(const char* p) { return (u32)(size_t)(LAS const char*)p; }
template <int OFF> DI void glds16(u32 m0v, u32 voff, rsrc_t rs, u32 soff) {
  asm volatile("s_mov_b32 m0, %0\n\ts_nop 0\n\tbuffer_load_dwordx4 %1, %2, %3 offen offset:%4 lds" :: "s"(m0v), "v"(voff), "s"(rs), "s"(soff), "n"(OFF) : "memory");
}
DI void glds16x4(u32 m0v, u32 voff, rsrc_t rs, u32 soff) {
  asm volatile("s_mov_b32 m0, %0\n\ts_nop 0\n\tbuffer_load_dwordx4 %1, %2, %3 offen lds\n\tbuffer_load_dwordx4 %1, %2, %3 offen offset:1024 lds\n\t"
               "buffer_load_dwordx4 %1, %2, %3 offen offset:2048 lds\n\tbuffer_load_dwordx4 %1, %2, %3 offen offset:3072 lds"
               :: "s"(m0v), "v"(voff), "s"(rs), "s"(soff) : "memory");
}
template <int OFF> DI void bld16(u4& d, u32 voff, rsrc_t rs, u32 soff) {
  asm volatile("buffer_load_dwordx4 %0, %1, %2, %3 offen offset:%4" : "=v"(d) : "v"(voff), "s"(rs), "s"(soff), "n"(OFF));
}
template <int OFF> DI void bld16_sc1(u4& d, u32 voff, rsrc_t rs, u32 soff) {
  asm volatile("buffer_load_dwordx4 %0, %1, %2, %3 offen offset:%4 sc1" : "=v"(d) : "v"(voff), "s"(rs), "s"(soff), "n"(OFF));
}
template <int OFF, class T> DI void gld16(T& d, const void* p) { asm volatile("global_load_dwordx4 %0, %1, off offset:%2" : "=v"(d) : "v"(p), "n"(OFF)); }
DI void pin(u32& v) { asm("" : "+v"(v)); }
DI void rawbar() { asm volatile("s_barrier" ::: "memory"); }
template <int N> DI void vmwait() { asm volatile("s_waitcnt vmcnt(%0)" :: "n"(N) : "memory"); }
template <int N> DI void vmwait(u4& d) { asm volatile("s_waitcnt vmcnt(%1)" : "+v"(d) : "n"(N)); }
DI void bar() { asm volatile("s_waitcnt lgkmcnt(0)\n\ts_barrier" ::: "memory"); }
#define SB() __builtin_amdgcn_sched_barrier(0)
#define SGB(m, n) __builtin_amdgcn_sched_group_barrier(m, n, 0)
template <int I, int N, class F> DI void sfor(F&& f) { if constexpr (I < N) { f(std::integral_constant<int, I>{}); sfor<I + 1, N>(f); } }

constexpr int KG_ROW = 544, KG_CH = 57 * KG_ROW, KG_T = 2 * KG_CH, KG_TS = 4160, K1_LDS = KG_T + 2 * KG_TS;

DI f2 cexp_pow2(float a, float fh, float fl, float p) {
  const float e = __builtin_amdgcn_exp2f(a * p);
  const float r = fmaf(fl, p, __builtin_amdgcn_fractf(fh * p));
  return (f2){e * __builtin_amdgcn_cosf(r), e * __builtin_amdgcn_sinf(r)};
}

DI void k1_kgen(char* sm, int b, const u16* lar, const u16* aim, const u16* cre, const u16* cim, const u16* ldt, const u16* dsk, u16* kx) {
  const int t = threadIdx.x, w = __builtin_amdgcn_readfirstlane(t >> 6), lane = t & 63, ch = w >> 1, half = w & 1;
  const int d0 = 64 * (b & 7) + 2 * (b >> 3), d = d0 + ch, n = 64 * half + lane;
  const u32 v_lar = ldnt(lar + d * NM + n), v_aim = ldnt(aim + d * NM + n);
  const u32 cr = ldnt(cre + d * NM + n), ci = ldnt(cim + d * NM + n), cr2 = ldnt(cre + d * NM + (n ^ 64)), ci2 = ldnt(cim + d * NM + (n ^ 64));
  const float dt = rb(exp_acc(b2f(ldt[d])));
  const float ds = b2f(dsk[d]);
  const float zr = -rb(exp_acc(blo(v_lar))) * dt, zi = blo(v_aim) * dt;
  const float a = zr * 1.44269502f;
  const float fh = zi * 0.159154937f, fl = fmaf(zi, 0.159154937f, -fh) + zi * 6.42063833e-9f;
  const f2 e1 = cexp_pow2(a, fh, fl, 1.f), e8 = cexp_pow2(a, -fh, -fl, 8.f), e128 = cexp_pow2(a, fh, fl, 128.f), e1024 = cexp_pow2(a, fh, fl, 1024.f);
  int mx = (int)max(max(cr & 0x7fffu, ci & 0x7fffu), max(cr2 & 0x7fffu, ci2 & 0x7fffu));
  mx = max(mx, __builtin_amdgcn_update_dpp(0, mx, 0x111, 0xf, 0xf, true));
  mx = max(mx, __builtin_amdgcn_update_dpp(0, mx, 0x112, 0xf, 0xf, true));
  mx = max(mx, __builtin_amdgcn_update_dpp(0, mx, 0x114, 0xf, 0xf, true));
  mx = max(mx, __builtin_amdgcn_update_dpp(0, mx, 0x118, 0xf, 0xf, true));
  mx = max(max(__builtin_amdgcn_readlane(mx, 15), __builtin_amdgcn_readlane(mx, 31)), max(__builtin_amdgcn_readlane(mx, 47), __builtin_amdgcn_readlane(mx, 63)));
  const int ex = min(max(mx >> 7, 1), 253);
  const float cs = __int_as_float((254 - ex) << 23), kf = 2.f * dt * __int_as_float(ex << 23);
  if (t < 2) *(u4*)(kx + (d0 + t) * KXS) = (u4){0u, 0u, 0u, 0u};
  constexpr u32 KEEP = 0x03020100u, LL = 0x01000100u, LH = 0x05040100u, HH = 0x05040504u;
  const u32 sel0 = t < 32 ? KEEP : LL, sel1 = t < 32 ? KEEP : t < 128 ? LH : LL;
  const u32 sel2 = t < 32 ? KEEP : t < 128 ? LL : (t & 1) ? HH : LH, sel3 = t < 32 ? KEEP : t < 64 ? LH : HH;
  char* base = sm + ch * KG_CH + n * 4;
  auto put = [&](int row, f2 v) { *(u32*)(base + row * KG_ROW) = pkh(v); };
  f2 lo[8], e[8], m[8], x0, x1, x2, x3, e16, e32, e64;
  lo[0] = (f2){blo(cr) * cs, -blo(ci) * cs};
  e[1] = e1; m[1] = e128;
  put(0, lo[0]); put(16, (f2){1.f, 0.f}); put(17, e1); put(24, e128); put(48, e1024);
  cmul3(e16, e8, e8, e[2], e1, e1, m[2], e128, e128);
  cmul2(lo[1], lo[0], e8, x0, e1024, e1024);
  put(18, e[2]); put(32, m[2]); put(1, lo[1]); put(56, x0);
  cmul3(e32, e16, e16, e[4], e[2], e[2], m[4], m[2], m[2]);
  cmul2(e[3], e[2], e1, m[3], m[2], e128);
  cmul2(lo[2], lo[0], e16, lo[3], lo[1], e16);
  put(20, e[4]); put(40, m[4]); put(19, e[3]); put(36, m[3]); put(2, lo[2]); put(3, lo[3]);
  cmul4(e[5], e[6], e[7], x0, e1, e[2], e[3], m[4], e[4]);
  put(21, e[5]); put(22, e[6]); put(23, e[7]); put(41, x0);
  cmul3(m[5], m[4], e128, m[6], m[4], m[2], m[7], m[4], m[3]);
  put(42, m[5]); put(44, m[6]); put(46, m[7]);
  cmul4(lo[4], lo[5], lo[6], lo[7], lo[0], lo[1], lo[2], lo[3], e32);
  put(4, lo[4]); put(5, lo[5]); put(6, lo[6]); put(7, lo[7]);
  cmul2(e64, e32, e32, x0, e128, e1);
  put(25, x0);
  cmul3(x0, m[5], e[4], x1, m[6], e[4], x2, m[7], e[4]);
  put(43, x0); put(45, x1); put(47, x2);
  cmul4(x0, x1, x2, x3, lo[0], lo[1], lo[2], lo[3], e64);
  put(8, x0); put(9, x1); put(10, x2); put(11, x3);
  cmul4(x0, x1, x2, x3, lo[4], lo[5], lo[6], lo[7], e64);
  put(12, x0); put(13, x1); put(14, x2); put(15, x3);
  cmul4(x0, x1, x2, x3, e[2], e[3], e[4], e[5], e128);
  put(26, x0); put(27, x1); put(28, x2); put(29, x3);
  cmul2(x0, e[6], e128, x1, e[7], e128);
  put(30, x0); put(31, x1);
  cmul3(x0, e[2], m[2], x1, e[4], m[2], x2, e[6], m[2]);
  put(33, x0); put(34, x1); put(35, x2);
  cmul3(x0, e[2], m[3], x1, e[4], m[3], x2, e[6], m[3]);
  put(37, x0); put(38, x1); put(39, x2);
  cmul4(x0, x1, x2, x3, m[1], m[2], m[3], m[4], e1024);
  put(49, x0); put(50, x1); put(51, x2); put(52, x3);
  cmul3(x0, m[5], e1024, x1, m[6], e1024, x2, m[7], e1024);
  put(53, x0); put(54, x1); put(55, x2);
  __syncthreads();
  {
    const int c = lane & 15, g = lane >> 4;
    const char* fb = sm + ch * KG_CH + c * KG_ROW + g * 16;
    u16* T = (u16*)(sm + KG_T + ch * KG_TS);
    f4 acc0 = {0.f, 0.f, 0.f, 0.f}, acc1 = acc0;
    u4 B[8], A[8];
#pragma unroll
    for (int s = 0; s < 8; ++s) { B[s] = *(const u4*)(fb + s * 64); A[s] = *(const u4*)(fb + (16 + 16 * half) * KG_ROW + s * 64); }
    if (!half) {
      u4 A2[8];
#pragma unroll
      for (int s = 0; s < 8; ++s) A2[s] = *(const u4*)(fb + 48 * KG_ROW + s * 64);
      SB();
#pragma unroll
      for (int s = 0; s < 8; ++s) acc0 = mfma16h(A[s], B[s], acc0);
#pragma unroll
      for (int s = 0; s < 8; ++s) acc1 = mfma16h(A2[s], B[s], acc1);
      acc0 *= kf; acc1 *= kf;
      if (lane == 0) acc0[0] += ds;
      *(u2*)(T + 8 * c + 4 * (g & 1) + 128 * (g >> 1)) = (u2){pk(acc0[0], acc0[1]), pk(acc0[2], acc0[3])};
      if (g < 2) {
#pragma unroll
        for (int e = 0; e < 4; ++e) T[1024 + 128 * (4 * g + e) + 8 * c] = (u16)pk(acc1[e], 0.f);
      } else if (lane == 32) T[2048] = (u16)pk(acc1[0], 0.f);
    } else {
      SB();
#pragma unroll
      for (int s = 0; s < 8; ++s) acc0 = mfma16h(A[s], B[s], acc0);
      acc0 *= kf;
#pragma unroll
      for (int e = 0; e < 4; ++e) {
        const int p = g < 2 ? 256 + 128 * g + 2 * e : 512 + 128 * (2 * (g - 2) + (e >> 1)) + 4 * (e & 1);
        T[p + 8 * c] = (u16)pk(acc0[e], 0.f);
      }
    }
  }
  __syncthreads();
  u4 v[2];
  u32 e8v[2];
#pragma unroll
  for (int c2 = 0; c2 < 2; ++c2) {
    const u16* T = (const u16*)(sm + KG_T + c2 * KG_TS);
    v[c2] = *(const u4*)(T + 8 * t);
    e8v[c2] = T[8 * t + 8];
  }
#pragma unroll
  for (int c2 = 0; c2 < 2; ++c2) {
    const u4 o = {__builtin_amdgcn_perm(v[c2].x, v[c2].x, sel0), __builtin_amdgcn_perm(v[c2].z, t < 64 ? v[c2].y : v[c2].x, sel1),
                  __builtin_amdgcn_perm(e8v[c2], t < 128 ? v[c2].z : v[c2].x, sel2), __builtin_amdgcn_perm(e8v[c2], v[c2].w, sel3)};
    *(u4*)(kx + (d0 + c2) * KXS + 8 + 8 * t) = o;
  }
}

DI void k1_xt(char* sm, int b, const u16* x, u16* xt) {
  const int t = threadIdx.x, w = t >> 6, lane = t & 63;
  const int c0 = 64 * (b & 7), r0 = 128 * (b >> 3);
  u4 v[4];
#pragma unroll
  for (int i = 0; i < 4; ++i) v[i] = ldnt((const u4*)(x + (r0 + 32 * i + (t >> 3)) * D + c0 + 8 * (t & 7)));
#pragma unroll
  for (int i = 0; i < 4; ++i) *(u4*)(sm + (32 * i + (t >> 3)) * 144 + 16 * (t & 7)) = v[i];
  __syncthreads();
  const int g = lane >> 4, q = (lane & 15) >> 2, p = lane & 3;
#pragma unroll
  for (int i = 0; i < 4; ++i) {
    const char* a = sm + (32 * i + 8 * g + q) * 144 + 32 * w + 8 * p;
    *(u4*)(xt + (c0 + 16 * w + (lane & 15)) * L + r0 + 32 * i + 8 * g) = cat(trd(a), trd(a + 4 * 144));
  }
}

DI void k1_stats(int b, const u16* x, char* ws) {
  const int t = threadIdx.x, row = 32 * b + (t >> 3);
  u4 v[8];
#pragma unroll
  for (int i = 0; i < 8; ++i) v[i] = ldnt((const u4*)(x + row * D + 64 * i + 8 * (t & 7)));
  float s1 = 0.f, s2 = 0.f, s3 = 0.f, s4 = 0.f;
#pragma unroll
  for (int i = 0; i < 8; ++i)
#pragma unroll
    for (int j = 0; j < 4; j += 2) {
      s1 = dot2(v[i][j], 0x3f803f80u, s1); s2 = dot2(v[i][j], v[i][j], s2);
      s3 = dot2(v[i][j + 1], 0x3f803f80u, s3); s4 = dot2(v[i][j + 1], v[i][j + 1], s4);
    }
  s1 += s3; s2 += s4;
  s1 += dppf<0xb1>(s1); s2 += dppf<0xb1>(s2); s1 += dppf<0x4e>(s1); s2 += dppf<0x4e>(s2); s1 += dppf<0x141>(s1); s2 += dppf<0x141>(s2);
  const f2 st = ln_stats(s1, s2);
  if ((t & 7) == 0) {
    ((u16*)(ws + WS_MEAN))[row] = (u16)(__float_as_uint(st.x) >> 16);
    ((u16*)(ws + WS_SD))[row] = (u16)(__float_as_uint(st.y) >> 16);
    ((u32*)(ws + WS_ST2))[row] = 0u;
  }
  if (t == 0 && b < 16) ((u32*)(ws + WS_CNT))[64 * b] = 0u;
}

DI void k1_mod(char* sm, int b, const u16* c, const u16* wm, char* ws) {
  const int t = threadIdx.x, rg = t >> 3;
  u4 v[16];
  const u32 cc = ((const u32*)c)[t];
#pragma unroll
  for (int i = 0; i < 16; ++i) v[i] = ldnt((const u4*)(wm + (16 * rg + i) * (6 * D) + 64 * b + 8 * (t & 7)));
  u32* sl = (u32*)(sm + 32 * 68 * 4);
  {
    const float c0 = blo(cc), c1 = bhi(cc);
    sl[t] = pk(c0 / (1.f + __expf(-c0)), c1 / (1.f + __expf(-c1)));
  }
  __syncthreads();
  const u4 sa = *(const u4*)(sl + 8 * rg), sb = *(const u4*)(sl + 8 * rg + 4);
  float acc[8] = {0.f, 0.f, 0.f, 0.f, 0.f, 0.f, 0.f, 0.f};
#pragma unroll
  for (int i = 0; i < 8; ++i) {
    const u32 sp = i < 4 ? sa[i & 3] : sb[i & 3];
#pragma unroll
    for (int j = 0; j < 4; ++j) {
      acc[2 * j] = dot2(__builtin_amdgcn_perm(v[2 * i + 1][j], v[2 * i][j], 0x05040100u), sp, acc[2 * j]);
      acc[2 * j + 1] = dot2(__builtin_amdgcn_perm(v[2 * i + 1][j], v[2 * i][j], 0x07060302u), sp, acc[2 * j + 1]);
    }
  }
  float* red = (float*)sm;
  *(f4*)(red + rg * 68 + 8 * (t & 7)) = (f4){acc[0], acc[1], acc[2], acc[3]};
  *(f4*)(red + rg * 68 + 8 * (t & 7) + 4) = (f4){acc[4], acc[5], acc[6], acc[7]};
  __syncthreads();
  float s = 0.f;
#pragma unroll
  for (int i = 0; i < 8; ++i) s += red[((t & 3) + 4 * i) * 68 + (t >> 2)];
  s += dppf<0xb1>(s); s += dppf<0x4e>(s);
  if ((t & 3) == 0) {
    const int j = 64 * b + (t >> 2), part = b >> 3;
    float v = rb(s);
    if (part == 1 || part == 4) v = rb(1.f + v);
    if (part == 4) {
      v = v == 0.f ? 0x1p-60f : v;
      ((float*)(ws + WS_INV))[j - 4 * D] = 1.f / v;
    }
    if (part == 3 || part == 4) ((u16*)(ws + WS_CV))[j - 3 * D] = (u16)(__float_as_uint(v) >> 16);
    ((float*)(ws + WS_MOD))[j] = v;
  }
}

__global__ __launch_bounds__(256, 2) void k1_prep(const u16* x, const u16* lar, const u16* aim, const u16* cre, const u16* cim, const u16* ldt,
                                                  const u16* dsk, const u16* c, const u16* wm, char* ws) {
  __shared__ __attribute__((aligned(16))) char sm[K1_LDS];
  const int b = blockIdx.x;
  if (b < 256) k1_kgen(sm, b, lar, aim, cre, cim, ldt, dsk, (u16*)(ws + WS_KX));
  else if (b < 384) k1_xt(sm, b - 256, x, (u16*)(ws + WS_XT));
  else if (b < 448) k1_stats(b - 384, x, ws);
  else k1_mod(sm, b - 448, c, wm, ws);
}

constexpr int CV_KC = 4128, CV_H = 8 * CV_KC, CV_HP = 1152, CV_X = CV_H + 8 * CV_HP, CV_EX = CV_X + 8192, CV_W = CV_EX + 16384,
              CV_FL = CV_W + 16384, K2_LDS = CV_FL + 16;

DI void k2_conv(char* sm, int b, char* ws) {
  const int t = threadIdx.x, w = __builtin_amdgcn_readfirstlane(t >> 6), lane = t & 63, ch = w >> 1, half = w & 1;
  const int d0 = 64 * (b & 7) + 2 * (b >> 3), d = d0 + ch;
  const u16* xt = (const u16*)(ws + WS_XT);
  const float* mod = (const float*)(ws + WS_MOD);
  const int c = lane & 15, g = lane >> 4;
  u4 xv[2];
  const u4 mv = *(const u4*)(ws + WS_MEAN + 16 * t), sv = *(const u4*)(ws + WS_SD + 16 * t);
#pragma unroll
  for (int i = 0; i < 2; ++i) xv[i] = *(const u4*)(xt + (d0 + i) * L + 8 * t);
  const float ga = mod[2 * D + d], c2 = mod[4 * D + d];
  const f2 c1 = *(const f2*)(mod + D + d0), sa = *(const f2*)(mod + d0);
  SB();
  float rs[8];
#pragma unroll
  for (int j = 0; j < 8; ++j) rs[j] = __builtin_amdgcn_rcpf((j & 1) ? bhi(sv[j >> 1]) : blo(sv[j >> 1]));
#pragma unroll
  for (int i = 0; i < 2; ++i) {
    float h[8];
#pragma unroll
    for (int j = 0; j < 8; ++j) {
      const float xx = (j & 1) ? bhi(xv[i][j >> 1]) : blo(xv[i][j >> 1]);
      const float mm = (j & 1) ? bhi(mv[j >> 1]) : blo(mv[j >> 1]);
      h[j] = fmaf((xx - mm) * rs[j], c1[i], sa[i]);
    }
#pragma unroll
    for (int r = 0; r < 4; ++r) *(u32*)(sm + CV_H + (4 * i + r) * CV_HP + 128 + 4 * t) = pk(h[r], h[r + 4]);
    *(u4*)(sm + CV_X + 4096 * i + 16 * t) = xv[i];
  }
  f4 acc[8];
#pragma unroll
  for (int i = 0; i < 8; ++i) acc[i] = (f4){0.f, 0.f, 0.f, 0.f};
  u32 ka = ldsaddr(sm) + (4 * ch + 2 * half) * CV_KC + 32 + 8 * (8 * g + (lane & 3) - ((lane & 15) >> 2));
  u32 hb = ldsaddr(sm) + CV_H + (4 * ch + 2 * half) * CV_HP + 128 + 8 * c - 16 * g - 8 - 64;
  bar();
  {
    u32 cnt;
    asm volatile(
      "ds_read_b64_tr_b16 v[64:65], %8 offset:32\n"
      "ds_read_b64_tr_b16 v[66:67], %8 offset:0\n"
      "ds_read_b64 v[120:121], %9 offset:832\n"
      "ds_read_b64 v[122:123], %9 offset:840\n"
      "ds_read_b64 v[124:125], %9 offset:960\n"
      "ds_read_b64 v[126:127], %9 offset:968\n"
      "ds_read_b64 v[112:113], %9 offset:576\n"
      "ds_read_b64 v[114:115], %9 offset:584\n"
      "ds_read_b64 v[116:117], %9 offset:704\n"
      "ds_read_b64 v[118:119], %9 offset:712\n"
      "ds_read_b64_tr_b16 v[68:69], %8 offset:544\n"
      "ds_read_b64_tr_b16 v[70:71], %8 offset:512\n"
      "ds_read_b64 v[108:109], %9 offset:448\n"
      "ds_read_b64 v[110:111], %9 offset:456\n"
      "ds_read_b64 v[104:105], %9 offset:320\n"
      "ds_read_b64 v[106:107], %9 offset:328\n"
      "ds_read_b64_tr_b16 v[72:73], %8 offset:1056\n"
      "ds_read_b64_tr_b16 v[74:75], %8 offset:1024\n"
      "ds_read_b64_tr_b16 v[76:77], %8 offset:1568\n"
      "ds_read_b64_tr_b16 v[78:79], %8 offset:1536\n"
      "ds_read_b64 v[100:101], %9 offset:192\n"
      "ds_read_b64 v[102:103], %9 offset:200\n"
      "ds_read_b64_tr_b16 v[80:81], %8 offset:2080\n"
      "ds_read_b64_tr_b16 v[82:83], %8 offset:2048\n"
      "ds_read_b64 v[96:97], %9 offset:64\n"
      "ds_read_b64 v[98:99], %9 offset:72\n"
      "ds_read_b64_tr_b16 v[84:85], %8 offset:2592\n"
      "ds_read_b64_tr_b16 v[86:87], %8 offset:2560\n"
      "ds_read_b64_tr_b16 v[88:89], %8 offset:3104\n"
      "ds_read_b64_tr_b16 v[90:91], %8 offset:3072\n"
      "ds_read_b64_tr_b16 v[92:93], %8 offset:3616\n"
      "ds_read_b64_tr_b16 v[94:95], %8 offset:3584\n"
      "s_mov_b32 %10, 2\n"
      "s_waitcnt lgkmcnt(0)\n"
      "Lconv%=:\n"
      "v_mfma_f32_16x16x32_bf16 %6, v[64:67], v[120:123], %6\n"
      "ds_read_b64_tr_b16 v[128:129], %8 offset:288\n"
      "v_mfma_f32_16x16x32_bf16 %7, v[64:67], v[124:127], %7\n"
      "ds_read_b64_tr_b16 v[130:131], %8 offset:256\n"
      "v_mfma_f32_16x16x32_bf16 %4, v[64:67], v[112:115], %4\n"
      "ds_read_b64 v[184:185], %9 offset:768\n"
      "v_mfma_f32_16x16x32_bf16 %5, v[64:67], v[116:119], %5\n"
      "ds_read_b64 v[186:187], %9 offset:776\n"
      "v_mfma_f32_16x16x32_bf16 %6, v[68:71], v[116:119], %6\n"
      "ds_read_b64 v[188:189], %9 offset:896\n"
      "v_mfma_f32_16x16x32_bf16 %7, v[68:71], v[120:123], %7\n"
      "ds_read_b64 v[190:191], %9 offset:904\n"
      "v_mfma_f32_16x16x32_bf16 %4, v[68:71], v[108:111], %4\n"
      "ds_read_b64 v[176:177], %9 offset:512\n"
      "v_mfma_f32_16x16x32_bf16 %2, v[64:67], v[104:107], %2\n"
      "ds_read_b64 v[178:179], %9 offset:520\n"
      "v_mfma_f32_16x16x32_bf16 %6, v[72:75], v[112:115], %6\n"
      "ds_read_b64 v[180:181], %9 offset:640\n"
      "v_mfma_f32_16x16x32_bf16 %3, v[64:67], v[108:111], %3\n"
      "ds_read_b64 v[182:183], %9 offset:648\n"
      "v_mfma_f32_16x16x32_bf16 %7, v[72:75], v[116:119], %7\n"
      "ds_read_b64_tr_b16 v[132:133], %8 offset:800\n"
      "v_mfma_f32_16x16x32_bf16 %4, v[72:75], v[104:107], %4\n"
      "ds_read_b64_tr_b16 v[134:135], %8 offset:768\n"
      "v_mfma_f32_16x16x32_bf16 %5, v[68:71], v[112:115], %5\n"
      "ds_read_b64 v[172:173], %9 offset:384\n"
      "v_mfma_f32_16x16x32_bf16 %6, v[76:79], v[108:111], %6\n"
      "ds_read_b64 v[174:175], %9 offset:392\n"
      "v_mfma_f32_16x16x32_bf16 %7, v[76:79], v[112:115], %7\n"
      "ds_read_b64 v[168:169], %9 offset:256\n"
      "v_mfma_f32_16x16x32_bf16 %4, v[76:79], v[100:103], %4\n"
      "ds_read_b64 v[170:171], %9 offset:264\n"
      "v_mfma_f32_16x16x32_bf16 %1, v[64:67], v[100:103], %1\n"
      "ds_read_b64_tr_b16 v[136:137], %8 offset:1312\n"
      "v_mfma_f32_16x16x32_bf16 %5, v[72:75], v[108:111], %5\n"
      "ds_read_b64_tr_b16 v[138:139], %8 offset:1280\n"
      "v_mfma_f32_16x16x32_bf16 %6, v[80:83], v[104:107], %6\n"
      "ds_read_b64_tr_b16 v[140:141], %8 offset:1824\n"
      "v_mfma_f32_16x16x32_bf16 %0, v[64:67], v[96:99], %0\n"
      "ds_read_b64_tr_b16 v[142:143], %8 offset:1792\n"
      "v_mfma_f32_16x16x32_bf16 %7, v[80:83], v[108:111], %7\n"
      "ds_read_b64 v[164:165], %9 offset:128\n"
      "v_mfma_f32_16x16x32_bf16 %5, v[76:79], v[104:107], %5\n"
      "ds_read_b64 v[166:167], %9 offset:136\n"
      "v_mfma_f32_16x16x32_bf16 %2, v[68:71], v[100:103], %2\n"
      "ds_read_b64_tr_b16 v[144:145], %8 offset:2336\n"
      "v_mfma_f32_16x16x32_bf16 %6, v[84:87], v[100:103], %6\n"
      "ds_read_b64_tr_b16 v[146:147], %8 offset:2304\n"
      "v_mfma_f32_16x16x32_bf16 %7, v[84:87], v[104:107], %7\n"
      "ds_read_b64 v[160:161], %9 offset:0\n"
      "v_mfma_f32_16x16x32_bf16 %3, v[68:71], v[104:107], %3\n"
      "ds_read_b64 v[162:163], %9 offset:8\n"
      "v_mfma_f32_16x16x32_bf16 %2, v[72:75], v[96:99], %2\n"
      "ds_read_b64_tr_b16 v[148:149], %8 offset:2848\n"
      "v_mfma_f32_16x16x32_bf16 %5, v[80:83], v[100:103], %5\n"
      "ds_read_b64_tr_b16 v[150:151], %8 offset:2816\n"
      "v_mfma_f32_16x16x32_bf16 %7, v[88:91], v[100:103], %7\n"
      "ds_read_b64_tr_b16 v[152:153], %8 offset:3360\n"
      "v_mfma_f32_16x16x32_bf16 %6, v[88:91], v[96:99], %6\n"
      "ds_read_b64_tr_b16 v[154:155], %8 offset:3328\n"
      "v_mfma_f32_16x16x32_bf16 %3, v[72:75], v[100:103], %3\n"
      "ds_read_b64_tr_b16 v[156:157], %8 offset:3872\n"
      "v_mfma_f32_16x16x32_bf16 %1, v[68:71], v[96:99], %1\n"
      "ds_read_b64_tr_b16 v[158:159], %8 offset:3840\n"
      "v_mfma_f32_16x16x32_bf16 %7, v[92:95], v[96:99], %7\n"
      "v_mfma_f32_16x16x32_bf16 %5, v[84:87], v[96:99], %5\n"
      "v_mfma_f32_16x16x32_bf16 %4, v[80:83], v[96:99], %4\n"
      "v_mfma_f32_16x16x32_bf16 %3, v[76:79], v[96:99], %3\n"
      "s_waitcnt lgkmcnt(0)\n"
      "v_add_u32 %8, 4128, %8\n"
      "v_add_u32 %9, 1152, %9\n"
      "v_mfma_f32_16x16x32_bf16 %6, v[128:131], v[184:187], %6\n"
      "ds_read_b64_tr_b16 v[64:65], %8 offset:32\n"
      "v_mfma_f32_16x16x32_bf16 %7, v[128:131], v[188:191], %7\n"
      "ds_read_b64_tr_b16 v[66:67], %8 offset:0\n"
      "v_mfma_f32_16x16x32_bf16 %4, v[128:131], v[176:179], %4\n"
      "ds_read_b64 v[120:121], %9 offset:832\n"
      "v_mfma_f32_16x16x32_bf16 %5, v[128:131], v[180:183], %5\n"
      "ds_read_b64 v[122:123], %9 offset:840\n"
      "v_mfma_f32_16x16x32_bf16 %6, v[132:135], v[180:183], %6\n"
      "ds_read_b64 v[124:125], %9 offset:960\n"
      "v_mfma_f32_16x16x32_bf16 %7, v[132:135], v[184:187], %7\n"
      "ds_read_b64 v[126:127], %9 offset:968\n"
      "v_mfma_f32_16x16x32_bf16 %4, v[132:135], v[172:175], %4\n"
      "ds_read_b64 v[112:113], %9 offset:576\n"
      "v_mfma_f32_16x16x32_bf16 %2, v[128:131], v[168:171], %2\n"
      "ds_read_b64 v[114:115], %9 offset:584\n"
      "v_mfma_f32_16x16x32_bf16 %6, v[136:139], v[176:179], %6\n"
      "ds_read_b64 v[116:117], %9 offset:704\n"
      "v_mfma_f32_16x16x32_bf16 %3, v[128:131], v[172:175], %3\n"
      "ds_read_b64 v[118:119], %9 offset:712\n"
      "v_mfma_f32_16x16x32_bf16 %7, v[136:139], v[180:183], %7\n"
      "ds_read_b64_tr_b16 v[68:69], %8 offset:544\n"
      "v_mfma_f32_16x16x32_bf16 %4, v[136:139], v[168:171], %4\n"
      "ds_read_b64_tr_b16 v[70:71], %8 offset:512\n"
      "v_mfma_f32_16x16x32_bf16 %5, v[132:135], v[176:179], %5\n"
      "ds_read_b64 v[108:109], %9 offset:448\n"
      "v_mfma_f32_16x16x32_bf16 %6, v[140:143], v[172:175], %6\n"
      "ds_read_b64 v[110:111], %9 offset:456\n"
      "v_mfma_f32_16x16x32_bf16 %7, v[140:143], v[176:179], %7\n"
      "ds_read_b64 v[104:105], %9 offset:320\n"
      "v_mfma_f32_16x16x32_bf16 %4, v[140:143], v[164:167], %4\n"
      "ds_read_b64 v[106:107], %9 offset:328\n"
      "v_mfma_f32_16x16x32_bf16 %1, v[128:131], v[164:167], %1\n"
      "ds_read_b64_tr_b16 v[72:73], %8 offset:1056\n"
      "v_mfma_f32_16x16x32_bf16 %5, v[136:139], v[172:175], %5\n"
      "ds_read_b64_tr_b16 v[74:75], %8 offset:1024\n"
      "v_mfma_f32_16x16x32_bf16 %6, v[144:147], v[168:171], %6\n"
      "ds_read_b64_tr_b16 v[76:77], %8 offset:1568\n"
      "v_mfma_f32_16x16x32_bf16 %0, v[128:131], v[160:163], %0\n"
      "ds_read_b64_tr_b16 v[78:79], %8 offset:1536\n"
      "v_mfma_f32_16x16x32_bf16 %7, v[144:147], v[172:175], %7\n"
      "ds_read_b64 v[100:101], %9 offset:192\n"
      "v_mfma_f32_16x16x32_bf16 %5, v[140:143], v[168:171], %5\n"
      "ds_read_b64 v[102:103], %9 offset:200\n"
      "v_mfma_f32_16x16x32_bf16 %2, v[132:135], v[164:167], %2\n"
      "ds_read_b64_tr_b16 v[80:81], %8 offset:2080\n"
      "v_mfma_f32_16x16x32_bf16 %6, v[148:151], v[164:167], %6\n"
      "ds_read_b64_tr_b16 v[82:83], %8 offset:2048\n"
      "v_mfma_f32_16x16x32_bf16 %7, v[148:151], v[168:171], %7\n"
      "ds_read_b64 v[96:97], %9 offset:64\n"
      "v_mfma_f32_16x16x32_bf16 %3, v[132:135], v[168:171], %3\n"
      "ds_read_b64 v[98:99], %9 offset:72\n"
      "v_mfma_f32_16x16x32_bf16 %2, v[136:139], v[160:163], %2\n"
      "ds_read_b64_tr_b16 v[84:85], %8 offset:2592\n"
      "v_mfma_f32_16x16x32_bf16 %5, v[144:147], v[164:167], %5\n"
      "ds_read_b64_tr_b16 v[86:87], %8 offset:2560\n"
      "v_mfma_f32_16x16x32_bf16 %7, v[152:155], v[164:167], %7\n"
      "ds_read_b64_tr_b16 v[88:89], %8 offset:3104\n"
      "v_mfma_f32_16x16x32_bf16 %6, v[152:155], v[160:163], %6\n"
      "ds_read_b64_tr_b16 v[90:91], %8 offset:3072\n"
      "v_mfma_f32_16x16x32_bf16 %3, v[136:139], v[164:167], %3\n"
      "ds_read_b64_tr_b16 v[92:93], %8 offset:3616\n"
      "v_mfma_f32_16x16x32_bf16 %1, v[132:135], v[160:163], %1\n"
      "ds_read_b64_tr_b16 v[94:95], %8 offset:3584\n"
      "v_mfma_f32_16x16x32_bf16 %7, v[156:159], v[160:163], %7\n"
      "v_mfma_f32_16x16x32_bf16 %5, v[148:151], v[160:163], %5\n"
      "v_mfma_f32_16x16x32_bf16 %4, v[144:147], v[160:163], %4\n"
      "v_mfma_f32_16x16x32_bf16 %3, v[140:143], v[160:163], %3\n"
      "s_waitcnt lgkmcnt(0)\n"
      "s_sub_u32 %10, %10, 1\n"
      "s_cmp_lg_u32 %10, 0\n"
      "s_cbranch_scc1 Lconv%=\n"
      "s_nop 7\n"
      : "+v"(acc[0]), "+v"(acc[1]), "+v"(acc[2]), "+v"(acc[3]), "+v"(acc[4]), "+v"(acc[5]), "+v"(acc[6]), "+v"(acc[7]), "+v"(ka), "+v"(hb), "=&s"(cnt)
      :: "memory", "scc", "v64", "v65", "v66", "v67", "v68", "v69", "v70", "v71", "v72", "v73", "v74", "v75", "v76", "v77", "v78", "v79", "v80", "v81", "v82", "v83", "v84", "v85", "v86", "v87", "v88", "v89", "v90", "v91", "v92", "v93", "v94", "v95", "v96", "v97", "v98", "v99", "v100", "v101", "v102", "v103", "v104", "v105", "v106", "v107", "v108", "v109", "v110", "v111", "v112", "v113", "v114", "v115", "v116", "v117", "v118", "v119", "v120", "v121", "v122", "v123", "v124", "v125", "v126", "v127", "v128", "v129", "v130", "v131", "v132", "v133", "v134", "v135", "v136", "v137", "v138", "v139", "v140", "v141", "v142", "v143", "v144", "v145", "v146", "v147", "v148", "v149", "v150", "v151", "v152", "v153", "v154", "v155", "v156", "v157", "v158", "v159", "v160", "v161", "v162", "v163", "v164", "v165", "v166", "v167", "v168", "v169", "v170", "v171", "v172", "v173", "v174", "v175", "v176", "v177", "v178", "v179", "v180", "v181", "v182", "v183", "v184", "v185", "v186", "v187", "v188", "v189", "v190", "v191");
  }
  f4* ex = (f4*)(sm + CV_EX);
  if (half) {
#pragma unroll
    for (int i = 0; i < 4; ++i) { ex[(w * 4 + i) * 64 + lane] = acc[i]; acc[i] = acc[4 + i]; }
  } else {
#pragma unroll
    for (int i = 0; i < 4; ++i) ex[(w * 4 + i) * 64 + lane] = acc[4 + i];
  }
  volatile LAS u32* fl = (volatile LAS u32*)(size_t)(ldsaddr(sm) + CV_FL);
  asm volatile("s_waitcnt lgkmcnt(0)" ::: "memory");
  fl[w] = 1u;
  u2 xr[4];
#pragma unroll
  for (int i = 0; i < 4; ++i) xr[i] = *(const u2*)(sm + CV_X + 4096 * ch + 2 * (256 * (4 * half + i) + 16 * c + 4 * g));
  while (fl[w ^ 1] == 0u) {}
  asm volatile("" ::: "memory");
  u16* x1t = (u16*)(ws + WS_X1T);
  const float gc = ga * c2;
#pragma unroll
  for (int i = 0; i < 4; ++i) {
    const f4 y = (acc[i] + ex[((w ^ 1) * 4 + i) * 64 + lane]) * gc + (f4){blo(xr[i].x), bhi(xr[i].x), blo(xr[i].y), bhi(xr[i].y)} * c2;
    *(u2*)(x1t + d * L + 256 * (4 * half + i) + 16 * c + 4 * g) = (u2){pk(y[0], y[1]), pk(y[2], y[3])};
  }
}

DI void k2_help(char* sm, int b, const u16* wg, const u16* wu, const u16* wd, char* ws) {
  const int t = threadIdx.x - 256, w = __builtin_amdgcn_readfirstlane(t >> 6), lane = t & 63;
  const int d0 = 64 * (b & 7) + 2 * (b >> 3);
  const u16* kx = (const u16*)(ws + WS_KX);
  u4 kv[2];
  u2 kp[2];
#pragma unroll
  for (int i = 0; i < 2; ++i) { kv[i] = *(const u4*)(kx + (d0 + i) * KXS + 8 + 8 * t); kp[i] = *(const u2*)(kx + (d0 + i) * KXS + 4 + 8 * t); }
  SB();
  if (t < 16) *(u4*)(sm + (t >> 1) * CV_KC + 16 * (t & 1)) = (u4){0u, 0u, 0u, 0u};
  if (w == 1) *(u4*)(sm + CV_H + (lane >> 3) * CV_HP + 16 * (t & 7)) = (u4){0u, 0u, 0u, 0u};
  if (t == 128) *(u4*)(sm + CV_FL) = (u4){0u, 0u, 0u, 0u};
#pragma unroll
  for (int i = 0; i < 2; ++i) {
    const u4 v = kv[i];
    const u32 a0 = __builtin_amdgcn_alignbit(kp[i].y, kp[i].x, 16), a1 = __builtin_amdgcn_alignbit(v.x, kp[i].y, 16), a2 = __builtin_amdgcn_alignbit(v.y, v.x, 16),
              a3 = __builtin_amdgcn_alignbit(v.z, v.y, 16), a4 = __builtin_amdgcn_alignbit(v.w, v.z, 16);
    char* kd = sm + 4 * i * CV_KC + 32 + 16 * t;
    *(u4*)kd = v;
    *(u4*)(kd + CV_KC) = (u4){a1, a2, a3, a4};
    *(u4*)(kd + 2 * CV_KC) = (u4){kp[i].y, v.x, v.y, v.z};
    *(u4*)(kd + 3 * CV_KC) = (u4){a0, a1, a2, a3};
  }
  bar();
  const int task = b + 256 * w, kq = task & 3, blk = task >> 2, mat = blk >= 43, jb = blk - 43 * mat;
  const bool work = task < 344;
  const int hh = lane >> 5;
  u4 vec[8];
  if (work) {
    const rsrc_t rw = mkrsrc(mat ? wu : wg, D * DF * 2);
    const u32 vo = (128 * kq + (lane >> 2)) * (DF * 2) + 64 * jb + 16 * (lane & 3);
    asm volatile("s_nop 4");
#pragma unroll
    for (int i = 0; i < 8; ++i) glds16<0>(ldsaddr(sm) + CV_W + 8192 * w + 1024 * i, vo, rw, i * 16 * DF * 2);
    const char* cv = ws + WS_CV + ((lane & 1) ? 0 : 1024) + 256 * kq + 16 * hh;
    sfor<0, 8>([&](auto i) { gld16<32 * i>(vec[i], cv); });
  }
  u32 dummy[4];
  {
    const int row = 32 * (b >> 3) + 8 * w + (lane >> 3);
    const char* base = (const char*)(row >= D ? wu : wg) + (row & (D - 1)) * (DF * 2);
#pragma unroll
    for (int i = 0; i < 2; ++i) {
      const int off = min((b & 1) * 1344 + 128 * ((lane & 7) + 8 * i), (b & 1) ? DF * 2 - 4 : 1340);
      asm volatile("global_load_dword %0, %1, off" : "=v"(dummy[i]) : "v"(base + off));
    }
  }
#pragma unroll
  for (int i = 0; i < 2; ++i)
    asm volatile("global_load_dword %0, %1, off" : "=v"(dummy[2 + i]) : "v"((const char*)wd + (b >> 3) * 44032 + w * 11008 + min(lane + 64 * i, 85) * 128));
  if (work) {
    asm volatile("s_waitcnt vmcnt(4)" : "+v"(vec[0]), "+v"(vec[1]), "+v"(vec[2]), "+v"(vec[3]), "+v"(vec[4]), "+v"(vec[5]), "+v"(vec[6]), "+v"(vec[7]) :: "memory");
    const u32 ra = ldsaddr(sm) + CV_W + 8192 * w + 64 * (8 * hh + ((lane & 15) >> 2)) + 32 * ((lane >> 4) & 1) + 8 * (lane & 3);
    f16v sums;
#pragma unroll
    for (int e = 0; e < 16; ++e) sums[e] = 0.f;
#pragma unroll
    for (int i = 0; i < 8; ++i) sums = mfma32(vec[i], cat(trd(ra + 1024 * i), trd(ra + 1024 * i + 256)), sums);
    if (lane < 32) {
      u32* exf = (u32*)(ws + WS_EX) + ((2 * jb + (lane >> 4)) * 64 + 16 * mat + (lane & 15)) * 4 + kq;
      const float h0 = rb(sums[0]), h1 = rb(sums[1]);
      exf[0] = pk(h0, sums[0] - h0);
      exf[128] = pk(h1, sums[1] - h1);
    }
  }
  asm volatile("s_waitcnt vmcnt(0)" : "+v"(dummy[0]), "+v"(dummy[1]), "+v"(dummy[2]), "+v"(dummy[3]) :: "memory");
}

__global__ __launch_bounds__(512, 2) void k2_conv_prep(const u16* wg, const u16* wu, const u16* wd, char* ws) {
  extern __shared__ __attribute__((aligned(16))) char sm[];
  const int b = blockIdx.x;
  if (__builtin_amdgcn_readfirstlane(threadIdx.x) < 256) k2_conv(sm, b, ws);
  else k2_help(sm, b, wg, wu, wd, ws);
}

constexpr int G1_LEAD = 3, G1_NS = 5, G1_SLOT = 20480;
constexpr int G2_RES = 19, G2_FL = 4 * G2_RES * 2048, G1_SV = G2_FL + 64, K3_LDS = G1_SV + 512;
constexpr int G2_LEAD = 6;
constexpr int HH_T = NU * 1024;

template <int K> DI u32 g1_pair(const f16v& acc, float rs, float sd) {
  const f2 g = (f2){acc[2 * K], acc[2 * K + 1]} * rs;
  const f2 z = g * (g * g * -0.10294324f + -2.30220819f);
  const f2 dn = (f2){__builtin_amdgcn_exp2f(z.x), __builtin_amdgcn_exp2f(z.y)} * sd + sd;
  const f2 r = g * (f2){acc[8 + 2 * K], acc[9 + 2 * K]} * (f2){__builtin_amdgcn_rcpf(dn.x), __builtin_amdgcn_rcpf(dn.y)};
  return pk(r.x, r.y);
}
DI u4 g1_tile(const f16v& acc, float rs, float sd) { return (u4){g1_pair<0>(acc, rs, sd), g1_pair<1>(acc, rs, sd), g1_pair<2>(acc, rs, sd), g1_pair<3>(acc, rs, sd)}; }

DI void g1_stats(char* sm, int rt, char* ws) {
  const int t = threadIdx.x;
  float* red = (float*)sm;
  if (t < 512) {
    const u16* xp = (const u16*)(ws + WS_X1T) + (t >> 4) * L + 128 * rt + 8 * (t & 15);
    u4 v[16];
    float iv[16];
#pragma unroll
    for (int i = 0; i < 16; ++i) { v[i] = *(const u4*)(xp + 32 * i * L); iv[i] = ((const float*)(ws + WS_INV))[(t >> 4) + 32 * i]; }
    f2 s1[4], s2[4];
#pragma unroll
    for (int j = 0; j < 4; ++j) s1[j] = s2[j] = (f2){0.f, 0.f};
#pragma unroll
    for (int i = 0; i < 16; ++i)
#pragma unroll
      for (int j = 0; j < 4; ++j) { const f2 x = (f2){blo(v[i][j]), bhi(v[i][j])} * iv[i]; s1[j] += x; s2[j] += x * x; }
#pragma unroll
    for (int j = 0; j < 4; ++j) *(f4*)(red + (t >> 4) * 260 + 16 * (t & 15) + 4 * j) = (f4){s1[j].x, s2[j].x, s1[j].y, s2[j].y};
  }
  __syncthreads();
  if (t < 512) {
    f2 p = {0.f, 0.f};
#pragma unroll
    for (int i = 0; i < 8; ++i) p += *(const f2*)(red + (8 * (t & 3) + i) * 260 + 2 * (t >> 2));
    p.x += dppf<0xb1>(p.x); p.y += dppf<0xb1>(p.y); p.x += dppf<0x4e>(p.x); p.y += dppf<0x4e>(p.y);
    const f2 ms = ln_stats(p.x, p.y);
    if ((t & 3) == 0)
      __hip_atomic_store((u32*)(ws + WS_ST2) + 128 * rt + (t >> 2), (__float_as_uint(ms.x) >> 16) | __float_as_uint(ms.y), __ATOMIC_RELAXED, __HIP_MEMORY_SCOPE_AGENT);
  }
  __syncthreads();
}

__global__ __launch_bounds__(768, 3) void k3_mlp(const u16* wg, const u16* wu, const u16* wd, char* ws, u16* out) {
  extern __shared__ __attribute__((aligned(16))) char sm[];
  const int b = blockIdx.x, j = b >> 3;
  const int t = threadIdx.x, w12 = __builtin_amdgcn_readfirstlane(t >> 6), w = w12 & 3, lane = t & 63, hh = lane >> 5;
  const u32 lb0 = ldsaddr(sm);
  const int q = (lane & 15) >> 2, p = lane & 3, g = lane >> 4;
  const int lt = 4 * (b & 7) + (j & 3), dt = j >> 2;
  auto kfirst = [&](int kq) { const u32 k = 22 * kq - (kq == 3) + 3 * dt; return min(k, k - NU); };
  auto knext = [](u32 k) { return min(k + 1, k + 1 - NU); };
  const rsrc_t rd = mkrsrc(wd, DF * D * 2);
  const u32 vd = (lane >> 3) * 1024 + 128 * dt + 16 * ((lane & 7) ^ (4 * ((lane >> 4) & 1)));
  volatile LAS u32* fl = (volatile LAS u32*)(size_t)(lb0 + G2_FL);
  auto fill2 = [&](int i0, int n, int slot0) {
    u32 kd = kfirst(w) + i0;
    kd = min(kd, kd - NU);
#pragma unroll 1
    for (int i = 0; i < n; ++i) {
      glds16<0>(lb0 + (G2_RES * w + slot0 + i) * 2048, vd, rd, kd * 16384);
      glds16<0>(lb0 + (G2_RES * w + slot0 + i) * 2048 + 1024, vd, rd, kd * 16384 + 8192);
      kd = knext(kd);
    }
  };
  {
    const int rt = 4 * ((b & 7) >> 1) + (j & 3), cg = (j >> 2) + 8 * (b & 1) - 1;
    if (t == 0) fl[0] = 0u;
    if (cg < 0) {
      g1_stats(sm, rt, ws);
    } else if (w12 >= 8) {
      const rsrc_t rx = mkrsrc(ws + WS_X1T, D * L * 2), rg = mkrsrc(wg, D * DF * 2), ru = mkrsrc(wu, D * DF * 2);
      const u32 vx = (4 * w + g) * 4096 + 256 * rt + 16 * ((lane & 15) ^ ((g << 2) | w));
      u32 vw[3];
#pragma unroll
      for (int i = 0; i < 3; ++i) {
        const int pc = (3 * (w & 1) + i) * 64 + lane, row = pc / 12, chk = pc - 12 * row;
        vw[i] = row * (DF * 2) + min(192 * cg + 16 * (chk ^ (w & 2)), DF * 2 - 16);
      }
      const rsrc_t rwm = w < 2 ? rg : ru;
      auto stage = [&](int i, int slot) {
        const u32 m = lb0 + slot * G1_SLOT;
        glds16<0>(m + 1024 * w, vx, rx, i * 131072);
        glds16<0>(m + 4096 + 1024 * w, vx, rx, i * 131072 + 65536);
#pragma unroll
        for (int k = 0; k < 3; ++k) glds16<0>(m + 8192 + 3072 * w + 1024 * k, vw[k], rwm, i * (32 * DF * 2));
      };
      asm volatile("s_nop 4");
      static_assert(G1_LEAD + 2 <= G1_NS, "ring too short");
#pragma unroll 1
      for (int i = 0; i < G1_LEAD - 1; ++i) stage(i, i);
      int slot = G1_LEAD - 1;
#pragma unroll 1
      for (int i = 0; i < 17 - G1_LEAD; ++i) {
        stage(i + G1_LEAD - 1, slot);
        slot = slot == G1_NS - 1 ? 0 : slot + 1;
        vmwait<5 * (G1_LEAD - 1)>();
        rawbar();
      }
      const u32* st2 = (const u32*)(ws + WS_ST2) + 128 * rt + 32 * w + (lane & 31);
      u32 v;
      asm volatile("global_load_dword %0, %1, off sc1" : "=v"(v) : "v"(st2));
      sfor<17 - G1_LEAD, 16>([&](auto i) { vmwait<5 * (15 - i) + 1>(); rawbar(); });
      asm volatile("s_waitcnt vmcnt(0)" : "+v"(v));
#pragma unroll 1
      for (int spin = 0; spin < (1 << 16) && !__all(v != 0u); ++spin) {
        __builtin_amdgcn_s_sleep(2);
        v = __hip_atomic_load(st2, __ATOMIC_RELAXED, __HIP_MEMORY_SCOPE_AGENT);
      }
      ((u32*)(sm + G1_SV))[32 * w + (lane & 31)] = v;
      bar();
      bar();
    } else if (w12 >= 4) {
      const char* pf = (const char*)wd + j * (43 * 1024) + w * (43 * 256);
#pragma unroll 1
      for (int i = 0; i < 18; ++i) {
        rawbar();
      }
    } else {
      const int wl = w & 1, wu3 = 3 * (w >> 1), u0 = 6 * cg + wu3;
      u32 xa[2][2], wa[3];
#pragma unroll
      for (int c = 0; c < 2; ++c)
#pragma unroll
        for (int tt = 0; tt < 2; ++tt)
          xa[c][tt] = lb0 + 256 * (8 * hh + 4 * tt + q) + 8 * (p & 1) + 16 * ((4 * (2 * wl + c) + 2 * (g & 1) + (p >> 1)) ^ ((q << 2) | (2 * hh + tt)));
#pragma unroll
      for (int v = 0; v < 3; ++v) wa[v] = lb0 + 8192 + 6144 * (g & 1) + 192 * (8 * hh + q) + 16 * ((2 * (wu3 + v) + (p >> 1)) ^ (2 * (g & 1))) + 8 * (p & 1);
      u4 ex[3];
#pragma unroll
      for (int v = 0; v < 3; ++v) ex[v] = *(const u4*)(ws + WS_EX + min(u0 + v, NU - 1) * 1024 + lane * 16);
      f16v acc[6];
#pragma unroll
      for (int i = 0; i < 6; ++i)
#pragma unroll
        for (int e = 0; e < 16; ++e) acc[i][e] = 0.f;
#pragma unroll
      for (int i = 0; i < 6; ++i) asm volatile("" : "+v"(acc[i]));
      rawbar();
      {
        u32 c0, c1, c2;
        asm volatile(
          "ds_read_b64_tr_b16 v[112:113], %6 offset:0\n"
          "ds_read_b64_tr_b16 v[114:115], %7 offset:0\n"
          "ds_read_b64_tr_b16 v[120:121], %10 offset:0\n"
          "ds_read_b64_tr_b16 v[122:123], %10 offset:768\n"
          "ds_read_b64_tr_b16 v[116:117], %8 offset:0\n"
          "ds_read_b64_tr_b16 v[118:119], %9 offset:0\n"
          "ds_read_b64_tr_b16 v[124:125], %11 offset:0\n"
          "ds_read_b64_tr_b16 v[126:127], %11 offset:768\n"
          "ds_read_b64_tr_b16 v[128:129], %12 offset:0\n"
          "ds_read_b64_tr_b16 v[130:131], %12 offset:768\n"
          "s_mov_b32 %13, 16\n"
          "s_mov_b32 %14, 0\n"
          "Lg1%=:\n"
          "s_add_u32 %14, %14, 1\n"
          "s_cmp_eq_u32 %14, 5\n"
          "s_cselect_b32 %15, -81920, %16\n"
          "s_cselect_b32 %14, 0, %14\n"
          "s_waitcnt lgkmcnt(6)\n"
          "v_mfma_f32_32x32x16_bf16 %0, v[120:123], v[112:115], %0\n"
          "ds_read_b64_tr_b16 v[132:133], %6 offset:4096\n"
          "ds_read_b64_tr_b16 v[134:135], %7 offset:4096\n"
          "s_waitcnt lgkmcnt(6)\n"
          "v_mfma_f32_32x32x16_bf16 %1, v[120:123], v[116:119], %1\n"
          "ds_read_b64_tr_b16 v[140:141], %10 offset:3072\n"
          "ds_read_b64_tr_b16 v[142:143], %10 offset:3840\n"
          "s_waitcnt lgkmcnt(6)\n"
          "v_mfma_f32_32x32x16_bf16 %2, v[124:127], v[112:115], %2\n"
          "ds_read_b64_tr_b16 v[136:137], %8 offset:4096\n"
          "ds_read_b64_tr_b16 v[138:139], %9 offset:4096\n"
          "v_mfma_f32_32x32x16_bf16 %3, v[124:127], v[116:119], %3\n"
          "ds_read_b64_tr_b16 v[144:145], %11 offset:3072\n"
          "ds_read_b64_tr_b16 v[146:147], %11 offset:3840\n"
          "s_waitcnt lgkmcnt(8)\n"
          "v_mfma_f32_32x32x16_bf16 %4, v[128:131], v[112:115], %4\n"
          "ds_read_b64_tr_b16 v[148:149], %12 offset:3072\n"
          "ds_read_b64_tr_b16 v[150:151], %12 offset:3840\n"
          "v_add_u32 %6, %15, %6\n"
          "v_add_u32 %7, %15, %7\n"
          "v_add_u32 %8, %15, %8\n"
          "v_add_u32 %9, %15, %9\n"
          "v_mfma_f32_32x32x16_bf16 %5, v[128:131], v[116:119], %5\n"
          "v_add_u32 %10, %15, %10\n"
          "v_add_u32 %11, %15, %11\n"
          "v_add_u32 %12, %15, %12\n"
          "s_barrier\n"
          "s_waitcnt lgkmcnt(6)\n"
          "v_mfma_f32_32x32x16_bf16 %0, v[140:143], v[132:135], %0\n"
          "ds_read_b64_tr_b16 v[112:113], %6 offset:0\n"
          "ds_read_b64_tr_b16 v[114:115], %7 offset:0\n"
          "s_waitcnt lgkmcnt(6)\n"
          "v_mfma_f32_32x32x16_bf16 %1, v[140:143], v[136:139], %1\n"
          "ds_read_b64_tr_b16 v[120:121], %10 offset:0\n"
          "ds_read_b64_tr_b16 v[122:123], %10 offset:768\n"
          "s_waitcnt lgkmcnt(6)\n"
          "v_mfma_f32_32x32x16_bf16 %2, v[144:147], v[132:135], %2\n"
          "ds_read_b64_tr_b16 v[116:117], %8 offset:0\n"
          "ds_read_b64_tr_b16 v[118:119], %9 offset:0\n"
          "v_mfma_f32_32x32x16_bf16 %3, v[144:147], v[136:139], %3\n"
          "ds_read_b64_tr_b16 v[124:125], %11 offset:0\n"
          "ds_read_b64_tr_b16 v[126:127], %11 offset:768\n"
          "s_waitcnt lgkmcnt(8)\n"
          "v_mfma_f32_32x32x16_bf16 %4, v[148:151], v[132:135], %4\n"
          "ds_read_b64_tr_b16 v[128:129], %12 offset:0\n"
          "ds_read_b64_tr_b16 v[130:131], %12 offset:768\n"
          "s_sub_u32 %13, %13, 1\n"
          "s_cmp_lg_u32 %13, 0\n"
          "v_mfma_f32_32x32x16_bf16 %5, v[148:151], v[136:139], %5\n"
          "s_cbranch_scc1 Lg1%=\n"
          "s_waitcnt lgkmcnt(0)\n"
          : "+v"(acc[0]), "+v"(acc[1]), "+v"(acc[2]), "+v"(acc[3]), "+v"(acc[4]), "+v"(acc[5]), "+v"(xa[0][0]), "+v"(xa[0][1]), "+v"(xa[1][0]), "+v"(xa[1][1]),
            "+v"(wa[0]), "+v"(wa[1]), "+v"(wa[2]), "=&s"(c0), "=&s"(c1), "=&s"(c2)
          : "s"(G1_SLOT) : "memory", "scc", "v112", "v113", "v114", "v115", "v116", "v117", "v118", "v119", "v120", "v121", "v122", "v123", "v124", "v125", "v126", "v127", "v128", "v129", "v130", "v131", "v132", "v133", "v134", "v135", "v136", "v137", "v138", "v139", "v140", "v141", "v142", "v143", "v144", "v145", "v146", "v147", "v148", "v149", "v150", "v151");
      }
      u32 sv[2];
#pragma unroll
      for (int c = 0; c < 2; ++c) sv[c] = ((const u32*)(sm + G1_SV))[64 * wl + 32 * c + (lane & 31)];
      bar();
#pragma unroll
      for (int c = 0; c < 2; ++c) {
        const u32 bv = hh ? __builtin_amdgcn_perm(sv[c], sv[c], 0x03020302u) : (__builtin_amdgcn_perm(sv[c], sv[c], 0x01000100u) ^ 0x80008000u);
#pragma unroll
        for (int v = 0; v < 3; ++v) acc[2 * v + c] = mfma32(ex[v], (u4){bv, bv, bv, bv}, acc[2 * v + c]);
      }
      char* hp = ws + WS_HH + (4 * rt + 2 * wl) * HH_T + u0 * 1024 + 16 * lane;
#pragma unroll
      for (int v = 0; v < 3; ++v) {
        if (u0 + v < NU) {
#pragma unroll
          for (int c = 0; c < 2; ++c) {
            const float sd = bhi(sv[c]);
            const u4 o = g1_tile(acc[2 * v + c], __builtin_amdgcn_rcpf(sd), sd);
            char* dst = hp + c * HH_T + v * 1024;
            asm volatile("global_store_dwordx4 %0, %1, off sc1\n\ts_nop 1" :: "v"(dst), "v"(o) : "memory");
          }
        }
      }
      {
        asm volatile("s_waitcnt vmcnt(0)" ::: "memory");
        if (lane == 0 && __hip_atomic_fetch_add((LAS u32*)fl, 1u, __ATOMIC_RELAXED, __HIP_MEMORY_SCOPE_WORKGROUP) == 3u)
          __hip_atomic_fetch_add((u32*)(ws + WS_CNT) + 64 * rt, 1u, __ATOMIC_RELAXED, __HIP_MEMORY_SCOPE_AGENT);
      }
    }
  }
  if (w12 >= 8) {
    asm volatile("s_nop 4");
    fill2(G2_RES, 3, 0);
    fill2(3, G2_RES - 3, 3);
    vmwait<2 * (G2_RES - 3)>();
    rawbar();
    rawbar();
    fill2(0, 3, 0);
    vmwait<0>();
    rawbar();
    rawbar();
    rawbar();
  } else {
    const int wl = w12 & 1, kq = w12 >> 1, dc = 64 * dt + 16 * kq + 4 * hh;
    f4 gb[2], xr[2];
#pragma unroll
    for (int e = 0; e < 2; ++e) {
      gb[e] = *(const f4*)((const float*)(ws + WS_MOD) + 5 * D + dc + 8 * e);
      xr[e] = *(const f4*)((const float*)(ws + WS_INV) + dc + 8 * e);
#pragma unroll
      for (int i = 0; i < 4; ++i) xr[e][i] *= b2f(((const u16*)(ws + WS_X1T))[(dc + 8 * e + i) * L + 64 * lt + 32 * wl + (lane & 31)]);
    }
    u32 pa[2];
#pragma unroll
    for (int e = 0; e < 2; ++e)
      pa[e] = lb0 + G2_RES * 2048 * kq + 128 * (4 * hh + q) + 16 * ((((4 * e + 2 * (g & 1)) ^ (2 * kq)) + (p >> 1)) ^ (4 * (q >> 1))) + 8 * (p & 1);
    f16v acc[2];
#pragma unroll
    for (int e = 0; e < 16; ++e) acc[0][e] = acc[1][e] = 0.f;
    asm volatile("" : "+v"(xr[0]), "+v"(xr[1]), "+v"(gb[0]), "+v"(gb[1]), "+v"(acc[0]), "+v"(acc[1]));
    rawbar();
    u4 A[2][2], AR[3][2];
#pragma unroll
    for (int i = 0; i < 6; ++i) AR[i >> 1][i & 1] = cat(trd(pa[i & 1] + (i >> 1) * 2048), trd(pa[i & 1] + (i >> 1) * 2048 + 1024));
    asm volatile("s_waitcnt lgkmcnt(0)\n\ts_barrier" : "+v"(AR[0][0]), "+v"(AR[0][1]), "+v"(AR[1][0]), "+v"(AR[1][1]), "+v"(AR[2][0]), "+v"(AR[2][1]) :: "memory");
    if (w12 == 0) {
      const u32* cnt = (const u32*)(ws + WS_CNT) + 64 * (lt >> 1);
#pragma unroll 1
      for (int spin = 0; spin < (1 << 18); ++spin)
        if (__hip_atomic_load(cnt, __ATOMIC_RELAXED, __HIP_MEMORY_SCOPE_AGENT) >= 15u) break;
    }
    rawbar();
    const rsrc_t rh = mkrsrc(ws + WS_HH + (2 * lt + wl) * HH_T, HH_T);
    constexpr int LEAD = G2_LEAD, NH = LEAD + 2;
    u4 H[NH];
    u32 kh = kfirst(kq);
    auto ldh = [&](auto i) { if constexpr (i < 22) { bld16_sc1<0>(H[i % NH], lane * 16, rh, kh * 1024); kh = knext(kh); } };
    sfor<0, LEAD>(ldh);
    auto rda = [&](auto i) {
#pragma unroll
      for (int e = 0; e < 2; ++e) A[i & 1][e] = cat(trd(pa[e] + i * 2048), trd(pa[e] + i * 2048 + 1024));
    };
    rda(std::integral_constant<int, 0>{});
    sfor<0, 22>([&](auto i) {
      vmwait<(i < 22 - LEAD ? LEAD - 1 : 21 - i)>(H[i % NH]);
      SB();
      if constexpr (i + 1 < G2_RES) rda(std::integral_constant<int, i + 1>{});
      if (i < 21 || kq < 2) {
#pragma unroll
        for (int e = 0; e < 2; ++e) acc[e] = mfma32(i < G2_RES ? A[i & 1][e] : AR[i < G2_RES ? 0 : i - G2_RES][e], H[i % NH], acc[e]);
      }
      if constexpr (i + 1 < G2_RES) { SGB(0x8, 1); SGB(0x100, 2); SGB(0x8, 1); SGB(0x100, 2); }
      SB();
      ldh(std::integral_constant<int, i + LEAD>{});
      if constexpr (i == G2_RES - 1) rawbar();
    });
    f4* red = (f4*)sm + wl * 1536 + lane;
#pragma unroll
    for (int k = 1; k < 4; ++k)
#pragma unroll
      for (int e = 0; e < 2; ++e) {
        const f16v& a = acc[k >> 1];
        const int r = 8 * (k & 1) + 4 * e;
        red[(kq ^ k) * 384 + (k - 1) * 128 + 64 * e] = (f4){a[r], a[r + 1], a[r + 2], a[r + 3]};
      }
    bar();
    u2 o[2];
#pragma unroll
    for (int e = 0; e < 2; ++e) {
      const f4* rp = red + kq * 384 + 64 * e;
      const f4 y = ((f4){acc[0][4 * e], acc[0][4 * e + 1], acc[0][4 * e + 2], acc[0][4 * e + 3]} + rp[0] + rp[128] + rp[256]) * gb[e] + xr[e];
      o[e] = (u2){pk(y[0], y[1]), pk(y[2], y[3])};
    }
    const auto r0 = __builtin_amdgcn_permlane32_swap(o[0].x, o[1].x, false, false);
    const auto r1 = __builtin_amdgcn_permlane32_swap(o[0].y, o[1].y, false, false);
    *(u4*)(out + (64 * lt + 32 * wl + (lane & 31)) * D + 64 * dt + 16 * kq + 8 * hh) = (u4){r0[0], r1[0], r0[1], r1[1]};
  }
}

extern "C" void kernel_launch(void* const* d_in, const int* in_sizes, int n_in, void* d_out, int out_size, void* d_ws, size_t ws_size,
                              hipStream_t stream) {
  const u16* x = (const u16*)d_in[0];
  const u16 *wg = (const u16*)d_in[8], *wu = (const u16*)d_in[9], *wd = (const u16*)d_in[10];
  char* ws = (char*)d_ws;
  (void)hipFuncSetAttribute((const void*)k2_conv_prep, hipFuncAttributeMaxDynamicSharedMemorySize, K2_LDS);
  (void)hipFuncSetAttribute((const void*)k3_mlp, hipFuncAttributeMaxDynamicSharedMemorySize, K3_LDS);
  k1_prep<<<496, 256, 0, stream>>>(x, (const u16*)d_in[1], (const u16*)d_in[2], (const u16*)d_in[3], (const u16*)d_in[4], (const u16*)d_in[5],
                                   (const u16*)d_in[6], (const u16*)d_in[11], (const u16*)d_in[12], ws);
  k2_conv_prep<<<256, 512, K2_LDS, stream>>>(wg, wu, wd, ws);
  k3_mlp<<<256, 768, K3_LDS, stream>>>(wg, wu, wd, ws, (u16*)d_out);
}
```

```cpp
#include <hip/hip_runtime.h>
#include <hip/hip_bf16.h>
#include <stdint.h>
#include <type_traits>

typedef unsigned short u16;
typedef unsigned u32;
typedef float f2 __attribute__((ext_vector_type(2)));
typedef float f4 __attribute__((ext_vector_type(4)));
typedef float f16v __attribute__((ext_vector_type(16)));
typedef u32 u2 __attribute__((ext_vector_type(2)));
typedef u32 u4 __attribute__((ext_vector_type(4)));
typedef u4 u4a8 __attribute__((aligned(8)));
typedef short s4 __attribute__((ext_vector_type(4)));
typedef __bf16 bf2 __attribute__((ext_vector_type(2)));
typedef __bf16 bf8 __attribute__((ext_vector_type(8)));
typedef _Float16 h2 __attribute__((ext_vector_type(2)));
typedef _Float16 h8 __attribute__((ext_vector_type(8)));
#define LAS __attribute__((address_space(3)))
#define DI __device__ __forceinline__

constexpr int L = 2048, D = 512, NM = 128, DF = 1376, NU = 86;
constexpr int KXS = 2056;

constexpr size_t WS_MOD = 0, WS_MEAN = 16 << 10, WS_SD = 20 << 10, WS_CV = 24 << 10, WS_INV = 26 << 10, WS_ST2 = 32 << 10, WS_CNT = 40 << 10,
                 WS_EX = 64 << 10, WS_XT = 256 << 10, WS_KX = WS_XT + (2 << 20), WS_X1T = WS_KX + (2112 << 10), WS_HH = WS_X1T + (2 << 20), WS_END = WS_HH + (5504 << 10);

template <class T> DI T ldnt(const T* p) { return __builtin_nontemporal_load(p); }
DI float blo(u32 v) { return __uint_as_float(v << 16); }
DI float bhi(u32 v) { return __uint_as_float(v & 0xffff0000u); }
DI float b2f(u16 v) { return __uint_as_float(((u32)v) << 16); }
DI u32 pk(float a, float b) { return __builtin_bit_cast(u32, __builtin_convertvector((f2){a, b}, bf2)); }
DI u32 pkh(f2 v) { return __builtin_bit_cast(u32, __builtin_convertvector(v, h2)); }
DI float rb(float a) { return blo(pk(a, a)); }
DI u2 trd(const char* p) { return __builtin_bit_cast(u2, __builtin_amdgcn_ds_read_tr16_b64_v4i16((LAS s4*)p)); }
DI u2 trd(u32 a) { return __builtin_bit_cast(u2, __builtin_amdgcn_ds_read_tr16_b64_v4i16((LAS s4*)(size_t)a)); }
DI u4 cat(u2 a, u2 b) { return (u4){a.x, a.y, b.x, b.y}; }
DI float dot2(u32 a, u32 b, float c) { return __builtin_amdgcn_fdot2_f32_bf16(__builtin_bit_cast(bf2, a), __builtin_bit_cast(bf2, b), c, false); }
DI f4 mfma16(u4 a, u4 b, f4 c) { return __builtin_amdgcn_mfma_f32_16x16x32_bf16(__builtin_bit_cast(bf8, a), __builtin_bit_cast(bf8, b), c, 0, 0, 0); }
DI f4 mfma16h(u4 a, u4 b, f4 c) { return __builtin_amdgcn_mfma_f32_16x16x32_f16(__builtin_bit_cast(h8, a), __builtin_bit_cast(h8, b), c, 0, 0, 0); }
DI f16v mfma32(u4 a, u4 b, f16v c) { return __builtin_amdgcn_mfma_f32_32x32x16_bf16(__builtin_bit_cast(bf8, a), __builtin_bit_cast(bf8, b), c, 0, 0, 0); }
#define CM_MUL(d, a, b) "v_pk_mul_f32 " d ", " a ", " b " op_sel_hi:[0,1]\n\t"
#define CM_FMA(d, a, b) "v_pk_fma_f32 " d ", " a ", " b ", " d " op_sel:[1,1,0] op_sel_hi:[1,0,1] neg_lo:[0,1,0]\n\t"
DI void cmul2(f2& r0, f2 a0, f2 b0, f2& r1, f2 a1, f2 b1) {
  asm(CM_MUL("%0", "%2", "%3") CM_MUL("%1", "%4", "%5") CM_FMA("%0", "%2", "%3") CM_FMA("%1", "%4", "%5")
      : "=&v"(r0), "=&v"(r1) : "v"(a0), "v"(b0), "v"(a1), "v"(b1));
}
DI void cmul3(f2& r0, f2 a0, f2 b0, f2& r1, f2 a1, f2 b1, f2& r2, f2 a2, f2 b2) {
  asm(CM_MUL("%0", "%3", "%4") CM_MUL("%1", "%5", "%6") CM_MUL("%2", "%7", "%8") CM_FMA("%0", "%3", "%4") CM_FMA("%1", "%5", "%6") CM_FMA("%2", "%7", "%8")
      : "=&v"(r0), "=&v"(r1), "=&v"(r2) : "v"(a0), "v"(b0), "v"(a1), "v"(b1), "v"(a2), "v"(b2));
}
DI void cmul4(f2& r0, f2& r1, f2& r2, f2& r3, f2 a0, f2 a1, f2 a2, f2 a3, f2 b) {
  asm(CM_MUL("%0", "%4", "%8") CM_MUL("%1", "%5", "%8") CM_MUL("%2", "%6", "%8") CM_MUL("%3", "%7", "%8")
      CM_FMA("%0", "%4", "%8") CM_FMA("%1", "%5", "%8") CM_FMA("%2", "%6", "%8") CM_FMA("%3", "%7", "%8")
      : "=&v"(r0), "=&v"(r1), "=&v"(r2), "=&v"(r3) : "v"(a0), "v"(a1), "v"(a2), "v"(a3), "v"(b));
}
DI float exp_acc(float x) {
  const float t = x * 1.44269502f, r = fmaf(x, 1.44269502f, -t) + x * 1.92596299e-8f, e = __builtin_amdgcn_exp2f(t);
  return fmaf(e, r * 0.693147182f, e);
}
template <int CTRL> DI float dppf(float v) { return __int_as_float(__builtin_amdgcn_update_dpp(0, __float_as_int(v), CTRL, 0xf, 0xf, true)); }
DI float sum32(float v) {
  auto r = __builtin_amdgcn_permlane32_swap(__float_as_uint(v), __float_as_uint(v), false, false);
  return __uint_as_float(r[0]) + __uint_as_float(r[1]);
}
DI f2 ln_stats(float s1, float s2) {
  const float mean = s1 * (1.f / D);
  const float var = fmaxf(fmaf(-mean, mean, s2 * (1.f / D)), 0.f);
  return (f2){rb(mean), rb(__builtin_amdgcn_sqrtf(rb(rb(var) + 1e-5f)))};
}
typedef __amdgpu_buffer_rsrc_t rsrc_t;
DI rsrc_t mkrsrc(const void* p, u32 bytes) { return __builtin_amdgcn_make_buffer_rsrc((void*)p, 0, bytes, 0x00020000); }
DI u32 ldsaddr(const char* p) { return (u32)(size_t)(LAS const char*)p; }
template <int OFF> DI void glds16(u32 m0v, u32 voff, rsrc_t rs, u32 soff) {
  asm volatile("s_mov_b32 m0, %0\n\ts_nop 0\n\tbuffer_load_dwordx4 %1, %2, %3 offen offset:%4 lds" :: "s"(m0v), "v"(voff), "s"(rs), "s"(soff), "n"(OFF) : "memory");
}
DI void glds16x4(u32 m0v, u32 voff, rsrc_t rs, u32 soff) {
  asm volatile("s_mov_b32 m0, %0\n\ts_nop 0\n\tbuffer_load_dwordx4 %1, %2, %3 offen lds\n\tbuffer_load_dwordx4 %1, %2, %3 offen offset:1024 lds\n\t"
               "buffer_load_dwordx4 %1, %2, %3 offen offset:2048 lds\n\tbuffer_load_dwordx4 %1, %2, %3 offen offset:3072 lds"
               :: "s"(m0v), "v"(voff), "s"(rs), "s"(soff) : "memory");
}
template <int OFF> DI void bld16(u4& d, u32 voff, rsrc_t rs, u32 soff) {
  asm volatile("buffer_load_dwordx4 %0, %1, %2, %3 offen offset:%4" : "=v"(d) : "v"(voff), "s"(rs), "s"(soff), "n"(OFF));
}
template <int OFF> DI void bld16_sc1(u4& d, u32 voff, rsrc_t rs, u32 soff) {
  asm volatile("buffer_load_dwordx4 %0, %1, %2, %3 offen offset:%4 sc1" : "=v"(d) : "v"(voff), "s"(rs), "s"(soff), "n"(OFF));
}
template <int OFF, class T> DI void gld16(T& d, const void* p) { asm volatile("global_load_dwordx4 %0, %1, off offset:%2" : "=v"(d) : "v"(p), "n"(OFF)); }
DI void pin(u32& v) { asm("" : "+v"(v)); }
DI void rawbar() { asm volatile("s_barrier" ::: "memory"); }
template <int N> DI void vmwait() { asm volatile("s_waitcnt vmcnt(%0)" :: "n"(N) : "memory"); }
template <int N> DI void vmwait(u4& d) { asm volatile("s_waitcnt vmcnt(%1)" : "+v"(d) : "n"(N)); }
DI void bar() { asm volatile("s_waitcnt lgkmcnt(0)\n\ts_barrier" ::: "memory"); }
#define SB() __builtin_amdgcn_sched_barrier(0)
#define SGB(m, n) __builtin_amdgcn_sched_group_barrier(m, n, 0)
template <int I, int N, class F> DI void sfor(F&& f) { if constexpr (I < N) { f(std::integral_constant<int, I>{}); sfor<I + 1, N>(f); } }

constexpr int KG_ROW = 544, KG_CH = 57 * KG_ROW, KG_T = 2 * KG_CH, KG_TS = 4160, K1_LDS = KG_T + 2 * KG_TS;

DI f2 cexp_pow2(float a, float fh, float fl, float p) {
  const float e = __builtin_amdgcn_exp2f(a * p);
  const float r = fmaf(fl, p, __builtin_amdgcn_fractf(fh * p));
  return (f2){e * __builtin_amdgcn_cosf(r), e * __builtin_amdgcn_sinf(r)};
}

DI void k1_kgen(char* sm, int b, const u16* lar, const u16* aim, const u16* cre, const u16* cim, const u16* ldt, const u16* dsk, u16* kx) {
  const int t = threadIdx.x, w = __builtin_amdgcn_readfirstlane(t >> 6), lane = t & 63, ch = w >> 1, half = w & 1;
  const int d0 = 64 * (b & 7) + 2 * (b >> 3), d = d0 + ch, n = 64 * half + lane;
  const u32 v_lar = ldnt(lar + d * NM + n), v_aim = ldnt(aim + d * NM + n);
  const u32 cr = ldnt(cre + d * NM + n), ci = ldnt(cim + d * NM + n), cr2 = ldnt(cre + d * NM + (n ^ 64)), ci2 = ldnt(cim + d * NM + (n ^ 64));
  const float dt = rb(exp_acc(b2f(ldt[d])));
  const float ds = b2f(dsk[d]);
  const float zr = -rb(exp_acc(blo(v_lar))) * dt, zi = blo(v_aim) * dt;
  const float a = zr * 1.44269502f;
  const float fh = zi * 0.159154937f, fl = fmaf(zi, 0.159154937f, -fh) + zi * 6.42063833e-9f;
  const f2 e1 = cexp_pow2(a, fh, fl, 1.f), e8 = cexp_pow2(a, -fh, -fl, 8.f), e128 = cexp_pow2(a, fh, fl, 128.f), e1024 = cexp_pow2(a, fh, fl, 1024.f);
  int mx = (int)max(max(cr & 0x7fffu, ci & 0x7fffu), max(cr2 & 0x7fffu, ci2 & 0x7fffu));
  mx = max(mx, __builtin_amdgcn_update_dpp(0, mx, 0x111, 0xf, 0xf, true));
  mx = max(mx, __builtin_amdgcn_update_dpp(0, mx, 0x112, 0xf, 0xf, true));
  mx = max(mx, __builtin_amdgcn_update_dpp(0, mx, 0x114, 0xf, 0xf, true));
  mx = max(mx, __builtin_amdgcn_update_dpp(0, mx, 0x118, 0xf, 0xf, true));
  mx = max(max(__builtin_amdgcn_readlane(mx, 15), __builtin_amdgcn_readlane(mx, 31)), max(__builtin_amdgcn_readlane(mx, 47), __builtin_amdgcn_readlane(mx, 63)));
  const int ex = min(max(mx >> 7, 1), 253);
  const float cs = __int_as_float((254 - ex) << 23), kf = 2.f * dt * __int_as_float(ex << 23);
  if (t < 2) *(u4*)(kx + (d0 + t) * KXS) = (u4){0u, 0u, 0u, 0u};
  constexpr u32 KEEP = 0x03020100u, LL = 0x01000100u, LH = 0x05040100u, HH = 0x05040504u;
  const u32 sel0 = t < 32 ? KEEP : LL, sel1 = t < 32 ? KEEP : t < 128 ? LH : LL;
  const u32 sel2 = t < 32 ? KEEP : t < 128 ? LL : (t & 1) ? HH : LH, sel3 = t < 32 ? KEEP : t < 64 ? LH : HH;
  char* base = sm + ch * KG_CH + n * 4;
  auto put = [&](int row, f2 v) { *(u32*)(base + row * KG_ROW) = pkh(v); };
  f2 lo[8], e[8], m[8], x0, x1, x2, x3, e16, e32, e64;
  lo[0] = (f2){blo(cr) * cs, -blo(ci) * cs};
  e[1] = e1; m[1] = e128;
  put(0, lo[0]); put(16, (f2){1.f, 0.f}); put(17, e1); put(24, e128); put(48, e1024);
  cmul3(e16, e8, e8, e[2], e1, e1, m[2], e128, e128);
  cmul2(lo[1], lo[0], e8, x0, e1024, e1024);
  put(18, e[2]); put(32, m[2]); put(1, lo[1]); put(56, x0);
  cmul3(e32, e16, e16, e[4], e[2], e[2], m[4], m[2], m[2]);
  cmul2(e[3], e[2], e1, m[3], m[2], e128);
  cmul2(lo[2], lo[0], e16, lo[3], lo[1], e16);
  put(20, e[4]); put(40, m[4]); put(19, e[3]); put(36, m[3]); put(2, lo[2]); put(3, lo[3]);
  cmul4(e[5], e[6], e[7], x0, e1, e[2], e[3], m[4], e[4]);
  put(21, e[5]); put(22, e[6]); put(23, e[7]); put(41, x0);
  cmul3(m[5], m[4], e128, m[6], m[4], m[2], m[7], m[4], m[3]);
  put(42, m[5]); put(44, m[6]); put(46, m[7]);
  cmul4(lo[4], lo[5], lo[6], lo[7], lo[0], lo[1], lo[2], lo[3], e32);
  put(4, lo[4]); put(5, lo[5]); put(6, lo[6]); put(7, lo[7]);
  cmul2(e64, e32, e32, x0, e128, e1);
  put(25, x0);
  cmul3(x0, m[5], e[4], x1, m[6], e[4], x2, m[7], e[4]);
  put(43, x0); put(45, x1); put(47, x2);
  cmul4(x0, x1, x2, x3, lo[0], lo[1], lo[2], lo[3], e64);
  put(8, x0); put(9, x1); put(10, x2); put(11, x3);
  cmul4(x0, x1, x2, x3, lo[4], lo[5], lo[6], lo[7], e64);
  put(12, x0); put(13, x1); put(14, x2); put(15, x3);
  cmul4(x0, x1, x2, x3, e[2], e[3], e[4], e[5], e128);
  put(26, x0); put(27, x1); put(28, x2); put(29, x3);
  cmul2(x0, e[6], e128, x1, e[7], e128);
  put(30, x0); put(31, x1);
  cmul3(x0, e[2], m[2], x1, e[4], m[2], x2, e[6], m[2]);
  put(33, x0); put(34, x1); put(35, x2);
  cmul3(x0, e[2], m[3], x1, e[4], m[3], x2, e[6], m[3]);
  put(37, x0); put(38, x1); put(39, x2);
  cmul4(x0, x1, x2, x3, m[1], m[2], m[3], m[4], e1024);
  put(49, x0); put(50, x1); put(51, x2); put(52, x3);
  cmul3(x0, m[5], e1024, x1, m[6], e1024, x2, m[7], e1024);
  put(53, x0); put(54, x1); put(55, x2);
  __syncthreads();
  {
    const int c = lane & 15, g = lane >> 4;
    const char* fb = sm + ch * KG_CH + c * KG_ROW + g * 16;
    u16* T = (u16*)(sm + KG_T + ch * KG_TS);
    f4 acc0 = {0.f, 0.f, 0.f, 0.f}, acc1 = acc0;
    u4 B[8], A[8];
#pragma unroll
    for (int s = 0; s < 8; ++s) { B[s] = *(const u4*)(fb + s * 64); A[s] = *(const u4*)(fb + (16 + 16 * half) * KG_ROW + s * 64); }
    if (!half) {
      u4 A2[8];
#pragma unroll
      for (int s = 0; s < 8; ++s) A2[s] = *(const u4*)(fb + 48 * KG_ROW + s * 64);
      SB();
#pragma unroll
      for (int s = 0; s < 8; ++s) acc0 = mfma16h(A[s], B[s], acc0);
#pragma unroll
      for (int s = 0; s < 8; ++s) acc1 = mfma16h(A2[s], B[s], acc1);
      acc0 *= kf; acc1 *= kf;
      if (lane == 0) acc0[0] += ds;
      *(u2*)(T + 8 * c + 4 * (g & 1) + 128 * (g >> 1)) = (u2){pk(acc0[0], acc0[1]), pk(acc0[2], acc0[3])};
      if (g < 2) {
#pragma unroll
        for (int e = 0; e < 4; ++e) T[1024 + 128 * (4 * g + e) + 8 * c] = (u16)pk(acc1[e], 0.f);
      } else if (lane == 32) T[2048] = (u16)pk(acc1[0], 0.f);
    } else {
      SB();
#pragma unroll
      for (int s = 0; s < 8; ++s) acc0 = mfma16h(A[s], B[s], acc0);
      acc0 *= kf;
#pragma unroll
      for (int e = 0; e < 4; ++e) {
        const int p = g < 2 ? 256 + 128 * g + 2 * e : 512 + 128 * (2 * (g - 2) + (e >> 1)) + 4 * (e & 1);
        T[p + 8 * c] = (u16)pk(acc0[e], 0.f);
      }
    }
  }
  __syncthreads();
  u4 v[2];
  u32 e8v[2];
#pragma unroll
  for (int c2 = 0; c2 < 2; ++c2) {
    const u16* T = (const u16*)(sm + KG_T + c2 * KG_TS);
    v[c2] = *(const u4*)(T + 8 * t);
    e8v[c2] = T[8 * t + 8];
  }
#pragma unroll
  for (int c2 = 0; c2 < 2; ++c2) {
    const u4 o = {__builtin_amdgcn_perm(v[c2].x, v[c2].x, sel0), __builtin_amdgcn_perm(v[c2].z, t < 64 ? v[c2].y : v[c2].x, sel1),
                  __builtin_amdgcn_perm(e8v[c2], t < 128 ? v[c2].z : v[c2].x, sel2), __builtin_amdgcn_perm(e8v[c2], v[c2].w, sel3)};
    *(u4*)(kx + (d0 + c2) * KXS + 8 + 8 * t) = o;
  }
}

DI void k1_xt(char* sm, int b, const u16* x, u16* xt) {
  const int t = threadIdx.x, w = t >> 6, lane = t & 63;
  const int c0 = 64 * (b & 7), r0 = 128 * (b >> 3);
  u4 v[4];
#pragma unroll
  for (int i = 0; i < 4; ++i) v[i] = ldnt((const u4*)(x + (r0 + 32 * i + (t >> 3)) * D + c0 + 8 * (t & 7)));
#pragma unroll
  for (int i = 0; i < 4; ++i) *(u4*)(sm + (32 * i + (t >> 3)) * 144 + 16 * (t & 7)) = v[i];
  __syncthreads();
  const int g = lane >> 4, q = (lane & 15) >> 2, p = lane & 3;
#pragma unroll
  for (int i = 0; i < 4; ++i) {
    const char* a = sm + (32 * i + 8 * g + q) * 144 + 32 * w + 8 * p;
    *(u4*)(xt + (c0 + 16 * w + (lane & 15)) * L + r0 + 32 * i + 8 * g) = cat(trd(a), trd(a + 4 * 144));
  }
}

DI void k1_stats(int b, const u16* x, char* ws) {
  const int t = threadIdx.x, row = 32 * b + (t >> 3);
  u4 v[8];
#pragma unroll
  for (int i = 0; i < 8; ++i) v[i] = ldnt((const u4*)(x + row * D + 64 * i + 8 * (t & 7)));
  float s1 = 0.f, s2 = 0.f, s3 = 0.f, s4 = 0.f;
#pragma unroll
  for (int i = 0; i < 8; ++i)
#pragma unroll
    for (int j = 0; j < 4; j += 2) {
      s1 = dot2(v[i][j], 0x3f803f80u, s1); s2 = dot2(v[i][j], v[i][j], s2);
      s3 = dot2(v[i][j + 1], 0x3f803f80u, s3); s4 = dot2(v[i][j + 1], v[i][j + 1], s4);
    }
  s1 += s3; s2 += s4;
  s1 += dppf<0xb1>(s1); s2 += dppf<0xb1>(s2); s1 += dppf<0x4e>(s1); s2 += dppf<0x4e>(s2); s1 += dppf<0x141>(s1); s2 += dppf<0x141>(s2);
  const f2 st = ln_stats(s1, s2);
  if ((t & 7) == 0) {
    ((u16*)(ws + WS_MEAN))[row] = (u16)(__float_as_uint(st.x) >> 16);
    ((u16*)(ws + WS_SD))[row] = (u16)(__float_as_uint(st.y) >> 16);
    ((u32*)(ws + WS_ST2))[row] = 0u;
  }
  if (t == 0 && b < 16) ((u32*)(ws + WS_CNT))[64 * b] = 0u;
}

DI void k1_mod(char* sm, int b, const u16* c, const u16* wm, char* ws) {
  const int t = threadIdx.x, rg = t >> 3;
  u4 v[16];
  const u32 cc = ((const u32*)c)[t];
#pragma unroll
  for (int i = 0; i < 16; ++i) v[i] = ldnt((const u4*)(wm + (16 * rg + i) * (6 * D) + 64 * b + 8 * (t & 7)));
  u32* sl = (u32*)(sm + 32 * 68 * 4);
  {
    const float c0 = blo(cc), c1 = bhi(cc);
    sl[t] = pk(c0 / (1.f + __expf(-c0)), c1 / (1.f + __expf(-c1)));
  }
  __syncthreads();
  const u4 sa = *(const u4*)(sl + 8 * rg), sb = *(const u4*)(sl + 8 * rg + 4);
  float acc[8] = {0.f, 0.f, 0.f, 0.f, 0.f, 0.f, 0.f, 0.f};
#pragma unroll
  for (int i = 0; i < 8; ++i) {
    const u32 sp = i < 4 ? sa[i & 3] : sb[i & 3];
#pragma unroll
    for (int j = 0; j < 4; ++j) {
      acc[2 * j] = dot2(__builtin_amdgcn_perm(v[2 * i + 1][j], v[2 * i][j], 0x05040100u), sp, acc[2 * j]);
      acc[2 * j + 1] = dot2(__builtin_amdgcn_perm(v[2 * i + 1][j], v[2 * i][j], 0x07060302u), sp, acc[2 * j + 1]);
    }
  }
  float* red = (float*)sm;
  *(f4*)(red + rg * 68 + 8 * (t & 7)) = (f4){acc[0], acc[1], acc[2], acc[3]};
  *(f4*)(red + rg * 68 + 8 * (t & 7) + 4) = (f4){acc[4], acc[5], acc[6], acc[7]};
  __syncthreads();
  float s = 0.f;
#pragma unroll
  for (int i = 0; i < 8; ++i) s += red[((t & 3) + 4 * i) * 68 + (t >> 2)];
  s += dppf<0xb1>(s); s += dppf<0x4e>(s);
  if ((t & 3) == 0) {
    const int j = 64 * b + (t >> 2), part = b >> 3;
    float v = rb(s);
    if (part == 1 || part == 4) v = rb(1.f + v);
    if (part == 4) {
      v = v == 0.f ? 0x1p-60f : v;
      ((float*)(ws + WS_INV))[j - 4 * D] = 1.f / v;
    }
    if (part == 3 || part == 4) ((u16*)(ws + WS_CV))[j - 3 * D] = (u16)(__float_as_uint(v) >> 16);
    ((float*)(ws + WS_MOD))[j] = v;
  }
}

__global__ __launch_bounds__(256, 2) void k1_prep(const u16* x, const u16* lar, const u16* aim, const u16* cre, const u16* cim, const u16* ldt,
                                                  const u16* dsk, const u16* c, const u16* wm, char* ws, const char* wg, const char* wu, const char* wd, const char* out) {
  __shared__ __attribute__((aligned(16))) char sm[K1_LDS];
  const int b = blockIdx.x;
  if (b < 256) k1_kgen(sm, b, lar, aim, cre, cim, ldt, dsk, (u16*)(ws + WS_KX));
  else if (b < 384) k1_xt(sm, b - 256, x, (u16*)(ws + WS_XT));
  else if (b < 448) k1_stats(b - 384, x, ws);
  else k1_mod(sm, b - 448, c, wm, ws);
  if (b >= 384 && b < 448) {
    constexpr u32 SH = 12, N0 = ((WS_END - WS_X1T - 1) >> SH) + 1, N1 = ((D * DF * 2 - 1) >> SH) + 1, N2 = ((L * D * 2 - 1) >> SH) + 1, NT = N0 + 3 * N1 + N2;
    u32 dummy[3] = {0, 0, 0};
#pragma unroll
    for (int k = 0; k < 3; ++k) {
      const u32 i = ((b - 384) >> 3) + 8 * (threadIdx.x + 256 * k);
      if (i < NT) {
        const char* p = i < N0 ? ws + WS_X1T + (i << SH) : i < N0 + N1 ? wg + ((i - N0) << SH) : i < N0 + 2 * N1 ? wu + ((i - N0 - N1) << SH)
                      : i < N0 + 3 * N1 ? wd + ((i - N0 - 2 * N1) << SH) : out + ((i - N0 - 3 * N1) << SH);
        asm volatile("global_load_dword %0, %1, off" : "+v"(dummy[k]) : "v"(p));
      }
    }
    asm volatile("s_waitcnt vmcnt(0)" : "+v"(dummy[0]), "+v"(dummy[1]), "+v"(dummy[2]));
  }
}

constexpr int CV_KC = 4128, CV_H = 8 * CV_KC, CV_HP = 1152, CV_X = CV_H + 8 * CV_HP, CV_EX = CV_X + 8192, CV_W = CV_EX + 16384,
              CV_FL = CV_W + 16384, K2_LDS = CV_FL + 16;

DI void k2_conv(char* sm, int b, char* ws) {
  const int t = threadIdx.x, w = __builtin_amdgcn_readfirstlane(t >> 6), lane = t & 63, ch = w >> 1, half = w & 1;
  const int d0 = 64 * (b & 7) + 2 * (b >> 3), d = d0 + ch;
  const u16* xt = (const u16*)(ws + WS_XT);
  const float* mod = (const float*)(ws + WS_MOD);
  const int c = lane & 15, g = lane >> 4;
  u4 xv[2];
  const u4 mv = *(const u4*)(ws + WS_MEAN + 16 * t), sv = *(const u4*)(ws + WS_SD + 16 * t);
#pragma unroll
  for (int i = 0; i < 2; ++i) xv[i] = *(const u4*)(xt + (d0 + i) * L + 8 * t);
  const float ga = mod[2 * D + d], c2 = mod[4 * D + d];
  const f2 c1 = *(const f2*)(mod + D + d0), sa = *(const f2*)(mod + d0);
  SB();
  float rs[8];
#pragma unroll
  for (int j = 0; j < 8; ++j) rs[j] = __builtin_amdgcn_rcpf((j & 1) ? bhi(sv[j >> 1]) : blo(sv[j >> 1]));
#pragma unroll
  for (int i = 0; i < 2; ++i) {
    float h[8];
#pragma unroll
    for (int j = 0; j < 8; ++j) {
      const float xx = (j & 1) ? bhi(xv[i][j >> 1]) : blo(xv[i][j >> 1]);
      const float mm = (j & 1) ? bhi(mv[j >> 1]) : blo(mv[j >> 1]);
      h[j] = fmaf((xx - mm) * rs[j], c1[i], sa[i]);
    }
#pragma unroll
    for (int r = 0; r < 4; ++r) *(u32*)(sm + CV_H + (4 * i + r) * CV_HP + 128 + 4 * t) = pk(h[r], h[r + 4]);
    *(u4*)(sm + CV_X + 4096 * i + 16 * t) = xv[i];
  }
  f4 acc[8];
#pragma unroll
  for (int i = 0; i < 8; ++i) acc[i] = (f4){0.f, 0.f, 0.f, 0.f};
  u32 ka = ldsaddr(sm) + (4 * ch + 2 * half) * CV_KC + 32 + 8 * (8 * g + (lane & 3) - ((lane & 15) >> 2));
  u32 hb = ldsaddr(sm) + CV_H + (4 * ch + 2 * half) * CV_HP + 128 + 8 * c - 16 * g - 8 - 64;
  bar();
  {
    u32 cnt;
    asm volatile(
      "ds_read_b64_tr_b16 v[64:65], %8 offset:32\n"
      "ds_read_b64_tr_b16 v[66:67], %8 offset:0\n"
      "ds_read_b64 v[120:121], %9 offset:832\n"
      "ds_read_b64 v[122:123], %9 offset:840\n"
      "ds_read_b64 v[124:125], %9 offset:960\n"
      "ds_read_b64 v[126:127], %9 offset:968\n"
      "ds_read_b64 v[112:113], %9 offset:576\n"
      "ds_read_b64 v[114:115], %9 offset:584\n"
      "ds_read_b64 v[116:117], %9 offset:704\n"
      "ds_read_b64 v[118:119], %9 offset:712\n"
      "ds_read_b64_tr_b16 v[68:69], %8 offset:544\n"
      "ds_read_b64_tr_b16 v[70:71], %8 offset:512\n"
      "ds_read_b64 v[108:109], %9 offset:448\n"
      "ds_read_b64 v[110:111], %9 offset:456\n"
      "ds_read_b64 v[104:105], %9 offset:320\n"
      "ds_read_b64 v[106:107], %9 offset:328\n"
      "ds_read_b64_tr_b16 v[72:73], %8 offset:1056\n"
      "ds_read_b64_tr_b16 v[74:75], %8 offset:1024\n"
      "ds_read_b64_tr_b16 v[76:77], %8 offset:1568\n"
      "ds_read_b64_tr_b16 v[78:79], %8 offset:1536\n"
      "ds_read_b64 v[100:101], %9 offset:192\n"
      "ds_read_b64 v[102:103], %9 offset:200\n"
      "ds_read_b64_tr_b16 v[80:81], %8 offset:2080\n"
      "ds_read_b64_tr_b16 v[82:83], %8 offset:2048\n"
      "ds_read_b64 v[96:97], %9 offset:64\n"
      "ds_read_b64 v[98:99], %9 offset:72\n"
      "ds_read_b64_tr_b16 v[84:85], %8 offset:2592\n"
      "ds_read_b64_tr_b16 v[86:87], %8 offset:2560\n"
      "ds_read_b64_tr_b16 v[88:89], %8 offset:3104\n"
      "ds_read_b64_tr_b16 v[90:91], %8 offset:3072\n"
      "ds_read_b64_tr_b16 v[92:93], %8 offset:3616\n"
      "ds_read_b64_tr_b16 v[94:95], %8 offset:3584\n"
      "s_mov_b32 %10, 2\n"
      "s_waitcnt lgkmcnt(0)\n"
      "Lconv%=:\n"
      "v_mfma_f32_16x16x32_bf16 %6, v[64:67], v[120:123], %6\n"
      "ds_read_b64_tr_b16 v[128:129], %8 offset:288\n"
      "v_mfma_f32_16x16x32_bf16 %7, v[64:67], v[124:127], %7\n"
      "ds_read_b64_tr_b16 v[130:131], %8 offset:256\n"
      "v_mfma_f32_16x16x32_bf16 %4, v[64:67], v[112:115], %4\n"
      "ds_read_b64 v[184:185], %9 offset:768\n"
      "v_mfma_f32_16x16x32_bf16 %5, v[64:67], v[116:119], %5\n"
      "ds_read_b64 v[186:187], %9 offset:776\n"
      "v_mfma_f32_16x16x32_bf16 %6, v[68:71], v[116:119], %6\n"
      "ds_read_b64 v[188:189], %9 offset:896\n"
      "v_mfma_f32_16x16x32_bf16 %7, v[68:71], v[120:123], %7\n"
      "ds_read_b64 v[190:191], %9 offset:904\n"
      "v_mfma_f32_16x16x32_bf16 %4, v[68:71], v[108:111], %4\n"
      "ds_read_b64 v[176:177], %9 offset:512\n"
      "v_mfma_f32_16x16x32_bf16 %2, v[64:67], v[104:107], %2\n"
      "ds_read_b64 v[178:179], %9 offset:520\n"
      "v_mfma_f32_16x16x32_bf16 %6, v[72:75], v[112:115], %6\n"
      "ds_read_b64 v[180:181], %9 offset:640\n"
      "v_mfma_f32_16x16x32_bf16 %3, v[64:67], v[108:111], %3\n"
      "ds_read_b64 v[182:183], %9 offset:648\n"
      "v_mfma_f32_16x16x32_bf16 %7, v[72:75], v[116:119], %7\n"
      "ds_read_b64_tr_b16 v[132:133], %8 offset:800\n"
      "v_mfma_f32_16x16x32_bf16 %4, v[72:75], v[104:107], %4\n"
      "ds_read_b64_tr_b16 v[134:135], %8 offset:768\n"
      "v_mfma_f32_16x16x32_bf16 %5, v[68:71], v[112:115], %5\n"
      "ds_read_b64 v[172:173], %9 offset:384\n"
      "v_mfma_f32_16x16x32_bf16 %6, v[76:79], v[108:111], %6\n"
      "ds_read_b64 v[174:175], %9 offset:392\n"
      "v_mfma_f32_16x16x32_bf16 %7, v[76:79], v[112:115], %7\n"
      "ds_read_b64 v[168:169], %9 offset:256\n"
      "v_mfma_f32_16x16x32_bf16 %4, v[76:79], v[100:103], %4\n"
      "ds_read_b64 v[170:171], %9 offset:264\n"
      "v_mfma_f32_16x16x32_bf16 %1, v[64:67], v[100:103], %1\n"
      "ds_read_b64_tr_b16 v[136:137], %8 offset:1312\n"
      "v_mfma_f32_16x16x32_bf16 %5, v[72:75], v[108:111], %5\n"
      "ds_read_b64_tr_b16 v[138:139], %8 offset:1280\n"
      "v_mfma_f32_16x16x32_bf16 %6, v[80:83], v[104:107], %6\n"
      "ds_read_b64_tr_b16 v[140:141], %8 offset:1824\n"
      "v_mfma_f32_16x16x32_bf16 %0, v[64:67], v[96:99], %0\n"
      "ds_read_b64_tr_b16 v[142:143], %8 offset:1792\n"
      "v_mfma_f32_16x16x32_bf16 %7, v[80:83], v[108:111], %7\n"
      "ds_read_b64 v[164:165], %9 offset:128\n"
      "v_mfma_f32_16x16x32_bf16 %5, v[76:79], v[104:107], %5\n"
      "ds_read_b64 v[166:167], %9 offset:136\n"
      "v_mfma_f32_16x16x32_bf16 %2, v[68:71], v[100:103], %2\n"
      "ds_read_b64_tr_b16 v[144:145], %8 offset:2336\n"
      "v_mfma_f32_16x16x32_bf16 %6, v[84:87], v[100:103], %6\n"
      "ds_read_b64_tr_b16 v[146:147], %8 offset:2304\n"
      "v_mfma_f32_16x16x32_bf16 %7, v[84:87], v[104:107], %7\n"
      "ds_read_b64 v[160:161], %9 offset:0\n"
      "v_mfma_f32_16x16x32_bf16 %3, v[68:71], v[104:107], %3\n"
      "ds_read_b64 v[162:163], %9 offset:8\n"
      "v_mfma_f32_16x16x32_bf16 %2, v[72:75], v[96:99], %2\n"
      "ds_read_b64_tr_b16 v[148:149], %8 offset:2848\n"
      "v_mfma_f32_16x16x32_bf16 %5, v[80:83], v[100:103], %5\n"
      "ds_read_b64_tr_b16 v[150:151], %8 offset:2816\n"
      "v_mfma_f32_16x16x32_bf16 %7, v[88:91], v[100:103], %7\n"
      "ds_read_b64_tr_b16 v[152:153], %8 offset:3360\n"
      "v_mfma_f32_16x16x32_bf16 %6, v[88:91], v[96:99], %6\n"
      "ds_read_b64_tr_b16 v[154:155], %8 offset:3328\n"
      "v_mfma_f32_16x16x32_bf16 %3, v[72:75], v[100:103], %3\n"
      "ds_read_b64_tr_b16 v[156:157], %8 offset:3872\n"
      "v_mfma_f32_16x16x32_bf16 %1, v[68:71], v[96:99], %1\n"
      "ds_read_b64_tr_b16 v[158:159], %8 offset:3840\n"
      "v_mfma_f32_16x16x32_bf16 %7, v[92:95], v[96:99], %7\n"
      "v_mfma_f32_16x16x32_bf16 %5, v[84:87], v[96:99], %5\n"
      "v_mfma_f32_16x16x32_bf16 %4, v[80:83], v[96:99], %4\n"
      "v_mfma_f32_16x16x32_bf16 %3, v[76:79], v[96:99], %3\n"
      "s_waitcnt lgkmcnt(0)\n"
      "v_add_u32 %8, 4128, %8\n"
      "v_add_u32 %9, 1152, %9\n"
      "v_mfma_f32_16x16x32_bf16 %6, v[128:131], v[184:187], %6\n"
      "ds_read_b64_tr_b16 v[64:65], %8 offset:32\n"
      "v_mfma_f32_16x16x32_bf16 %7, v[128:131], v[188:191], %7\n"
      "ds_read_b64_tr_b16 v[66:67], %8 offset:0\n"
      "v_mfma_f32_16x16x32_bf16 %4, v[128:131], v[176:179], %4\n"
      "ds_read_b64 v[120:121], %9 offset:832\n"
      "v_mfma_f32_16x16x32_bf16 %5, v[128:131], v[180:183], %5\n"
      "ds_read_b64 v[122:123], %9 offset:840\n"
      "v_mfma_f32_16x16x32_bf16 %6, v[132:135], v[180:183], %6\n"
      "ds_read_b64 v[124:125], %9 offset:960\n"
      "v_mfma_f32_16x16x32_bf16 %7, v[132:135], v[184:187], %7\n"
      "ds_read_b64 v[126:127], %9 offset:968\n"
      "v_mfma_f32_16x16x32_bf16 %4, v[132:135], v[172:175], %4\n"
      "ds_read_b64 v[112:113], %9 offset:576\n"
      "v_mfma_f32_16x16x32_bf16 %2, v[128:131], v[168:171], %2\n"
      "ds_read_b64 v[114:115], %9 offset:584\n"
      "v_mfma_f32_16x16x32_bf16 %6, v[136:139], v[176:179], %6\n"
      "ds_read_b64 v[116:117], %9 offset:704\n"
      "v_mfma_f32_16x16x32_bf16 %3, v[128:131], v[172:175], %3\n"
      "ds_read_b64 v[118:119], %9 offset:712\n"
      "v_mfma_f32_16x16x32_bf16 %7, v[136:139], v[180:183], %7\n"
      "ds_read_b64_tr_b16 v[68:69], %8 offset:544\n"
      "v_mfma_f32_16x16x32_bf16 %4, v[136:139], v[168:171], %4\n"
      "ds_read_b64_tr_b16 v[70:71], %8 offset:512\n"
      "v_mfma_f32_16x16x32_bf16 %5, v[132:135], v[176:179], %5\n"
      "ds_read_b64 v[108:109], %9 offset:448\n"
      "v_mfma_f32_16x16x32_bf16 %6, v[140:143], v[172:175], %6\n"
      "ds_read_b64 v[110:111], %9 offset:456\n"
      "v_mfma_f32_16x16x32_bf16 %7, v[140:143], v[176:179], %7\n"
      "ds_read_b64 v[104:105], %9 offset:320\n"
      "v_mfma_f32_16x16x32_bf16 %4, v[140:143], v[164:167], %4\n"
      "ds_read_b64 v[106:107], %9 offset:328\n"
      "v_mfma_f32_16x16x32_bf16 %1, v[128:131], v[164:167], %1\n"
      "ds_read_b64_tr_b16 v[72:73], %8 offset:1056\n"
      "v_mfma_f32_16x16x32_bf16 %5, v[136:139], v[172:175], %5\n"
      "ds_read_b64_tr_b16 v[74:75], %8 offset:1024\n"
      "v_mfma_f32_16x16x32_bf16 %6, v[144:147], v[168:171], %6\n"
      "ds_read_b64_tr_b16 v[76:77], %8 offset:1568\n"
      "v_mfma_f32_16x16x32_bf16 %0, v[128:131], v[160:163], %0\n"
      "ds_read_b64_tr_b16 v[78:79], %8 offset:1536\n"
      "v_mfma_f32_16x16x32_bf16 %7, v[144:147], v[172:175], %7\n"
      "ds_read_b64 v[100:101], %9 offset:192\n"
      "v_mfma_f32_16x16x32_bf16 %5, v[140:143], v[168:171], %5\n"
      "ds_read_b64 v[102:103], %9 offset:200\n"
      "v_mfma_f32_16x16x32_bf16 %2, v[132:135], v[164:167], %2\n"
      "ds_read_b64_tr_b16 v[80:81], %8 offset:2080\n"
      "v_mfma_f32_16x16x32_bf16 %6, v[148:151], v[164:167], %6\n"
      "ds_read_b64_tr_b16 v[82:83], %8 offset:2048\n"
      "v_mfma_f32_16x16x32_bf16 %7, v[148:151], v[168:171], %7\n"
      "ds_read_b64 v[96:97], %9 offset:64\n"
      "v_mfma_f32_16x16x32_bf16 %3, v[132:135], v[168:171], %3\n"
      "ds_read_b64 v[98:99], %9 offset:72\n"
      "v_mfma_f32_16x16x32_bf16 %2, v[136:139], v[160:163], %2\n"
      "ds_read_b64_tr_b16 v[84:85], %8 offset:2592\n"
      "v_mfma_f32_16x16x32_bf16 %5, v[144:147], v[164:167], %5\n"
      "ds_read_b64_tr_b16 v[86:87], %8 offset:2560\n"
      "v_mfma_f32_16x16x32_bf16 %7, v[152:155], v[164:167], %7\n"
      "ds_read_b64_tr_b16 v[88:89], %8 offset:3104\n"
      "v_mfma_f32_16x16x32_bf16 %6, v[152:155], v[160:163], %6\n"
      "ds_read_b64_tr_b16 v[90:91], %8 offset:3072\n"
      "v_mfma_f32_16x16x32_bf16 %3, v[136:139], v[164:167], %3\n"
      "ds_read_b64_tr_b16 v[92:93], %8 offset:3616\n"
      "v_mfma_f32_16x16x32_bf16 %1, v[132:135], v[160:163], %1\n"
      "ds_read_b64_tr_b16 v[94:95], %8 offset:3584\n"
      "v_mfma_f32_16x16x32_bf16 %7, v[156:159], v[160:163], %7\n"
      "v_mfma_f32_16x16x32_bf16 %5, v[148:151], v[160:163], %5\n"
      "v_mfma_f32_16x16x32_bf16 %4, v[144:147], v[160:163], %4\n"
      "v_mfma_f32_16x16x32_bf16 %3, v[140:143], v[160:163], %3\n"
      "s_waitcnt lgkmcnt(0)\n"
      "s_sub_u32 %10, %10, 1\n"
      "s_cmp_lg_u32 %10, 0\n"
      "s_cbranch_scc1 Lconv%=\n"
      "s_nop 7\n"
      : "+v"(acc[0]), "+v"(acc[1]), "+v"(acc[2]), "+v"(acc[3]), "+v"(acc[4]), "+v"(acc[5]), "+v"(acc[6]), "+v"(acc[7]), "+v"(ka), "+v"(hb), "=&s"(cnt)
      :: "memory", "scc", "v64", "v65", "v66", "v67", "v68", "v69", "v70", "v71", "v72", "v73", "v74", "v75", "v76", "v77", "v78", "v79", "v80", "v81", "v82", "v83", "v84", "v85", "v86", "v87", "v88", "v89", "v90", "v91", "v92", "v93", "v94", "v95", "v96", "v97", "v98", "v99", "v100", "v101", "v102", "v103", "v104", "v105", "v106", "v107", "v108", "v109", "v110", "v111", "v112", "v113", "v114", "v115", "v116", "v117", "v118", "v119", "v120", "v121", "v122", "v123", "v124", "v125", "v126", "v127", "v128", "v129", "v130", "v131", "v132", "v133", "v134", "v135", "v136", "v137", "v138", "v139", "v140", "v141", "v142", "v143", "v144", "v145", "v146", "v147", "v148", "v149", "v150", "v151", "v152", "v153", "v154", "v155", "v156", "v157", "v158", "v159", "v160", "v161", "v162", "v163", "v164", "v165", "v166", "v167", "v168", "v169", "v170", "v171", "v172", "v173", "v174", "v175", "v176", "v177", "v178", "v179", "v180", "v181", "v182", "v183", "v184", "v185", "v186", "v187", "v188", "v189", "v190", "v191");
  }
  f4* ex = (f4*)(sm + CV_EX);
  if (half) {
#pragma unroll
    for (int i = 0; i < 4; ++i) { ex[(w * 4 + i) * 64 + lane] = acc[i]; acc[i] = acc[4 + i]; }
  } else {
#pragma unroll
    for (int i = 0; i < 4; ++i) ex[(w * 4 + i) * 64 + lane] = acc[4 + i];
  }
  volatile LAS u32* fl = (volatile LAS u32*)(size_t)(ldsaddr(sm) + CV_FL);
  asm volatile("s_waitcnt lgkmcnt(0)" ::: "memory");
  fl[w] = 1u;
  u2 xr[4];
#pragma unroll
  for (int i = 0; i < 4; ++i) xr[i] = *(const u2*)(sm + CV_X + 4096 * ch + 2 * (256 * (4 * half + i) + 16 * c + 4 * g));
  while (fl[w ^ 1] == 0u) {}
  asm volatile("" ::: "memory");
  u16* x1t = (u16*)(ws + WS_X1T);
  const float gc = ga * c2;
#pragma unroll
  for (int i = 0; i < 4; ++i) {
    const f4 y = (acc[i] + ex[((w ^ 1) * 4 + i) * 64 + lane]) * gc + (f4){blo(xr[i].x), bhi(xr[i].x), blo(xr[i].y), bhi(xr[i].y)} * c2;
    *(u2*)(x1t + d * L + 256 * (4 * half + i) + 16 * c + 4 * g) = (u2){pk(y[0], y[1]), pk(y[2], y[3])};
  }
}

DI void k2_help(char* sm, int b, const u16* wg, const u16* wu, const u16* wd, char* ws) {
  const int t = threadIdx.x - 256, w = __builtin_amdgcn_readfirstlane(t >> 6), lane = t & 63;
  const int d0 = 64 * (b & 7) + 2 * (b >> 3);
  const u16* kx = (const u16*)(ws + WS_KX);
  u4 kv[2];
  u2 kp[2];
#pragma unroll
  for (int i = 0; i < 2; ++i) { kv[i] = *(const u4*)(kx + (d0 + i) * KXS + 8 + 8 * t); kp[i] = *(const u2*)(kx + (d0 + i) * KXS + 4 + 8 * t); }
  SB();
  if (t < 16) *(u4*)(sm + (t >> 1) * CV_KC + 16 * (t & 1)) = (u4){0u, 0u, 0u, 0u};
  if (w == 1) *(u4*)(sm + CV_H + (lane >> 3) * CV_HP + 16 * (t & 7)) = (u4){0u, 0u, 0u, 0u};
  if (t == 128) *(u4*)(sm + CV_FL) = (u4){0u, 0u, 0u, 0u};
#pragma unroll
  for (int i = 0; i < 2; ++i) {
    const u4 v = kv[i];
    const u32 a0 = __builtin_amdgcn_alignbit(kp[i].y, kp[i].x, 16), a1 = __builtin_amdgcn_alignbit(v.x, kp[i].y, 16), a2 = __builtin_amdgcn_alignbit(v.y, v.x, 16),
              a3 = __builtin_amdgcn_alignbit(v.z, v.y, 16), a4 = __builtin_amdgcn_alignbit(v.w, v.z, 16);
    char* kd = sm + 4 * i * CV_KC + 32 + 16 * t;
    *(u4*)kd = v;
    *(u4*)(kd + CV_KC) = (u4){a1, a2, a3, a4};
    *(u4*)(kd + 2 * CV_KC) = (u4){kp[i].y, v.x, v.y, v.z};
    *(u4*)(kd + 3 * CV_KC) = (u4){a0, a1, a2, a3};
  }
  bar();
  const int task = b + 256 * w, kq = task & 3, blk = task >> 2, mat = blk >= 43, jb = blk - 43 * mat;
  const bool work = task < 344;
  const int hh = lane >> 5;
  u4 vec[8];
  if (work) {
    const rsrc_t rw = mkrsrc(mat ? wu : wg, D * DF * 2);
    const u32 vo = (128 * kq + (lane >> 2)) * (DF * 2) + 64 * jb + 16 * (lane & 3);
    asm volatile("s_nop 4");
#pragma unroll
    for (int i = 0; i < 8; ++i) glds16<0>(ldsaddr(sm) + CV_W + 8192 * w + 1024 * i, vo, rw, i * 16 * DF * 2);
    const char* cv = ws + WS_CV + ((lane & 1) ? 0 : 1024) + 256 * kq + 16 * hh;
    sfor<0, 8>([&](auto i) { gld16<32 * i>(vec[i], cv); });
  }
  u32 dummy[4];
  {
    const int row = 32 * (b >> 3) + 8 * w + (lane >> 3);
    const char* base = (const char*)(row >= D ? wu : wg) + (row & (D - 1)) * (DF * 2);
#pragma unroll
    for (int i = 0; i < 2; ++i) {
      const int off = min((b & 1) * 1344 + 128 * ((lane & 7) + 8 * i), (b & 1) ? DF * 2 - 4 : 1340);
      asm volatile("global_load_dword %0, %1, off" : "=v"(dummy[i]) : "v"(base + off));
    }
  }
#pragma unroll
  for (int i = 0; i < 2; ++i)
    asm volatile("global_load_dword %0, %1, off" : "=v"(dummy[2 + i]) : "v"((const char*)wd + (b >> 3) * 44032 + w * 11008 + min(lane + 64 * i, 85) * 128));
  if (work) {
    asm volatile("s_waitcnt vmcnt(4)" : "+v"(vec[0]), "+v"(vec[1]), "+v"(vec[2]), "+v"(vec[3]), "+v"(vec[4]), "+v"(vec[5]), "+v"(vec[6]), "+v"(vec[7]) :: "memory");
    const u32 ra = ldsaddr(sm) + CV_W + 8192 * w + 64 * (8 * hh + ((lane & 15) >> 2)) + 32 * ((lane >> 4) & 1) + 8 * (lane & 3);
    f16v sums;
#pragma unroll
    for (int e = 0; e < 16; ++e) sums[e] = 0.f;
#pragma unroll
    for (int i = 0; i < 8; ++i) sums = mfma32(vec[i], cat(trd(ra + 1024 * i), trd(ra + 1024 * i + 256)), sums);
    if (lane < 32) {
      u32* exf = (u32*)(ws + WS_EX) + ((2 * jb + (lane >> 4)) * 64 + 16 * mat + (lane & 15)) * 4 + kq;
      const float h0 = rb(sums[0]), h1 = rb(sums[1]);
      exf[0] = pk(h0, sums[0] - h0);
      exf[128] = pk(h1, sums[1] - h1);
    }
  }
  asm volatile("s_waitcnt vmcnt(0)" : "+v"(dummy[0]), "+v"(dummy[1]), "+v"(dummy[2]), "+v"(dummy[3]) :: "memory");
}

__global__ __launch_bounds__(512, 2) void k2_conv_prep(const u16* wg, const u16* wu, const u16* wd, char* ws) {
  extern __shared__ __attribute__((aligned(16))) char sm[];
  const int b = blockIdx.x;
  if (__builtin_amdgcn_readfirstlane(threadIdx.x) < 256) k2_conv(sm, b, ws);
  else k2_help(sm, b, wg, wu, wd, ws);
}

constexpr int G1_LEAD = 3, G1_NS = 5, G1_SLOT = 20480;
constexpr int G2_RES = 19, G2_FL = 4 * G2_RES * 2048, G1_SV = G2_FL + 64, K3_LDS = G1_SV + 512;
constexpr int G2_LEAD = 6;
constexpr int HH_T = NU * 1024;

template <int K> DI u32 g1_pair(const f16v& acc, float rs, float sd) {
  const f2 g = (f2){acc[2 * K], acc[2 * K + 1]} * rs;
  const f2 z = g * (g * g * -0.10294324f + -2.30220819f);
  const f2 dn = (f2){__builtin_amdgcn_exp2f(z.x), __builtin_amdgcn_exp2f(z.y)} * sd + sd;
  const f2 r = g * (f2){acc[8 + 2 * K], acc[9 + 2 * K]} * (f2){__builtin_amdgcn_rcpf(dn.x), __builtin_amdgcn_rcpf(dn.y)};
  return pk(r.x, r.y);
}
DI u4 g1_tile(const f16v& acc, float rs, float sd) { return (u4){g1_pair<0>(acc, rs, sd), g1_pair<1>(acc, rs, sd), g1_pair<2>(acc, rs, sd), g1_pair<3>(acc, rs, sd)}; }

DI void g1_stats(char* sm, int rt, char* ws) {
  const int t = threadIdx.x;
  float* red = (float*)sm;
  if (t < 512) {
    const u16* xp = (const u16*)(ws + WS_X1T) + (t >> 4) * L + 128 * rt + 8 * (t & 15);
    u4 v[16];
    float iv[16];
#pragma unroll
    for (int i = 0; i < 16; ++i) { v[i] = *(const u4*)(xp + 32 * i * L); iv[i] = ((const float*)(ws + WS_INV))[(t >> 4) + 32 * i]; }
    f2 s1[4], s2[4];
#pragma unroll
    for (int j = 0; j < 4; ++j) s1[j] = s2[j] = (f2){0.f, 0.f};
#pragma unroll
    for (int i = 0; i < 16; ++i)
#pragma unroll
      for (int j = 0; j < 4; ++j) { const f2 x = (f2){blo(v[i][j]), bhi(v[i][j])} * iv[i]; s1[j] += x; s2[j] += x * x; }
#pragma unroll
    for (int j = 0; j < 4; ++j) *(f4*)(red + (t >> 4) * 260 + 16 * (t & 15) + 4 * j) = (f4){s1[j].x, s2[j].x, s1[j].y, s2[j].y};
  }
  __syncthreads();
  if (t < 512) {
    f2 p = {0.f, 0.f};
#pragma unroll
    for (int i = 0; i < 8; ++i) p += *(const f2*)(red + (8 * (t & 3) + i) * 260 + 2 * (t >> 2));
    p.x += dppf<0xb1>(p.x); p.y += dppf<0xb1>(p.y); p.x += dppf<0x4e>(p.x); p.y += dppf<0x4e>(p.y);
    const f2 ms = ln_stats(p.x, p.y);
    if ((t & 3) == 0)
      __hip_atomic_store((u32*)(ws + WS_ST2) + 128 * rt + (t >> 2), (__float_as_uint(ms.x) >> 16) | __float_as_uint(ms.y), __ATOMIC_RELAXED, __HIP_MEMORY_SCOPE_AGENT);
  }
  __syncthreads();
}

__global__ __launch_bounds__(768, 3) void k3_mlp(const u16* wg, const u16* wu, const u16* wd, char* ws, u16* out) {
  extern __shared__ __attribute__((aligned(16))) char sm[];
  const int b = blockIdx.x, j = b >> 3;
  const int t = threadIdx.x, w12 = __builtin_amdgcn_readfirstlane(t >> 6), w = w12 & 3, lane = t & 63, hh = lane >> 5;
  const u32 lb0 = ldsaddr(sm);
  const int q = (lane & 15) >> 2, p = lane & 3, g = lane >> 4;
  const int lt = 4 * (b & 7) + (j & 3), dt = j >> 2;
  auto kfirst = [&](int kq) { const u32 k = 22 * kq - (kq == 3) + 3 * dt; return min(k, k - NU); };
  auto knext = [](u32 k) { return min(k + 1, k + 1 - NU); };
  const rsrc_t rd = mkrsrc(wd, DF * D * 2);
  const u32 vd = (lane >> 3) * 1024 + 128 * dt + 16 * ((lane & 7) ^ (4 * ((lane >> 4) & 1)));
  volatile LAS u32* fl = (volatile LAS u32*)(size_t)(lb0 + G2_FL);
  auto fill2 = [&](int i0, int n, int slot0) {
    u32 kd = kfirst(w) + i0;
    kd = min(kd, kd - NU);
#pragma unroll 1
    for (int i = 0; i < n; ++i) {
      glds16<0>(lb0 + (G2_RES * w + slot0 + i) * 2048, vd, rd, kd * 16384);
      glds16<0>(lb0 + (G2_RES * w + slot0 + i) * 2048 + 1024, vd, rd, kd * 16384 + 8192);
      kd = knext(kd);
    }
  };
  {
    const int rt = 4 * ((b & 7) >> 1) + (j & 3), cg = (j >> 2) + 8 * (b & 1) - 1;
    if (t == 0) fl[0] = 0u;
    if (cg < 0) {
      g1_stats(sm, rt, ws);
    } else if (w12 >= 8) {
      const rsrc_t rx = mkrsrc(ws + WS_X1T, D * L * 2), rg = mkrsrc(wg, D * DF * 2), ru = mkrsrc(wu, D * DF * 2);
      const u32 vx = (4 * w + g) * 4096 + 256 * rt + 16 * ((lane & 15) ^ ((g << 2) | w));
      u32 vw[3];
#pragma unroll
      for (int i = 0; i < 3; ++i) {
        const int pc = (3 * (w & 1) + i) * 64 + lane, row = pc / 12, chk = pc - 12 * row;
        vw[i] = row * (DF * 2) + min(192 * cg + 16 * (chk ^ (w & 2)), DF * 2 - 16);
      }
      const rsrc_t rwm = w < 2 ? rg : ru;
      auto stage = [&](int i, int slot) {
        const u32 m = lb0 + slot * G1_SLOT;
        glds16<0>(m + 1024 * w, vx, rx, i * 131072);
        glds16<0>(m + 4096 + 1024 * w, vx, rx, i * 131072 + 65536);
#pragma unroll
        for (int k = 0; k < 3; ++k) glds16<0>(m + 8192 + 3072 * w + 1024 * k, vw[k], rwm, i * (32 * DF * 2));
      };
      asm volatile("s_nop 4");
      static_assert(G1_LEAD + 2 <= G1_NS, "ring too short");
#pragma unroll 1
      for (int i = 0; i < G1_LEAD - 1; ++i) stage(i, i);
      int slot = G1_LEAD - 1;
#pragma unroll 1
      for (int i = 0; i < 17 - G1_LEAD; ++i) {
        stage(i + G1_LEAD - 1, slot);
        slot = slot == G1_NS - 1 ? 0 : slot + 1;
        vmwait<5 * (G1_LEAD - 1)>();
        rawbar();
      }
      const u32* st2 = (const u32*)(ws + WS_ST2) + 128 * rt + 32 * w + (lane & 31);
      u32 v;
      asm volatile("global_load_dword %0, %1, off sc1" : "=v"(v) : "v"(st2));
      sfor<17 - G1_LEAD, 16>([&](auto i) { vmwait<5 * (15 - i) + 1>(); rawbar(); });
      asm volatile("s_waitcnt vmcnt(0)" : "+v"(v));
#pragma unroll 1
      for (int spin = 0; spin < (1 << 16) && !__all(v != 0u); ++spin) {
        __builtin_amdgcn_s_sleep(2);
        v = __hip_atomic_load(st2, __ATOMIC_RELAXED, __HIP_MEMORY_SCOPE_AGENT);
      }
      ((u32*)(sm + G1_SV))[32 * w + (lane & 31)] = v;
      bar();
      bar();
    } else if (w12 >= 4) {
      const char* pf = (const char*)wd + j * (43 * 1024) + w * (43 * 256);
#pragma unroll 1
      for (int i = 0; i < 18; ++i) {
        rawbar();
      }
    } else {
      const int wl = w & 1, wu3 = 3 * (w >> 1), u0 = 6 * cg + wu3;
      u32 xa[2][2], wa[3];
#pragma unroll
      for (int c = 0; c < 2; ++c)
#pragma unroll
        for (int tt = 0; tt < 2; ++tt)
          xa[c][tt] = lb0 + 256 * (8 * hh + 4 * tt + q) + 8 * (p & 1) + 16 * ((4 * (2 * wl + c) + 2 * (g & 1) + (p >> 1)) ^ ((q << 2) | (2 * hh + tt)));
#pragma unroll
      for (int v = 0; v < 3; ++v) wa[v] = lb0 + 8192 + 6144 * (g & 1) + 192 * (8 * hh + q) + 16 * ((2 * (wu3 + v) + (p >> 1)) ^ (2 * (g & 1))) + 8 * (p & 1);
      u4 ex[3];
#pragma unroll
      for (int v = 0; v < 3; ++v) ex[v] = *(const u4*)(ws + WS_EX + min(u0 + v, NU - 1) * 1024 + lane * 16);
      f16v acc[6];
#pragma unroll
      for (int i = 0; i < 6; ++i)
#pragma unroll
        for (int e = 0; e < 16; ++e) acc[i][e] = 0.f;
#pragma unroll
      for (int i = 0; i < 6; ++i) asm volatile("" : "+v"(acc[i]));
      rawbar();
      {
        u32 c0, c1, c2;
        asm volatile(
          "ds_read_b64_tr_b16 v[112:113], %6 offset:0\n"
          "ds_read_b64_tr_b16 v[114:115], %7 offset:0\n"
          "ds_read_b64_tr_b16 v[120:121], %10 offset:0\n"
          "ds_read_b64_tr_b16 v[122:123], %10 offset:768\n"
          "ds_read_b64_tr_b16 v[116:117], %8 offset:0\n"
          "ds_read_b64_tr_b16 v[118:119], %9 offset:0\n"
          "ds_read_b64_tr_b16 v[124:125], %11 offset:0\n"
          "ds_read_b64_tr_b16 v[126:127], %11 offset:768\n"
          "ds_read_b64_tr_b16 v[128:129], %12 offset:0\n"
          "ds_read_b64_tr_b16 v[130:131], %12 offset:768\n"
          "s_mov_b32 %13, 16\n"
          "s_mov_b32 %14, 0\n"
          "Lg1%=:\n"
          "s_add_u32 %14, %14, 1\n"
          "s_cmp_eq_u32 %14, 5\n"
          "s_cselect_b32 %15, -81920, %16\n"
          "s_cselect_b32 %14, 0, %14\n"
          "s_waitcnt lgkmcnt(6)\n"
          "v_mfma_f32_32x32x16_bf16 %0, v[120:123], v[112:115], %0\n"
          "ds_read_b64_tr_b16 v[132:133], %6 offset:4096\n"
          "ds_read_b64_tr_b16 v[134:135], %7 offset:4096\n"
          "s_waitcnt lgkmcnt(6)\n"
          "v_mfma_f32_32x32x16_bf16 %1, v[120:123], v[116:119], %1\n"
          "ds_read_b64_tr_b16 v[140:141], %10 offset:3072\n"
          "ds_read_b64_tr_b16 v[142:143], %10 offset:3840\n"
          "s_waitcnt lgkmcnt(6)\n"
          "v_mfma_f32_32x32x16_bf16 %2, v[124:127], v[112:115], %2\n"
          "ds_read_b64_tr_b16 v[136:137], %8 offset:4096\n"
          "ds_read_b64_tr_b16 v[138:139], %9 offset:4096\n"
          "v_mfma_f32_32x32x16_bf16 %3, v[124:127], v[116:119], %3\n"
          "ds_read_b64_tr_b16 v[144:145], %11 offset:3072\n"
          "ds_read_b64_tr_b16 v[146:147], %11 offset:3840\n"
          "s_waitcnt lgkmcnt(8)\n"
          "v_mfma_f32_32x32x16_bf16 %4, v[128:131], v[112:115], %4\n"
          "ds_read_b64_tr_b16 v[148:149], %12 offset:3072\n"
          "ds_read_b64_tr_b16 v[150:151], %12 offset:3840\n"
          "v_add_u32 %6, %15, %6\n"
          "v_add_u32 %7, %15, %7\n"
          "v_add_u32 %8, %15, %8\n"
          "v_add_u32 %9, %15, %9\n"
          "v_mfma_f32_32x32x16_bf16 %5, v[128:131], v[116:119], %5\n"
          "v_add_u32 %10, %15, %10\n"
          "v_add_u32 %11, %15, %11\n"
          "v_add_u32 %12, %15, %12\n"
          "s_barrier\n"
          "s_waitcnt lgkmcnt(6)\n"
          "v_mfma_f32_32x32x16_bf16 %0, v[140:143], v[132:135], %0\n"
          "ds_read_b64_tr_b16 v[112:113], %6 offset:0\n"
          "ds_read_b64_tr_b16 v[114:115], %7 offset:0\n"
          "s_waitcnt lgkmcnt(6)\n"
          "v_mfma_f32_32x32x16_bf16 %1, v[140:143], v[136:139], %1\n"
          "ds_read_b64_tr_b16 v[120:121], %10 offset:0\n"
          "ds_read_b64_tr_b16 v[122:123], %10 offset:768\n"
          "s_waitcnt lgkmcnt(6)\n"
          "v_mfma_f32_32x32x16_bf16 %2, v[144:147], v[132:135], %2\n"
          "ds_read_b64_tr_b16 v[116:117], %8 offset:0\n"
          "ds_read_b64_tr_b16 v[118:119], %9 offset:0\n"
          "v_mfma_f32_32x32x16_bf16 %3, v[144:147], v[136:139], %3\n"
          "ds_read_b64_tr_b16 v[124:125], %11 offset:0\n"
          "ds_read_b64_tr_b16 v[126:127], %11 offset:768\n"
          "s_waitcnt lgkmcnt(8)\n"
          "v_mfma_f32_32x32x16_bf16 %4, v[148:151], v[132:135], %4\n"
          "ds_read_b64_tr_b16 v[128:129], %12 offset:0\n"
          "ds_read_b64_tr_b16 v[130:131], %12 offset:768\n"
          "s_sub_u32 %13, %13, 1\n"
          "s_cmp_lg_u32 %13, 0\n"
          "v_mfma_f32_32x32x16_bf16 %5, v[148:151], v[136:139], %5\n"
          "s_cbranch_scc1 Lg1%=\n"
          "s_waitcnt lgkmcnt(0)\n"
          : "+v"(acc[0]), "+v"(acc[1]), "+v"(acc[2]), "+v"(acc[3]), "+v"(acc[4]), "+v"(acc[5]), "+v"(xa[0][0]), "+v"(xa[0][1]), "+v"(xa[1][0]), "+v"(xa[1][1]),
            "+v"(wa[0]), "+v"(wa[1]), "+v"(wa[2]), "=&s"(c0), "=&s"(c1), "=&s"(c2)
          : "s"(G1_SLOT) : "memory", "scc", "v112", "v113", "v114", "v115", "v116", "v117", "v118", "v119", "v120", "v121", "v122", "v123", "v124", "v125", "v126", "v127", "v128", "v129", "v130", "v131", "v132", "v133", "v134", "v135", "v136", "v137", "v138", "v139", "v140", "v141", "v142", "v143", "v144", "v145", "v146", "v147", "v148", "v149", "v150", "v151");
      }
      u32 sv[2];
#pragma unroll
      for (int c = 0; c < 2; ++c) sv[c] = ((const u32*)(sm + G1_SV))[64 * wl + 32 * c + (lane & 31)];
      bar();
#pragma unroll
      for (int c = 0; c < 2; ++c) {
        const u32 bv = hh ? __builtin_amdgcn_perm(sv[c], sv[c], 0x03020302u) : (__builtin_amdgcn_perm(sv[c], sv[c], 0x01000100u) ^ 0x80008000u);
#pragma unroll
        for (int v = 0; v < 3; ++v) acc[2 * v + c] = mfma32(ex[v], (u4){bv, bv, bv, bv}, acc[2 * v + c]);
      }
      char* hp = ws + WS_HH + (4 * rt + 2 * wl) * HH_T + u0 * 1024 + 16 * lane;
#pragma unroll
      for (int v = 0; v < 3; ++v) {
        if (u0 + v < NU) {
#pragma unroll
          for (int c = 0; c < 2; ++c) {
            const float sd = bhi(sv[c]);
            const u4 o = g1_tile(acc[2 * v + c], __builtin_amdgcn_rcpf(sd), sd);
            char* dst = hp + c * HH_T + v * 1024;
            asm volatile("global_store_dwordx4 %0, %1, off sc1\n\ts_nop 1" :: "v"(dst), "v"(o) : "memory");
          }
        }
      }
      {
        asm volatile("s_waitcnt vmcnt(0)" ::: "memory");
        if (lane == 0 && __hip_atomic_fetch_add((LAS u32*)fl, 1u, __ATOMIC_RELAXED, __HIP_MEMORY_SCOPE_WORKGROUP) == 3u)
          __hip_atomic_fetch_add((u32*)(ws + WS_CNT) + 64 * rt, 1u, __ATOMIC_RELAXED, __HIP_MEMORY_SCOPE_AGENT);
      }
    }
  }
  if (w12 >= 8) {
    asm volatile("s_nop 4");
    fill2(G2_RES, 3, 0);
    fill2(3, G2_RES - 3, 3);
    vmwait<2 * (G2_RES - 3)>();
    rawbar();
    rawbar();
    fill2(0, 3, 0);
    vmwait<0>();
    rawbar();
    rawbar();
    rawbar();
  } else {
    const int wl = w12 & 1, kq = w12 >> 1, dc = 64 * dt + 16 * kq + 4 * hh;
    f4 gb[2], xr[2];
#pragma unroll
    for (int e = 0; e < 2; ++e) {
      gb[e] = *(const f4*)((const float*)(ws + WS_MOD) + 5 * D + dc + 8 * e);
      xr[e] = *(const f4*)((const float*)(ws + WS_INV) + dc + 8 * e);
#pragma unroll
      for (int i = 0; i < 4; ++i) xr[e][i] *= b2f(((const u16*)(ws + WS_X1T))[(dc + 8 * e + i) * L + 64 * lt + 32 * wl + (lane & 31)]);
    }
    u32 pa[2];
#pragma unroll
    for (int e = 0; e < 2; ++e)
      pa[e] = lb0 + G2_RES * 2048 * kq + 128 * (4 * hh + q) + 16 * ((((4 * e + 2 * (g & 1)) ^ (2 * kq)) + (p >> 1)) ^ (4 * (q >> 1))) + 8 * (p & 1);
    f16v acc[2];
#pragma unroll
    for (int e = 0; e < 16; ++e) acc[0][e] = acc[1][e] = 0.f;
    asm volatile("" : "+v"(xr[0]), "+v"(xr[1]), "+v"(gb[0]), "+v"(gb[1]), "+v"(acc[0]), "+v"(acc[1]));
    rawbar();
    u4 A[2][2], AR[3][2];
#pragma unroll
    for (int i = 0; i < 6; ++i) AR[i >> 1][i & 1] = cat(trd(pa[i & 1] + (i >> 1) * 2048), trd(pa[i & 1] + (i >> 1) * 2048 + 1024));
    asm volatile("s_waitcnt lgkmcnt(0)\n\ts_barrier" : "+v"(AR[0][0]), "+v"(AR[0][1]), "+v"(AR[1][0]), "+v"(AR[1][1]), "+v"(AR[2][0]), "+v"(AR[2][1]) :: "memory");
    if (w12 == 0) {
      const u32* cnt = (const u32*)(ws + WS_CNT) + 64 * (lt >> 1);
#pragma unroll 1
      for (int spin = 0; spin < (1 << 18); ++spin)
        if (__hip_atomic_load(cnt, __ATOMIC_RELAXED, __HIP_MEMORY_SCOPE_AGENT) >= 15u) break;
    }
    rawbar();
    const rsrc_t rh = mkrsrc(ws + WS_HH + (2 * lt + wl) * HH_T, HH_T);
    constexpr int LEAD = G2_LEAD, NH = LEAD + 2;
    u4 H[NH];
    u32 kh = kfirst(kq);
    auto ldh = [&](auto i) { if constexpr (i < 22) { bld16_sc1<0>(H[i % NH], lane * 16, rh, kh * 1024); kh = knext(kh); } };
    sfor<0, LEAD>(ldh);
    auto rda = [&](auto i) {
#pragma unroll
      for (int e = 0; e < 2; ++e) A[i & 1][e] = cat(trd(pa[e] + i * 2048), trd(pa[e] + i * 2048 + 1024));
    };
    rda(std::integral_constant<int, 0>{});
    sfor<0, 22>([&](auto i) {
      vmwait<(i < 22 - LEAD ? LEAD - 1 : 21 - i)>(H[i % NH]);
      SB();
      if constexpr (i + 1 < G2_RES) rda(std::integral_constant<int, i + 1>{});
      if (i < 21 || kq < 2) {
#pragma unroll
        for (int e = 0; e < 2; ++e) acc[e] = mfma32(i < G2_RES ? A[i & 1][e] : AR[i < G2_RES ? 0 : i - G2_RES][e], H[i % NH], acc[e]);
      }
      if constexpr (i + 1 < G2_RES) { SGB(0x8, 1); SGB(0x100, 2); SGB(0x8, 1); SGB(0x100, 2); }
      SB();
      ldh(std::integral_constant<int, i + LEAD>{});
      if constexpr (i == G2_RES - 1) rawbar();
    });
    f4* red = (f4*)sm + wl * 1536 + lane;
#pragma unroll
    for (int k = 1; k < 4; ++k)
#pragma unroll
      for (int e = 0; e < 2; ++e) {
        const f16v& a = acc[k >> 1];
        const int r = 8 * (k & 1) + 4 * e;
        red[(kq ^ k) * 384 + (k - 1) * 128 + 64 * e] = (f4){a[r], a[r + 1], a[r + 2], a[r + 3]};
      }
    bar();
    u2 o[2];
#pragma unroll
    for (int e = 0; e < 2; ++e) {
      const f4* rp = red + kq * 384 + 64 * e;
      const f4 y = ((f4){acc[0][4 * e], acc[0][4 * e + 1], acc[0][4 * e + 2], acc[0][4 * e + 3]} + rp[0] + rp[128] + rp[256]) * gb[e] + xr[e];
      o[e] = (u2){pk(y[0], y[1]), pk(y[2], y[3])};
    }
    const auto r0 = __builtin_amdgcn_permlane32_swap(o[0].x, o[1].x, false, false);
    const auto r1 = __builtin_amdgcn_permlane32_swap(o[0].y, o[1].y, false, false);
    *(u4*)(out + (64 * lt + 32 * wl + (lane & 31)) * D + 64 * dt + 16 * kq + 8 * hh) = (u4){r0[0], r1[0], r0[1], r1[1]};
  }
}

extern "C" void kernel_launch(void* const* d_in, const int* in_sizes, int n_in, void* d_out, int out_size, void* d_ws, size_t ws_size,
                              hipStream_t stream) {
  const u16* x = (const u16*)d_in[0];
  const u16 *wg = (const u16*)d_in[8], *wu = (const u16*)d_in[9], *wd = (const u16*)d_in[10];
  char* ws = (char*)d_ws;
  (void)hipFuncSetAttribute((const void*)k2_conv_prep, hipFuncAttributeMaxDynamicSharedMemorySize, K2_LDS);
  (void)hipFuncSetAttribute((const void*)k3_mlp, hipFuncAttributeMaxDynamicSharedMemorySize, K3_LDS);
  k1_prep<<<496, 256, 0, stream>>>(x, (const u16*)d_in[1], (const u16*)d_in[2], (const u16*)d_in[3], (const u16*)d_in[4], (const u16*)d_in[5],
                                   (const u16*)d_in[6], (const u16*)d_in[11], (const u16*)d_in[12], ws, (const char*)wg, (const char*)wu, (const char*)wd, (const char*)d_out);
  k2_conv_prep<<<256, 512, K2_LDS, stream>>>(wg, wu, wd, ws);
  k3_mlp<<<256, 768, K3_LDS, stream>>>(wg, wu, wd, ws, (u16*)d_out);
}
```

```cpp
#include <hip/hip_runtime.h>
#include <hip/hip_bf16.h>
#include <stdint.h>
#include <type_traits>

typedef unsigned short u16;
typedef unsigned u32;
typedef float f2 __attribute__((ext_vector_type(2)));
typedef float f4 __attribute__((ext_vector_type(4)));
typedef float f16v __attribute__((ext_vector_type(16)));
typedef u32 u2 __attribute__((ext_vector_type(2)));
typedef u32 u4 __attribute__((ext_vector_type(4)));
typedef u4 u4a8 __attribute__((aligned(8)));
typedef short s4 __attribute__((ext_vector_type(4)));
typedef __bf16 bf2 __attribute__((ext_vector_type(2)));
typedef __bf16 bf8 __attribute__((ext_vector_type(8)));
typedef _Float16 h2 __attribute__((ext_vector_type(2)));
typedef _Float16 h8 __attribute__((ext_vector_type(8)));
#define LAS __attribute__((address_space(3)))
#define DI __device__ __forceinline__

constexpr int L = 2048, D = 512, NM = 128, DF = 1376, NU = 86;
constexpr int KXS = 2056;

constexpr size_t WS_MOD = 0, WS_MEAN = 16 << 10, WS_SD = 20 << 10, WS_CV = 24 << 10, WS_INV = 26 << 10, WS_ST2 = 32 << 10, WS_CNT = 40 << 10,
                 WS_EX = 64 << 10, WS_XT = 256 << 10, WS_KX = WS_XT + (2 << 20), WS_X1T = WS_KX + (2112 << 10), WS_HH = WS_X1T + (2 << 20), WS_END = WS_HH + (5504 << 10);

template <class T> DI T ldnt(const T* p) { return __builtin_nontemporal_load(p); }
DI float blo(u32 v) { return __uint_as_float(v << 16); }
DI float bhi(u32 v) { return __uint_as_float(v & 0xffff0000u); }
DI float b2f(u16 v) { return __uint_as_float(((u32)v) << 16); }
DI u32 pk(float a, float b) { return __builtin_bit_cast(u32, __builtin_convertvector((f2){a, b}, bf2)); }
DI u32 pkh(f2 v) { return __builtin_bit_cast(u32, __builtin_convertvector(v, h2)); }
DI float rb(float a) { return blo(pk(a, a)); }
DI u2 trd(const char* p) { return __builtin_bit_cast(u2, __builtin_amdgcn_ds_read_tr16_b64_v4i16((LAS s4*)p)); }
DI u2 trd(u32 a) { return __builtin_bit_cast(u2, __builtin_amdgcn_ds_read_tr16_b64_v4i16((LAS s4*)(size_t)a)); }
DI u4 cat(u2 a, u2 b) { return (u4){a.x, a.y, b.x, b.y}; }
DI float dot2(u32 a, u32 b, float c) { return __builtin_amdgcn_fdot2_f32_bf16(__builtin_bit_cast(bf2, a), __builtin_bit_cast(bf2, b), c, false); }
DI f4 mfma16(u4 a, u4 b, f4 c) { return __builtin_amdgcn_mfma_f32_16x16x32_bf16(__builtin_bit_cast(bf8, a), __builtin_bit_cast(bf8, b), c, 0, 0, 0); }
DI f4 mfma16h(u4 a, u4 b, f4 c) { return __builtin_amdgcn_mfma_f32_16x16x32_f16(__builtin_bit_cast(h8, a), __builtin_bit_cast(h8, b), c, 0, 0, 0); }
DI f16v mfma32(u4 a, u4 b, f16v c) { return __builtin_amdgcn_mfma_f32_32x32x16_bf16(__builtin_bit_cast(bf8, a), __builtin_bit_cast(bf8, b), c, 0, 0, 0); }
#define CM_MUL(d, a, b) "v_pk_mul_f32 " d ", " a ", " b " op_sel_hi:[0,1]\n\t"
#define CM_FMA(d, a, b) "v_pk_fma_f32 " d ", " a ", " b ", " d " op_sel:[1,1,0] op_sel_hi:[1,0,1] neg_lo:[0,1,0]\n\t"
DI void cmul2(f2& r0, f2 a0, f2 b0, f2& r1, f2 a1, f2 b1) {
  asm(CM_MUL("%0", "%2", "%3") CM_MUL("%1", "%4", "%5") CM_FMA("%0", "%2", "%3") CM_FMA("%1", "%4", "%5")
      : "=&v"(r0), "=&v"(r1) : "v"(a0), "v"(b0), "v"(a1), "v"(b1));
}
DI void cmul3(f2& r0, f2 a0, f2 b0, f2& r1, f2 a1, f2 b1, f2& r2, f2 a2, f2 b2) {
  asm(CM_MUL("%0", "%3", "%4") CM_MUL("%1", "%5", "%6") CM_MUL("%2", "%7", "%8") CM_FMA("%0", "%3", "%4") CM_FMA("%1", "%5", "%6") CM_FMA("%2", "%7", "%8")
      : "=&v"(r0), "=&v"(r1), "=&v"(r2) : "v"(a0), "v"(b0), "v"(a1), "v"(b1), "v"(a2), "v"(b2));
}
DI void cmul4(f2& r0, f2& r1, f2& r2, f2& r3, f2 a0, f2 a1, f2 a2, f2 a3, f2 b) {
  asm(CM_MUL("%0", "%4", "%8") CM_MUL("%1", "%5", "%8") CM_MUL("%2", "%6", "%8") CM_MUL("%3", "%7", "%8")
      CM_FMA("%0", "%4", "%8") CM_FMA("%1", "%5", "%8") CM_FMA("%2", "%6", "%8") CM_FMA("%3", "%7", "%8")
      : "=&v"(r0), "=&v"(r1), "=&v"(r2), "=&v"(r3) : "v"(a0), "v"(a1), "v"(a2), "v"(a3), "v"(b));
}
DI float exp_acc(float x) {
  const float t = x * 1.44269502f, r = fmaf(x, 1.44269502f, -t) + x * 1.92596299e-8f, e = __builtin_amdgcn_exp2f(t);
  return fmaf(e, r * 0.693147182f, e);
}
template <int CTRL> DI float dppf(float v) { return __int_as_float(__builtin_amdgcn_update_dpp(0, __float_as_int(v), CTRL, 0xf, 0xf, true)); }
DI float sum32(float v) {
  auto r = __builtin_amdgcn_permlane32_swap(__float_as_uint(v), __float_as_uint(v), false, false);
  return __uint_as_float(r[0]) + __uint_as_float(r[1]);
}
DI f2 ln_stats(float s1, float s2) {
  const float mean = s1 * (1.f / D);
  const float var = fmaxf(fmaf(-mean, mean, s2 * (1.f / D)), 0.f);
  return (f2){rb(mean), rb(__builtin_amdgcn_sqrtf(rb(rb(var) + 1e-5f)))};
}
typedef __amdgpu_buffer_rsrc_t rsrc_t;
DI rsrc_t mkrsrc(const void* p, u32 bytes) { return __builtin_amdgcn_make_buffer_rsrc((void*)p, 0, bytes, 0x00020000); }
DI u32 ldsaddr(const char* p) { return (u32)(size_t)(LAS const char*)p; }
template <int OFF> DI void glds16(u32 m0v, u32 voff, rsrc_t rs, u32 soff) {
  asm volatile("s_mov_b32 m0, %0\n\ts_nop 0\n\tbuffer_load_dwordx4 %1, %2, %3 offen offset:%4 lds" :: "s"(m0v), "v"(voff), "s"(rs), "s"(soff), "n"(OFF) : "memory");
}
DI void glds16x4(u32 m0v, u32 voff, rsrc_t rs, u32 soff) {
  asm volatile("s_mov_b32 m0, %0\n\ts_nop 0\n\tbuffer_load_dwordx4 %1, %2, %3 offen lds\n\tbuffer_load_dwordx4 %1, %2, %3 offen offset:1024 lds\n\t"
               "buffer_load_dwordx4 %1, %2, %3 offen offset:2048 lds\n\tbuffer_load_dwordx4 %1, %2, %3 offen offset:3072 lds"
               :: "s"(m0v), "v"(voff), "s"(rs), "s"(soff) : "memory");
}
template <int OFF> DI void bld16(u4& d, u32 voff, rsrc_t rs, u32 soff) {
  asm volatile("buffer_load_dwordx4 %0, %1, %2, %3 offen offset:%4" : "=v"(d) : "v"(voff), "s"(rs), "s"(soff), "n"(OFF));
}
template <int OFF> DI void bld16_sc1(u4& d, u32 voff, rsrc_t rs, u32 soff) {
  asm volatile("buffer_load_dwordx4 %0, %1, %2, %3 offen offset:%4 sc1" : "=v"(d) : "v"(voff), "s"(rs), "s"(soff), "n"(OFF));
}
template <int OFF, class T> DI void gld16(T& d, const void* p) { asm volatile("global_load_dwordx4 %0, %1, off offset:%2" : "=v"(d) : "v"(p), "n"(OFF)); }
DI void pin(u32& v) { asm("" : "+v"(v)); }
DI void rawbar() { asm volatile("s_barrier" ::: "memory"); }
template <int N> DI void vmwait() { asm volatile("s_waitcnt vmcnt(%0)" :: "n"(N) : "memory"); }
template <int N> DI void vmwait(u4& d) { asm volatile("s_waitcnt vmcnt(%1)" : "+v"(d) : "n"(N)); }
DI void bar() { asm volatile("s_waitcnt lgkmcnt(0)\n\ts_barrier" ::: "memory"); }
#define SB() __builtin_amdgcn_sched_barrier(0)
#define SGB(m, n) __builtin_amdgcn_sched_group_barrier(m, n, 0)
template <int I, int N, class F> DI void sfor(F&& f) { if constexpr (I < N) { f(std::integral_constant<int, I>{}); sfor<I + 1, N>(f); } }

constexpr int KG_ROW = 544, KG_CH = 57 * KG_ROW, KG_T = 2 * KG_CH, KG_TS = 4160, K1_LDS = KG_T + 2 * KG_TS;

DI f2 cexp_pow2(float a, float fh, float fl, float p) {
  const float e = __builtin_amdgcn_exp2f(a * p);
  const float r = fmaf(fl, p, __builtin_amdgcn_fractf(fh * p));
  return (f2){e * __builtin_amdgcn_cosf(r), e * __builtin_amdgcn_sinf(r)};
}

DI void k1_kgen(char* sm, int b, const u16* lar, const u16* aim, const u16* cre, const u16* cim, const u16* ldt, const u16* dsk, u16* kx) {
  const int t = threadIdx.x, w = __builtin_amdgcn_readfirstlane(t >> 6), lane = t & 63, ch = w >> 1, half = w & 1;
  const int d0 = 64 * (b & 7) + 2 * (b >> 3), d = d0 + ch, n = 64 * half + lane;
  const u32 v_lar = ldnt(lar + d * NM + n), v_aim = ldnt(aim + d * NM + n);
  const u32 cr = ldnt(cre + d * NM + n), ci = ldnt(cim + d * NM + n), cr2 = ldnt(cre + d * NM + (n ^ 64)), ci2 = ldnt(cim + d * NM + (n ^ 64));
  const float dt = rb(exp_acc(b2f(ldt[d])));
  const float ds = b2f(dsk[d]);
  const float zr = -rb(exp_acc(blo(v_lar))) * dt, zi = blo(v_aim) * dt;
  const float a = zr * 1.44269502f;
  const float fh = zi * 0.159154937f, fl = fmaf(zi, 0.159154937f, -fh) + zi * 6.42063833e-9f;
  const f2 e1 = cexp_pow2(a, fh, fl, 1.f), e8 = cexp_pow2(a, -fh, -fl, 8.f), e128 = cexp_pow2(a, fh, fl, 128.f), e1024 = cexp_pow2(a, fh, fl, 1024.f);
  int mx = (int)max(max(cr & 0x7fffu, ci & 0x7fffu), max(cr2 & 0x7fffu, ci2 & 0x7fffu));
  mx = max(mx, __builtin_amdgcn_update_dpp(0, mx, 0x111, 0xf, 0xf, true));
  mx = max(mx, __builtin_amdgcn_update_dpp(0, mx, 0x112, 0xf, 0xf, true));
  mx = max(mx, __builtin_amdgcn_update_dpp(0, mx, 0x114, 0xf, 0xf, true));
  mx = max(mx, __builtin_amdgcn_update_dpp(0, mx, 0x118, 0xf, 0xf, true));
  mx = max(max(__builtin_amdgcn_readlane(mx, 15), __builtin_amdgcn_readlane(mx, 31)), max(__builtin_amdgcn_readlane(mx, 47), __builtin_amdgcn_readlane(mx, 63)));
  const int ex = min(max(mx >> 7, 1), 253);
  const float cs = __int_as_float((254 - ex) << 23), kf = 2.f * dt * __int_as_float(ex << 23);
  if (t < 2) *(u4*)(kx + (d0 + t) * KXS) = (u4){0u, 0u, 0u, 0u};
  constexpr u32 KEEP = 0x03020100u, LL = 0x01000100u, LH = 0x05040100u, HH = 0x05040504u;
  const u32 sel0 = t < 32 ? KEEP : LL, sel1 = t < 32 ? KEEP : t < 128 ? LH : LL;
  const u32 sel2 = t < 32 ? KEEP : t < 128 ? LL : (t & 1) ? HH : LH, sel3 = t < 32 ? KEEP : t < 64 ? LH : HH;
  char* base = sm + ch * KG_CH + n * 4;
  auto put = [&](int row, f2 v) { *(u32*)(base + row * KG_ROW) = pkh(v); };
  f2 lo[8], e[8], m[8], x0, x1, x2, x3, e16, e32, e64;
  lo[0] = (f2){blo(cr) * cs, -blo(ci) * cs};
  e[1] = e1; m[1] = e128;
  put(0, lo[0]); put(16, (f2){1.f, 0.f}); put(17, e1); put(24, e128); put(48, e1024);
  cmul3(e16, e8, e8, e[2], e1, e1, m[2], e128, e128);
  cmul2(lo[1], lo[0], e8, x0, e1024, e1024);
  put(18, e[2]); put(32, m[2]); put(1, lo[1]); put(56, x0);
  cmul3(e32, e16, e16, e[4], e[2], e[2], m[4], m[2], m[2]);
  cmul2(e[3], e[2], e1, m[3], m[2], e128);
  cmul2(lo[2], lo[0], e16, lo[3], lo[1], e16);
  put(20, e[4]); put(40, m[4]); put(19, e[3]); put(36, m[3]); put(2, lo[2]); put(3, lo[3]);
  cmul4(e[5], e[6], e[7], x0, e1, e[2], e[3], m[4], e[4]);
  put(21, e[5]); put(22, e[6]); put(23, e[7]); put(41, x0);
  cmul3(m[5], m[4], e128, m[6], m[4], m[2], m[7], m[4], m[3]);
  put(42, m[5]); put(44, m[6]); put(46, m[7]);
  cmul4(lo[4], lo[5], lo[6], lo[7], lo[0], lo[1], lo[2], lo[3], e32);
  put(4, lo[4]); put(5, lo[5]); put(6, lo[6]); put(7, lo[7]);
  cmul2(e64, e32, e32, x0, e128, e1);
  put(25, x0);
  cmul3(x0, m[5], e[4], x1, m[6], e[4], x2, m[7], e[4]);
  put(43, x0); put(45, x1); put(47, x2);
  cmul4(x0, x1, x2, x3, lo[0], lo[1], lo[2], lo[3], e64);
  put(8, x0); put(9, x1); put(10, x2); put(11, x3);
  cmul4(x0, x1, x2, x3, lo[4], lo[5], lo[6], lo[7], e64);
  put(12, x0); put(13, x1); put(14, x2); put(15, x3);
  cmul4(x0, x1, x2, x3, e[2], e[3], e[4], e[5], e128);
  put(26, x0); put(27, x1); put(28, x2); put(29, x3);
  cmul2(x0, e[6], e128, x1, e[7], e128);
  put(30, x0); put(31, x1);
  cmul3(x0, e[2], m[2], x1, e[4], m[2], x2, e[6], m[2]);
  put(33, x0); put(34, x1); put(35, x2);
  cmul3(x0, e[2], m[3], x1, e[4], m[3], x2, e[6], m[3]);
  put(37, x0); put(38, x1); put(39, x2);
  cmul4(x0, x1, x2, x3, m[1], m[2], m[3], m[4], e1024);
  put(49, x0); put(50, x1); put(51, x2); put(52, x3);
  cmul3(x0, m[5], e1024, x1, m[6], e1024, x2, m[7], e1024);
  put(53, x0); put(54, x1); put(55, x2);
  __syncthreads();
  {
    const int c = lane & 15, g = lane >> 4;
    const char* fb = sm + ch * KG_CH + c * KG_ROW + g * 16;
    u16* T = (u16*)(sm + KG_T + ch * KG_TS);
    f4 acc0 = {0.f, 0.f, 0.f, 0.f}, acc1 = acc0;
    u4 B[8], A[8];
#pragma unroll
    for (int s = 0; s < 8; ++s) { B[s] = *(const u4*)(fb + s * 64); A[s] = *(const u4*)(fb + (16 + 16 * half) * KG_ROW + s * 64); }
    if (!half) {
      u4 A2[8];
#pragma unroll
      for (int s = 0; s < 8; ++s) A2[s] = *(const u4*)(fb + 48 * KG_ROW + s * 64);
      SB();
#pragma unroll
      for (int s = 0; s < 8; ++s) acc0 = mfma16h(A[s], B[s], acc0);
#pragma unroll
      for (int s = 0; s < 8; ++s) acc1 = mfma16h(A2[s], B[s], acc1);
      acc0 *= kf; acc1 *= kf;
      if (lane == 0) acc0[0] += ds;
      *(u2*)(T + 8 * c + 4 * (g & 1) + 128 * (g >> 1)) = (u2){pk(acc0[0], acc0[1]), pk(acc0[2], acc0[3])};
      if (g < 2) {
#pragma unroll
        for (int e = 0; e < 4; ++e) T[1024 + 128 * (4 * g + e) + 8 * c] = (u16)pk(acc1[e], 0.f);
      } else if (lane == 32) T[2048] = (u16)pk(acc1[0], 0.f);
    } else {
      SB();
#pragma unroll
      for (int s = 0; s < 8; ++s) acc0 = mfma16h(A[s], B[s], acc0);
      acc0 *= kf;
#pragma unroll
      for (int e = 0; e < 4; ++e) {
        const int p = g < 2 ? 256 + 128 * g + 2 * e : 512 + 128 * (2 * (g - 2) + (e >> 1)) + 4 * (e & 1);
        T[p + 8 * c] = (u16)pk(acc0[e], 0.f);
      }
    }
  }
  __syncthreads();
  u4 v[2];
  u32 e8v[2];
#pragma unroll
  for (int c2 = 0; c2 < 2; ++c2) {
    const u16* T = (const u16*)(sm + KG_T + c2 * KG_TS);
    v[c2] = *(const u4*)(T + 8 * t);
    e8v[c2] = T[8 * t + 8];
  }
#pragma unroll
  for (int c2 = 0; c2 < 2; ++c2) {
    const u4 o = {__builtin_amdgcn_perm(v[c2].x, v[c2].x, sel0), __builtin_amdgcn_perm(v[c2].z, t < 64 ? v[c2].y : v[c2].x, sel1),
                  __builtin_amdgcn_perm(e8v[c2], t < 128 ? v[c2].z : v[c2].x, sel2), __builtin_amdgcn_perm(e8v[c2], v[c2].w, sel3)};
    *(u4*)(kx + (d0 + c2) * KXS + 8 + 8 * t) = o;
  }
}

DI void k1_xt(char* sm, int b, const u16* x, u16* xt) {
  const int t = threadIdx.x, w = t >> 6, lane = t & 63;
  const int c0 = 64 * (b & 7), r0 = 128 * (b >> 3);
  u4 v[4];
#pragma unroll
  for (int i = 0; i < 4; ++i) v[i] = ldnt((const u4*)(x + (r0 + 32 * i + (t >> 3)) * D + c0 + 8 * (t & 7)));
#pragma unroll
  for (int i = 0; i < 4; ++i) *(u4*)(sm + (32 * i + (t >> 3)) * 144 + 16 * (t & 7)) = v[i];
  __syncthreads();
  const int g = lane >> 4, q = (lane & 15) >> 2, p = lane & 3;
#pragma unroll
  for (int i = 0; i < 4; ++i) {
    const char* a = sm + (32 * i + 8 * g + q) * 144 + 32 * w + 8 * p;
    *(u4*)(xt + (c0 + 16 * w + (lane & 15)) * L + r0 + 32 * i + 8 * g) = cat(trd(a), trd(a + 4 * 144));
  }
}

DI void k1_stats(int b, const u16* x, char* ws) {
  const int t = threadIdx.x, row = 32 * b + (t >> 3);
  u4 v[8];
#pragma unroll
  for (int i = 0; i < 8; ++i) v[i] = ldnt((const u4*)(x + row * D + 64 * i + 8 * (t & 7)));
  float s1 = 0.f, s2 = 0.f, s3 = 0.f, s4 = 0.f;
#pragma unroll
  for (int i = 0; i < 8; ++i)
#pragma unroll
    for (int j = 0; j < 4; j += 2) {
      s1 = dot2(v[i][j], 0x3f803f80u, s1); s2 = dot2(v[i][j], v[i][j], s2);
      s3 = dot2(v[i][j + 1], 0x3f803f80u, s3); s4 = dot2(v[i][j + 1], v[i][j + 1], s4);
    }
  s1 += s3; s2 += s4;
  s1 += dppf<0xb1>(s1); s2 += dppf<0xb1>(s2); s1 += dppf<0x4e>(s1); s2 += dppf<0x4e>(s2); s1 += dppf<0x141>(s1); s2 += dppf<0x141>(s2);
  const f2 st = ln_stats(s1, s2);
  if ((t & 7) == 0) {
    ((u16*)(ws + WS_MEAN))[row] = (u16)(__float_as_uint(st.x) >> 16);
    ((u16*)(ws + WS_SD))[row] = (u16)(__float_as_uint(st.y) >> 16);
    ((u32*)(ws + WS_ST2))[row] = 0u;
  }
  if (t == 0 && b < 16) ((u32*)(ws + WS_CNT))[64 * b] = 0u;
}

DI void k1_mod(char* sm, int b, const u16* c, const u16* wm, char* ws) {
  const int t = threadIdx.x, rg = t >> 3;
  u4 v[16];
  const u32 cc = ((const u32*)c)[t];
#pragma unroll
  for (int i = 0; i < 16; ++i) v[i] = ldnt((const u4*)(wm + (16 * rg + i) * (6 * D) + 64 * b + 8 * (t & 7)));
  u32* sl = (u32*)(sm + 32 * 68 * 4);
  {
    const float c0 = blo(cc), c1 = bhi(cc);
    sl[t] = pk(c0 / (1.f + __expf(-c0)), c1 / (1.f + __expf(-c1)));
  }
  __syncthreads();
  const u4 sa = *(const u4*)(sl + 8 * rg), sb = *(const u4*)(sl + 8 * rg + 4);
  float acc[8] = {0.f, 0.f, 0.f, 0.f, 0.f, 0.f, 0.f, 0.f};
#pragma unroll
  for (int i = 0; i < 8; ++i) {
    const u32 sp = i < 4 ? sa[i & 3] : sb[i & 3];
#pragma unroll
    for (int j = 0; j < 4; ++j) {
      acc[2 * j] = dot2(__builtin_amdgcn_perm(v[2 * i + 1][j], v[2 * i][j], 0x05040100u), sp, acc[2 * j]);
      acc[2 * j + 1] = dot2(__builtin_amdgcn_perm(v[2 * i + 1][j], v[2 * i][j], 0x07060302u), sp, acc[2 * j + 1]);
    }
  }
  float* red = (float*)sm;
  *(f4*)(red + rg * 68 + 8 * (t & 7)) = (f4){acc[0], acc[1], acc[2], acc[3]};
  *(f4*)(red + rg * 68 + 8 * (t & 7) + 4) = (f4){acc[4], acc[5], acc[6], acc[7]};
  __syncthreads();
  float s = 0.f;
#pragma unroll
  for (int i = 0; i < 8; ++i) s += red[((t & 3) + 4 * i) * 68 + (t >> 2)];
  s += dppf<0xb1>(s); s += dppf<0x4e>(s);
  if ((t & 3) == 0) {
    const int j = 64 * b + (t >> 2), part = b >> 3;
    float v = rb(s);
    if (part == 1 || part == 4) v = rb(1.f + v);
    if (part == 4) {
      v = v == 0.f ? 0x1p-60f : v;
      ((float*)(ws + WS_INV))[j - 4 * D] = 1.f / v;
    }
    if (part == 3 || part == 4) ((u16*)(ws + WS_CV))[j - 3 * D] = (u16)(__float_as_uint(v) >> 16);
    ((float*)(ws + WS_MOD))[j] = v;
  }
}

__global__ __launch_bounds__(256, 2) void k1_prep(const u16* x, const u16* lar, const u16* aim, const u16* cre, const u16* cim, const u16* ldt,
                                                  const u16* dsk, const u16* c, const u16* wm, char* ws) {
  __shared__ __attribute__((aligned(16))) char sm[K1_LDS];
  const int b = blockIdx.x;
  if (b < 256) k1_kgen(sm, b, lar, aim, cre, cim, ldt, dsk, (u16*)(ws + WS_KX));
  else if (b < 384) k1_xt(sm, b - 256, x, (u16*)(ws + WS_XT));
  else if (b < 448) k1_stats(b - 384, x, ws);
  else k1_mod(sm, b - 448, c, wm, ws);
}

constexpr int CV_KC = 4128, CV_H = 8 * CV_KC, CV_HP = 1152, CV_X = CV_H + 8 * CV_HP, CV_EX = CV_X + 8192, CV_W = CV_EX + 16384,
              CV_FL = CV_W + 16384, K2_LDS = CV_FL + 16;

DI void k2_conv(char* sm, int b, char* ws) {
  const int t = threadIdx.x, w = __builtin_amdgcn_readfirstlane(t >> 6), lane = t & 63, ch = w >> 1, half = w & 1;
  const int d0 = 64 * (b & 7) + 2 * (b >> 3), d = d0 + ch;
  const u16* xt = (const u16*)(ws + WS_XT);
  const float* mod = (const float*)(ws + WS_MOD);
  const int c = lane & 15, g = lane >> 4;
  u4 xv[2];
  const u4 mv = *(const u4*)(ws + WS_MEAN + 16 * t), sv = *(const u4*)(ws + WS_SD + 16 * t);
#pragma unroll
  for (int i = 0; i < 2; ++i) xv[i] = *(const u4*)(xt + (d0 + i) * L + 8 * t);
  const float ga = mod[2 * D + d], c2 = mod[4 * D + d];
  const f2 c1 = *(const f2*)(mod + D + d0), sa = *(const f2*)(mod + d0);
  SB();
  float rs[8];
#pragma unroll
  for (int j = 0; j < 8; ++j) rs[j] = __builtin_amdgcn_rcpf((j & 1) ? bhi(sv[j >> 1]) : blo(sv[j >> 1]));
#pragma unroll
  for (int i = 0; i < 2; ++i) {
    float h[8];
#pragma unroll
    for (int j = 0; j < 8; ++j) {
      const float xx = (j & 1) ? bhi(xv[i][j >> 1]) : blo(xv[i][j >> 1]);
      const float mm = (j & 1) ? bhi(mv[j >> 1]) : blo(mv[j >> 1]);
      h[j] = fmaf((xx - mm) * rs[j], c1[i], sa[i]);
    }
#pragma unroll
    for (int r = 0; r < 4; ++r) *(u32*)(sm + CV_H + (4 * i + r) * CV_HP + 128 + 4 * t) = pk(h[r], h[r + 4]);
    *(u4*)(sm + CV_X + 4096 * i + 16 * t) = xv[i];
  }
  f4 acc[8];
#pragma unroll
  for (int i = 0; i < 8; ++i) acc[i] = (f4){0.f, 0.f, 0.f, 0.f};
  u32 ka = ldsaddr(sm) + (4 * ch + 2 * half) * CV_KC + 32 + 8 * (8 * g + (lane & 3) - ((lane & 15) >> 2));
  u32 hb = ldsaddr(sm) + CV_H + (4 * ch + 2 * half) * CV_HP + 128 + 8 * c - 16 * g - 8 - 64;
  bar();
  {
    u32 cnt;
    asm volatile(
      "ds_read_b64_tr_b16 v[64:65], %8 offset:32\n"
      "ds_read_b64_tr_b16 v[66:67], %8 offset:0\n"
      "ds_read_b64 v[120:121], %9 offset:832\n"
      "ds_read_b64 v[122:123], %9 offset:840\n"
      "ds_read_b64 v[124:125], %9 offset:960\n"
      "ds_read_b64 v[126:127], %9 offset:968\n"
      "ds_read_b64 v[112:113], %9 offset:576\n"
      "ds_read_b64 v[114:115], %9 offset:584\n"
      "ds_read_b64 v[116:117], %9 offset:704\n"
      "ds_read_b64 v[118:119], %9 offset:712\n"
      "ds_read_b64_tr_b16 v[68:69], %8 offset:544\n"
      "ds_read_b64_tr_b16 v[70:71], %8 offset:512\n"
      "ds_read_b64 v[108:109], %9 offset:448\n"
      "ds_read_b64 v[110:111], %9 offset:456\n"
      "ds_read_b64 v[104:105], %9 offset:320\n"
      "ds_read_b64 v[106:107], %9 offset:328\n"
      "ds_read_b64_tr_b16 v[72:73], %8 offset:1056\n"
      "ds_read_b64_tr_b16 v[74:75], %8 offset:1024\n"
      "ds_read_b64_tr_b16 v[76:77], %8 offset:1568\n"
      "ds_read_b64_tr_b16 v[78:79], %8 offset:1536\n"
      "ds_read_b64 v[100:101], %9 offset:192\n"
      "ds_read_b64 v[102:103], %9 offset:200\n"
      "ds_read_b64_tr_b16 v[80:81], %8 offset:2080\n"
      "ds_read_b64_tr_b16 v[82:83], %8 offset:2048\n"
      "ds_read_b64 v[96:97], %9 offset:64\n"
      "ds_read_b64 v[98:99], %9 offset:72\n"
      "ds_read_b64_tr_b16 v[84:85], %8 offset:2592\n"
      "ds_read_b64_tr_b16 v[86:87], %8 offset:2560\n"
      "ds_read_b64_tr_b16 v[88:89], %8 offset:3104\n"
      "ds_read_b64_tr_b16 v[90:91], %8 offset:3072\n"
      "ds_read_b64_tr_b16 v[92:93], %8 offset:3616\n"
      "ds_read_b64_tr_b16 v[94:95], %8 offset:3584\n"
      "s_mov_b32 %10, 2\n"
      "s_waitcnt lgkmcnt(0)\n"
      "Lconv%=:\n"
      "v_mfma_f32_16x16x32_bf16 %6, v[64:67], v[120:123], %6\n"
      "ds_read_b64_tr_b16 v[128:129], %8 offset:288\n"
      "v_mfma_f32_16x16x32_bf16 %7, v[64:67], v[124:127], %7\n"
      "ds_read_b64_tr_b16 v[130:131], %8 offset:256\n"
      "v_mfma_f32_16x16x32_bf16 %4, v[64:67], v[112:115], %4\n"
      "ds_read_b64 v[184:185], %9 offset:768\n"
      "v_mfma_f32_16x16x32_bf16 %5, v[64:67], v[116:119], %5\n"
      "ds_read_b64 v[186:187], %9 offset:776\n"
      "v_mfma_f32_16x16x32_bf16 %6, v[68:71], v[116:119], %6\n"
      "ds_read_b64 v[188:189], %9 offset:896\n"
      "v_mfma_f32_16x16x32_bf16 %7, v[68:71], v[120:123], %7\n"
      "ds_read_b64 v[190:191], %9 offset:904\n"
      "v_mfma_f32_16x16x32_bf16 %4, v[68:71], v[108:111], %4\n"
      "ds_read_b64 v[176:177], %9 offset:512\n"
      "v_mfma_f32_16x16x32_bf16 %2, v[64:67], v[104:107], %2\n"
      "ds_read_b64 v[178:179], %9 offset:520\n"
      "v_mfma_f32_16x16x32_bf16 %6, v[72:75], v[112:115], %6\n"
      "ds_read_b64 v[180:181], %9 offset:640\n"
      "v_mfma_f32_16x16x32_bf16 %3, v[64:67], v[108:111], %3\n"
      "ds_read_b64 v[182:183], %9 offset:648\n"
      "v_mfma_f32_16x16x32_bf16 %7, v[72:75], v[116:119], %7\n"
      "ds_read_b64_tr_b16 v[132:133], %8 offset:800\n"
      "v_mfma_f32_16x16x32_bf16 %4, v[72:75], v[104:107], %4\n"
      "ds_read_b64_tr_b16 v[134:135], %8 offset:768\n"
      "v_mfma_f32_16x16x32_bf16 %5, v[68:71], v[112:115], %5\n"
      "ds_read_b64 v[172:173], %9 offset:384\n"
      "v_mfma_f32_16x16x32_bf16 %6, v[76:79], v[108:111], %6\n"
      "ds_read_b64 v[174:175], %9 offset:392\n"
      "v_mfma_f32_16x16x32_bf16 %7, v[76:79], v[112:115], %7\n"
      "ds_read_b64 v[168:169], %9 offset:256\n"
      "v_mfma_f32_16x16x32_bf16 %4, v[76:79], v[100:103], %4\n"
      "ds_read_b64 v[170:171], %9 offset:264\n"
      "v_mfma_f32_16x16x32_bf16 %1, v[64:67], v[100:103], %1\n"
      "ds_read_b64_tr_b16 v[136:137], %8 offset:1312\n"
      "v_mfma_f32_16x16x32_bf16 %5, v[72:75], v[108:111], %5\n"
      "ds_read_b64_tr_b16 v[138:139], %8 offset:1280\n"
      "v_mfma_f32_16x16x32_bf16 %6, v[80:83], v[104:107], %6\n"
      "ds_read_b64_tr_b16 v[140:141], %8 offset:1824\n"
      "v_mfma_f32_16x16x32_bf16 %0, v[64:67], v[96:99], %0\n"
      "ds_read_b64_tr_b16 v[142:143], %8 offset:1792\n"
      "v_mfma_f32_16x16x32_bf16 %7, v[80:83], v[108:111], %7\n"
      "ds_read_b64 v[164:165], %9 offset:128\n"
      "v_mfma_f32_16x16x32_bf16 %5, v[76:79], v[104:107], %5\n"
      "ds_read_b64 v[166:167], %9 offset:136\n"
      "v_mfma_f32_16x16x32_bf16 %2, v[68:71], v[100:103], %2\n"
      "ds_read_b64_tr_b16 v[144:145], %8 offset:2336\n"
      "v_mfma_f32_16x16x32_bf16 %6, v[84:87], v[100:103], %6\n"
      "ds_read_b64_tr_b16 v[146:147], %8 offset:2304\n"
      "v_mfma_f32_16x16x32_bf16 %7, v[84:87], v[104:107], %7\n"
      "ds_read_b64 v[160:161], %9 offset:0\n"
      "v_mfma_f32_16x16x32_bf16 %3, v[68:71], v[104:107], %3\n"
      "ds_read_b64 v[162:163], %9 offset:8\n"
      "v_mfma_f32_16x16x32_bf16 %2, v[72:75], v[96:99], %2\n"
      "ds_read_b64_tr_b16 v[148:149], %8 offset:2848\n"
      "v_mfma_f32_16x16x32_bf16 %5, v[80:83], v[100:103], %5\n"
      "ds_read_b64_tr_b16 v[150:151], %8 offset:2816\n"
      "v_mfma_f32_16x16x32_bf16 %7, v[88:91], v[100:103], %7\n"
      "ds_read_b64_tr_b16 v[152:153], %8 offset:3360\n"
      "v_mfma_f32_16x16x32_bf16 %6, v[88:91], v[96:99], %6\n"
      "ds_read_b64_tr_b16 v[154:155], %8 offset:3328\n"
      "v_mfma_f32_16x16x32_bf16 %3, v[72:75], v[100:103], %3\n"
      "ds_read_b64_tr_b16 v[156:157], %8 offset:3872\n"
      "v_mfma_f32_16x16x32_bf16 %1, v[68:71], v[96:99], %1\n"
      "ds_read_b64_tr_b16 v[158:159], %8 offset:3840\n"
      "v_mfma_f32_16x16x32_bf16 %7, v[92:95], v[96:99], %7\n"
      "v_mfma_f32_16x16x32_bf16 %5, v[84:87], v[96:99], %5\n"
      "v_mfma_f32_16x16x32_bf16 %4, v[80:83], v[96:99], %4\n"
      "v_mfma_f32_16x16x32_bf16 %3, v[76:79], v[96:99], %3\n"
      "s_waitcnt lgkmcnt(0)\n"
      "v_add_u32 %8, 4128, %8\n"
      "v_add_u32 %9, 1152, %9\n"
      "v_mfma_f32_16x16x32_bf16 %6, v[128:131], v[184:187], %6\n"
      "ds_read_b64_tr_b16 v[64:65], %8 offset:32\n"
      "v_mfma_f32_16x16x32_bf16 %7, v[128:131], v[188:191], %7\n"
      "ds_read_b64_tr_b16 v[66:67], %8 offset:0\n"
      "v_mfma_f32_16x16x32_bf16 %4, v[128:131], v[176:179], %4\n"
      "ds_read_b64 v[120:121], %9 offset:832\n"
      "v_mfma_f32_16x16x32_bf16 %5, v[128:131], v[180:183], %5\n"
      "ds_read_b64 v[122:123], %9 offset:840\n"
      "v_mfma_f32_16x16x32_bf16 %6, v[132:135], v[180:183], %6\n"
      "ds_read_b64 v[124:125], %9 offset:960\n"
      "v_mfma_f32_16x16x32_bf16 %7, v[132:135], v[184:187], %7\n"
      "ds_read_b64 v[126:127], %9 offset:968\n"
      "v_mfma_f32_16x16x32_bf16 %4, v[132:135], v[172:175], %4\n"
      "ds_read_b64 v[112:113], %9 offset:576\n"
      "v_mfma_f32_16x16x32_bf16 %2, v[128:131], v[168:171], %2\n"
      "ds_read_b64 v[114:115], %9 offset:584\n"
      "v_mfma_f32_16x16x32_bf16 %6, v[136:139], v[176:179], %6\n"
      "ds_read_b64 v[116:117], %9 offset:704\n"
      "v_mfma_f32_16x16x32_bf16 %3, v[128:131], v[172:175], %3\n"
      "ds_read_b64 v[118:119], %9 offset:712\n"
      "v_mfma_f32_16x16x32_bf16 %7, v[136:139], v[180:183], %7\n"
      "ds_read_b64_tr_b16 v[68:69], %8 offset:544\n"
      "v_mfma_f32_16x16x32_bf16 %4, v[136:139], v[168:171], %4\n"
      "ds_read_b64_tr_b16 v[70:71], %8 offset:512\n"
      "v_mfma_f32_16x16x32_bf16 %5, v[132:135], v[176:179], %5\n"
      "ds_read_b64 v[108:109], %9 offset:448\n"
      "v_mfma_f32_16x16x32_bf16 %6, v[140:143], v[172:175], %6\n"
      "ds_read_b64 v[110:111], %9 offset:456\n"
      "v_mfma_f32_16x16x32_bf16 %7, v[140:143], v[176:179], %7\n"
      "ds_read_b64 v[104:105], %9 offset:320\n"
      "v_mfma_f32_16x16x32_bf16 %4, v[140:143], v[164:167], %4\n"
      "ds_read_b64 v[106:107], %9 offset:328\n"
      "v_mfma_f32_16x16x32_bf16 %1, v[128:131], v[164:167], %1\n"
      "ds_read_b64_tr_b16 v[72:73], %8 offset:1056\n"
      "v_mfma_f32_16x16x32_bf16 %5, v[136:139], v[172:175], %5\n"
      "ds_read_b64_tr_b16 v[74:75], %8 offset:1024\n"
      "v_mfma_f32_16x16x32_bf16 %6, v[144:147], v[168:171], %6\n"
      "ds_read_b64_tr_b16 v[76:77], %8 offset:1568\n"
      "v_mfma_f32_16x16x32_bf16 %0, v[128:131], v[160:163], %0\n"
      "ds_read_b64_tr_b16 v[78:79], %8 offset:1536\n"
      "v_mfma_f32_16x16x32_bf16 %7, v[144:147], v[172:175], %7\n"
      "ds_read_b64 v[100:101], %9 offset:192\n"
      "v_mfma_f32_16x16x32_bf16 %5, v[140:143], v[168:171], %5\n"
      "ds_read_b64 v[102:103], %9 offset:200\n"
      "v_mfma_f32_16x16x32_bf16 %2, v[132:135], v[164:167], %2\n"
      "ds_read_b64_tr_b16 v[80:81], %8 offset:2080\n"
      "v_mfma_f32_16x16x32_bf16 %6, v[148:151], v[164:167], %6\n"
      "ds_read_b64_tr_b16 v[82:83], %8 offset:2048\n"
      "v_mfma_f32_16x16x32_bf16 %7, v[148:151], v[168:171], %7\n"
      "ds_read_b64 v[96:97], %9 offset:64\n"
      "v_mfma_f32_16x16x32_bf16 %3, v[132:135], v[168:171], %3\n"
      "ds_read_b64 v[98:99], %9 offset:72\n"
      "v_mfma_f32_16x16x32_bf16 %2, v[136:139], v[160:163], %2\n"
      "ds_read_b64_tr_b16 v[84:85], %8 offset:2592\n"
      "v_mfma_f32_16x16x32_bf16 %5, v[144:147], v[164:167], %5\n"
      "ds_read_b64_tr_b16 v[86:87], %8 offset:2560\n"
      "v_mfma_f32_16x16x32_bf16 %7, v[152:155], v[164:167], %7\n"
      "ds_read_b64_tr_b16 v[88:89], %8 offset:3104\n"
      "v_mfma_f32_16x16x32_bf16 %6, v[152:155], v[160:163], %6\n"
      "ds_read_b64_tr_b16 v[90:91], %8 offset:3072\n"
      "v_mfma_f32_16x16x32_bf16 %3, v[136:139], v[164:167], %3\n"
      "ds_read_b64_tr_b16 v[92:93], %8 offset:3616\n"
      "v_mfma_f32_16x16x32_bf16 %1, v[132:135], v[160:163], %1\n"
      "ds_read_b64_tr_b16 v[94:95], %8 offset:3584\n"
      "v_mfma_f32_16x16x32_bf16 %7, v[156:159], v[160:163], %7\n"
      "v_mfma_f32_16x16x32_bf16 %5, v[148:151], v[160:163], %5\n"
      "v_mfma_f32_16x16x32_bf16 %4, v[144:147], v[160:163], %4\n"
      "v_mfma_f32_16x16x32_bf16 %3, v[140:143], v[160:163], %3\n"
      "s_waitcnt lgkmcnt(0)\n"
      "s_sub_u32 %10, %10, 1\n"
      "s_cmp_lg_u32 %10, 0\n"
      "s_cbranch_scc1 Lconv%=\n"
      "s_nop 7\n"
      : "+v"(acc[0]), "+v"(acc[1]), "+v"(acc[2]), "+v"(acc[3]), "+v"(acc[4]), "+v"(acc[5]), "+v"(acc[6]), "+v"(acc[7]), "+v"(ka), "+v"(hb), "=&s"(cnt)
      :: "memory", "scc", "v64", "v65", "v66", "v67", "v68", "v69", "v70", "v71", "v72", "v73", "v74", "v75", "v76", "v77", "v78", "v79", "v80", "v81", "v82", "v83", "v84", "v85", "v86", "v87", "v88", "v89", "v90", "v91", "v92", "v93", "v94", "v95", "v96", "v97", "v98", "v99", "v100", "v101", "v102", "v103", "v104", "v105", "v106", "v107", "v108", "v109", "v110", "v111", "v112", "v113", "v114", "v115", "v116", "v117", "v118", "v119", "v120", "v121", "v122", "v123", "v124", "v125", "v126", "v127", "v128", "v129", "v130", "v131", "v132", "v133", "v134", "v135", "v136", "v137", "v138", "v139", "v140", "v141", "v142", "v143", "v144", "v145", "v146", "v147", "v148", "v149", "v150", "v151", "v152", "v153", "v154", "v155", "v156", "v157", "v158", "v159", "v160", "v161", "v162", "v163", "v164", "v165", "v166", "v167", "v168", "v169", "v170", "v171", "v172", "v173", "v174", "v175", "v176", "v177", "v178", "v179", "v180", "v181", "v182", "v183", "v184", "v185", "v186", "v187", "v188", "v189", "v190", "v191");
  }
  f4* ex = (f4*)(sm + CV_EX);
  if (half) {
#pragma unroll
    for (int i = 0; i < 4; ++i) { ex[(w * 4 + i) * 64 + lane] = acc[i]; acc[i] = acc[4 + i]; }
  } else {
#pragma unroll
    for (int i = 0; i < 4; ++i) ex[(w * 4 + i) * 64 + lane] = acc[4 + i];
  }
  volatile LAS u32* fl = (volatile LAS u32*)(size_t)(ldsaddr(sm) + CV_FL);
  asm volatile("s_waitcnt lgkmcnt(0)" ::: "memory");
  fl[w] = 1u;
  u2 xr[4];
#pragma unroll
  for (int i = 0; i < 4; ++i) xr[i] = *(const u2*)(sm + CV_X + 4096 * ch + 2 * (256 * (4 * half + i) + 16 * c + 4 * g));
  while (fl[w ^ 1] == 0u) {}
  asm volatile("" ::: "memory");
  u16* x1t = (u16*)(ws + WS_X1T);
  const float gc = ga * c2;
#pragma unroll
  for (int i = 0; i < 4; ++i) {
    const f4 y = (acc[i] + ex[((w ^ 1) * 4 + i) * 64 + lane]) * gc + (f4){blo(xr[i].x), bhi(xr[i].x), blo(xr[i].y), bhi(xr[i].y)} * c2;
    *(u2*)(x1t + d * L + 256 * (4 * half + i) + 16 * c + 4 * g) = (u2){pk(y[0], y[1]), pk(y[2], y[3])};
  }
}

DI void k2_help(char* sm, int b, const u16* wg, const u16* wu, const u16* wd, char* ws) {
  const int t = threadIdx.x - 256, w = __builtin_amdgcn_readfirstlane(t >> 6), lane = t & 63;
  const int d0 = 64 * (b & 7) + 2 * (b >> 3);
  const u16* kx = (const u16*)(ws + WS_KX);
  u4 kv[2];
  u2 kp[2];
#pragma unroll
  for (int i = 0; i < 2; ++i) { kv[i] = *(const u4*)(kx + (d0 + i) * KXS + 8 + 8 * t); kp[i] = *(const u2*)(kx + (d0 + i) * KXS + 4 + 8 * t); }
  SB();
  if (t < 16) *(u4*)(sm + (t >> 1) * CV_KC + 16 * (t & 1)) = (u4){0u, 0u, 0u, 0u};
  if (w == 1) *(u4*)(sm + CV_H + (lane >> 3) * CV_HP + 16 * (t & 7)) = (u4){0u, 0u, 0u, 0u};
  if (t == 128) *(u4*)(sm + CV_FL) = (u4){0u, 0u, 0u, 0u};
#pragma unroll
  for (int i = 0; i < 2; ++i) {
    const u4 v = kv[i];
    const u32 a0 = __builtin_amdgcn_alignbit(kp[i].y, kp[i].x, 16), a1 = __builtin_amdgcn_alignbit(v.x, kp[i].y, 16), a2 = __builtin_amdgcn_alignbit(v.y, v.x, 16),
              a3 = __builtin_amdgcn_alignbit(v.z, v.y, 16), a4 = __builtin_amdgcn_alignbit(v.w, v.z, 16);
    char* kd = sm + 4 * i * CV_KC + 32 + 16 * t;
    *(u4*)kd = v;
    *(u4*)(kd + CV_KC) = (u4){a1, a2, a3, a4};
    *(u4*)(kd + 2 * CV_KC) = (u4){kp[i].y, v.x, v.y, v.z};
    *(u4*)(kd + 3 * CV_KC) = (u4){a0, a1, a2, a3};
  }
  bar();
  const int task = b + 256 * w, kq = task & 3, blk = task >> 2, mat = blk >= 43, jb = blk - 43 * mat;
  const bool work = task < 344;
  const int hh = lane >> 5;
  u4 vec[8];
  if (work) {
    const rsrc_t rw = mkrsrc(mat ? wu : wg, D * DF * 2);
    const u32 vo = (128 * kq + (lane >> 2)) * (DF * 2) + 64 * jb + 16 * (lane & 3);
    asm volatile("s_nop 4");
#pragma unroll
    for (int i = 0; i < 8; ++i) glds16<0>(ldsaddr(sm) + CV_W + 8192 * w + 1024 * i, vo, rw, i * 16 * DF * 2);
    const char* cv = ws + WS_CV + ((lane & 1) ? 0 : 1024) + 256 * kq + 16 * hh;
    sfor<0, 8>([&](auto i) { gld16<32 * i>(vec[i], cv); });
  }
  u32 dummy[4];
  {
    const int row = 32 * (b >> 3) + 8 * w + (lane >> 3);
    const char* base = (const char*)(row >= D ? wu : wg) + (row & (D - 1)) * (DF * 2);
#pragma unroll
    for (int i = 0; i < 2; ++i) {
      const int off = min((b & 1) * 1344 + 128 * ((lane & 7) + 8 * i), (b & 1) ? DF * 2 - 4 : 1340);
      asm volatile("global_load_dword %0, %1, off" : "=v"(dummy[i]) : "v"(base + off));
    }
  }
#pragma unroll
  for (int i = 0; i < 2; ++i)
    asm volatile("global_load_dword %0, %1, off" : "=v"(dummy[2 + i]) : "v"((const char*)wd + (b >> 3) * 44032 + w * 11008 + min(lane + 64 * i, 85) * 128));
  if (work) {
    asm volatile("s_waitcnt vmcnt(4)" : "+v"(vec[0]), "+v"(vec[1]), "+v"(vec[2]), "+v"(vec[3]), "+v"(vec[4]), "+v"(vec[5]), "+v"(vec[6]), "+v"(vec[7]) :: "memory");
    const u32 ra = ldsaddr(sm) + CV_W + 8192 * w + 64 * (8 * hh + ((lane & 15) >> 2)) + 32 * ((lane >> 4) & 1) + 8 * (lane & 3);
    f16v sums;
#pragma unroll
    for (int e = 0; e < 16; ++e) sums[e] = 0.f;
#pragma unroll
    for (int i = 0; i < 8; ++i) sums = mfma32(vec[i], cat(trd(ra + 1024 * i), trd(ra + 1024 * i + 256)), sums);
    if (lane < 32) {
      u32* exf = (u32*)(ws + WS_EX) + ((2 * jb + (lane >> 4)) * 64 + 16 * mat + (lane & 15)) * 4 + kq;
      const float h0 = rb(sums[0]), h1 = rb(sums[1]);
      exf[0] = pk(h0, sums[0] - h0);
      exf[128] = pk(h1, sums[1] - h1);
    }
  }
  asm volatile("s_waitcnt vmcnt(0)" : "+v"(dummy[0]), "+v"(dummy[1]), "+v"(dummy[2]), "+v"(dummy[3]) :: "memory");
}

__global__ __launch_bounds__(512, 2) void k2_conv_prep(const u16* wg, const u16* wu, const u16* wd, char* ws) {
  extern __shared__ __attribute__((aligned(16))) char sm[];
  const int b = blockIdx.x;
  if (__builtin_amdgcn_readfirstlane(threadIdx.x) < 256) k2_conv(sm, b, ws);
  else k2_help(sm, b, wg, wu, wd, ws);
}

constexpr int G1_LEAD = 3, G1_NS = 5, G1_SLOT = 20480;
constexpr int G2_RES = 19, G2_FL = 4 * G2_RES * 2048, G1_SV = G2_FL + 64, K3_LDS = G1_SV + 512;
constexpr int G2_LEAD = 6;
constexpr int HH_T = NU * 1024;

template <int K> DI u32 g1_pair(const f16v& acc, float rs, float sd) {
  const f2 g = (f2){acc[2 * K], acc[2 * K + 1]} * rs;
  const f2 z = g * (g * g * -0.10294324f + -2.30220819f);
  const f2 dn = (f2){__builtin_amdgcn_exp2f(z.x), __builtin_amdgcn_exp2f(z.y)} * sd + sd;
  const f2 r = g * (f2){acc[8 + 2 * K], acc[9 + 2 * K]} * (f2){__builtin_amdgcn_rcpf(dn.x), __builtin_amdgcn_rcpf(dn.y)};
  return pk(r.x, r.y);
}
DI u4 g1_tile(const f16v& acc, float rs, float sd) { return (u4){g1_pair<0>(acc, rs, sd), g1_pair<1>(acc, rs, sd), g1_pair<2>(acc, rs, sd), g1_pair<3>(acc, rs, sd)}; }

DI void g1_stats(char* sm, int rt, char* ws) {
  const int t = threadIdx.x;
  float* red = (float*)sm;
  if (t < 512) {
    const u16* xp = (const u16*)(ws + WS_X1T) + (t >> 4) * L + 128 * rt + 8 * (t & 15);
    u4 v[16];
    float iv[16];
#pragma unroll
    for (int i = 0; i < 16; ++i) { v[i] = *(const u4*)(xp + 32 * i * L); iv[i] = ((const float*)(ws + WS_INV))[(t >> 4) + 32 * i]; }
    f2 s1[4], s2[4];
#pragma unroll
    for (int j = 0; j < 4; ++j) s1[j] = s2[j] = (f2){0.f, 0.f};
#pragma unroll
    for (int i = 0; i < 16; ++i)
#pragma unroll
      for (int j = 0; j < 4; ++j) { const f2 x = (f2){blo(v[i][j]), bhi(v[i][j])} * iv[i]; s1[j] += x; s2[j] += x * x; }
#pragma unroll
    for (int j = 0; j < 4; ++j) *(f4*)(red + (t >> 4) * 260 + 16 * (t & 15) + 4 * j) = (f4){s1[j].x, s2[j].x, s1[j].y, s2[j].y};
  }
  __syncthreads();
  if (t < 512) {
    f2 p = {0.f, 0.f};
#pragma unroll
    for (int i = 0; i < 8; ++i) p += *(const f2*)(red + (8 * (t & 3) + i) * 260 + 2 * (t >> 2));
    p.x += dppf<0xb1>(p.x); p.y += dppf<0xb1>(p.y); p.x += dppf<0x4e>(p.x); p.y += dppf<0x4e>(p.y);
    const f2 ms = ln_stats(p.x, p.y);
    if ((t & 3) == 0)
      __hip_atomic_store((u32*)(ws + WS_ST2) + 128 * rt + (t >> 2), (__float_as_uint(ms.x) >> 16) | __float_as_uint(ms.y), __ATOMIC_RELAXED, __HIP_MEMORY_SCOPE_AGENT);
  }
  __syncthreads();
}

__global__ __launch_bounds__(768, 3) void k3_mlp(const u16* wg, const u16* wu, const u16* wd, char* ws, u16* out) {
  extern __shared__ __attribute__((aligned(16))) char sm[];
  const int b = blockIdx.x, j = b >> 3;
  const int t = threadIdx.x, w12 = __builtin_amdgcn_readfirstlane(t >> 6), w = w12 & 3, lane = t & 63, hh = lane >> 5;
  const u32 lb0 = ldsaddr(sm);
  const int q = (lane & 15) >> 2, p = lane & 3, g = lane >> 4;
  const int lt = 4 * (b & 7) + (j & 3), dt = j >> 2;
  auto kfirst = [&](int kq) { const u32 k = 22 * kq - (kq == 3) + 3 * dt; return min(k, k - NU); };
  auto knext = [](u32 k) { return min(k + 1, k + 1 - NU); };
  const rsrc_t rd = mkrsrc(wd, DF * D * 2);
  const u32 vd = (lane >> 3) * 1024 + 128 * dt + 16 * ((lane & 7) ^ (4 * ((lane >> 4) & 1)));
  volatile LAS u32* fl = (volatile LAS u32*)(size_t)(lb0 + G2_FL);
  auto fill2 = [&](int i0, int n, int slot0) {
    u32 kd = kfirst(w) + i0;
    kd = min(kd, kd - NU);
#pragma unroll 1
    for (int i = 0; i < n; ++i) {
      glds16<0>(lb0 + (G2_RES * w + slot0 + i) * 2048, vd, rd, kd * 16384);
      glds16<0>(lb0 + (G2_RES * w + slot0 + i) * 2048 + 1024, vd, rd, kd * 16384 + 8192);
      kd = knext(kd);
    }
  };
  {
    const int rt = 4 * ((b & 7) >> 1) + (j & 3), cg = (j >> 2) + 8 * (b & 1) - 1;
    if (t == 0) fl[0] = 0u;
    if (cg < 0) {
      g1_stats(sm, rt, ws);
    } else if (w12 >= 8) {
      const rsrc_t rx = mkrsrc(ws + WS_X1T, D * L * 2), rg = mkrsrc(wg, D * DF * 2), ru = mkrsrc(wu, D * DF * 2);
      const u32 vx = (4 * w + g) * 4096 + 256 * rt + 16 * ((lane & 15) ^ ((g << 2) | w));
      u32 vw[3];
#pragma unroll
      for (int i = 0; i < 3; ++i) {
        const int pc = (3 * (w & 1) + i) * 64 + lane, row = pc / 12, chk = pc - 12 * row;
        vw[i] = row * (DF * 2) + min(192 * cg + 16 * (chk ^ (w & 2)), DF * 2 - 16);
      }
      const rsrc_t rwm = w < 2 ? rg : ru;
      auto stage = [&](int i, int slot) {
        const u32 m = lb0 + slot * G1_SLOT;
        glds16<0>(m + 1024 * w, vx, rx, i * 131072);
        glds16<0>(m + 4096 + 1024 * w, vx, rx, i * 131072 + 65536);
#pragma unroll
        for (int k = 0; k < 3; ++k) glds16<0>(m + 8192 + 3072 * w + 1024 * k, vw[k], rwm, i * (32 * DF * 2));
      };
      asm volatile("s_nop 4");
      static_assert(G1_LEAD + 2 <= G1_NS, "ring too short");
#pragma unroll 1
      for (int i = 0; i < G1_LEAD - 1; ++i) stage(i, i);
      int slot = G1_LEAD - 1;
#pragma unroll 1
      for (int i = 0; i < 17 - G1_LEAD; ++i) {
        stage(i + G1_LEAD - 1, slot);
        slot = slot == G1_NS - 1 ? 0 : slot + 1;
        vmwait<5 * (G1_LEAD - 1)>();
        rawbar();
      }
      const u32* st2 = (const u32*)(ws + WS_ST2) + 128 * rt + 32 * w + (lane & 31);
      u32 v;
      asm volatile("global_load_dword %0, %1, off sc1" : "=v"(v) : "v"(st2));
      sfor<17 - G1_LEAD, 16>([&](auto i) { vmwait<5 * (15 - i) + 1>(); rawbar(); });
      asm volatile("s_waitcnt vmcnt(0)" : "+v"(v));
#pragma unroll 1
      for (int spin = 0; spin < (1 << 16) && !__all(v != 0u); ++spin) {
        __builtin_amdgcn_s_sleep(2);
        v = __hip_atomic_load(st2, __ATOMIC_RELAXED, __HIP_MEMORY_SCOPE_AGENT);
      }
      ((u32*)(sm + G1_SV))[32 * w + (lane & 31)] = v;
      bar();
      bar();
    } else if (w12 >= 4) {
      const char* pf = (const char*)wd + j * (43 * 1024) + w * (43 * 256);
#pragma unroll 1
      for (int i = 0; i < 18; ++i) {
        rawbar();
      }
    } else {
      const int wl = w & 1, wu3 = 3 * (w >> 1), u0 = 6 * cg + wu3;
      u32 xa[2][2], wa[3];
#pragma unroll
      for (int c = 0; c < 2; ++c)
#pragma unroll
        for (int tt = 0; tt < 2; ++tt)
          xa[c][tt] = lb0 + 256 * (8 * hh + 4 * tt + q) + 8 * (p & 1) + 16 * ((4 * (2 * wl + c) + 2 * (g & 1) + (p >> 1)) ^ ((q << 2) | (2 * hh + tt)));
#pragma unroll
      for (int v = 0; v < 3; ++v) wa[v] = lb0 + 8192 + 6144 * (g & 1) + 192 * (8 * hh + q) + 16 * ((2 * (wu3 + v) + (p >> 1)) ^ (2 * (g & 1))) + 8 * (p & 1);
      u4 ex[3];
#pragma unroll
      for (int v = 0; v < 3; ++v) ex[v] = *(const u4*)(ws + WS_EX + min(u0 + v, NU - 1) * 1024 + lane * 16);
      f16v acc[6];
#pragma unroll
      for (int i = 0; i < 6; ++i)
#pragma unroll
        for (int e = 0; e < 16; ++e) acc[i][e] = 0.f;
#pragma unroll
      for (int i = 0; i < 6; ++i) asm volatile("" : "+v"(acc[i]));
      rawbar();
      {
        u32 c0, c1, c2;
        asm volatile(
          "ds_read_b64_tr_b16 v[112:113], %6 offset:0\n"
          "ds_read_b64_tr_b16 v[114:115], %7 offset:0\n"
          "ds_read_b64_tr_b16 v[120:121], %10 offset:0\n"
          "ds_read_b64_tr_b16 v[122:123], %10 offset:768\n"
          "ds_read_b64_tr_b16 v[116:117], %8 offset:0\n"
          "ds_read_b64_tr_b16 v[118:119], %9 offset:0\n"
          "ds_read_b64_tr_b16 v[124:125], %11 offset:0\n"
          "ds_read_b64_tr_b16 v[126:127], %11 offset:768\n"
          "ds_read_b64_tr_b16 v[128:129], %12 offset:0\n"
          "ds_read_b64_tr_b16 v[130:131], %12 offset:768\n"
          "s_mov_b32 %13, 16\n"
          "s_mov_b32 %14, 0\n"
          "Lg1%=:\n"
          "s_add_u32 %14, %14, 1\n"
          "s_cmp_eq_u32 %14, 5\n"
          "s_cselect_b32 %15, -81920, %16\n"
          "s_cselect_b32 %14, 0, %14\n"
          "s_waitcnt lgkmcnt(6)\n"
          "v_mfma_f32_32x32x16_bf16 %0, v[120:123], v[112:115], %0\n"
          "ds_read_b64_tr_b16 v[132:133], %6 offset:4096\n"
          "ds_read_b64_tr_b16 v[134:135], %7 offset:4096\n"
          "s_waitcnt lgkmcnt(6)\n"
          "v_mfma_f32_32x32x16_bf16 %1, v[120:123], v[116:119], %1\n"
          "ds_read_b64_tr_b16 v[140:141], %10 offset:3072\n"
          "ds_read_b64_tr_b16 v[142:143], %10 offset:3840\n"
          "s_waitcnt lgkmcnt(6)\n"
          "v_mfma_f32_32x32x16_bf16 %2, v[124:127], v[112:115], %2\n"
          "ds_read_b64_tr_b16 v[136:137], %8 offset:4096\n"
          "ds_read_b64_tr_b16 v[138:139], %9 offset:4096\n"
          "v_mfma_f32_32x32x16_bf16 %3, v[124:127], v[116:119], %3\n"
          "ds_read_b64_tr_b16 v[144:145], %11 offset:3072\n"
          "ds_read_b64_tr_b16 v[146:147], %11 offset:3840\n"
          "s_waitcnt lgkmcnt(8)\n"
          "v_mfma_f32_32x32x16_bf16 %4, v[128:131], v[112:115], %4\n"
          "ds_read_b64_tr_b16 v[148:149], %12 offset:3072\n"
          "ds_read_b64_tr_b16 v[150:151], %12 offset:3840\n"
          "v_add_u32 %6, %15, %6\n"
          "v_add_u32 %7, %15, %7\n"
          "v_add_u32 %8, %15, %8\n"
          "v_add_u32 %9, %15, %9\n"
          "v_mfma_f32_32x32x16_bf16 %5, v[128:131], v[116:119], %5\n"
          "v_add_u32 %10, %15, %10\n"
          "v_add_u32 %11, %15, %11\n"
          "v_add_u32 %12, %15, %12\n"
          "s_barrier\n"
          "s_waitcnt lgkmcnt(6)\n"
          "v_mfma_f32_32x32x16_bf16 %0, v[140:143], v[132:135], %0\n"
          "ds_read_b64_tr_b16 v[112:113], %6 offset:0\n"
          "ds_read_b64_tr_b16 v[114:115], %7 offset:0\n"
          "s_waitcnt lgkmcnt(6)\n"
          "v_mfma_f32_32x32x16_bf16 %1, v[140:143], v[136:139], %1\n"
          "ds_read_b64_tr_b16 v[120:121], %10 offset:0\n"
          "ds_read_b64_tr_b16 v[122:123], %10 offset:768\n"
          "s_waitcnt lgkmcnt(6)\n"
          "v_mfma_f32_32x32x16_bf16 %2, v[144:147], v[132:135], %2\n"
          "ds_read_b64_tr_b16 v[116:117], %8 offset:0\n"
          "ds_read_b64_tr_b16 v[118:119], %9 offset:0\n"
          "v_mfma_f32_32x32x16_bf16 %3, v[144:147], v[136:139], %3\n"
          "ds_read_b64_tr_b16 v[124:125], %11 offset:0\n"
          "ds_read_b64_tr_b16 v[126:127], %11 offset:768\n"
          "s_waitcnt lgkmcnt(8)\n"
          "v_mfma_f32_32x32x16_bf16 %4, v[148:151], v[132:135], %4\n"
          "ds_read_b64_tr_b16 v[128:129], %12 offset:0\n"
          "ds_read_b64_tr_b16 v[130:131], %12 offset:768\n"
          "s_sub_u32 %13, %13, 1\n"
          "s_cmp_lg_u32 %13, 0\n"
          "v_mfma_f32_32x32x16_bf16 %5, v[148:151], v[136:139], %5\n"
          "s_cbranch_scc1 Lg1%=\n"
          "s_waitcnt lgkmcnt(0)\n"
          : "+v"(acc[0]), "+v"(acc[1]), "+v"(acc[2]), "+v"(acc[3]), "+v"(acc[4]), "+v"(acc[5]), "+v"(xa[0][0]), "+v"(xa[0][1]), "+v"(xa[1][0]), "+v"(xa[1][1]),
            "+v"(wa[0]), "+v"(wa[1]), "+v"(wa[2]), "=&s"(c0), "=&s"(c1), "=&s"(c2)
          : "s"(G1_SLOT) : "memory", "scc", "v112", "v113", "v114", "v115", "v116", "v117", "v118", "v119", "v120", "v121", "v122", "v123", "v124", "v125", "v126", "v127", "v128", "v129", "v130", "v131", "v132", "v133", "v134", "v135", "v136", "v137", "v138", "v139", "v140", "v141", "v142", "v143", "v144", "v145", "v146", "v147", "v148", "v149", "v150", "v151");
      }
      u32 sv[2];
#pragma unroll
      for (int c = 0; c < 2; ++c) sv[c] = ((const u32*)(sm + G1_SV))[64 * wl + 32 * c + (lane & 31)];
      bar();
#pragma unroll
      for (int c = 0; c < 2; ++c) {
        const u32 bv = hh ? __builtin_amdgcn_perm(sv[c], sv[c], 0x03020302u) : (__builtin_amdgcn_perm(sv[c], sv[c], 0x01000100u) ^ 0x80008000u);
#pragma unroll
        for (int v = 0; v < 3; ++v) acc[2 * v + c] = mfma32(ex[v], (u4){bv, bv, bv, bv}, acc[2 * v + c]);
      }
      char* hp = ws + WS_HH + (4 * rt + 2 * wl) * HH_T + u0 * 1024 + 16 * lane;
#pragma unroll
      for (int v = 0; v < 3; ++v) {
        if (u0 + v < NU) {
#pragma unroll
          for (int c = 0; c < 2; ++c) {
            const float sd = bhi(sv[c]);
            const u4 o = g1_tile(acc[2 * v + c], __builtin_amdgcn_rcpf(sd), sd);
            char* dst = hp + c * HH_T + v * 1024;
            asm volatile("global_store_dwordx4 %0, %1, off sc1\n\ts_nop 1" :: "v"(dst), "v"(o) : "memory");
          }
        }
      }
      {
        asm volatile("s_waitcnt vmcnt(0)" ::: "memory");
        if (lane == 0 && __hip_atomic_fetch_add((LAS u32*)fl, 1u, __ATOMIC_RELAXED, __HIP_MEMORY_SCOPE_WORKGROUP) == 3u)
          __hip_atomic_fetch_add((u32*)(ws + WS_CNT) + 64 * rt, 1u, __ATOMIC_RELAXED, __HIP_MEMORY_SCOPE_AGENT);
      }
    }
  }
  if (w12 >= 8) {
    asm volatile("s_nop 4");
    fill2(G2_RES, 3, 0);
    fill2(3, G2_RES - 3, 3);
    vmwait<2 * (G2_RES - 3)>();
    rawbar();
    rawbar();
    fill2(0, 3, 0);
    vmwait<0>();
    rawbar();
    rawbar();
    rawbar();
  } else {
    const int wl = w12 & 1, kq = w12 >> 1, dc = 64 * dt + 16 * kq + 4 * hh;
    f4 gb[2], xr[2];
#pragma unroll
    for (int e = 0; e < 2; ++e) {
      gb[e] = *(const f4*)((const float*)(ws + WS_MOD) + 5 * D + dc + 8 * e);
      xr[e] = *(const f4*)((const float*)(ws + WS_INV) + dc + 8 * e);
#pragma unroll
      for (int i = 0; i < 4; ++i) xr[e][i] *= b2f(((const u16*)(ws + WS_X1T))[(dc + 8 * e + i) * L + 64 * lt + 32 * wl + (lane & 31)]);
    }
    u32 pa[2];
#pragma unroll
    for (int e = 0; e < 2; ++e)
      pa[e] = lb0 + G2_RES * 2048 * kq + 128 * (4 * hh + q) + 16 * ((((4 * e + 2 * (g & 1)) ^ (2 * kq)) + (p >> 1)) ^ (4 * (q >> 1))) + 8 * (p & 1);
    f16v acc[2];
#pragma unroll
    for (int e = 0; e < 16; ++e) acc[0][e] = acc[1][e] = 0.f;
    asm volatile("" : "+v"(xr[0]), "+v"(xr[1]), "+v"(gb[0]), "+v"(gb[1]), "+v"(acc[0]), "+v"(acc[1]));
    rawbar();
    u4 A[2][2], AR[3][2];
#pragma unroll
    for (int i = 0; i < 6; ++i) AR[i >> 1][i & 1] = cat(trd(pa[i & 1] + (i >> 1) * 2048), trd(pa[i & 1] + (i >> 1) * 2048 + 1024));
    asm volatile("s_waitcnt lgkmcnt(0)\n\ts_barrier" : "+v"(AR[0][0]), "+v"(AR[0][1]), "+v"(AR[1][0]), "+v"(AR[1][1]), "+v"(AR[2][0]), "+v"(AR[2][1]) :: "memory");
    if (w12 == 0) {
      const u32* cnt = (const u32*)(ws + WS_CNT) + 64 * (lt >> 1);
#pragma unroll 1
      for (int spin = 0; spin < (1 << 18); ++spin)
        if (__hip_atomic_load(cnt, __ATOMIC_RELAXED, __HIP_MEMORY_SCOPE_AGENT) >= 15u) break;
    }
    rawbar();
    const rsrc_t rh = mkrsrc(ws + WS_HH + (2 * lt + wl) * HH_T, HH_T);
    constexpr int LEAD = G2_LEAD, NH = LEAD + 2;
    u4 H[NH];
    u32 kh = kfirst(kq);
    auto ldh = [&](auto i) { if constexpr (i < 22) { bld16_sc1<0>(H[i % NH], lane * 16, rh, kh * 1024); kh = knext(kh); } };
    sfor<0, LEAD>(ldh);
    auto rda = [&](auto i) {
#pragma unroll
      for (int e = 0; e < 2; ++e) A[i & 1][e] = cat(trd(pa[e] + i * 2048), trd(pa[e] + i * 2048 + 1024));
    };
    rda(std::integral_constant<int, 0>{});
    sfor<0, 22>([&](auto i) {
      vmwait<(i < 22 - LEAD ? LEAD - 1 : 21 - i)>(H[i % NH]);
      SB();
      if constexpr (i + 1 < G2_RES) rda(std::integral_constant<int, i + 1>{});
      if (i < 21 || kq < 2) {
#pragma unroll
        for (int e = 0; e < 2; ++e) acc[e] = mfma32(i < G2_RES ? A[i & 1][e] : AR[i < G2_RES ? 0 : i - G2_RES][e], H[i % NH], acc[e]);
      }
      if constexpr (i + 1 < G2_RES) { SGB(0x8, 1); SGB(0x100, 2); SGB(0x8, 1); SGB(0x100, 2); }
      SB();
      ldh(std::integral_constant<int, i + LEAD>{});
      if constexpr (i == G2_RES - 1) rawbar();
    });
    f4* red = (f4*)sm + wl * 1536 + lane;
#pragma unroll
    for (int k = 1; k < 4; ++k)
#pragma unroll
      for (int e = 0; e < 2; ++e) {
        const f16v& a = acc[k >> 1];
        const int r = 8 * (k & 1) + 4 * e;
        red[(kq ^ k) * 384 + (k - 1) * 128 + 64 * e] = (f4){a[r], a[r + 1], a[r + 2], a[r + 3]};
      }
    bar();
    u2 o[2];
#pragma unroll
    for (int e = 0; e < 2; ++e) {
      const f4* rp = red + kq * 384 + 64 * e;
      const f4 y = ((f4){acc[0][4 * e], acc[0][4 * e + 1], acc[0][4 * e + 2], acc[0][4 * e + 3]} + rp[0] + rp[128] + rp[256]) * gb[e] + xr[e];
      o[e] = (u2){pk(y[0], y[1]), pk(y[2], y[3])};
    }
    const auto r0 = __builtin_amdgcn_permlane32_swap(o[0].x, o[1].x, false, false);
    const auto r1 = __builtin_amdgcn_permlane32_swap(o[0].y, o[1].y, false, false);
    *(u4*)(out + (64 * lt + 32 * wl + (lane & 31)) * D + 64 * dt + 16 * kq + 8 * hh) = (u4){r0[0], r1[0], r0[1], r1[1]};
  }
}

extern "C" void kernel_launch(void* const* d_in, const int* in_sizes, int n_in, void* d_out, int out_size, void* d_ws, size_t ws_size,
                              hipStream_t stream) {
  const u16* x = (const u16*)d_in[0];
  const u16 *wg = (const u16*)d_in[8], *wu = (const u16*)d_in[9], *wd = (const u16*)d_in[10];
  char* ws = (char*)d_ws + (size_t(240)  __H0__ );
  (void)hipFuncSetAttribute((const void*)k2_conv_prep, hipFuncAttributeMaxDynamicSharedMemorySize, K2_LDS);
  (void)hipFuncSetAttribute((const void*)k3_mlp, hipFuncAttributeMaxDynamicSharedMemorySize, K3_LDS);
  k1_prep<<<496, 256, 0, stream>>>(x, (const u16*)d_in[1], (const u16*)d_in[2], (const u16*)d_in[3], (const u16*)d_in[4], (const u16*)d_in[5],
                                   (const u16*)d_in[6], (const u16*)d_in[11], (const u16*)d_in[12], ws);
  k2_conv_prep<<<256, 512, K2_LDS, stream>>>(wg, wu, wd, ws);
  k3_mlp<<<256, 768, K3_LDS, stream>>>(wg, wu, wd, ws, (u16*)d_out);
}
```

```cpp
#include <hip/hip_runtime.h>
#include <hip/hip_bf16.h>
#include <stdint.h>

__device__ unsigned long long g_stamps[2 * 4 * 512 * 8 * 8];
struct Stamps { unsigned long long r[8], c[8]; };
#define STAMP(kid, idx) do { __builtin_amdgcn_sched_barrier(0); asm volatile("s_memrealtime %0\n s_memtime %1\n s_waitcnt lgkmcnt(0)" : "=s"(ts_.r[idx]), "=s"(ts_.c[idx]) :: "memory"); __builtin_amdgcn_sched_barrier(0); } while (0)
#define STAMPV(kid, idx) do { asm volatile("s_waitcnt vmcnt(0)" ::: "memory"); STAMP(kid, idx); \
  if ((threadIdx.x & 63) == 0 && (blockDim.x < 768 || (threadIdx.x >> 6) < 4 || (threadIdx.x >> 6) >= 8)) { for (int i_ = 0; i_ < 8; ++i_) { g_stamps[(((kid) * 512 + blockIdx.x) * 8 + ((threadIdx.x >> 6) >= 8 ? (threadIdx.x >> 6) - 4 : (threadIdx.x >> 6))) * 8 + i_] = ts_.r[i_]; \
     g_stamps[4 * 512 * 64 + (((kid) * 512 + blockIdx.x) * 8 + ((threadIdx.x >> 6) >= 8 ? (threadIdx.x >> 6) - 4 : (threadIdx.x >> 6))) * 8 + i_] = ts_.c[i_]; } } } while (0)
#include <type_traits>

typedef unsigned short u16;
typedef unsigned u32;
typedef float f2 __attribute__((ext_vector_type(2)));
typedef float f4 __attribute__((ext_vector_type(4)));
typedef float f16v __attribute__((ext_vector_type(16)));
typedef u32 u2 __attribute__((ext_vector_type(2)));
typedef u32 u4 __attribute__((ext_vector_type(4)));
typedef u4 u4a8 __attribute__((aligned(8)));
typedef short s4 __attribute__((ext_vector_type(4)));
typedef __bf16 bf2 __attribute__((ext_vector_type(2)));
typedef __bf16 bf8 __attribute__((ext_vector_type(8)));
typedef _Float16 h2 __attribute__((ext_vector_type(2)));
typedef _Float16 h8 __attribute__((ext_vector_type(8)));
#define LAS __attribute__((address_space(3)))
#define DI __device__ __forceinline__

constexpr int L = 2048, D = 512, NM = 128, DF = 1376, NU = 86;
constexpr int KXS = 2056;

constexpr size_t WS_MOD = 0, WS_MEAN = 16 << 10, WS_SD = 20 << 10, WS_CV = 24 << 10, WS_INV = 26 << 10, WS_ST2 = 32 << 10, WS_CNT = 40 << 10,
                 WS_EX = 64 << 10, WS_XT = 256 << 10, WS_KX = WS_XT + (2 << 20), WS_X1T = WS_KX + (2112 << 10), WS_HH = WS_X1T + (2 << 20), WS_END = WS_HH + (5504 << 10);

template <class T> DI T ldnt(const T* p) { return __builtin_nontemporal_load(p); }
DI float blo(u32 v) { return __uint_as_float(v << 16); }
DI float bhi(u32 v) { return __uint_as_float(v & 0xffff0000u); }
DI float b2f(u16 v) { return __uint_as_float(((u32)v) << 16); }
DI u32 pk(float a, float b) { return __builtin_bit_cast(u32, __builtin_convertvector((f2){a, b}, bf2)); }
DI u32 pkh(f2 v) { return __builtin_bit_cast(u32, __builtin_convertvector(v, h2)); }
DI float rb(float a) { return blo(pk(a, a)); }
DI u2 trd(const char* p) { return __builtin_bit_cast(u2, __builtin_amdgcn_ds_read_tr16_b64_v4i16((LAS s4*)p)); }
DI u2 trd(u32 a) { return __builtin_bit_cast(u2, __builtin_amdgcn_ds_read_tr16_b64_v4i16((LAS s4*)(size_t)a)); }
DI u4 cat(u2 a, u2 b) { return (u4){a.x, a.y, b.x, b.y}; }
DI float dot2(u32 a, u32 b, float c) { return __builtin_amdgcn_fdot2_f32_bf16(__builtin_bit_cast(bf2, a), __builtin_bit_cast(bf2, b), c, false); }
DI f4 mfma16(u4 a, u4 b, f4 c) { return __builtin_amdgcn_mfma_f32_16x16x32_bf16(__builtin_bit_cast(bf8, a), __builtin_bit_cast(bf8, b), c, 0, 0, 0); }
DI f4 mfma16h(u4 a, u4 b, f4 c) { return __builtin_amdgcn_mfma_f32_16x16x32_f16(__builtin_bit_cast(h8, a), __builtin_bit_cast(h8, b), c, 0, 0, 0); }
DI f16v mfma32(u4 a, u4 b, f16v c) { return __builtin_amdgcn_mfma_f32_32x32x16_bf16(__builtin_bit_cast(bf8, a), __builtin_bit_cast(bf8, b), c, 0, 0, 0); }
#define CM_MUL(d, a, b) "v_pk_mul_f32 " d ", " a ", " b " op_sel_hi:[0,1]\n\t"
#define CM_FMA(d, a, b) "v_pk_fma_f32 " d ", " a ", " b ", " d " op_sel:[1,1,0] op_sel_hi:[1,0,1] neg_lo:[0,1,0]\n\t"
DI void cmul2(f2& r0, f2 a0, f2 b0, f2& r1, f2 a1, f2 b1) {
  asm(CM_MUL("%0", "%2", "%3") CM_MUL("%1", "%4", "%5") CM_FMA("%0", "%2", "%3") CM_FMA("%1", "%4", "%5")
      : "=&v"(r0), "=&v"(r1) : "v"(a0), "v"(b0), "v"(a1), "v"(b1));
}
DI void cmul3(f2& r0, f2 a0, f2 b0, f2& r1, f2 a1, f2 b1, f2& r2, f2 a2, f2 b2) {
  asm(CM_MUL("%0", "%3", "%4") CM_MUL("%1", "%5", "%6") CM_MUL("%2", "%7", "%8") CM_FMA("%0", "%3", "%4") CM_FMA("%1", "%5", "%6") CM_FMA("%2", "%7", "%8")
      : "=&v"(r0), "=&v"(r1), "=&v"(r2) : "v"(a0), "v"(b0), "v"(a1), "v"(b1), "v"(a2), "v"(b2));
}
DI void cmul4(f2& r0, f2& r1, f2& r2, f2& r3, f2 a0, f2 a1, f2 a2, f2 a3, f2 b) {
  asm(CM_MUL("%0", "%4", "%8") CM_MUL("%1", "%5", "%8") CM_MUL("%2", "%6", "%8") CM_MUL("%3", "%7", "%8")
      CM_FMA("%0", "%4", "%8") CM_FMA("%1", "%5", "%8") CM_FMA("%2", "%6", "%8") CM_FMA("%3", "%7", "%8")
      : "=&v"(r0), "=&v"(r1), "=&v"(r2), "=&v"(r3) : "v"(a0), "v"(a1), "v"(a2), "v"(a3), "v"(b));
}
DI float exp_acc(float x) {
  const float t = x * 1.44269502f, r = fmaf(x, 1.44269502f, -t) + x * 1.92596299e-8f, e = __builtin_amdgcn_exp2f(t);
  return fmaf(e, r * 0.693147182f, e);
}
template <int CTRL> DI float dppf(float v) { return __int_as_float(__builtin_amdgcn_update_dpp(0, __float_as_int(v), CTRL, 0xf, 0xf, true)); }
DI float sum32(float v) {
  auto r = __builtin_amdgcn_permlane32_swap(__float_as_uint(v), __float_as_uint(v), false, false);
  return __uint_as_float(r[0]) + __uint_as_float(r[1]);
}
DI f2 ln_stats(float s1, float s2) {
  const float mean = s1 * (1.f / D);
  const float var = fmaxf(fmaf(-mean, mean, s2 * (1.f / D)), 0.f);
  return (f2){rb(mean), rb(__builtin_amdgcn_sqrtf(rb(rb(var) + 1e-5f)))};
}
typedef __amdgpu_buffer_rsrc_t rsrc_t;
DI rsrc_t mkrsrc(const void* p, u32 bytes) { return __builtin_amdgcn_make_buffer_rsrc((void*)p, 0, bytes, 0x00020000); }
DI u32 ldsaddr(const char* p) { return (u32)(size_t)(LAS const char*)p; }
template <int OFF> DI void glds16(u32 m0v, u32 voff, rsrc_t rs, u32 soff) {
  asm volatile("s_mov_b32 m0, %0\n\ts_nop 0\n\tbuffer_load_dwordx4 %1, %2, %3 offen offset:%4 lds" :: "s"(m0v), "v"(voff), "s"(rs), "s"(soff), "n"(OFF) : "memory");
}
DI void glds16x4(u32 m0v, u32 voff, rsrc_t rs, u32 soff) {
  asm volatile("s_mov_b32 m0, %0\n\ts_nop 0\n\tbuffer_load_dwordx4 %1, %2, %3 offen lds\n\tbuffer_load_dwordx4 %1, %2, %3 offen offset:1024 lds\n\t"
               "buffer_load_dwordx4 %1, %2, %3 offen offset:2048 lds\n\tbuffer_load_dwordx4 %1, %2, %3 offen offset:3072 lds"
               :: "s"(m0v), "v"(voff), "s"(rs), "s"(soff) : "memory");
}
template <int OFF> DI void bld16(u4& d, u32 voff, rsrc_t rs, u32 soff) {
  asm volatile("buffer_load_dwordx4 %0, %1, %2, %3 offen offset:%4" : "=v"(d) : "v"(voff), "s"(rs), "s"(soff), "n"(OFF));
}
template <int OFF> DI void bld16_sc1(u4& d, u32 voff, rsrc_t rs, u32 soff) {
  asm volatile("buffer_load_dwordx4 %0, %1, %2, %3 offen offset:%4 sc1" : "=v"(d) : "v"(voff), "s"(rs), "s"(soff), "n"(OFF));
}
template <int OFF, class T> DI void gld16(T& d, const void* p) { asm volatile("global_load_dwordx4 %0, %1, off offset:%2" : "=v"(d) : "v"(p), "n"(OFF)); }
DI void pin(u32& v) { asm("" : "+v"(v)); }
DI void rawbar() { asm volatile("s_barrier" ::: "memory"); }
template <int N> DI void vmwait() { asm volatile("s_waitcnt vmcnt(%0)" :: "n"(N) : "memory"); }
template <int N> DI void vmwait(u4& d) { asm volatile("s_waitcnt vmcnt(%1)" : "+v"(d) : "n"(N)); }
DI void bar() { asm volatile("s_waitcnt lgkmcnt(0)\n\ts_barrier" ::: "memory"); }
#define SB() __builtin_amdgcn_sched_barrier(0)
#define SGB(m, n) __builtin_amdgcn_sched_group_barrier(m, n, 0)
template <int I, int N, class F> DI void sfor(F&& f) { if constexpr (I < N) { f(std::integral_constant<int, I>{}); sfor<I + 1, N>(f); } }

constexpr int KG_ROW = 544, KG_CH = 57 * KG_ROW, KG_T = 2 * KG_CH, KG_TS = 4160, K1_LDS = KG_T + 2 * KG_TS;

DI f2 cexp_pow2(float a, float fh, float fl, float p) {
  const float e = __builtin_amdgcn_exp2f(a * p);
  const float r = fmaf(fl, p, __builtin_amdgcn_fractf(fh * p));
  return (f2){e * __builtin_amdgcn_cosf(r), e * __builtin_amdgcn_sinf(r)};
}

DI void k1_kgen(Stamps& ts_, char* sm, int b, const u16* lar, const u16* aim, const u16* cre, const u16* cim, const u16* ldt, const u16* dsk, u16* kx) {
  const int t = threadIdx.x, w = __builtin_amdgcn_readfirstlane(t >> 6), lane = t & 63, ch = w >> 1, half = w & 1;
  const int d0 = 64 * (b & 7) + 2 * (b >> 3), d = d0 + ch, n = 64 * half + lane;
  const u32 v_lar = ldnt(lar + d * NM + n), v_aim = ldnt(aim + d * NM + n);
  const u32 cr = ldnt(cre + d * NM + n), ci = ldnt(cim + d * NM + n), cr2 = ldnt(cre + d * NM + (n ^ 64)), ci2 = ldnt(cim + d * NM + (n ^ 64));
  const float dt = rb(exp_acc(b2f(ldt[d])));
  const float ds = b2f(dsk[d]);
  const float zr = -rb(exp_acc(blo(v_lar))) * dt, zi = blo(v_aim) * dt;
  const float a = zr * 1.44269502f;
  const float fh = zi * 0.159154937f, fl = fmaf(zi, 0.159154937f, -fh) + zi * 6.42063833e-9f;
  const f2 e1 = cexp_pow2(a, fh, fl, 1.f), e8 = cexp_pow2(a, -fh, -fl, 8.f), e128 = cexp_pow2(a, fh, fl, 128.f), e1024 = cexp_pow2(a, fh, fl, 1024.f);
  int mx = (int)max(max(cr & 0x7fffu, ci & 0x7fffu), max(cr2 & 0x7fffu, ci2 & 0x7fffu));
  mx = max(mx, __builtin_amdgcn_update_dpp(0, mx, 0x111, 0xf, 0xf, true));
  mx = max(mx, __builtin_amdgcn_update_dpp(0, mx, 0x112, 0xf, 0xf, true));
  mx = max(mx, __builtin_amdgcn_update_dpp(0, mx, 0x114, 0xf, 0xf, true));
  mx = max(mx, __builtin_amdgcn_update_dpp(0, mx, 0x118, 0xf, 0xf, true));
  mx = max(max(__builtin_amdgcn_readlane(mx, 15), __builtin_amdgcn_readlane(mx, 31)), max(__builtin_amdgcn_readlane(mx, 47), __builtin_amdgcn_readlane(mx, 63)));
  const int ex = min(max(mx >> 7, 1), 253);
  const float cs = __int_as_float((254 - ex) << 23), kf = 2.f * dt * __int_as_float(ex << 23);
  if (t < 2) *(u4*)(kx + (d0 + t) * KXS) = (u4){0u, 0u, 0u, 0u};
  constexpr u32 KEEP = 0x03020100u, LL = 0x01000100u, LH = 0x05040100u, HH = 0x05040504u;
  const u32 sel0 = t < 32 ? KEEP : LL, sel1 = t < 32 ? KEEP : t < 128 ? LH : LL;
  const u32 sel2 = t < 32 ? KEEP : t < 128 ? LL : (t & 1) ? HH : LH, sel3 = t < 32 ? KEEP : t < 64 ? LH : HH;
  char* base = sm + ch * KG_CH + n * 4;
  auto put = [&](int row, f2 v) { *(u32*)(base + row * KG_ROW) = pkh(v); };
  f2 lo[8], e[8], m[8], x0, x1, x2, x3, e16, e32, e64;
  lo[0] = (f2){blo(cr) * cs, -blo(ci) * cs};
  e[1] = e1; m[1] = e128;
  put(0, lo[0]); put(16, (f2){1.f, 0.f}); put(17, e1); put(24, e128); put(48, e1024);
  cmul3(e16, e8, e8, e[2], e1, e1, m[2], e128, e128);
  cmul2(lo[1], lo[0], e8, x0, e1024, e1024);
  put(18, e[2]); put(32, m[2]); put(1, lo[1]); put(56, x0);
  cmul3(e32, e16, e16, e[4], e[2], e[2], m[4], m[2], m[2]);
  cmul2(e[3], e[2], e1, m[3], m[2], e128);
  cmul2(lo[2], lo[0], e16, lo[3], lo[1], e16);
  put(20, e[4]); put(40, m[4]); put(19, e[3]); put(36, m[3]); put(2, lo[2]); put(3, lo[3]);
  cmul4(e[5], e[6], e[7], x0, e1, e[2], e[3], m[4], e[4]);
  put(21, e[5]); put(22, e[6]); put(23, e[7]); put(41, x0);
  cmul3(m[5], m[4], e128, m[6], m[4], m[2], m[7], m[4], m[3]);
  put(42, m[5]); put(44, m[6]); put(46, m[7]);
  cmul4(lo[4], lo[5], lo[6], lo[7], lo[0], lo[1], lo[2], lo[3], e32);
  put(4, lo[4]); put(5, lo[5]); put(6, lo[6]); put(7, lo[7]);
  cmul2(e64, e32, e32, x0, e128, e1);
  put(25, x0);
  cmul3(x0, m[5], e[4], x1, m[6], e[4], x2, m[7], e[4]);
  put(43, x0); put(45, x1); put(47, x2);
  cmul4(x0, x1, x2, x3, lo[0], lo[1], lo[2], lo[3], e64);
  put(8, x0); put(9, x1); put(10, x2); put(11, x3);
  cmul4(x0, x1, x2, x3, lo[4], lo[5], lo[6], lo[7], e64);
  put(12, x0); put(13, x1); put(14, x2); put(15, x3);
  cmul4(x0, x1, x2, x3, e[2], e[3], e[4], e[5], e128);
  put(26, x0); put(27, x1); put(28, x2); put(29, x3);
  cmul2(x0, e[6], e128, x1, e[7], e128);
  put(30, x0); put(31, x1);
  cmul3(x0, e[2], m[2], x1, e[4], m[2], x2, e[6], m[2]);
  put(33, x0); put(34, x1); put(35, x2);
  cmul3(x0, e[2], m[3], x1, e[4], m[3], x2, e[6], m[3]);
  put(37, x0); put(38, x1); put(39, x2);
  cmul4(x0, x1, x2, x3, m[1], m[2], m[3], m[4], e1024);
  put(49, x0); put(50, x1); put(51, x2); put(52, x3);
  cmul3(x0, m[5], e1024, x1, m[6], e1024, x2, m[7], e1024);
  put(53, x0); put(54, x1); put(55, x2);

  __syncthreads();
  {
    const int c = lane & 15, g = lane >> 4;
    const char* fb = sm + ch * KG_CH + c * KG_ROW + g * 16;
    u16* T = (u16*)(sm + KG_T + ch * KG_TS);
    f4 acc0 = {0.f, 0.f, 0.f, 0.f}, acc1 = acc0;
    u4 B[8], A[8];
#pragma unroll
    for (int s = 0; s < 8; ++s) { B[s] = *(const u4*)(fb + s * 64); A[s] = *(const u4*)(fb + (16 + 16 * half) * KG_ROW + s * 64); }
    if (!half) {
      u4 A2[8];
#pragma unroll
      for (int s = 0; s < 8; ++s) A2[s] = *(const u4*)(fb + 48 * KG_ROW + s * 64);
      SB();
#pragma unroll
      for (int s = 0; s < 8; ++s) acc0 = mfma16h(A[s], B[s], acc0);
#pragma unroll
      for (int s = 0; s < 8; ++s) acc1 = mfma16h(A2[s], B[s], acc1);
      acc0 *= kf; acc1 *= kf;
      if (lane == 0) acc0[0] += ds;
      *(u2*)(T + 8 * c + 4 * (g & 1) + 128 * (g >> 1)) = (u2){pk(acc0[0], acc0[1]), pk(acc0[2], acc0[3])};
      if (g < 2) {
#pragma unroll
        for (int e = 0; e < 4; ++e) T[1024 + 128 * (4 * g + e) + 8 * c] = (u16)pk(acc1[e], 0.f);
      } else if (lane == 32) T[2048] = (u16)pk(acc1[0], 0.f);
    } else {
      SB();
#pragma unroll
      for (int s = 0; s < 8; ++s) acc0 = mfma16h(A[s], B[s], acc0);
      acc0 *= kf;
#pragma unroll
      for (int e = 0; e < 4; ++e) {
        const int p = g < 2 ? 256 + 128 * g + 2 * e : 512 + 128 * (2 * (g - 2) + (e >> 1)) + 4 * (e & 1);
        T[p + 8 * c] = (u16)pk(acc0[e], 0.f);
      }
    }
  }

  __syncthreads();
  u4 v[2];
  u32 e8v[2];
#pragma unroll
  for (int c2 = 0; c2 < 2; ++c2) {
    const u16* T = (const u16*)(sm + KG_T + c2 * KG_TS);
    v[c2] = *(const u4*)(T + 8 * t);
    e8v[c2] = T[8 * t + 8];
  }
#pragma unroll
  for (int c2 = 0; c2 < 2; ++c2) {
    const u4 o = {__builtin_amdgcn_perm(v[c2].x, v[c2].x, sel0), __builtin_amdgcn_perm(v[c2].z, t < 64 ? v[c2].y : v[c2].x, sel1),
                  __builtin_amdgcn_perm(e8v[c2], t < 128 ? v[c2].z : v[c2].x, sel2), __builtin_amdgcn_perm(e8v[c2], v[c2].w, sel3)};
    *(u4*)(kx + (d0 + c2) * KXS + 8 + 8 * t) = o;
  }
}

DI void k1_xt(Stamps& ts_, char* sm, int b, const u16* x, u16* xt) {
  const int t = threadIdx.x, w = t >> 6, lane = t & 63;
  const int c0 = 64 * (b & 7), r0 = 128 * (b >> 3);
  u4 v[4];
#pragma unroll
  for (int i = 0; i < 4; ++i) v[i] = ldnt((const u4*)(x + (r0 + 32 * i + (t >> 3)) * D + c0 + 8 * (t & 7)));
#pragma unroll
  for (int i = 0; i < 4; ++i) *(u4*)(sm + (32 * i + (t >> 3)) * 144 + 16 * (t & 7)) = v[i];

  __syncthreads();
  const int g = lane >> 4, q = (lane & 15) >> 2, p = lane & 3;
#pragma unroll
  for (int i = 0; i < 4; ++i) {
    const char* a = sm + (32 * i + 8 * g + q) * 144 + 32 * w + 8 * p;
    *(u4*)(xt + (c0 + 16 * w + (lane & 15)) * L + r0 + 32 * i + 8 * g) = cat(trd(a), trd(a + 4 * 144));
  }
}

DI void k1_stats(Stamps& ts_, int b, const u16* x, char* ws) {
  const int t = threadIdx.x, row = 32 * b + (t >> 3);
  u4 v[8];
#pragma unroll
  for (int i = 0; i < 8; ++i) v[i] = ldnt((const u4*)(x + row * D + 64 * i + 8 * (t & 7)));
  float s1 = 0.f, s2 = 0.f, s3 = 0.f, s4 = 0.f;
#pragma unroll
  for (int i = 0; i < 8; ++i)
#pragma unroll
    for (int j = 0; j < 4; j += 2) {
      s1 = dot2(v[i][j], 0x3f803f80u, s1); s2 = dot2(v[i][j], v[i][j], s2);
      s3 = dot2(v[i][j + 1], 0x3f803f80u, s3); s4 = dot2(v[i][j + 1], v[i][j + 1], s4);
    }
  s1 += s3; s2 += s4;
  s1 += dppf<0xb1>(s1); s2 += dppf<0xb1>(s2); s1 += dppf<0x4e>(s1); s2 += dppf<0x4e>(s2); s1 += dppf<0x141>(s1); s2 += dppf<0x141>(s2);
  const f2 st = ln_stats(s1, s2);
  if ((t & 7) == 0) {
    ((u16*)(ws + WS_MEAN))[row] = (u16)(__float_as_uint(st.x) >> 16);
    ((u16*)(ws + WS_SD))[row] = (u16)(__float_as_uint(st.y) >> 16);
    ((u32*)(ws + WS_ST2))[row] = 0u;
  }
  if (t == 0 && b < 16) ((u32*)(ws + WS_CNT))[64 * b] = 0u;
}

DI void k1_mod(Stamps& ts_, char* sm, int b, const u16* c, const u16* wm, char* ws) {
  const int t = threadIdx.x, rg = t >> 3;
  u4 v[16];
  const u32 cc = ((const u32*)c)[t];
#pragma unroll
  for (int i = 0; i < 16; ++i) v[i] = ldnt((const u4*)(wm + (16 * rg + i) * (6 * D) + 64 * b + 8 * (t & 7)));
  u32* sl = (u32*)(sm + 32 * 68 * 4);
  {
    const float c0 = blo(cc), c1 = bhi(cc);
    sl[t] = pk(c0 / (1.f + __expf(-c0)), c1 / (1.f + __expf(-c1)));
  }
  __syncthreads();
  const u4 sa = *(const u4*)(sl + 8 * rg), sb = *(const u4*)(sl + 8 * rg + 4);
  float acc[8] = {0.f, 0.f, 0.f, 0.f, 0.f, 0.f, 0.f, 0.f};
#pragma unroll
  for (int i = 0; i < 8; ++i) {
    const u32 sp = i < 4 ? sa[i & 3] : sb[i & 3];
#pragma unroll
    for (int j = 0; j < 4; ++j) {
      acc[2 * j] = dot2(__builtin_amdgcn_perm(v[2 * i + 1][j], v[2 * i][j], 0x05040100u), sp, acc[2 * j]);
      acc[2 * j + 1] = dot2(__builtin_amdgcn_perm(v[2 * i + 1][j], v[2 * i][j], 0x07060302u), sp, acc[2 * j + 1]);
    }
  }
  float* red = (float*)sm;
  *(f4*)(red + rg * 68 + 8 * (t & 7)) = (f4){acc[0], acc[1], acc[2], acc[3]};
  *(f4*)(red + rg * 68 + 8 * (t & 7) + 4) = (f4){acc[4], acc[5], acc[6], acc[7]};

  __syncthreads();
  float s = 0.f;
#pragma unroll
  for (int i = 0; i < 8; ++i) s += red[((t & 3) + 4 * i) * 68 + (t >> 2)];
  s += dppf<0xb1>(s); s += dppf<0x4e>(s);
  if ((t & 3) == 0) {
    const int j = 64 * b + (t >> 2), part = b >> 3;
    float v = rb(s);
    if (part == 1 || part == 4) v = rb(1.f + v);
    if (part == 4) {
      v = v == 0.f ? 0x1p-60f : v;
      ((float*)(ws + WS_INV))[j - 4 * D] = 1.f / v;
    }
    if (part == 3 || part == 4) ((u16*)(ws + WS_CV))[j - 3 * D] = (u16)(__float_as_uint(v) >> 16);
    ((float*)(ws + WS_MOD))[j] = v;
  }
}

__global__ __launch_bounds__(256, 2) void k1_prep(const u16* x, const u16* lar, const u16* aim, const u16* cre, const u16* cim, const u16* ldt,
                                                  const u16* dsk, const u16* c, const u16* wm, char* ws) {
  __shared__ __attribute__((aligned(16))) char sm[K1_LDS];
  const int b = blockIdx.x;
  Stamps ts_ = {}; STAMP(0,0);
  if (b < 256) k1_kgen(ts_, sm, b, lar, aim, cre, cim, ldt, dsk, (u16*)(ws + WS_KX));
  else if (b < 384) k1_xt(ts_, sm, b - 256, x, (u16*)(ws + WS_XT));
  else if (b < 448) k1_stats(ts_, b - 384, x, ws);
  else k1_mod(ts_, sm, b - 448, c, wm, ws);

  STAMPV(0,7);
}

constexpr int CV_KC = 4128, CV_H = 8 * CV_KC, CV_HP = 1152, CV_X = CV_H + 8 * CV_HP, CV_EX = CV_X + 8192, CV_W = CV_EX + 16384,
              CV_FL = CV_W + 16384, K2_LDS = CV_FL + 16;

DI void k2_conv(Stamps& ts_, char* sm, int b, char* ws) {
  const int t = threadIdx.x, w = __builtin_amdgcn_readfirstlane(t >> 6), lane = t & 63, ch = w >> 1, half = w & 1;
  const int d0 = 64 * (b & 7) + 2 * (b >> 3), d = d0 + ch;
  const u16* xt = (const u16*)(ws + WS_XT);
  const float* mod = (const float*)(ws + WS_MOD);
  const int c = lane & 15, g = lane >> 4;
  u4 xv[2];
  const u4 mv = *(const u4*)(ws + WS_MEAN + 16 * t), sv = *(const u4*)(ws + WS_SD + 16 * t);
#pragma unroll
  for (int i = 0; i < 2; ++i) xv[i] = *(const u4*)(xt + (d0 + i) * L + 8 * t);
  const float ga = mod[2 * D + d], c2 = mod[4 * D + d];
  const f2 c1 = *(const f2*)(mod + D + d0), sa = *(const f2*)(mod + d0);
  SB();
  float rs[8];
#pragma unroll
  for (int j = 0; j < 8; ++j) rs[j] = __builtin_amdgcn_rcpf((j & 1) ? bhi(sv[j >> 1]) : blo(sv[j >> 1]));
#pragma unroll
  for (int i = 0; i < 2; ++i) {
    float h[8];
#pragma unroll
    for (int j = 0; j < 8; ++j) {
      const float xx = (j & 1) ? bhi(xv[i][j >> 1]) : blo(xv[i][j >> 1]);
      const float mm = (j & 1) ? bhi(mv[j >> 1]) : blo(mv[j >> 1]);
      h[j] = fmaf((xx - mm) * rs[j], c1[i], sa[i]);
    }
#pragma unroll
    for (int r = 0; r < 4; ++r) *(u32*)(sm + CV_H + (4 * i + r) * CV_HP + 128 + 4 * t) = pk(h[r], h[r + 4]);
    *(u4*)(sm + CV_X + 4096 * i + 16 * t) = xv[i];
  }
  f4 acc[8];
#pragma unroll
  for (int i = 0; i < 8; ++i) acc[i] = (f4){0.f, 0.f, 0.f, 0.f};
  u32 ka = ldsaddr(sm) + (4 * ch + 2 * half) * CV_KC + 32 + 8 * (8 * g + (lane & 3) - ((lane & 15) >> 2));
  u32 hb = ldsaddr(sm) + CV_H + (4 * ch + 2 * half) * CV_HP + 128 + 8 * c - 16 * g - 8 - 64;

  bar();
  {
    u32 cnt;
    asm volatile(
      "ds_read_b64_tr_b16 v[64:65], %8 offset:32\n"
      "ds_read_b64_tr_b16 v[66:67], %8 offset:0\n"
      "ds_read_b64 v[120:121], %9 offset:832\n"
      "ds_read_b64 v[122:123], %9 offset:840\n"
      "ds_read_b64 v[124:125], %9 offset:960\n"
      "ds_read_b64 v[126:127], %9 offset:968\n"
      "ds_read_b64 v[112:113], %9 offset:576\n"
      "ds_read_b64 v[114:115], %9 offset:584\n"
      "ds_read_b64 v[116:117], %9 offset:704\n"
      "ds_read_b64 v[118:119], %9 offset:712\n"
      "ds_read_b64_tr_b16 v[68:69], %8 offset:544\n"
      "ds_read_b64_tr_b16 v[70:71], %8 offset:512\n"
      "ds_read_b64 v[108:109], %9 offset:448\n"
      "ds_read_b64 v[110:111], %9 offset:456\n"
      "ds_read_b64 v[104:105], %9 offset:320\n"
      "ds_read_b64 v[106:107], %9 offset:328\n"
      "ds_read_b64_tr_b16 v[72:73], %8 offset:1056\n"
      "ds_read_b64_tr_b16 v[74:75], %8 offset:1024\n"
      "ds_read_b64_tr_b16 v[76:77], %8 offset:1568\n"
      "ds_read_b64_tr_b16 v[78:79], %8 offset:1536\n"
      "ds_read_b64 v[100:101], %9 offset:192\n"
      "ds_read_b64 v[102:103], %9 offset:200\n"
      "ds_read_b64_tr_b16 v[80:81], %8 offset:2080\n"
      "ds_read_b64_tr_b16 v[82:83], %8 offset:2048\n"
      "ds_read_b64 v[96:97], %9 offset:64\n"
      "ds_read_b64 v[98:99], %9 offset:72\n"
      "ds_read_b64_tr_b16 v[84:85], %8 offset:2592\n"
      "ds_read_b64_tr_b16 v[86:87], %8 offset:2560\n"
      "ds_read_b64_tr_b16 v[88:89], %8 offset:3104\n"
      "ds_read_b64_tr_b16 v[90:91], %8 offset:3072\n"
      "ds_read_b64_tr_b16 v[92:93], %8 offset:3616\n"
      "ds_read_b64_tr_b16 v[94:95], %8 offset:3584\n"
      "s_mov_b32 %10, 2\n"
      "s_waitcnt lgkmcnt(0)\n"
      "Lconv%=:\n"
      "v_mfma_f32_16x16x32_bf16 %6, v[64:67], v[120:123], %6\n"
      "ds_read_b64_tr_b16 v[128:129], %8 offset:288\n"
      "v_mfma_f32_16x16x32_bf16 %7, v[64:67], v[124:127], %7\n"
      "ds_read_b64_tr_b16 v[130:131], %8 offset:256\n"
      "v_mfma_f32_16x16x32_bf16 %4, v[64:67], v[112:115], %4\n"
      "ds_read_b64 v[184:185], %9 offset:768\n"
      "v_mfma_f32_16x16x32_bf16 %5, v[64:67], v[116:119], %5\n"
      "ds_read_b64 v[186:187], %9 offset:776\n"
      "v_mfma_f32_16x16x32_bf16 %6, v[68:71], v[116:119], %6\n"
      "ds_read_b64 v[188:189], %9 offset:896\n"
      "v_mfma_f32_16x16x32_bf16 %7, v[68:71], v[120:123], %7\n"
      "ds_read_b64 v[190:191], %9 offset:904\n"
      "v_mfma_f32_16x16x32_bf16 %4, v[68:71], v[108:111], %4\n"
      "ds_read_b64 v[176:177], %9 offset:512\n"
      "v_mfma_f32_16x16x32_bf16 %2, v[64:67], v[104:107], %2\n"
      "ds_read_b64 v[178:179], %9 offset:520\n"
      "v_mfma_f32_16x16x32_bf16 %6, v[72:75], v[112:115], %6\n"
      "ds_read_b64 v[180:181], %9 offset:640\n"
      "v_mfma_f32_16x16x32_bf16 %3, v[64:67], v[108:111], %3\n"
      "ds_read_b64 v[182:183], %9 offset:648\n"
      "v_mfma_f32_16x16x32_bf16 %7, v[72:75], v[116:119], %7\n"
      "ds_read_b64_tr_b16 v[132:133], %8 offset:800\n"
      "v_mfma_f32_16x16x32_bf16 %4, v[72:75], v[104:107], %4\n"
      "ds_read_b64_tr_b16 v[134:135], %8 offset:768\n"
      "v_mfma_f32_16x16x32_bf16 %5, v[68:71], v[112:115], %5\n"
      "ds_read_b64 v[172:173], %9 offset:384\n"
      "v_mfma_f32_16x16x32_bf16 %6, v[76:79], v[108:111], %6\n"
      "ds_read_b64 v[174:175], %9 offset:392\n"
      "v_mfma_f32_16x16x32_bf16 %7, v[76:79], v[112:115], %7\n"
      "ds_read_b64 v[168:169], %9 offset:256\n"
      "v_mfma_f32_16x16x32_bf16 %4, v[76:79], v[100:103], %4\n"
      "ds_read_b64 v[170:171], %9 offset:264\n"
      "v_mfma_f32_16x16x32_bf16 %1, v[64:67], v[100:103], %1\n"
      "ds_read_b64_tr_b16 v[136:137], %8 offset:1312\n"
      "v_mfma_f32_16x16x32_bf16 %5, v[72:75], v[108:111], %5\n"
      "ds_read_b64_tr_b16 v[138:139], %8 offset:1280\n"
      "v_mfma_f32_16x16x32_bf16 %6, v[80:83], v[104:107], %6\n"
      "ds_read_b64_tr_b16 v[140:141], %8 offset:1824\n"
      "v_mfma_f32_16x16x32_bf16 %0, v[64:67], v[96:99], %0\n"
      "ds_read_b64_tr_b16 v[142:143], %8 offset:1792\n"
      "v_mfma_f32_16x16x32_bf16 %7, v[80:83], v[108:111], %7\n"
      "ds_read_b64 v[164:165], %9 offset:128\n"
      "v_mfma_f32_16x16x32_bf16 %5, v[76:79], v[104:107], %5\n"
      "ds_read_b64 v[166:167], %9 offset:136\n"
      "v_mfma_f32_16x16x32_bf16 %2, v[68:71], v[100:103], %2\n"
      "ds_read_b64_tr_b16 v[144:145], %8 offset:2336\n"
      "v_mfma_f32_16x16x32_bf16 %6, v[84:87], v[100:103], %6\n"
      "ds_read_b64_tr_b16 v[146:147], %8 offset:2304\n"
      "v_mfma_f32_16x16x32_bf16 %7, v[84:87], v[104:107], %7\n"
      "ds_read_b64 v[160:161], %9 offset:0\n"
      "v_mfma_f32_16x16x32_bf16 %3, v[68:71], v[104:107], %3\n"
      "ds_read_b64 v[162:163], %9 offset:8\n"
      "v_mfma_f32_16x16x32_bf16 %2, v[72:75], v[96:99], %2\n"
      "ds_read_b64_tr_b16 v[148:149], %8 offset:2848\n"
      "v_mfma_f32_16x16x32_bf16 %5, v[80:83], v[100:103], %5\n"
      "ds_read_b64_tr_b16 v[150:151], %8 offset:2816\n"
      "v_mfma_f32_16x16x32_bf16 %7, v[88:91], v[100:103], %7\n"
      "ds_read_b64_tr_b16 v[152:153], %8 offset:3360\n"
      "v_mfma_f32_16x16x32_bf16 %6, v[88:91], v[96:99], %6\n"
      "ds_read_b64_tr_b16 v[154:155], %8 offset:3328\n"
      "v_mfma_f32_16x16x32_bf16 %3, v[72:75], v[100:103], %3\n"
      "ds_read_b64_tr_b16 v[156:157], %8 offset:3872\n"
      "v_mfma_f32_16x16x32_bf16 %1, v[68:71], v[96:99], %1\n"
      "ds_read_b64_tr_b16 v[158:159], %8 offset:3840\n"
      "v_mfma_f32_16x16x32_bf16 %7, v[92:95], v[96:99], %7\n"
      "v_mfma_f32_16x16x32_bf16 %5, v[84:87], v[96:99], %5\n"
      "v_mfma_f32_16x16x32_bf16 %4, v[80:83], v[96:99], %4\n"
      "v_mfma_f32_16x16x32_bf16 %3, v[76:79], v[96:99], %3\n"
      "s_waitcnt lgkmcnt(0)\n"
      "v_add_u32 %8, 4128, %8\n"
      "v_add_u32 %9, 1152, %9\n"
      "v_mfma_f32_16x16x32_bf16 %6, v[128:131], v[184:187], %6\n"
      "ds_read_b64_tr_b16 v[64:65], %8 offset:32\n"
      "v_mfma_f32_16x16x32_bf16 %7, v[128:131], v[188:191], %7\n"
      "ds_read_b64_tr_b16 v[66:67], %8 offset:0\n"
      "v_mfma_f32_16x16x32_bf16 %4, v[128:131], v[176:179], %4\n"
      "ds_read_b64 v[120:121], %9 offset:832\n"
      "v_mfma_f32_16x16x32_bf16 %5, v[128:131], v[180:183], %5\n"
      "ds_read_b64 v[122:123], %9 offset:840\n"
      "v_mfma_f32_16x16x32_bf16 %6, v[132:135], v[180:183], %6\n"
      "ds_read_b64 v[124:125], %9 offset:960\n"
      "v_mfma_f32_16x16x32_bf16 %7, v[132:135], v[184:187], %7\n"
      "ds_read_b64 v[126:127], %9 offset:968\n"
      "v_mfma_f32_16x16x32_bf16 %4, v[132:135], v[172:175], %4\n"
      "ds_read_b64 v[112:113], %9 offset:576\n"
      "v_mfma_f32_16x16x32_bf16 %2, v[128:131], v[168:171], %2\n"
      "ds_read_b64 v[114:115], %9 offset:584\n"
      "v_mfma_f32_16x16x32_bf16 %6, v[136:139], v[176:179], %6\n"
      "ds_read_b64 v[116:117], %9 offset:704\n"
      "v_mfma_f32_16x16x32_bf16 %3, v[128:131], v[172:175], %3\n"
      "ds_read_b64 v[118:119], %9 offset:712\n"
      "v_mfma_f32_16x16x32_bf16 %7, v[136:139], v[180:183], %7\n"
      "ds_read_b64_tr_b16 v[68:69], %8 offset:544\n"
      "v_mfma_f32_16x16x32_bf16 %4, v[136:139], v[168:171], %4\n"
      "ds_read_b64_tr_b16 v[70:71], %8 offset:512\n"
      "v_mfma_f32_16x16x32_bf16 %5, v[132:135], v[176:179], %5\n"
      "ds_read_b64 v[108:109], %9 offset:448\n"
      "v_mfma_f32_16x16x32_bf16 %6, v[140:143], v[172:175], %6\n"
      "ds_read_b64 v[110:111], %9 offset:456\n"
      "v_mfma_f32_16x16x32_bf16 %7, v[140:143], v[176:179], %7\n"
      "ds_read_b64 v[104:105], %9 offset:320\n"
      "v_mfma_f32_16x16x32_bf16 %4, v[140:143], v[164:167], %4\n"
      "ds_read_b64 v[106:107], %9 offset:328\n"
      "v_mfma_f32_16x16x32_bf16 %1, v[128:131], v[164:167], %1\n"
      "ds_read_b64_tr_b16 v[72:73], %8 offset:1056\n"
      "v_mfma_f32_16x16x32_bf16 %5, v[136:139], v[172:175], %5\n"
      "ds_read_b64_tr_b16 v[74:75], %8 offset:1024\n"
      "v_mfma_f32_16x16x32_bf16 %6, v[144:147], v[168:171], %6\n"
      "ds_read_b64_tr_b16 v[76:77], %8 offset:1568\n"
      "v_mfma_f32_16x16x32_bf16 %0, v[128:131], v[160:163], %0\n"
      "ds_read_b64_tr_b16 v[78:79], %8 offset:1536\n"
      "v_mfma_f32_16x16x32_bf16 %7, v[144:147], v[172:175], %7\n"
      "ds_read_b64 v[100:101], %9 offset:192\n"
      "v_mfma_f32_16x16x32_bf16 %5, v[140:143], v[168:171], %5\n"
      "ds_read_b64 v[102:103], %9 offset:200\n"
      "v_mfma_f32_16x16x32_bf16 %2, v[132:135], v[164:167], %2\n"
      "ds_read_b64_tr_b16 v[80:81], %8 offset:2080\n"
      "v_mfma_f32_16x16x32_bf16 %6, v[148:151], v[164:167], %6\n"
      "ds_read_b64_tr_b16 v[82:83], %8 offset:2048\n"
      "v_mfma_f32_16x16x32_bf16 %7, v[148:151], v[168:171], %7\n"
      "ds_read_b64 v[96:97], %9 offset:64\n"
      "v_mfma_f32_16x16x32_bf16 %3, v[132:135], v[168:171], %3\n"
      "ds_read_b64 v[98:99], %9 offset:72\n"
      "v_mfma_f32_16x16x32_bf16 %2, v[136:139], v[160:163], %2\n"
      "ds_read_b64_tr_b16 v[84:85], %8 offset:2592\n"
      "v_mfma_f32_16x16x32_bf16 %5, v[144:147], v[164:167], %5\n"
      "ds_read_b64_tr_b16 v[86:87], %8 offset:2560\n"
      "v_mfma_f32_16x16x32_bf16 %7, v[152:155], v[164:167], %7\n"
      "ds_read_b64_tr_b16 v[88:89], %8 offset:3104\n"
      "v_mfma_f32_16x16x32_bf16 %6, v[152:155], v[160:163], %6\n"
      "ds_read_b64_tr_b16 v[90:91], %8 offset:3072\n"
      "v_mfma_f32_16x16x32_bf16 %3, v[136:139], v[164:167], %3\n"
      "ds_read_b64_tr_b16 v[92:93], %8 offset:3616\n"
      "v_mfma_f32_16x16x32_bf16 %1, v[132:135], v[160:163], %1\n"
      "ds_read_b64_tr_b16 v[94:95], %8 offset:3584\n"
      "v_mfma_f32_16x16x32_bf16 %7, v[156:159], v[160:163], %7\n"
      "v_mfma_f32_16x16x32_bf16 %5, v[148:151], v[160:163], %5\n"
      "v_mfma_f32_16x16x32_bf16 %4, v[144:147], v[160:163], %4\n"
      "v_mfma_f32_16x16x32_bf16 %3, v[140:143], v[160:163], %3\n"
      "s_waitcnt lgkmcnt(0)\n"
      "s_sub_u32 %10, %10, 1\n"
      "s_cmp_lg_u32 %10, 0\n"
      "s_cbranch_scc1 Lconv%=\n"
      "s_nop 7\n"
      : "+v"(acc[0]), "+v"(acc[1]), "+v"(acc[2]), "+v"(acc[3]), "+v"(acc[4]), "+v"(acc[5]), "+v"(acc[6]), "+v"(acc[7]), "+v"(ka), "+v"(hb), "=&s"(cnt)
      :: "memory", "scc", "v64", "v65", "v66", "v67", "v68", "v69", "v70", "v71", "v72", "v73", "v74", "v75", "v76", "v77", "v78", "v79", "v80", "v81", "v82", "v83", "v84", "v85", "v86", "v87", "v88", "v89", "v90", "v91", "v92", "v93", "v94", "v95", "v96", "v97", "v98", "v99", "v100", "v101", "v102", "v103", "v104", "v105", "v106", "v107", "v108", "v109", "v110", "v111", "v112", "v113", "v114", "v115", "v116", "v117", "v118", "v119", "v120", "v121", "v122", "v123", "v124", "v125", "v126", "v127", "v128", "v129", "v130", "v131", "v132", "v133", "v134", "v135", "v136", "v137", "v138", "v139", "v140", "v141", "v142", "v143", "v144", "v145", "v146", "v147", "v148", "v149", "v150", "v151", "v152", "v153", "v154", "v155", "v156", "v157", "v158", "v159", "v160", "v161", "v162", "v163", "v164", "v165", "v166", "v167", "v168", "v169", "v170", "v171", "v172", "v173", "v174", "v175", "v176", "v177", "v178", "v179", "v180", "v181", "v182", "v183", "v184", "v185", "v186", "v187", "v188", "v189", "v190", "v191");
  }

  f4* ex = (f4*)(sm + CV_EX);
  if (half) {
#pragma unroll
    for (int i = 0; i < 4; ++i) { ex[(w * 4 + i) * 64 + lane] = acc[i]; acc[i] = acc[4 + i]; }
  } else {
#pragma unroll
    for (int i = 0; i < 4; ++i) ex[(w * 4 + i) * 64 + lane] = acc[4 + i];
  }
  volatile LAS u32* fl = (volatile LAS u32*)(size_t)(ldsaddr(sm) + CV_FL);
  asm volatile("s_waitcnt lgkmcnt(0)" ::: "memory");
  fl[w] = 1u;
  u2 xr[4];
#pragma unroll
  for (int i = 0; i < 4; ++i) xr[i] = *(const u2*)(sm + CV_X + 4096 * ch + 2 * (256 * (4 * half + i) + 16 * c + 4 * g));
  while (fl[w ^ 1] == 0u) {}
  asm volatile("" ::: "memory");
  u16* x1t = (u16*)(ws + WS_X1T);
  const float gc = ga * c2;
#pragma unroll
  for (int i = 0; i < 4; ++i) {
    const f4 y = (acc[i] + ex[((w ^ 1) * 4 + i) * 64 + lane]) * gc + (f4){blo(xr[i].x), bhi(xr[i].x), blo(xr[i].y), bhi(xr[i].y)} * c2;
    *(u2*)(x1t + d * L + 256 * (4 * half + i) + 16 * c + 4 * g) = (u2){pk(y[0], y[1]), pk(y[2], y[3])};
  }
}

DI void k2_help(Stamps& ts_, char* sm, int b, const u16* wg, const u16* wu, const u16* wd, char* ws) {
  const int t = threadIdx.x - 256, w = __builtin_amdgcn_readfirstlane(t >> 6), lane = t & 63;
  const int d0 = 64 * (b & 7) + 2 * (b >> 3);
  const u16* kx = (const u16*)(ws + WS_KX);
  u4 kv[2];
  u2 kp[2];
#pragma unroll
  for (int i = 0; i < 2; ++i) { kv[i] = *(const u4*)(kx + (d0 + i) * KXS + 8 + 8 * t); kp[i] = *(const u2*)(kx + (d0 + i) * KXS + 4 + 8 * t); }
  SB();
  if (t < 16) *(u4*)(sm + (t >> 1) * CV_KC + 16 * (t & 1)) = (u4){0u, 0u, 0u, 0u};
  if (w == 1) *(u4*)(sm + CV_H + (lane >> 3) * CV_HP + 16 * (t & 7)) = (u4){0u, 0u, 0u, 0u};
  if (t == 128) *(u4*)(sm + CV_FL) = (u4){0u, 0u, 0u, 0u};
#pragma unroll
  for (int i = 0; i < 2; ++i) {
    const u4 v = kv[i];
    const u32 a0 = __builtin_amdgcn_alignbit(kp[i].y, kp[i].x, 16), a1 = __builtin_amdgcn_alignbit(v.x, kp[i].y, 16), a2 = __builtin_amdgcn_alignbit(v.y, v.x, 16),
              a3 = __builtin_amdgcn_alignbit(v.z, v.y, 16), a4 = __builtin_amdgcn_alignbit(v.w, v.z, 16);
    char* kd = sm + 4 * i * CV_KC + 32 + 16 * t;
    *(u4*)kd = v;
    *(u4*)(kd + CV_KC) = (u4){a1, a2, a3, a4};
    *(u4*)(kd + 2 * CV_KC) = (u4){kp[i].y, v.x, v.y, v.z};
    *(u4*)(kd + 3 * CV_KC) = (u4){a0, a1, a2, a3};
  }

  bar();
  const int task = b + 256 * w, kq = task & 3, blk = task >> 2, mat = blk >= 43, jb = blk - 43 * mat;
  const bool work = task < 344;
  const int hh = lane >> 5;
  u4 vec[8];
  if (work) {
    const rsrc_t rw = mkrsrc(mat ? wu : wg, D * DF * 2);
    const u32 vo = (128 * kq + (lane >> 2)) * (DF * 2) + 64 * jb + 16 * (lane & 3);
    asm volatile("s_nop 4");
#pragma unroll
    for (int i = 0; i < 8; ++i) glds16<0>(ldsaddr(sm) + CV_W + 8192 * w + 1024 * i, vo, rw, i * 16 * DF * 2);
    const char* cv = ws + WS_CV + ((lane & 1) ? 0 : 1024) + 256 * kq + 16 * hh;
    sfor<0, 8>([&](auto i) { gld16<32 * i>(vec[i], cv); });
  }
  u32 dummy[4];
  {
    const int row = 32 * (b >> 3) + 8 * w + (lane >> 3);
    const char* base = (const char*)(row >= D ? wu : wg) + (row & (D - 1)) * (DF * 2);
#pragma unroll
    for (int i = 0; i < 2; ++i) {
      const int off = min((b & 1) * 1344 + 128 * ((lane & 7) + 8 * i), (b & 1) ? DF * 2 - 4 : 1340);
      asm volatile("global_load_dword %0, %1, off" : "=v"(dummy[i]) : "v"(base + off));
    }
  }
#pragma unroll
  for (int i = 0; i < 2; ++i)
    asm volatile("global_load_dword %0, %1, off" : "=v"(dummy[2 + i]) : "v"((const char*)wd + (b >> 3) * 44032 + w * 11008 + min(lane + 64 * i, 85) * 128));
  if (work) {
    asm volatile("s_waitcnt vmcnt(4)" : "+v"(vec[0]), "+v"(vec[1]), "+v"(vec[2]), "+v"(vec[3]), "+v"(vec[4]), "+v"(vec[5]), "+v"(vec[6]), "+v"(vec[7]) :: "memory");
    const u32 ra = ldsaddr(sm) + CV_W + 8192 * w + 64 * (8 * hh + ((lane & 15) >> 2)) + 32 * ((lane >> 4) & 1) + 8 * (lane & 3);
    f16v sums;
#pragma unroll
    for (int e = 0; e < 16; ++e) sums[e] = 0.f;
#pragma unroll
    for (int i = 0; i < 8; ++i) sums = mfma32(vec[i], cat(trd(ra + 1024 * i), trd(ra + 1024 * i + 256)), sums);
    if (lane < 32) {
      u32* exf = (u32*)(ws + WS_EX) + ((2 * jb + (lane >> 4)) * 64 + 16 * mat + (lane & 15)) * 4 + kq;
      const float h0 = rb(sums[0]), h1 = rb(sums[1]);
      exf[0] = pk(h0, sums[0] - h0);
      exf[128] = pk(h1, sums[1] - h1);
    }
  }
  asm volatile("s_waitcnt vmcnt(0)" : "+v"(dummy[0]), "+v"(dummy[1]), "+v"(dummy[2]), "+v"(dummy[3]) :: "memory");
}

__global__ __launch_bounds__(512, 2) void k2_conv_prep(const u16* wg, const u16* wu, const u16* wd, char* ws) {
  extern __shared__ __attribute__((aligned(16))) char sm[];
  const int b = blockIdx.x;
  Stamps ts_ = {}; STAMP(1,0);
  if (__builtin_amdgcn_readfirstlane(threadIdx.x) < 256) k2_conv(ts_, sm, b, ws);
  else k2_help(ts_, sm, b, wg, wu, wd, ws);

  STAMPV(1,7);
}

constexpr int G1_LEAD = 3, G1_NS = 5, G1_SLOT = 20480;
constexpr int G2_RES = 19, G2_FL = 4 * G2_RES * 2048, G1_SV = G2_FL + 64, K3_LDS = G1_SV + 512;
constexpr int G2_LEAD = 6;
constexpr int HH_T = NU * 1024;

template <int K> DI u32 g1_pair(const f16v& acc, float rs, float sd) {
  const f2 g = (f2){acc[2 * K], acc[2 * K + 1]} * rs;
  const f2 z = g * (g * g * -0.10294324f + -2.30220819f);
  const f2 dn = (f2){__builtin_amdgcn_exp2f(z.x), __builtin_amdgcn_exp2f(z.y)} * sd + sd;
  const f2 r = g * (f2){acc[8 + 2 * K], acc[9 + 2 * K]} * (f2){__builtin_amdgcn_rcpf(dn.x), __builtin_amdgcn_rcpf(dn.y)};
  return pk(r.x, r.y);
}
DI u4 g1_tile(const f16v& acc, float rs, float sd) { return (u4){g1_pair<0>(acc, rs, sd), g1_pair<1>(acc, rs, sd), g1_pair<2>(acc, rs, sd), g1_pair<3>(acc, rs, sd)}; }

DI void g1_stats(char* sm, int rt, char* ws) {
  const int t = threadIdx.x;
  float* red = (float*)sm;
  if (t < 512) {
    const u16* xp = (const u16*)(ws + WS_X1T) + (t >> 4) * L + 128 * rt + 8 * (t & 15);
    u4 v[16];
    float iv[16];
#pragma unroll
    for (int i = 0; i < 16; ++i) { v[i] = *(const u4*)(xp + 32 * i * L); iv[i] = ((const float*)(ws + WS_INV))[(t >> 4) + 32 * i]; }
    f2 s1[4], s2[4];
#pragma unroll
    for (int j = 0; j < 4; ++j) s1[j] = s2[j] = (f2){0.f, 0.f};
#pragma unroll
    for (int i = 0; i < 16; ++i)
#pragma unroll
      for (int j = 0; j < 4; ++j) { const f2 x = (f2){blo(v[i][j]), bhi(v[i][j])} * iv[i]; s1[j] += x; s2[j] += x * x; }
#pragma unroll
    for (int j = 0; j < 4; ++j) *(f4*)(red + (t >> 4) * 260 + 16 * (t & 15) + 4 * j) = (f4){s1[j].x, s2[j].x, s1[j].y, s2[j].y};
  }
  __syncthreads();
  if (t < 512) {
    f2 p = {0.f, 0.f};
#pragma unroll
    for (int i = 0; i < 8; ++i) p += *(const f2*)(red + (8 * (t & 3) + i) * 260 + 2 * (t >> 2));
    p.x += dppf<0xb1>(p.x); p.y += dppf<0xb1>(p.y); p.x += dppf<0x4e>(p.x); p.y += dppf<0x4e>(p.y);
    const f2 ms = ln_stats(p.x, p.y);
    if ((t & 3) == 0)
      __hip_atomic_store((u32*)(ws + WS_ST2) + 128 * rt + (t >> 2), (__float_as_uint(ms.x) >> 16) | __float_as_uint(ms.y), __ATOMIC_RELAXED, __HIP_MEMORY_SCOPE_AGENT);
  }
  __syncthreads();
}

__global__ __launch_bounds__(768, 3) void k3_mlp(const u16* wg, const u16* wu, const u16* wd, char* ws, u16* out) {
  extern __shared__ __attribute__((aligned(16))) char sm[];
  const int b = blockIdx.x, j = b >> 3;
  Stamps ts_ = {}; STAMP(2,0);
  const int t = threadIdx.x, w12 = __builtin_amdgcn_readfirstlane(t >> 6), w = w12 & 3, lane = t & 63, hh = lane >> 5;
  const u32 lb0 = ldsaddr(sm);
  const int q = (lane & 15) >> 2, p = lane & 3, g = lane >> 4;
  const int lt = 4 * (b & 7) + (j & 3), dt = j >> 2;
  auto kfirst = [&](int kq) { const u32 k = 22 * kq - (kq == 3) + 3 * dt; return min(k, k - NU); };
  auto knext = [](u32 k) { return min(k + 1, k + 1 - NU); };
  const rsrc_t rd = mkrsrc(wd, DF * D * 2);
  const u32 vd = (lane >> 3) * 1024 + 128 * dt + 16 * ((lane & 7) ^ (4 * ((lane >> 4) & 1)));
  volatile LAS u32* fl = (volatile LAS u32*)(size_t)(lb0 + G2_FL);
  auto fill2 = [&](int i0, int n, int slot0) {
    u32 kd = kfirst(w) + i0;
    kd = min(kd, kd - NU);
#pragma unroll 1
    for (int i = 0; i < n; ++i) {
      glds16<0>(lb0 + (G2_RES * w + slot0 + i) * 2048, vd, rd, kd * 16384);
      glds16<0>(lb0 + (G2_RES * w + slot0 + i) * 2048 + 1024, vd, rd, kd * 16384 + 8192);
      kd = knext(kd);
    }
  };
  {
    const int rt = 4 * ((b & 7) >> 1) + (j & 3), cg = (j >> 2) + 8 * (b & 1) - 1;
    if (t == 0) fl[0] = 0u;
    if (cg < 0) {
      g1_stats(sm, rt, ws);
    } else if (w12 >= 8) {
      const rsrc_t rx = mkrsrc(ws + WS_X1T, D * L * 2), rg = mkrsrc(wg, D * DF * 2), ru = mkrsrc(wu, D * DF * 2);
      const u32 vx = (4 * w + g) * 4096 + 256 * rt + 16 * ((lane & 15) ^ ((g << 2) | w));
      u32 vw[3];
#pragma unroll
      for (int i = 0; i < 3; ++i) {
        const int pc = (3 * (w & 1) + i) * 64 + lane, row = pc / 12, chk = pc - 12 * row;
        vw[i] = row * (DF * 2) + min(192 * cg + 16 * (chk ^ (w & 2)), DF * 2 - 16);
      }
      const rsrc_t rwm = w < 2 ? rg : ru;
      auto stage = [&](int i, int slot) {
        const u32 m = lb0 + slot * G1_SLOT;
        glds16<0>(m + 1024 * w, vx, rx, i * 131072);
        glds16<0>(m + 4096 + 1024 * w, vx, rx, i * 131072 + 65536);
#pragma unroll
        for (int k = 0; k < 3; ++k) glds16<0>(m + 8192 + 3072 * w + 1024 * k, vw[k], rwm, i * (32 * DF * 2));
      };
      asm volatile("s_nop 4");
      static_assert(G1_LEAD + 2 <= G1_NS, "ring too short");
#pragma unroll 1
      for (int i = 0; i < G1_LEAD - 1; ++i) stage(i, i);
      int slot = G1_LEAD - 1;
#pragma unroll 1
      for (int i = 0; i < 17 - G1_LEAD; ++i) {
        stage(i + G1_LEAD - 1, slot);
        slot = slot == G1_NS - 1 ? 0 : slot + 1;
        vmwait<5 * (G1_LEAD - 1)>();
        rawbar();
      }
      const u32* st2 = (const u32*)(ws + WS_ST2) + 128 * rt + 32 * w + (lane & 31);
      u32 v;
      asm volatile("global_load_dword %0, %1, off sc1" : "=v"(v) : "v"(st2));
      sfor<17 - G1_LEAD, 16>([&](auto i) { vmwait<5 * (15 - i) + 1>(); rawbar(); });
      asm volatile("s_waitcnt vmcnt(0)" : "+v"(v));
#pragma unroll 1
      for (int spin = 0; spin < (1 << 16) && !__all(v != 0u); ++spin) {
        __builtin_amdgcn_s_sleep(2);
        v = __hip_atomic_load(st2, __ATOMIC_RELAXED, __HIP_MEMORY_SCOPE_AGENT);
      }
      ((u32*)(sm + G1_SV))[32 * w + (lane & 31)] = v;
      bar();
      bar();
    } else if (w12 >= 4) {
      const char* pf = (const char*)wd + j * (43 * 1024) + w * (43 * 256);
#pragma unroll 1
      for (int i = 0; i < 18; ++i) {
        rawbar();
      }
    } else {
      const int wl = w & 1, wu3 = 3 * (w >> 1), u0 = 6 * cg + wu3;
      u32 xa[2][2], wa[3];
#pragma unroll
      for (int c = 0; c < 2; ++c)
#pragma unroll
        for (int tt = 0; tt < 2; ++tt)
          xa[c][tt] = lb0 + 256 * (8 * hh + 4 * tt + q) + 8 * (p & 1) + 16 * ((4 * (2 * wl + c) + 2 * (g & 1) + (p >> 1)) ^ ((q << 2) | (2 * hh + tt)));
#pragma unroll
      for (int v = 0; v < 3; ++v) wa[v] = lb0 + 8192 + 6144 * (g & 1) + 192 * (8 * hh + q) + 16 * ((2 * (wu3 + v) + (p >> 1)) ^ (2 * (g & 1))) + 8 * (p & 1);
      u4 ex[3];
#pragma unroll
      for (int v = 0; v < 3; ++v) ex[v] = *(const u4*)(ws + WS_EX + min(u0 + v, NU - 1) * 1024 + lane * 16);
      f16v acc[6];
#pragma unroll
      for (int i = 0; i < 6; ++i)
#pragma unroll
        for (int e = 0; e < 16; ++e) acc[i][e] = 0.f;
#pragma unroll
      for (int i = 0; i < 6; ++i) asm volatile("" : "+v"(acc[i]));
      rawbar();

      {
        u32 c0, c1, c2;
        asm volatile(
          "ds_read_b64_tr_b16 v[112:113], %6 offset:0\n"
          "ds_read_b64_tr_b16 v[114:115], %7 offset:0\n"
          "ds_read_b64_tr_b16 v[120:121], %10 offset:0\n"
          "ds_read_b64_tr_b16 v[122:123], %10 offset:768\n"
          "ds_read_b64_tr_b16 v[116:117], %8 offset:0\n"
          "ds_read_b64_tr_b16 v[118:119], %9 offset:0\n"
          "ds_read_b64_tr_b16 v[124:125], %11 offset:0\n"
          "ds_read_b64_tr_b16 v[126:127], %11 offset:768\n"
          "ds_read_b64_tr_b16 v[128:129], %12 offset:0\n"
          "ds_read_b64_tr_b16 v[130:131], %12 offset:768\n"
          "s_mov_b32 %13, 16\n"
          "s_mov_b32 %14, 0\n"
          "Lg1%=:\n"
          "s_add_u32 %14, %14, 1\n"
          "s_cmp_eq_u32 %14, 5\n"
          "s_cselect_b32 %15, -81920, %16\n"
          "s_cselect_b32 %14, 0, %14\n"
          "s_waitcnt lgkmcnt(6)\n"
          "v_mfma_f32_32x32x16_bf16 %0, v[120:123], v[112:115], %0\n"
          "ds_read_b64_tr_b16 v[132:133], %6 offset:4096\n"
          "ds_read_b64_tr_b16 v[134:135], %7 offset:4096\n"
          "s_waitcnt lgkmcnt(6)\n"
          "v_mfma_f32_32x32x16_bf16 %1, v[120:123], v[116:119], %1\n"
          "ds_read_b64_tr_b16 v[140:141], %10 offset:3072\n"
          "ds_read_b64_tr_b16 v[142:143], %10 offset:3840\n"
          "s_waitcnt lgkmcnt(6)\n"
          "v_mfma_f32_32x32x16_bf16 %2, v[124:127], v[112:115], %2\n"
          "ds_read_b64_tr_b16 v[136:137], %8 offset:4096\n"
          "ds_read_b64_tr_b16 v[138:139], %9 offset:4096\n"
          "v_mfma_f32_32x32x16_bf16 %3, v[124:127], v[116:119], %3\n"
          "ds_read_b64_tr_b16 v[144:145], %11 offset:3072\n"
          "ds_read_b64_tr_b16 v[146:147], %11 offset:3840\n"
          "s_waitcnt lgkmcnt(8)\n"
          "v_mfma_f32_32x32x16_bf16 %4, v[128:131], v[112:115], %4\n"
          "ds_read_b64_tr_b16 v[148:149], %12 offset:3072\n"
          "ds_read_b64_tr_b16 v[150:151], %12 offset:3840\n"
          "v_add_u32 %6, %15, %6\n"
          "v_add_u32 %7, %15, %7\n"
          "v_add_u32 %8, %15, %8\n"
          "v_add_u32 %9, %15, %9\n"
          "v_mfma_f32_32x32x16_bf16 %5, v[128:131], v[116:119], %5\n"
          "v_add_u32 %10, %15, %10\n"
          "v_add_u32 %11, %15, %11\n"
          "v_add_u32 %12, %15, %12\n"
          "s_barrier\n"
          "s_waitcnt lgkmcnt(6)\n"
          "v_mfma_f32_32x32x16_bf16 %0, v[140:143], v[132:135], %0\n"
          "ds_read_b64_tr_b16 v[112:113], %6 offset:0\n"
          "ds_read_b64_tr_b16 v[114:115], %7 offset:0\n"
          "s_waitcnt lgkmcnt(6)\n"
          "v_mfma_f32_32x32x16_bf16 %1, v[140:143], v[136:139], %1\n"
          "ds_read_b64_tr_b16 v[120:121], %10 offset:0\n"
          "ds_read_b64_tr_b16 v[122:123], %10 offset:768\n"
          "s_waitcnt lgkmcnt(6)\n"
          "v_mfma_f32_32x32x16_bf16 %2, v[144:147], v[132:135], %2\n"
          "ds_read_b64_tr_b16 v[116:117], %8 offset:0\n"
          "ds_read_b64_tr_b16 v[118:119], %9 offset:0\n"
          "v_mfma_f32_32x32x16_bf16 %3, v[144:147], v[136:139], %3\n"
          "ds_read_b64_tr_b16 v[124:125], %11 offset:0\n"
          "ds_read_b64_tr_b16 v[126:127], %11 offset:768\n"
          "s_waitcnt lgkmcnt(8)\n"
          "v_mfma_f32_32x32x16_bf16 %4, v[148:151], v[132:135], %4\n"
          "ds_read_b64_tr_b16 v[128:129], %12 offset:0\n"
          "ds_read_b64_tr_b16 v[130:131], %12 offset:768\n"
          "s_sub_u32 %13, %13, 1\n"
          "s_cmp_lg_u32 %13, 0\n"
          "v_mfma_f32_32x32x16_bf16 %5, v[148:151], v[136:139], %5\n"
          "s_cbranch_scc1 Lg1%=\n"
          "s_waitcnt lgkmcnt(0)\n"
          : "+v"(acc[0]), "+v"(acc[1]), "+v"(acc[2]), "+v"(acc[3]), "+v"(acc[4]), "+v"(acc[5]), "+v"(xa[0][0]), "+v"(xa[0][1]), "+v"(xa[1][0]), "+v"(xa[1][1]),
            "+v"(wa[0]), "+v"(wa[1]), "+v"(wa[2]), "=&s"(c0), "=&s"(c1), "=&s"(c2)
          : "s"(G1_SLOT) : "memory", "scc", "v112", "v113", "v114", "v115", "v116", "v117", "v118", "v119", "v120", "v121", "v122", "v123", "v124", "v125", "v126", "v127", "v128", "v129", "v130", "v131", "v132", "v133", "v134", "v135", "v136", "v137", "v138", "v139", "v140", "v141", "v142", "v143", "v144", "v145", "v146", "v147", "v148", "v149", "v150", "v151");
      }
      STAMP(2,2);
      u32 sv[2];
#pragma unroll
      for (int c = 0; c < 2; ++c) sv[c] = ((const u32*)(sm + G1_SV))[64 * wl + 32 * c + (lane & 31)];
      bar();
#pragma unroll
      for (int c = 0; c < 2; ++c) {
        const u32 bv = hh ? __builtin_amdgcn_perm(sv[c], sv[c], 0x03020302u) : (__builtin_amdgcn_perm(sv[c], sv[c], 0x01000100u) ^ 0x80008000u);
#pragma unroll
        for (int v = 0; v < 3; ++v) acc[2 * v + c] = mfma32(ex[v], (u4){bv, bv, bv, bv}, acc[2 * v + c]);
      }

      char* hp = ws + WS_HH + (4 * rt + 2 * wl) * HH_T + u0 * 1024 + 16 * lane;
#pragma unroll
      for (int v = 0; v < 3; ++v) {
        if (u0 + v < NU) {
#pragma unroll
          for (int c = 0; c < 2; ++c) {
            const float sd = bhi(sv[c]);
            const u4 o = g1_tile(acc[2 * v + c], __builtin_amdgcn_rcpf(sd), sd);
            char* dst = hp + c * HH_T + v * 1024;
            asm volatile("global_store_dwordx4 %0, %1, off sc1\n\ts_nop 1" :: "v"(dst), "v"(o) : "memory");
          }
        }
      }
      STAMP(2,4);
      {
        asm volatile("s_waitcnt vmcnt(0)" ::: "memory");
        if (lane == 0 && __hip_atomic_fetch_add((LAS u32*)fl, 1u, __ATOMIC_RELAXED, __HIP_MEMORY_SCOPE_WORKGROUP) == 3u)
          __hip_atomic_fetch_add((u32*)(ws + WS_CNT) + 64 * rt, 1u, __ATOMIC_RELAXED, __HIP_MEMORY_SCOPE_AGENT);
      }
    }
  }
  if (w12 >= 8) {
    asm volatile("s_nop 4");
    fill2(G2_RES, 3, 0);
    fill2(3, G2_RES - 3, 3);
    vmwait<2 * (G2_RES - 3)>();
    rawbar();
    rawbar();
    fill2(0, 3, 0);
    vmwait<0>();
    rawbar();
    rawbar();
    rawbar();
  } else {
    const int wl = w12 & 1, kq = w12 >> 1, dc = 64 * dt + 16 * kq + 4 * hh;
    f4 gb[2], xr[2];
#pragma unroll
    for (int e = 0; e < 2; ++e) {
      gb[e] = *(const f4*)((const float*)(ws + WS_MOD) + 5 * D + dc + 8 * e);
      xr[e] = *(const f4*)((const float*)(ws + WS_INV) + dc + 8 * e);
#pragma unroll
      for (int i = 0; i < 4; ++i) xr[e][i] *= b2f(((const u16*)(ws + WS_X1T))[(dc + 8 * e + i) * L + 64 * lt + 32 * wl + (lane & 31)]);
    }
    u32 pa[2];
#pragma unroll
    for (int e = 0; e < 2; ++e)
      pa[e] = lb0 + G2_RES * 2048 * kq + 128 * (4 * hh + q) + 16 * ((((4 * e + 2 * (g & 1)) ^ (2 * kq)) + (p >> 1)) ^ (4 * (q >> 1))) + 8 * (p & 1);
    f16v acc[2];
#pragma unroll
    for (int e = 0; e < 16; ++e) acc[0][e] = acc[1][e] = 0.f;
    asm volatile("" : "+v"(xr[0]), "+v"(xr[1]), "+v"(gb[0]), "+v"(gb[1]), "+v"(acc[0]), "+v"(acc[1]));
    rawbar();
    u4 A[2][2], AR[3][2];
#pragma unroll
    for (int i = 0; i < 6; ++i) AR[i >> 1][i & 1] = cat(trd(pa[i & 1] + (i >> 1) * 2048), trd(pa[i & 1] + (i >> 1) * 2048 + 1024));
    asm volatile("s_waitcnt lgkmcnt(0)\n\ts_barrier" : "+v"(AR[0][0]), "+v"(AR[0][1]), "+v"(AR[1][0]), "+v"(AR[1][1]), "+v"(AR[2][0]), "+v"(AR[2][1]) :: "memory");
    if (w12 == 0) {
      const u32* cnt = (const u32*)(ws + WS_CNT) + 64 * (lt >> 1);
#pragma unroll 1
      for (int spin = 0; spin < (1 << 18); ++spin)
        if (__hip_atomic_load(cnt, __ATOMIC_RELAXED, __HIP_MEMORY_SCOPE_AGENT) >= 15u) break;
    }
    rawbar();
    STAMP(2,5);
    const rsrc_t rh = mkrsrc(ws + WS_HH + (2 * lt + wl) * HH_T, HH_T);
    constexpr int LEAD = G2_LEAD, NH = LEAD + 2;
    u4 H[NH];
    u32 kh = kfirst(kq);
    auto ldh = [&](auto i) { if constexpr (i < 22) { bld16_sc1<0>(H[i % NH], lane * 16, rh, kh * 1024); kh = knext(kh); } };
    sfor<0, LEAD>(ldh);
    auto rda = [&](auto i) {
#pragma unroll
      for (int e = 0; e < 2; ++e) A[i & 1][e] = cat(trd(pa[e] + i * 2048), trd(pa[e] + i * 2048 + 1024));
    };
    rda(std::integral_constant<int, 0>{});
    sfor<0, 22>([&](auto i) {
      vmwait<(i < 22 - LEAD ? LEAD - 1 : 21 - i)>(H[i % NH]);
      SB();
      if constexpr (i + 1 < G2_RES) rda(std::integral_constant<int, i + 1>{});
      if (i < 21 || kq < 2) {
#pragma unroll
        for (int e = 0; e < 2; ++e) acc[e] = mfma32(i < G2_RES ? A[i & 1][e] : AR[i < G2_RES ? 0 : i - G2_RES][e], H[i % NH], acc[e]);
      }
      if constexpr (i + 1 < G2_RES) { SGB(0x8, 1); SGB(0x100, 2); SGB(0x8, 1); SGB(0x100, 2); }
      SB();
      ldh(std::integral_constant<int, i + LEAD>{});
      if constexpr (i == G2_RES - 1) rawbar();
    });

    f4* red = (f4*)sm + wl * 1536 + lane;
#pragma unroll
    for (int k = 1; k < 4; ++k)
#pragma unroll
      for (int e = 0; e < 2; ++e) {
        const f16v& a = acc[k >> 1];
        const int r = 8 * (k & 1) + 4 * e;
        red[(kq ^ k) * 384 + (k - 1) * 128 + 64 * e] = (f4){a[r], a[r + 1], a[r + 2], a[r + 3]};
      }
    bar();
    u2 o[2];
#pragma unroll
    for (int e = 0; e < 2; ++e) {
      const f4* rp = red + kq * 384 + 64 * e;
      const f4 y = ((f4){acc[0][4 * e], acc[0][4 * e + 1], acc[0][4 * e + 2], acc[0][4 * e + 3]} + rp[0] + rp[128] + rp[256]) * gb[e] + xr[e];
      o[e] = (u2){pk(y[0], y[1]), pk(y[2], y[3])};
    }
    const auto r0 = __builtin_amdgcn_permlane32_swap(o[0].x, o[1].x, false, false);
    const auto r1 = __builtin_amdgcn_permlane32_swap(o[0].y, o[1].y, false, false);
    *(u4*)(out + (64 * lt + 32 * wl + (lane & 31)) * D + 64 * dt + 16 * kq + 8 * hh) = (u4){r0[0], r1[0], r0[1], r1[1]};
  }
  STAMPV(2,7);
}


__global__ void k_amp() {
  __shared__ unsigned long long r[2][256];
  const int t = threadIdx.x;
  const int kk[2] = {0, 0}, ii[2] = {0, 0}, mm[2] = {0, 0};
  for (int z = 0; z < 2; ++z) {
    unsigned long long v = mm[z] ? 0ull : ~0ull;
    for (int i = t; i < 512 * 8; i += 256) { unsigned long long x = g_stamps[(kk[z] * 512 * 8 + i) * 8 + ii[z]]; if (x) v = mm[z] ? (x > v ? x : v) : (x < v ? x : v); }
    r[z][t] = v;
  }
  __syncthreads();
  if (t == 0) {
    unsigned long long a = r[0][0], b = r[1][0];
    for (int i = 1; i < 256; ++i) { a = mm[0] ? (r[0][i] > a ? r[0][i] : a) : (r[0][i] < a ? r[0][i] : a); b = mm[1] ? (r[1][i] > b ? r[1][i] : b) : (r[1][i] < b ? r[1][i] : b); }
    unsigned long long d = b > a ? b - a : 0; if (d > 5000) d = 5000;
    const unsigned long long t0 = __builtin_amdgcn_s_memrealtime();
    while (__builtin_amdgcn_s_memrealtime() - t0 < 10 * d) {}
  }
}
static const int g_mask = 15;
extern "C" void kernel_launch(void* const* d_in, const int* in_sizes, int n_in, void* d_out, int out_size, void* d_ws, size_t ws_size,
                              hipStream_t stream) {
  const u16* x = (const u16*)d_in[0];
  const u16 *wg = (const u16*)d_in[8], *wu = (const u16*)d_in[9], *wd = (const u16*)d_in[10];
  char* ws = (char*)d_ws;
  (void)hipFuncSetAttribute((const void*)k2_conv_prep, hipFuncAttributeMaxDynamicSharedMemorySize, K2_LDS);
  (void)hipFuncSetAttribute((const void*)k3_mlp, hipFuncAttributeMaxDynamicSharedMemorySize, K3_LDS);
  if (g_mask & (1 << 0)) k1_prep<<<496, 256, 0, stream>>>(x, (const u16*)d_in[1], (const u16*)d_in[2], (const u16*)d_in[3], (const u16*)d_in[4], (const u16*)d_in[5],
                                   (const u16*)d_in[6], (const u16*)d_in[11], (const u16*)d_in[12], ws);
  if (g_mask & (1 << 1)) k2_conv_prep<<<256, 512, K2_LDS, stream>>>(wg, wu, wd, ws);
  if (g_mask & (1 << 2)) k3_mlp<<<256, 768, K3_LDS, stream>>>(wg, wu, wd, ws, (u16*)d_out);
  k_amp<<<1, 256, 0, stream>>>();
}
```

```cpp
#include <hip/hip_runtime.h>
#include <hip/hip_bf16.h>
#include <stdint.h>
#include <type_traits>

typedef unsigned short u16;
typedef unsigned u32;
typedef float f2 __attribute__((ext_vector_type(2)));
typedef float f4 __attribute__((ext_vector_type(4)));
typedef float f16v __attribute__((ext_vector_type(16)));
typedef u32 u2 __attribute__((ext_vector_type(2)));
typedef u32 u4 __attribute__((ext_vector_type(4)));
typedef u4 u4a8 __attribute__((aligned(8)));
typedef short s4 __attribute__((ext_vector_type(4)));
typedef __bf16 bf2 __attribute__((ext_vector_type(2)));
typedef __bf16 bf8 __attribute__((ext_vector_type(8)));
typedef _Float16 h2 __attribute__((ext_vector_type(2)));
typedef _Float16 h8 __attribute__((ext_vector_type(8)));
#define LAS __attribute__((address_space(3)))
#define DI __device__ __forceinline__

constexpr int L = 2048, D = 512, NM = 128, DF = 1376, NU = 86;
constexpr int KXS = 2056;

constexpr size_t WS_MOD = 0, WS_MEAN = 16 << 10, WS_SD = 20 << 10, WS_CV = 24 << 10, WS_INV = 26 << 10, WS_ST2 = 32 << 10, WS_CNT = 40 << 10,
                 WS_EX = 64 << 10, WS_XT = 256 << 10, WS_KX = WS_XT + (2 << 20), WS_X1T = WS_KX + (2112 << 10), WS_HH = WS_X1T + (2 << 20), WS_END = WS_HH + (5504 << 10);

template <class T> DI T ldnt(const T* p) { return __builtin_nontemporal_load(p); }
DI float blo(u32 v) { return __uint_as_float(v << 16); }
DI float bhi(u32 v) { return __uint_as_float(v & 0xffff0000u); }
DI float b2f(u16 v) { return __uint_as_float(((u32)v) << 16); }
DI u32 pk(float a, float b) { return __builtin_bit_cast(u32, __builtin_convertvector((f2){a, b}, bf2)); }
DI u32 pkh(f2 v) { return __builtin_bit_cast(u32, __builtin_convertvector(v, h2)); }
DI float rb(float a) { return blo(pk(a, a)); }
DI u2 trd(const char* p) { return __builtin_bit_cast(u2, __builtin_amdgcn_ds_read_tr16_b64_v4i16((LAS s4*)p)); }
DI u2 trd(u32 a) { return __builtin_bit_cast(u2, __builtin_amdgcn_ds_read_tr16_b64_v4i16((LAS s4*)(size_t)a)); }
DI u4 cat(u2 a, u2 b) { return (u4){a.x, a.y, b.x, b.y}; }
DI float dot2(u32 a, u32 b, float c) { return __builtin_amdgcn_fdot2_f32_bf16(__builtin_bit_cast(bf2, a), __builtin_bit_cast(bf2, b), c, false); }
DI f4 mfma16(u4 a, u4 b, f4 c) { return __builtin_amdgcn_mfma_f32_16x16x32_bf16(__builtin_bit_cast(bf8, a), __builtin_bit_cast(bf8, b), c, 0, 0, 0); }
DI f4 mfma16h(u4 a, u4 b, f4 c) { return __builtin_amdgcn_mfma_f32_16x16x32_f16(__builtin_bit_cast(h8, a), __builtin_bit_cast(h8, b), c, 0, 0, 0); }
DI f16v mfma32(u4 a, u4 b, f16v c) { return __builtin_amdgcn_mfma_f32_32x32x16_bf16(__builtin_bit_cast(bf8, a), __builtin_bit_cast(bf8, b), c, 0, 0, 0); }
#define CM_MUL(d, a, b) "v_pk_mul_f32 " d ", " a ", " b " op_sel_hi:[0,1]\n\t"
#define CM_FMA(d, a, b) "v_pk_fma_f32 " d ", " a ", " b ", " d " op_sel:[1,1,0] op_sel_hi:[1,0,1] neg_lo:[0,1,0]\n\t"
DI void cmul2(f2& r0, f2 a0, f2 b0, f2& r1, f2 a1, f2 b1) {
  asm(CM_MUL("%0", "%2", "%3") CM_MUL("%1", "%4", "%5") CM_FMA("%0", "%2", "%3") CM_FMA("%1", "%4", "%5")
      : "=&v"(r0), "=&v"(r1) : "v"(a0), "v"(b0), "v"(a1), "v"(b1));
}
DI void cmul3(f2& r0, f2 a0, f2 b0, f2& r1, f2 a1, f2 b1, f2& r2, f2 a2, f2 b2) {
  asm(CM_MUL("%0", "%3", "%4") CM_MUL("%1", "%5", "%6") CM_MUL("%2", "%7", "%8") CM_FMA("%0", "%3", "%4") CM_FMA("%1", "%5", "%6") CM_FMA("%2", "%7", "%8")
      : "=&v"(r0), "=&v"(r1), "=&v"(r2) : "v"(a0), "v"(b0), "v"(a1), "v"(b1), "v"(a2), "v"(b2));
}
DI void cmul4(f2& r0, f2& r1, f2& r2, f2& r3, f2 a0, f2 a1, f2 a2, f2 a3, f2 b) {
  asm(CM_MUL("%0", "%4", "%8") CM_MUL("%1", "%5", "%8") CM_MUL("%2", "%6", "%8") CM_MUL("%3", "%7", "%8")
      CM_FMA("%0", "%4", "%8") CM_FMA("%1", "%5", "%8") CM_FMA("%2", "%6", "%8") CM_FMA("%3", "%7", "%8")
      : "=&v"(r0), "=&v"(r1), "=&v"(r2), "=&v"(r3) : "v"(a0), "v"(a1), "v"(a2), "v"(a3), "v"(b));
}
DI float exp_acc(float x) {
  const float t = x * 1.44269502f, r = fmaf(x, 1.44269502f, -t) + x * 1.92596299e-8f, e = __builtin_amdgcn_exp2f(t);
  return fmaf(e, r * 0.693147182f, e);
}
template <int CTRL> DI float dppf(float v) { return __int_as_float(__builtin_amdgcn_update_dpp(0, __float_as_int(v), CTRL, 0xf, 0xf, true)); }
DI float sum32(float v) {
  auto r = __builtin_amdgcn_permlane32_swap(__float_as_uint(v), __float_as_uint(v), false, false);
  return __uint_as_float(r[0]) + __uint_as_float(r[1]);
}
DI f2 ln_stats(float s1, float s2) {
  const float mean = s1 * (1.f / D);
  const float var = fmaxf(fmaf(-mean, mean, s2 * (1.f / D)), 0.f);
  return (f2){rb(mean), rb(__builtin_amdgcn_sqrtf(rb(rb(var) + 1e-5f)))};
}
typedef __amdgpu_buffer_rsrc_t rsrc_t;
DI rsrc_t mkrsrc(const void* p, u32 bytes) { return __builtin_amdgcn_make_buffer_rsrc((void*)p, 0, bytes, 0x00020000); }
DI u32 ldsaddr(const char* p) { return (u32)(size_t)(LAS const char*)p; }
template <int OFF> DI void glds16(u32 m0v, u32 voff, rsrc_t rs, u32 soff) {
  asm volatile("s_mov_b32 m0, %0\n\ts_nop 0\n\tbuffer_load_dwordx4 %1, %2, %3 offen offset:%4 lds" :: "s"(m0v), "v"(voff), "s"(rs), "s"(soff), "n"(OFF) : "memory");
}
DI void glds16x4(u32 m0v, u32 voff, rsrc_t rs, u32 soff) {
  asm volatile("s_mov_b32 m0, %0\n\ts_nop 0\n\tbuffer_load_dwordx4 %1, %2, %3 offen lds\n\tbuffer_load_dwordx4 %1, %2, %3 offen offset:1024 lds\n\t"
               "buffer_load_dwordx4 %1, %2, %3 offen offset:2048 lds\n\tbuffer_load_dwordx4 %1, %2, %3 offen offset:3072 lds"
               :: "s"(m0v), "v"(voff), "s"(rs), "s"(soff) : "memory");
}
template <int OFF> DI void bld16(u4& d, u32 voff, rsrc_t rs, u32 soff) {
  asm volatile("buffer_load_dwordx4 %0, %1, %2, %3 offen offset:%4" : "=v"(d) : "v"(voff), "s"(rs), "s"(soff), "n"(OFF));
}
template <int OFF> DI void bld16_sc1(u4& d, u32 voff, rsrc_t rs, u32 soff) {
  asm volatile("buffer_load_dwordx4 %0, %1, %2, %3 offen offset:%4 sc1" : "=v"(d) : "v"(voff), "s"(rs), "s"(soff), "n"(OFF));
}
template <int OFF, class T> DI void gld16(T& d, const void* p) { asm volatile("global_load_dwordx4 %0, %1, off offset:%2" : "=v"(d) : "v"(p), "n"(OFF)); }
DI void pin(u32& v) { asm("" : "+v"(v)); }
DI void rawbar() { asm volatile("s_barrier" ::: "memory"); }
template <int N> DI void vmwait() { asm volatile("s_waitcnt vmcnt(%0)" :: "n"(N) : "memory"); }
template <int N> DI void vmwait(u4& d) { asm volatile("s_waitcnt vmcnt(%1)" : "+v"(d) : "n"(N)); }
DI void bar() { asm volatile("s_waitcnt lgkmcnt(0)\n\ts_barrier" ::: "memory"); }
#define SB() __builtin_amdgcn_sched_barrier(0)
#define SGB(m, n) __builtin_amdgcn_sched_group_barrier(m, n, 0)
template <int I, int N, class F> DI void sfor(F&& f) { if constexpr (I < N) { f(std::integral_constant<int, I>{}); sfor<I + 1, N>(f); } }

constexpr int KG_ROW = 544, KG_CH = 57 * KG_ROW, KG_T = 2 * KG_CH, KG_TS = 4160, K1_LDS = KG_T + 2 * KG_TS;

DI f2 cexp_pow2(float a, float fh, float fl, float p) {
  const float e = __builtin_amdgcn_exp2f(a * p);
  const float r = fmaf(fl, p, __builtin_amdgcn_fractf(fh * p));
  return (f2){e * __builtin_amdgcn_cosf(r), e * __builtin_amdgcn_sinf(r)};
}

DI void k1_kgen(char* sm, int b, const u16* lar, const u16* aim, const u16* cre, const u16* cim, const u16* ldt, const u16* dsk, u16* kx) {
  const int t = threadIdx.x, w = __builtin_amdgcn_readfirstlane(t >> 6), lane = t & 63, ch = w >> 1, half = w & 1;
  const int d0 = 64 * (b & 7) + 2 * (b >> 3), d = d0 + ch, n = 64 * half + lane;
  const u32 v_lar = ldnt(lar + d * NM + n), v_aim = ldnt(aim + d * NM + n);
  const u32 cr = ldnt(cre + d * NM + n), ci = ldnt(cim + d * NM + n), cr2 = ldnt(cre + d * NM + (n ^ 64)), ci2 = ldnt(cim + d * NM + (n ^ 64));
  const float dt = rb(exp_acc(b2f(ldt[d])));
  const float ds = b2f(dsk[d]);
  const float zr = -rb(exp_acc(blo(v_lar))) * dt, zi = blo(v_aim) * dt;
  const float a = zr * 1.44269502f;
  const float fh = zi * 0.159154937f, fl = fmaf(zi, 0.159154937f, -fh) + zi * 6.42063833e-9f;
  const f2 e1 = cexp_pow2(a, fh, fl, 1.f), e8 = cexp_pow2(a, -fh, -fl, 8.f), e128 = cexp_pow2(a, fh, fl, 128.f), e1024 = cexp_pow2(a, fh, fl, 1024.f);
  int mx = (int)max(max(cr & 0x7fffu, ci & 0x7fffu), max(cr2 & 0x7fffu, ci2 & 0x7fffu));
  mx = max(mx, __builtin_amdgcn_update_dpp(0, mx, 0x111, 0xf, 0xf, true));
  mx = max(mx, __builtin_amdgcn_update_dpp(0, mx, 0x112, 0xf, 0xf, true));
  mx = max(mx, __builtin_amdgcn_update_dpp(0, mx, 0x114, 0xf, 0xf, true));
  mx = max(mx, __builtin_amdgcn_update_dpp(0, mx, 0x118, 0xf, 0xf, true));
  mx = max(max(__builtin_amdgcn_readlane(mx, 15), __builtin_amdgcn_readlane(mx, 31)), max(__builtin_amdgcn_readlane(mx, 47), __builtin_amdgcn_readlane(mx, 63)));
  const int ex = min(max(mx >> 7, 1), 253);
  const float cs = __int_as_float((254 - ex) << 23), kf = 2.f * dt * __int_as_float(ex << 23);
  if (t < 2) *(u4*)(kx + (d0 + t) * KXS) = (u4){0u, 0u, 0u, 0u};
  constexpr u32 KEEP = 0x03020100u, LL = 0x01000100u, LH = 0x05040100u, HH = 0x05040504u;
  const u32 sel0 = t < 32 ? KEEP : LL, sel1 = t < 32 ? KEEP : t < 128 ? LH : LL;
  const u32 sel2 = t < 32 ? KEEP : t < 128 ? LL : (t & 1) ? HH : LH, sel3 = t < 32 ? KEEP : t < 64 ? LH : HH;
  char* base = sm + ch * KG_CH + n * 4;
  auto put = [&](int row, f2 v) { *(u32*)(base + row * KG_ROW) = pkh(v); };
  f2 lo[8], e[8], m[8], x0, x1, x2, x3, e16, e32, e64;
  lo[0] = (f2){blo(cr) * cs, -blo(ci) * cs};
  e[1] = e1; m[1] = e128;
  put(0, lo[0]); put(16, (f2){1.f, 0.f}); put(17, e1); put(24, e128); put(48, e1024);
  cmul3(e16, e8, e8, e[2], e1, e1, m[2], e128, e128);
  cmul2(lo[1], lo[0], e8, x0, e1024, e1024);
  put(18, e[2]); put(32, m[2]); put(1, lo[1]); put(56, x0);
  cmul3(e32, e16, e16, e[4], e[2], e[2], m[4], m[2], m[2]);
  cmul2(e[3], e[2], e1, m[3], m[2], e128);
  cmul2(lo[2], lo[0], e16, lo[3], lo[1], e16);
  put(20, e[4]); put(40, m[4]); put(19, e[3]); put(36, m[3]); put(2, lo[2]); put(3, lo[3]);
  cmul4(e[5], e[6], e[7], x0, e1, e[2], e[3], m[4], e[4]);
  put(21, e[5]); put(22, e[6]); put(23, e[7]); put(41, x0);
  cmul3(m[5], m[4], e128, m[6], m[4], m[2], m[7], m[4], m[3]);
  put(42, m[5]); put(44, m[6]); put(46, m[7]);
  cmul4(lo[4], lo[5], lo[6], lo[7], lo[0], lo[1], lo[2], lo[3], e32);
  put(4, lo[4]); put(5, lo[5]); put(6, lo[6]); put(7, lo[7]);
  cmul2(e64, e32, e32, x0, e128, e1);
  put(25, x0);
  cmul3(x0, m[5], e[4], x1, m[6], e[4], x2, m[7], e[4]);
  put(43, x0); put(45, x1); put(47, x2);
  cmul4(x0, x1, x2, x3, lo[0], lo[1], lo[2], lo[3], e64);
  put(8, x0); put(9, x1); put(10, x2); put(11, x3);
  cmul4(x0, x1, x2, x3, lo[4], lo[5], lo[6], lo[7], e64);
  put(12, x0); put(13, x1); put(14, x2); put(15, x3);
  cmul4(x0, x1, x2, x3, e[2], e[3], e[4], e[5], e128);
  put(26, x0); put(27, x1); put(28, x2); put(29, x3);
  cmul2(x0, e[6], e128, x1, e[7], e128);
  put(30, x0); put(31, x1);
  cmul3(x0, e[2], m[2], x1, e[4], m[2], x2, e[6], m[2]);
  put(33, x0); put(34, x1); put(35, x2);
  cmul3(x0, e[2], m[3], x1, e[4], m[3], x2, e[6], m[3]);
  put(37, x0); put(38, x1); put(39, x2);
  cmul4(x0, x1, x2, x3, m[1], m[2], m[3], m[4], e1024);
  put(49, x0); put(50, x1); put(51, x2); put(52, x3);
  cmul3(x0, m[5], e1024, x1, m[6], e1024, x2, m[7], e1024);
  put(53, x0); put(54, x1); put(55, x2);
  __syncthreads();
  {
    const int c = lane & 15, g = lane >> 4;
    const char* fb = sm + ch * KG_CH + c * KG_ROW + g * 16;
    u16* T = (u16*)(sm + KG_T + ch * KG_TS);
    f4 acc0 = {0.f, 0.f, 0.f, 0.f}, acc1 = acc0;
    u4 B[8], A[8];
#pragma unroll
    for (int s = 0; s < 8; ++s) { B[s] = *(const u4*)(fb + s * 64); A[s] = *(const u4*)(fb + (16 + 16 * half) * KG_ROW + s * 64); }
    if (!half) {
      u4 A2[8];
#pragma unroll
      for (int s = 0; s < 8; ++s) A2[s] = *(const u4*)(fb + 48 * KG_ROW + s * 64);
      SB();
#pragma unroll
      for (int s = 0; s < 8; ++s) acc0 = mfma16h(A[s], B[s], acc0);
#pragma unroll
      for (int s = 0; s < 8; ++s) acc1 = mfma16h(A2[s], B[s], acc1);
      acc0 *= kf; acc1 *= kf;
      if (lane == 0) acc0[0] += ds;
      *(u2*)(T + 8 * c + 4 * (g & 1) + 128 * (g >> 1)) = (u2){pk(acc0[0], acc0[1]), pk(acc0[2], acc0[3])};
      if (g < 2) {
#pragma unroll
        for (int e = 0; e < 4; ++e) T[1024 + 128 * (4 * g + e) + 8 * c] = (u16)pk(acc1[e], 0.f);
      } else if (lane == 32) T[2048] = (u16)pk(acc1[0], 0.f);
    } else {
      SB();
#pragma unroll
      for (int s = 0; s < 8; ++s) acc0 = mfma16h(A[s], B[s], acc0);
      acc0 *= kf;
#pragma unroll
      for (int e = 0; e < 4; ++e) {
        const int p = g < 2 ? 256 + 128 * g + 2 * e : 512 + 128 * (2 * (g - 2) + (e >> 1)) + 4 * (e & 1);
        T[p + 8 * c] = (u16)pk(acc0[e], 0.f);
      }
    }
  }
  __syncthreads();
  u4 v[2];
  u32 e8v[2];
#pragma unroll
  for (int c2 = 0; c2 < 2; ++c2) {
    const u16* T = (const u16*)(sm + KG_T + c2 * KG_TS);
    v[c2] = *(const u4*)(T + 8 * t);
    e8v[c2] = T[8 * t + 8];
  }
#pragma unroll
  for (int c2 = 0; c2 < 2; ++c2) {
    const u4 o = {__builtin_amdgcn_perm(v[c2].x, v[c2].x, sel0), __builtin_amdgcn_perm(v[c2].z, t < 64 ? v[c2].y : v[c2].x, sel1),
                  __builtin_amdgcn_perm(e8v[c2], t < 128 ? v[c2].z : v[c2].x, sel2), __builtin_amdgcn_perm(e8v[c2], v[c2].w, sel3)};
    *(u4*)(kx + (d0 + c2) * KXS + 8 + 8 * t) = o;
  }
}

DI void k1_xt(char* sm, int b, const u16* x, u16* xt) {
  const int t = threadIdx.x, w = t >> 6, lane = t & 63;
  const int c0 = 64 * (b & 7), r0 = 128 * (b >> 3);
  u4 v[4];
#pragma unroll
  for (int i = 0; i < 4; ++i) v[i] = ldnt((const u4*)(x + (r0 + 32 * i + (t >> 3)) * D + c0 + 8 * (t & 7)));
#pragma unroll
  for (int i = 0; i < 4; ++i) *(u4*)(sm + (32 * i + (t >> 3)) * 144 + 16 * (t & 7)) = v[i];
  __syncthreads();
  const int g = lane >> 4, q = (lane & 15) >> 2, p = lane & 3;
#pragma unroll
  for (int i = 0; i < 4; ++i) {
    const char* a = sm + (32 * i + 8 * g + q) * 144 + 32 * w + 8 * p;
    *(u4*)(xt + (c0 + 16 * w + (lane & 15)) * L + r0 + 32 * i + 8 * g) = cat(trd(a), trd(a + 4 * 144));
  }
}

DI void k1_stats(int b, const u16* x, char* ws) {
  const int t = threadIdx.x, row = 32 * b + (t >> 3);
  u4 v[8];
#pragma unroll
  for (int i = 0; i < 8; ++i) v[i] = ldnt((const u4*)(x + row * D + 64 * i + 8 * (t & 7)));
  float s1 = 0.f, s2 = 0.f, s3 = 0.f, s4 = 0.f;
#pragma unroll
  for (int i = 0; i < 8; ++i)
#pragma unroll
    for (int j = 0; j < 4; j += 2) {
      s1 = dot2(v[i][j], 0x3f803f80u, s1); s2 = dot2(v[i][j], v[i][j], s2);
      s3 = dot2(v[i][j + 1], 0x3f803f80u, s3); s4 = dot2(v[i][j + 1], v[i][j + 1], s4);
    }
  s1 += s3; s2 += s4;
  s1 += dppf<0xb1>(s1); s2 += dppf<0xb1>(s2); s1 += dppf<0x4e>(s1); s2 += dppf<0x4e>(s2); s1 += dppf<0x141>(s1); s2 += dppf<0x141>(s2);
  const f2 st = ln_stats(s1, s2);
  if ((t & 7) == 0) {
    ((u16*)(ws + WS_MEAN))[row] = (u16)(__float_as_uint(st.x) >> 16);
    ((u16*)(ws + WS_SD))[row] = (u16)(__float_as_uint(st.y) >> 16);
    ((u32*)(ws + WS_ST2))[row] = 0u;
  }
  if (t == 0 && b < 16) ((u32*)(ws + WS_CNT))[64 * b] = 0u;
}

DI void k1_mod(char* sm, int b, const u16* c, const u16* wm, char* ws) {
  const int t = threadIdx.x, rg = t >> 3;
  u4 v[16];
  const u32 cc = ((const u32*)c)[t];
#pragma unroll
  for (int i = 0; i < 16; ++i) v[i] = ldnt((const u4*)(wm + (16 * rg + i) * (6 * D) + 64 * b + 8 * (t & 7)));
  u32* sl = (u32*)(sm + 32 * 68 * 4);
  {
    const float c0 = blo(cc), c1 = bhi(cc);
    sl[t] = pk(c0 / (1.f + __expf(-c0)), c1 / (1.f + __expf(-c1)));
  }
  __syncthreads();
  const u4 sa = *(const u4*)(sl + 8 * rg), sb = *(const u4*)(sl + 8 * rg + 4);
  float acc[8] = {0.f, 0.f, 0.f, 0.f, 0.f, 0.f, 0.f, 0.f};
#pragma unroll
  for (int i = 0; i < 8; ++i) {
    const u32 sp = i < 4 ? sa[i & 3] : sb[i & 3];
#pragma unroll
    for (int j = 0; j < 4; ++j) {
      acc[2 * j] = dot2(__builtin_amdgcn_perm(v[2 * i + 1][j], v[2 * i][j], 0x05040100u), sp, acc[2 * j]);
      acc[2 * j + 1] = dot2(__builtin_amdgcn_perm(v[2 * i + 1][j], v[2 * i][j], 0x07060302u), sp, acc[2 * j + 1]);
    }
  }
  float* red = (float*)sm;
  *(f4*)(red + rg * 68 + 8 * (t & 7)) = (f4){acc[0], acc[1], acc[2], acc[3]};
  *(f4*)(red + rg * 68 + 8 * (t & 7) + 4) = (f4){acc[4], acc[5], acc[6], acc[7]};
  __syncthreads();
  float s = 0.f;
#pragma unroll
  for (int i = 0; i < 8; ++i) s += red[((t & 3) + 4 * i) * 68 + (t >> 2)];
  s += dppf<0xb1>(s); s += dppf<0x4e>(s);
  if ((t & 3) == 0) {
    const int j = 64 * b + (t >> 2), part = b >> 3;
    float v = rb(s);
    if (part == 1 || part == 4) v = rb(1.f + v);
    if (part == 4) {
      v = v == 0.f ? 0x1p-60f : v;
      ((float*)(ws + WS_INV))[j - 4 * D] = 1.f / v;
    }
    if (part == 3 || part == 4) ((u16*)(ws + WS_CV))[j - 3 * D] = (u16)(__float_as_uint(v) >> 16);
    ((float*)(ws + WS_MOD))[j] = v;
  }
}

__global__ __launch_bounds__(256, 2) void k1_prep(const u16* x, const u16* lar, const u16* aim, const u16* cre, const u16* cim, const u16* ldt,
                                                  const u16* dsk, const u16* c, const u16* wm, char* ws) {
  __shared__ __attribute__((aligned(16))) char sm[K1_LDS];
  const int b = blockIdx.x;
  if (b < 256) k1_kgen(sm, b, lar, aim, cre, cim, ldt, dsk, (u16*)(ws + WS_KX));
  else if (b < 384) k1_xt(sm, b - 256, x, (u16*)(ws + WS_XT));
  else if (b < 448) k1_stats(b - 384, x, ws);
  else k1_mod(sm, b - 448, c, wm, ws);
}

constexpr int CV_KC = 4128, CV_H = 8 * CV_KC, CV_HP = 1152, CV_X = CV_H + 8 * CV_HP, CV_EX = CV_X + 8192, CV_W = CV_EX + 16384,
              CV_FL = CV_W + 16384, K2_LDS = CV_FL + 16;

DI void k2_conv(char* sm, int b, char* ws) {
  const int t = threadIdx.x, w = __builtin_amdgcn_readfirstlane(t >> 6), lane = t & 63, ch = w >> 1, half = w & 1;
  const int d0 = 64 * (b & 7) + 2 * (b >> 3), d = d0 + ch;
  const u16* xt = (const u16*)(ws + WS_XT);
  const float* mod = (const float*)(ws + WS_MOD);
  const int c = lane & 15, g = lane >> 4;
  u4 xv[2];
  const u4 mv = *(const u4*)(ws + WS_MEAN + 16 * t), sv = *(const u4*)(ws + WS_SD + 16 * t);
#pragma unroll
  for (int i = 0; i < 2; ++i) xv[i] = *(const u4*)(xt + (d0 + i) * L + 8 * t);
  const float ga = mod[2 * D + d], c2 = mod[4 * D + d];
  const f2 c1 = *(const f2*)(mod + D + d0), sa = *(const f2*)(mod + d0);
  SB();
  float rs[8];
#pragma unroll
  for (int j = 0; j < 8; ++j) rs[j] = __builtin_amdgcn_rcpf((j & 1) ? bhi(sv[j >> 1]) : blo(sv[j >> 1]));
#pragma unroll
  for (int i = 0; i < 2; ++i) {
    float h[8];
#pragma unroll
    for (int j = 0; j < 8; ++j) {
      const float xx = (j & 1) ? bhi(xv[i][j >> 1]) : blo(xv[i][j >> 1]);
      const float mm = (j & 1) ? bhi(mv[j >> 1]) : blo(mv[j >> 1]);
      h[j] = fmaf((xx - mm) * rs[j], c1[i], sa[i]);
    }
#pragma unroll
    for (int r = 0; r < 4; ++r) *(u32*)(sm + CV_H + (4 * i + r) * CV_HP + 128 + 4 * t) = pk(h[r], h[r + 4]);
    *(u4*)(sm + CV_X + 4096 * i + 16 * t) = xv[i];
  }
  f4 acc[8];
#pragma unroll
  for (int i = 0; i < 8; ++i) acc[i] = (f4){0.f, 0.f, 0.f, 0.f};
  u32 ka = ldsaddr(sm) + (4 * ch + 2 * half) * CV_KC + 32 + 8 * (8 * g + (lane & 3) - ((lane & 15) >> 2));
  u32 hb = ldsaddr(sm) + CV_H + (4 * ch + 2 * half) * CV_HP + 128 + 8 * c - 16 * g - 8 - 64;
  bar();
  {
    u32 cnt;
    asm volatile(
      "ds_read_b64_tr_b16 v[64:65], %8 offset:32\n"
      "ds_read_b64_tr_b16 v[66:67], %8 offset:0\n"
      "ds_read_b64 v[120:121], %9 offset:832\n"
      "ds_read_b64 v[122:123], %9 offset:840\n"
      "ds_read_b64 v[124:125], %9 offset:960\n"
      "ds_read_b64 v[126:127], %9 offset:968\n"
      "ds_read_b64 v[112:113], %9 offset:576\n"
      "ds_read_b64 v[114:115], %9 offset:584\n"
      "ds_read_b64 v[116:117], %9 offset:704\n"
      "ds_read_b64 v[118:119], %9 offset:712\n"
      "ds_read_b64_tr_b16 v[68:69], %8 offset:544\n"
      "ds_read_b64_tr_b16 v[70:71], %8 offset:512\n"
      "ds_read_b64 v[108:109], %9 offset:448\n"
      "ds_read_b64 v[110:111], %9 offset:456\n"
      "ds_read_b64 v[104:105], %9 offset:320\n"
      "ds_read_b64 v[106:107], %9 offset:328\n"
      "ds_read_b64_tr_b16 v[72:73], %8 offset:1056\n"
      "ds_read_b64_tr_b16 v[74:75], %8 offset:1024\n"
      "ds_read_b64_tr_b16 v[76:77], %8 offset:1568\n"
      "ds_read_b64_tr_b16 v[78:79], %8 offset:1536\n"
      "ds_read_b64 v[100:101], %9 offset:192\n"
      "ds_read_b64 v[102:103], %9 offset:200\n"
      "ds_read_b64_tr_b16 v[80:81], %8 offset:2080\n"
      "ds_read_b64_tr_b16 v[82:83], %8 offset:2048\n"
      "ds_read_b64 v[96:97], %9 offset:64\n"
      "ds_read_b64 v[98:99], %9 offset:72\n"
      "ds_read_b64_tr_b16 v[84:85], %8 offset:2592\n"
      "ds_read_b64_tr_b16 v[86:87], %8 offset:2560\n"
      "ds_read_b64_tr_b16 v[88:89], %8 offset:3104\n"
      "ds_read_b64_tr_b16 v[90:91], %8 offset:3072\n"
      "ds_read_b64_tr_b16 v[92:93], %8 offset:3616\n"
      "ds_read_b64_tr_b16 v[94:95], %8 offset:3584\n"
      "s_mov_b32 %10, 2\n"
      "s_waitcnt lgkmcnt(0)\n"
      "Lconv%=:\n"
      "v_mfma_f32_16x16x32_bf16 %6, v[64:67], v[120:123], %6\n"
      "ds_read_b64_tr_b16 v[128:129], %8 offset:288\n"
      "v_mfma_f32_16x16x32_bf16 %7, v[64:67], v[124:127], %7\n"
      "ds_read_b64_tr_b16 v[130:131], %8 offset:256\n"
      "v_mfma_f32_16x16x32_bf16 %4, v[64:67], v[112:115], %4\n"
      "ds_read_b64 v[184:185], %9 offset:768\n"
      "v_mfma_f32_16x16x32_bf16 %5, v[64:67], v[116:119], %5\n"
      "ds_read_b64 v[186:187], %9 offset:776\n"
      "v_mfma_f32_16x16x32_bf16 %6, v[68:71], v[116:119], %6\n"
      "ds_read_b64 v[188:189], %9 offset:896\n"
      "v_mfma_f32_16x16x32_bf16 %7, v[68:71], v[120:123], %7\n"
      "ds_read_b64 v[190:191], %9 offset:904\n"
      "v_mfma_f32_16x16x32_bf16 %4, v[68:71], v[108:111], %4\n"
      "ds_read_b64 v[176:177], %9 offset:512\n"
      "v_mfma_f32_16x16x32_bf16 %2, v[64:67], v[104:107], %2\n"
      "ds_read_b64 v[178:179], %9 offset:520\n"
      "v_mfma_f32_16x16x32_bf16 %6, v[72:75], v[112:115], %6\n"
      "ds_read_b64 v[180:181], %9 offset:640\n"
      "v_mfma_f32_16x16x32_bf16 %3, v[64:67], v[108:111], %3\n"
      "ds_read_b64 v[182:183], %9 offset:648\n"
      "v_mfma_f32_16x16x32_bf16 %7, v[72:75], v[116:119], %7\n"
      "ds_read_b64_tr_b16 v[132:133], %8 offset:800\n"
      "v_mfma_f32_16x16x32_bf16 %4, v[72:75], v[104:107], %4\n"
      "ds_read_b64_tr_b16 v[134:135], %8 offset:768\n"
      "v_mfma_f32_16x16x32_bf16 %5, v[68:71], v[112:115], %5\n"
      "ds_read_b64 v[172:173], %9 offset:384\n"
      "v_mfma_f32_16x16x32_bf16 %6, v[76:79], v[108:111], %6\n"
      "ds_read_b64 v[174:175], %9 offset:392\n"
      "v_mfma_f32_16x16x32_bf16 %7, v[76:79], v[112:115], %7\n"
      "ds_read_b64 v[168:169], %9 offset:256\n"
      "v_mfma_f32_16x16x32_bf16 %4, v[76:79], v[100:103], %4\n"
      "ds_read_b64 v[170:171], %9 offset:264\n"
      "v_mfma_f32_16x16x32_bf16 %1, v[64:67], v[100:103], %1\n"
      "ds_read_b64_tr_b16 v[136:137], %8 offset:1312\n"
      "v_mfma_f32_16x16x32_bf16 %5, v[72:75], v[108:111], %5\n"
      "ds_read_b64_tr_b16 v[138:139], %8 offset:1280\n"
      "v_mfma_f32_16x16x32_bf16 %6, v[80:83], v[104:107], %6\n"
      "ds_read_b64_tr_b16 v[140:141], %8 offset:1824\n"
      "v_mfma_f32_16x16x32_bf16 %0, v[64:67], v[96:99], %0\n"
      "ds_read_b64_tr_b16 v[142:143], %8 offset:1792\n"
      "v_mfma_f32_16x16x32_bf16 %7, v[80:83], v[108:111], %7\n"
      "ds_read_b64 v[164:165], %9 offset:128\n"
      "v_mfma_f32_16x16x32_bf16 %5, v[76:79], v[104:107], %5\n"
      "ds_read_b64 v[166:167], %9 offset:136\n"
      "v_mfma_f32_16x16x32_bf16 %2, v[68:71], v[100:103], %2\n"
      "ds_read_b64_tr_b16 v[144:145], %8 offset:2336\n"
      "v_mfma_f32_16x16x32_bf16 %6, v[84:87], v[100:103], %6\n"
      "ds_read_b64_tr_b16 v[146:147], %8 offset:2304\n"
      "v_mfma_f32_16x16x32_bf16 %7, v[84:87], v[104:107], %7\n"
      "ds_read_b64 v[160:161], %9 offset:0\n"
      "v_mfma_f32_16x16x32_bf16 %3, v[68:71], v[104:107], %3\n"
      "ds_read_b64 v[162:163], %9 offset:8\n"
      "v_mfma_f32_16x16x32_bf16 %2, v[72:75], v[96:99], %2\n"
      "ds_read_b64_tr_b16 v[148:149], %8 offset:2848\n"
      "v_mfma_f32_16x16x32_bf16 %5, v[80:83], v[100:103], %5\n"
      "ds_read_b64_tr_b16 v[150:151], %8 offset:2816\n"
      "v_mfma_f32_16x16x32_bf16 %7, v[88:91], v[100:103], %7\n"
      "ds_read_b64_tr_b16 v[152:153], %8 offset:3360\n"
      "v_mfma_f32_16x16x32_bf16 %6, v[88:91], v[96:99], %6\n"
      "ds_read_b64_tr_b16 v[154:155], %8 offset:3328\n"
      "v_mfma_f32_16x16x32_bf16 %3, v[72:75], v[100:103], %3\n"
      "ds_read_b64_tr_b16 v[156:157], %8 offset:3872\n"
      "v_mfma_f32_16x16x32_bf16 %1, v[68:71], v[96:99], %1\n"
      "ds_read_b64_tr_b16 v[158:159], %8 offset:3840\n"
      "v_mfma_f32_16x16x32_bf16 %7, v[92:95], v[96:99], %7\n"
      "v_mfma_f32_16x16x32_bf16 %5, v[84:87], v[96:99], %5\n"
      "v_mfma_f32_16x16x32_bf16 %4, v[80:83], v[96:99], %4\n"
      "v_mfma_f32_16x16x32_bf16 %3, v[76:79], v[96:99], %3\n"
      "s_waitcnt lgkmcnt(0)\n"
      "v_add_u32 %8, 4128, %8\n"
      "v_add_u32 %9, 1152, %9\n"
      "v_mfma_f32_16x16x32_bf16 %6, v[128:131], v[184:187], %6\n"
      "ds_read_b64_tr_b16 v[64:65], %8 offset:32\n"
      "v_mfma_f32_16x16x32_bf16 %7, v[128:131], v[188:191], %7\n"
      "ds_read_b64_tr_b16 v[66:67], %8 offset:0\n"
      "v_mfma_f32_16x16x32_bf16 %4, v[128:131], v[176:179], %4\n"
      "ds_read_b64 v[120:121], %9 offset:832\n"
      "v_mfma_f32_16x16x32_bf16 %5, v[128:131], v[180:183], %5\n"
      "ds_read_b64 v[122:123], %9 offset:840\n"
      "v_mfma_f32_16x16x32_bf16 %6, v[132:135], v[180:183], %6\n"
      "ds_read_b64 v[124:125], %9 offset:960\n"
      "v_mfma_f32_16x16x32_bf16 %7, v[132:135], v[184:187], %7\n"
      "ds_read_b64 v[126:127], %9 offset:968\n"
      "v_mfma_f32_16x16x32_bf16 %4, v[132:135], v[172:175], %4\n"
      "ds_read_b64 v[112:113], %9 offset:576\n"
      "v_mfma_f32_16x16x32_bf16 %2, v[128:131], v[168:171], %2\n"
      "ds_read_b64 v[114:115], %9 offset:584\n"
      "v_mfma_f32_16x16x32_bf16 %6, v[136:139], v[176:179], %6\n"
      "ds_read_b64 v[116:117], %9 offset:704\n"
      "v_mfma_f32_16x16x32_bf16 %3, v[128:131], v[172:175], %3\n"
      "ds_read_b64 v[118:119], %9 offset:712\n"
      "v_mfma_f32_16x16x32_bf16 %7, v[136:139], v[180:183], %7\n"
      "ds_read_b64_tr_b16 v[68:69], %8 offset:544\n"
      "v_mfma_f32_16x16x32_bf16 %4, v[136:139], v[168:171], %4\n"
      "ds_read_b64_tr_b16 v[70:71], %8 offset:512\n"
      "v_mfma_f32_16x16x32_bf16 %5, v[132:135], v[176:179], %5\n"
      "ds_read_b64 v[108:109], %9 offset:448\n"
      "v_mfma_f32_16x16x32_bf16 %6, v[140:143], v[172:175], %6\n"
      "ds_read_b64 v[110:111], %9 offset:456\n"
      "v_mfma_f32_16x16x32_bf16 %7, v[140:143], v[176:179], %7\n"
      "ds_read_b64 v[104:105], %9 offset:320\n"
      "v_mfma_f32_16x16x32_bf16 %4, v[140:143], v[164:167], %4\n"
      "ds_read_b64 v[106:107], %9 offset:328\n"
      "v_mfma_f32_16x16x32_bf16 %1, v[128:131], v[164:167], %1\n"
      "ds_read_b64_tr_b16 v[72:73], %8 offset:1056\n"
      "v_mfma_f32_16x16x32_bf16 %5, v[136:139], v[172:175], %5\n"
      "ds_read_b64_tr_b16 v[74:75], %8 offset:1024\n"
      "v_mfma_f32_16x16x32_bf16 %6, v[144:147], v[168:171], %6\n"
      "ds_read_b64_tr_b16 v[76:77], %8 offset:1568\n"
      "v_mfma_f32_16x16x32_bf16 %0, v[128:131], v[160:163], %0\n"
      "ds_read_b64_tr_b16 v[78:79], %8 offset:1536\n"
      "v_mfma_f32_16x16x32_bf16 %7, v[144:147], v[172:175], %7\n"
      "ds_read_b64 v[100:101], %9 offset:192\n"
      "v_mfma_f32_16x16x32_bf16 %5, v[140:143], v[168:171], %5\n"
      "ds_read_b64 v[102:103], %9 offset:200\n"
      "v_mfma_f32_16x16x32_bf16 %2, v[132:135], v[164:167], %2\n"
      "ds_read_b64_tr_b16 v[80:81], %8 offset:2080\n"
      "v_mfma_f32_16x16x32_bf16 %6, v[148:151], v[164:167], %6\n"
      "ds_read_b64_tr_b16 v[82:83], %8 offset:2048\n"
      "v_mfma_f32_16x16x32_bf16 %7, v[148:151], v[168:171], %7\n"
      "ds_read_b64 v[96:97], %9 offset:64\n"
      "v_mfma_f32_16x16x32_bf16 %3, v[132:135], v[168:171], %3\n"
      "ds_read_b64 v[98:99], %9 offset:72\n"
      "v_mfma_f32_16x16x32_bf16 %2, v[136:139], v[160:163], %2\n"
      "ds_read_b64_tr_b16 v[84:85], %8 offset:2592\n"
      "v_mfma_f32_16x16x32_bf16 %5, v[144:147], v[164:167], %5\n"
      "ds_read_b64_tr_b16 v[86:87], %8 offset:2560\n"
      "v_mfma_f32_16x16x32_bf16 %7, v[152:155], v[164:167], %7\n"
      "ds_read_b64_tr_b16 v[88:89], %8 offset:3104\n"
      "v_mfma_f32_16x16x32_bf16 %6, v[152:155], v[160:163], %6\n"
      "ds_read_b64_tr_b16 v[90:91], %8 offset:3072\n"
      "v_mfma_f32_16x16x32_bf16 %3, v[136:139], v[164:167], %3\n"
      "ds_read_b64_tr_b16 v[92:93], %8 offset:3616\n"
      "v_mfma_f32_16x16x32_bf16 %1, v[132:135], v[160:163], %1\n"
      "ds_read_b64_tr_b16 v[94:95], %8 offset:3584\n"
      "v_mfma_f32_16x16x32_bf16 %7, v[156:159], v[160:163], %7\n"
      "v_mfma_f32_16x16x32_bf16 %5, v[148:151], v[160:163], %5\n"
      "v_mfma_f32_16x16x32_bf16 %4, v[144:147], v[160:163], %4\n"
      "v_mfma_f32_16x16x32_bf16 %3, v[140:143], v[160:163], %3\n"
      "s_waitcnt lgkmcnt(0)\n"
      "s_sub_u32 %10, %10, 1\n"
      "s_cmp_lg_u32 %10, 0\n"
      "s_cbranch_scc1 Lconv%=\n"
      "s_nop 7\n"
      : "+v"(acc[0]), "+v"(acc[1]), "+v"(acc[2]), "+v"(acc[3]), "+v"(acc[4]), "+v"(acc[5]), "+v"(acc[6]), "+v"(acc[7]), "+v"(ka), "+v"(hb), "=&s"(cnt)
      :: "memory", "scc", "v64", "v65", "v66", "v67", "v68", "v69", "v70", "v71", "v72", "v73", "v74", "v75", "v76", "v77", "v78", "v79", "v80", "v81", "v82", "v83", "v84", "v85", "v86", "v87", "v88", "v89", "v90", "v91", "v92", "v93", "v94", "v95", "v96", "v97", "v98", "v99", "v100", "v101", "v102", "v103", "v104", "v105", "v106", "v107", "v108", "v109", "v110", "v111", "v112", "v113", "v114", "v115", "v116", "v117", "v118", "v119", "v120", "v121", "v122", "v123", "v124", "v125", "v126", "v127", "v128", "v129", "v130", "v131", "v132", "v133", "v134", "v135", "v136", "v137", "v138", "v139", "v140", "v141", "v142", "v143", "v144", "v145", "v146", "v147", "v148", "v149", "v150", "v151", "v152", "v153", "v154", "v155", "v156", "v157", "v158", "v159", "v160", "v161", "v162", "v163", "v164", "v165", "v166", "v167", "v168", "v169", "v170", "v171", "v172", "v173", "v174", "v175", "v176", "v177", "v178", "v179", "v180", "v181", "v182", "v183", "v184", "v185", "v186", "v187", "v188", "v189", "v190", "v191");
  }
  f4* ex = (f4*)(sm + CV_EX);
  if (half) {
#pragma unroll
    for (int i = 0; i < 4; ++i) { ex[(w * 4 + i) * 64 + lane] = acc[i]; acc[i] = acc[4 + i]; }
  } else {
#pragma unroll
    for (int i = 0; i < 4; ++i) ex[(w * 4 + i) * 64 + lane] = acc[4 + i];
  }
  volatile LAS u32* fl = (volatile LAS u32*)(size_t)(ldsaddr(sm) + CV_FL);
  asm volatile("s_waitcnt lgkmcnt(0)" ::: "memory");
  fl[w] = 1u;
  u2 xr[4];
#pragma unroll
  for (int i = 0; i < 4; ++i) xr[i] = *(const u2*)(sm + CV_X + 4096 * ch + 2 * (256 * (4 * half + i) + 16 * c + 4 * g));
  while (fl[w ^ 1] == 0u) {}
  asm volatile("" ::: "memory");
  u16* x1t = (u16*)(ws + WS_X1T);
  const float gc = ga * c2;
#pragma unroll
  for (int i = 0; i < 4; ++i) {
    const f4 y = (acc[i] + ex[((w ^ 1) * 4 + i) * 64 + lane]) * gc + (f4){blo(xr[i].x), bhi(xr[i].x), blo(xr[i].y), bhi(xr[i].y)} * c2;
    *(u2*)(x1t + d * L + 256 * (4 * half + i) + 16 * c + 4 * g) = (u2){pk(y[0], y[1]), pk(y[2], y[3])};
  }
}

DI void k2_help(char* sm, int b, const u16* wg, const u16* wu, const u16* wd, char* ws) {
  const int t = threadIdx.x - 256, w = __builtin_amdgcn_readfirstlane(t >> 6), lane = t & 63;
  const int d0 = 64 * (b & 7) + 2 * (b >> 3);
  const u16* kx = (const u16*)(ws + WS_KX);
  u4 kv[2];
  u2 kp[2];
#pragma unroll
  for (int i = 0; i < 2; ++i) { kv[i] = *(const u4*)(kx + (d0 + i) * KXS + 8 + 8 * t); kp[i] = *(const u2*)(kx + (d0 + i) * KXS + 4 + 8 * t); }
  SB();
  if (t < 16) *(u4*)(sm + (t >> 1) * CV_KC + 16 * (t & 1)) = (u4){0u, 0u, 0u, 0u};
  if (w == 1) *(u4*)(sm + CV_H + (lane >> 3) * CV_HP + 16 * (t & 7)) = (u4){0u, 0u, 0u, 0u};
  if (t == 128) *(u4*)(sm + CV_FL) = (u4){0u, 0u, 0u, 0u};
#pragma unroll
  for (int i = 0; i < 2; ++i) {
    const u4 v = kv[i];
    const u32 a0 = __builtin_amdgcn_alignbit(kp[i].y, kp[i].x, 16), a1 = __builtin_amdgcn_alignbit(v.x, kp[i].y, 16), a2 = __builtin_amdgcn_alignbit(v.y, v.x, 16),
              a3 = __builtin_amdgcn_alignbit(v.z, v.y, 16), a4 = __builtin_amdgcn_alignbit(v.w, v.z, 16);
    char* kd = sm + 4 * i * CV_KC + 32 + 16 * t;
    *(u4*)kd = v;
    *(u4*)(kd + CV_KC) = (u4){a1, a2, a3, a4};
    *(u4*)(kd + 2 * CV_KC) = (u4){kp[i].y, v.x, v.y, v.z};
    *(u4*)(kd + 3 * CV_KC) = (u4){a0, a1, a2, a3};
  }
  bar();
  const int task = b + 256 * w, kq = task & 3, blk = task >> 2, mat = blk >= 43, jb = blk - 43 * mat;
  const bool work = task < 344;
  const int hh = lane >> 5;
  u4 vec[8];
  if (work) {
    const rsrc_t rw = mkrsrc(mat ? wu : wg, D * DF * 2);
    const u32 vo = (128 * kq + (lane >> 2)) * (DF * 2) + 64 * jb + 16 * (lane & 3);
    asm volatile("s_nop 4");
#pragma unroll
    for (int i = 0; i < 8; ++i) glds16<0>(ldsaddr(sm) + CV_W + 8192 * w + 1024 * i, vo, rw, i * 16 * DF * 2);
    const char* cv = ws + WS_CV + ((lane & 1) ? 0 : 1024) + 256 * kq + 16 * hh;
    sfor<0, 8>([&](auto i) { gld16<32 * i>(vec[i], cv); });
  }
  u32 dummy[4];
  {
    const int row = 32 * (b >> 3) + 8 * w + (lane >> 3);
    const char* base = (const char*)(row >= D ? wu : wg) + (row & (D - 1)) * (DF * 2);
#pragma unroll
    for (int i = 0; i < 2; ++i) {
      const int off = min((b & 1) * 1344 + 128 * ((lane & 7) + 8 * i), (b & 1) ? DF * 2 - 4 : 1340);
      asm volatile("global_load_dword %0, %1, off" : "=v"(dummy[i]) : "v"(base + off));
    }
  }
#pragma unroll
  for (int i = 0; i < 2; ++i)
    asm volatile("global_load_dword %0, %1, off" : "=v"(dummy[2 + i]) : "v"((const char*)wd + (b >> 3) * 44032 + w * 11008 + min(lane + 64 * i, 85) * 128));
  if (work) {
    asm volatile("s_waitcnt vmcnt(4)" : "+v"(vec[0]), "+v"(vec[1]), "+v"(vec[2]), "+v"(vec[3]), "+v"(vec[4]), "+v"(vec[5]), "+v"(vec[6]), "+v"(vec[7]) :: "memory");
    const u32 ra = ldsaddr(sm) + CV_W + 8192 * w + 64 * (8 * hh + ((lane & 15) >> 2)) + 32 * ((lane >> 4) & 1) + 8 * (lane & 3);
    f16v sums;
#pragma unroll
    for (int e = 0; e < 16; ++e) sums[e] = 0.f;
#pragma unroll
    for (int i = 0; i < 8; ++i) sums = mfma32(vec[i], cat(trd(ra + 1024 * i), trd(ra + 1024 * i + 256)), sums);
    if (lane < 32) {
      u32* exf = (u32*)(ws + WS_EX) + ((2 * jb + (lane >> 4)) * 64 + 16 * mat + (lane & 15)) * 4 + kq;
      const float h0 = rb(sums[0]), h1 = rb(sums[1]);
      exf[0] = pk(h0, sums[0] - h0);
      exf[128] = pk(h1, sums[1] - h1);
    }
  }
  asm volatile("s_waitcnt vmcnt(0)" : "+v"(dummy[0]), "+v"(dummy[1]), "+v"(dummy[2]), "+v"(dummy[3]) :: "memory");
}

__global__ __launch_bounds__(512, 2) void k2_conv_prep(const u16* wg, const u16* wu, const u16* wd, char* ws) {
  extern __shared__ __attribute__((aligned(16))) char sm[];
  const int b = blockIdx.x;
  if (__builtin_amdgcn_readfirstlane(threadIdx.x) < 256) k2_conv(sm, b, ws);
  else k2_help(sm, b, wg, wu, wd, ws);
}

constexpr int G1_LEAD = 3, G1_NS = 5, G1_SLOT = 20480;
constexpr int G2_RES = 19, G2_FL = 4 * G2_RES * 2048, G1_SV = G2_FL + 64, K3_LDS = G1_SV + 512;
constexpr int G2_LEAD = 6;
constexpr int HH_T = NU * 1024;

template <int K> DI u32 g1_pair(const f16v& acc, float rs, float sd) {
  const f2 g = (f2){acc[2 * K], acc[2 * K + 1]} * rs;
  const f2 z = g * (g * g * -0.10294324f + -2.30220819f);
  const f2 dn = (f2){__builtin_amdgcn_exp2f(z.x), __builtin_amdgcn_exp2f(z.y)} * sd + sd;
  const f2 r = g * (f2){acc[8 + 2 * K], acc[9 + 2 * K]} * (f2){__builtin_amdgcn_rcpf(dn.x), __builtin_amdgcn_rcpf(dn.y)};
  return pk(r.x, r.y);
}
DI u4 g1_tile(const f16v& acc, float rs, float sd) { return (u4){g1_pair<0>(acc, rs, sd), g1_pair<1>(acc, rs, sd), g1_pair<2>(acc, rs, sd), g1_pair<3>(acc, rs, sd)}; }

DI void g1_stats(char* sm, int rt, char* ws) {
  const int t = threadIdx.x;
  float* red = (float*)sm;
  if (t < 512) {
    const u16* xp = (const u16*)(ws + WS_X1T) + (t >> 4) * L + 128 * rt + 8 * (t & 15);
    u4 v[16];
    float iv[16];
#pragma unroll
    for (int i = 0; i < 16; ++i) { v[i] = *(const u4*)(xp + 32 * i * L); iv[i] = ((const float*)(ws + WS_INV))[(t >> 4) + 32 * i]; }
    f2 s1[4], s2[4];
#pragma unroll
    for (int j = 0; j < 4; ++j) s1[j] = s2[j] = (f2){0.f, 0.f};
#pragma unroll
    for (int i = 0; i < 16; ++i)
#pragma unroll
      for (int j = 0; j < 4; ++j) { const f2 x = (f2){blo(v[i][j]), bhi(v[i][j])} * iv[i]; s1[j] += x; s2[j] += x * x; }
#pragma unroll
    for (int j = 0; j < 4; ++j) *(f4*)(red + (t >> 4) * 260 + 16 * (t & 15) + 4 * j) = (f4){s1[j].x, s2[j].x, s1[j].y, s2[j].y};
  }
  __syncthreads();
  if (t < 512) {
    f2 p = {0.f, 0.f};
#pragma unroll
    for (int i = 0; i < 8; ++i) p += *(const f2*)(red + (8 * (t & 3) + i) * 260 + 2 * (t >> 2));
    p.x += dppf<0xb1>(p.x); p.y += dppf<0xb1>(p.y); p.x += dppf<0x4e>(p.x); p.y += dppf<0x4e>(p.y);
    const f2 ms = ln_stats(p.x, p.y);
    if ((t & 3) == 0)
      __hip_atomic_store((u32*)(ws + WS_ST2) + 128 * rt + (t >> 2), (__float_as_uint(ms.x) >> 16) | __float_as_uint(ms.y), __ATOMIC_RELAXED, __HIP_MEMORY_SCOPE_AGENT);
  }
  __syncthreads();
}

__global__ __launch_bounds__(768, 3) void k3_mlp(const u16* wg, const u16* wu, const u16* wd, char* ws, u16* out) {
  extern __shared__ __attribute__((aligned(16))) char sm[];
  const int b = blockIdx.x, j = b >> 3;
  const int t = threadIdx.x, w12 = __builtin_amdgcn_readfirstlane(t >> 6), w = w12 & 3, lane = t & 63, hh = lane >> 5;
  const u32 lb0 = ldsaddr(sm);
  const int q = (lane & 15) >> 2, p = lane & 3, g = lane >> 4;
  const int lt = 4 * (b & 7) + (j & 3), dt = j >> 2;
  auto kfirst = [&](int kq) { const u32 k = 22 * kq - (kq == 3) + 3 * dt; return min(k, k - NU); };
  auto knext = [](u32 k) { return min(k + 1, k + 1 - NU); };
  const rsrc_t rd = mkrsrc(wd, DF * D * 2);
  const u32 vd = (lane >> 3) * 1024 + 128 * dt + 16 * ((lane & 7) ^ (4 * ((lane >> 4) & 1)));
  volatile LAS u32* fl = (volatile LAS u32*)(size_t)(lb0 + G2_FL);
  auto fill2 = [&](int i0, int n, int slot0) {
    u32 kd = kfirst(w) + i0;
    kd = min(kd, kd - NU);
#pragma unroll 1
    for (int i = 0; i < n; ++i) {
      glds16<0>(lb0 + (G2_RES * w + slot0 + i) * 2048, vd, rd, kd * 16384);
      glds16<0>(lb0 + (G2_RES * w + slot0 + i) * 2048 + 1024, vd, rd, kd * 16384 + 8192);
      kd = knext(kd);
    }
  };
  {
    const int rt = 4 * ((b & 7) >> 1) + (j & 3), cg = (j >> 2) + 8 * (b & 1) - 1;
    if (t == 0) fl[0] = 0u;
    if (cg < 0) {
      g1_stats(sm, rt, ws);
    } else if (w12 >= 8) {
      const rsrc_t rx = mkrsrc(ws + WS_X1T, D * L * 2), rg = mkrsrc(wg, D * DF * 2), ru = mkrsrc(wu, D * DF * 2);
      const u32 vx = (4 * w + g) * 4096 + 256 * rt + 16 * ((lane & 15) ^ ((g << 2) | w));
      u32 vw[3];
#pragma unroll
      for (int i = 0; i < 3; ++i) {
        const int pc = (3 * (w & 1) + i) * 64 + lane, row = pc / 12, chk = pc - 12 * row;
        vw[i] = row * (DF * 2) + min(192 * cg + 16 * (chk ^ (w & 2)), DF * 2 - 16);
      }
      const rsrc_t rwm = w < 2 ? rg : ru;
      auto stage = [&](int i, int slot) {
        const u32 m = lb0 + slot * G1_SLOT;
        glds16<0>(m + 1024 * w, vx, rx, i * 131072);
        glds16<0>(m + 4096 + 1024 * w, vx, rx, i * 131072 + 65536);
#pragma unroll
        for (int k = 0; k < 3; ++k) glds16<0>(m + 8192 + 3072 * w + 1024 * k, vw[k], rwm, i * (32 * DF * 2));
      };
      asm volatile("s_nop 4");
      static_assert(G1_LEAD + 2 <= G1_NS, "ring too short");
#pragma unroll 1
      for (int i = 0; i < G1_LEAD - 1; ++i) stage(i, i);
      int slot = G1_LEAD - 1;
#pragma unroll 1
      for (int i = 0; i < 17 - G1_LEAD; ++i) {
        stage(i + G1_LEAD - 1, slot);
        slot = slot == G1_NS - 1 ? 0 : slot + 1;
        vmwait<5 * (G1_LEAD - 1)>();
        rawbar();
      }
      const u32* st2 = (const u32*)(ws + WS_ST2) + 128 * rt + 32 * w + (lane & 31);
      u32 v;
      asm volatile("global_load_dword %0, %1, off sc1" : "=v"(v) : "v"(st2));
      sfor<17 - G1_LEAD, 16>([&](auto i) { vmwait<5 * (15 - i) + 1>(); rawbar(); });
      asm volatile("s_waitcnt vmcnt(0)" : "+v"(v));
#pragma unroll 1
      for (int spin = 0; spin < (1 << 16) && !__all(v != 0u); ++spin) {
        __builtin_amdgcn_s_sleep(2);
        v = __hip_atomic_load(st2, __ATOMIC_RELAXED, __HIP_MEMORY_SCOPE_AGENT);
      }
      ((u32*)(sm + G1_SV))[32 * w + (lane & 31)] = v;
      bar();
      bar();
    } else if (w12 >= 4) {
      const char* pf = (const char*)wd + j * (43 * 1024) + w * (43 * 256);
#pragma unroll 1
      for (int i = 0; i < 18; ++i) {
        rawbar();
      }
    } else {
      const int wl = w & 1, wu3 = 3 * (w >> 1), u0 = 6 * cg + wu3;
      u32 xa[2][2], wa[3];
#pragma unroll
      for (int c = 0; c < 2; ++c)
#pragma unroll
        for (int tt = 0; tt < 2; ++tt)
          xa[c][tt] = lb0 + 256 * (8 * hh + 4 * tt + q) + 8 * (p & 1) + 16 * ((4 * (2 * wl + c) + 2 * (g & 1) + (p >> 1)) ^ ((q << 2) | (2 * hh + tt)));
#pragma unroll
      for (int v = 0; v < 3; ++v) wa[v] = lb0 + 8192 + 6144 * (g & 1) + 192 * (8 * hh + q) + 16 * ((2 * (wu3 + v) + (p >> 1)) ^ (2 * (g & 1))) + 8 * (p & 1);
      u4 ex[3];
#pragma unroll
      for (int v = 0; v < 3; ++v) ex[v] = *(const u4*)(ws + WS_EX + min(u0 + v, NU - 1) * 1024 + lane * 16);
      f16v acc[6];
#pragma unroll
      for (int i = 0; i < 6; ++i)
#pragma unroll
        for (int e = 0; e < 16; ++e) acc[i][e] = 0.f;
#pragma unroll
      for (int i = 0; i < 6; ++i) asm volatile("" : "+v"(acc[i]));
      rawbar();
      {
        u32 c0, c1, c2;
        asm volatile(
          "ds_read_b64_tr_b16 v[112:113], %6 offset:0\n"
          "ds_read_b64_tr_b16 v[114:115], %7 offset:0\n"
          "ds_read_b64_tr_b16 v[120:121], %10 offset:0\n"
          "ds_read_b64_tr_b16 v[122:123], %10 offset:768\n"
          "ds_read_b64_tr_b16 v[116:117], %8 offset:0\n"
          "ds_read_b64_tr_b16 v[118:119], %9 offset:0\n"
          "ds_read_b64_tr_b16 v[124:125], %11 offset:0\n"
          "ds_read_b64_tr_b16 v[126:127], %11 offset:768\n"
          "ds_read_b64_tr_b16 v[128:129], %12 offset:0\n"
          "ds_read_b64_tr_b16 v[130:131], %12 offset:768\n"
          "s_mov_b32 %13, 16\n"
          "s_mov_b32 %14, 0\n"
          "Lg1%=:\n"
          "s_add_u32 %14, %14, 1\n"
          "s_cmp_eq_u32 %14, 5\n"
          "s_cselect_b32 %15, -81920, %16\n"
          "s_cselect_b32 %14, 0, %14\n"
          "s_waitcnt lgkmcnt(6)\n"
          "v_mfma_f32_32x32x16_bf16 %0, v[120:123], v[112:115], %0\n"
          "ds_read_b64_tr_b16 v[132:133], %6 offset:4096\n"
          "ds_read_b64_tr_b16 v[134:135], %7 offset:4096\n"
          "s_waitcnt lgkmcnt(6)\n"
          "v_mfma_f32_32x32x16_bf16 %1, v[120:123], v[116:119], %1\n"
          "ds_read_b64_tr_b16 v[140:141], %10 offset:3072\n"
          "ds_read_b64_tr_b16 v[142:143], %10 offset:3840\n"
          "s_waitcnt lgkmcnt(6)\n"
          "v_mfma_f32_32x32x16_bf16 %2, v[124:127], v[112:115], %2\n"
          "ds_read_b64_tr_b16 v[136:137], %8 offset:4096\n"
          "ds_read_b64_tr_b16 v[138:139], %9 offset:4096\n"
          "v_mfma_f32_32x32x16_bf16 %3, v[124:127], v[116:119], %3\n"
          "ds_read_b64_tr_b16 v[144:145], %11 offset:3072\n"
          "ds_read_b64_tr_b16 v[146:147], %11 offset:3840\n"
          "s_waitcnt lgkmcnt(8)\n"
          "v_mfma_f32_32x32x16_bf16 %4, v[128:131], v[112:115], %4\n"
          "ds_read_b64_tr_b16 v[148:149], %12 offset:3072\n"
          "ds_read_b64_tr_b16 v[150:151], %12 offset:3840\n"
          "v_add_u32 %6, %15, %6\n"
          "v_add_u32 %7, %15, %7\n"
          "v_add_u32 %8, %15, %8\n"
          "v_add_u32 %9, %15, %9\n"
          "v_mfma_f32_32x32x16_bf16 %5, v[128:131], v[116:119], %5\n"
          "v_add_u32 %10, %15, %10\n"
          "v_add_u32 %11, %15, %11\n"
          "v_add_u32 %12, %15, %12\n"
          "s_barrier\n"
          "s_waitcnt lgkmcnt(6)\n"
          "v_mfma_f32_32x32x16_bf16 %0, v[140:143], v[132:135], %0\n"
          "ds_read_b64_tr_b16 v[112:113], %6 offset:0\n"
          "ds_read_b64_tr_b16 v[114:115], %7 offset:0\n"
          "s_waitcnt lgkmcnt(6)\n"
          "v_mfma_f32_32x32x16_bf16 %1, v[140:143], v[136:139], %1\n"
          "ds_read_b64_tr_b16 v[120:121], %10 offset:0\n"
          "ds_read_b64_tr_b16 v[122:123], %10 offset:768\n"
          "s_waitcnt lgkmcnt(6)\n"
          "v_mfma_f32_32x32x16_bf16 %2, v[144:147], v[132:135], %2\n"
          "ds_read_b64_tr_b16 v[116:117], %8 offset:0\n"
          "ds_read_b64_tr_b16 v[118:119], %9 offset:0\n"
          "v_mfma_f32_32x32x16_bf16 %3, v[144:147], v[136:139], %3\n"
          "ds_read_b64_tr_b16 v[124:125], %11 offset:0\n"
          "ds_read_b64_tr_b16 v[126:127], %11 offset:768\n"
          "s_waitcnt lgkmcnt(8)\n"
          "v_mfma_f32_32x32x16_bf16 %4, v[148:151], v[132:135], %4\n"
          "ds_read_b64_tr_b16 v[128:129], %12 offset:0\n"
          "ds_read_b64_tr_b16 v[130:131], %12 offset:768\n"
          "s_sub_u32 %13, %13, 1\n"
          "s_cmp_lg_u32 %13, 0\n"
          "v_mfma_f32_32x32x16_bf16 %5, v[148:151], v[136:139], %5\n"
          "s_cbranch_scc1 Lg1%=\n"
          "s_waitcnt lgkmcnt(0)\n"
          : "+v"(acc[0]), "+v"(acc[1]), "+v"(acc[2]), "+v"(acc[3]), "+v"(acc[4]), "+v"(acc[5]), "+v"(xa[0][0]), "+v"(xa[0][1]), "+v"(xa[1][0]), "+v"(xa[1][1]),
            "+v"(wa[0]), "+v"(wa[1]), "+v"(wa[2]), "=&s"(c0), "=&s"(c1), "=&s"(c2)
          : "s"(G1_SLOT) : "memory", "scc", "v112", "v113", "v114", "v115", "v116", "v117", "v118", "v119", "v120", "v121", "v122", "v123", "v124", "v125", "v126", "v127", "v128", "v129", "v130", "v131", "v132", "v133", "v134", "v135", "v136", "v137", "v138", "v139", "v140", "v141", "v142", "v143", "v144", "v145", "v146", "v147", "v148", "v149", "v150", "v151");
      }
      u32 sv[2];
#pragma unroll
      for (int c = 0; c < 2; ++c) sv[c] = ((const u32*)(sm + G1_SV))[64 * wl + 32 * c + (lane & 31)];
      bar();
#pragma unroll
      for (int c = 0; c < 2; ++c) {
        const u32 bv = hh ? __builtin_amdgcn_perm(sv[c], sv[c], 0x03020302u) : (__builtin_amdgcn_perm(sv[c], sv[c], 0x01000100u) ^ 0x80008000u);
#pragma unroll
        for (int v = 0; v < 3; ++v) acc[2 * v + c] = mfma32(ex[v], (u4){bv, bv, bv, bv}, acc[2 * v + c]);
      }
      char* hp = ws + WS_HH + (4 * rt + 2 * wl) * HH_T + u0 * 1024 + 16 * lane;
#pragma unroll
      for (int v = 0; v < 3; ++v) {
        if (u0 + v < NU) {
#pragma unroll
          for (int c = 0; c < 2; ++c) {
            const float sd = bhi(sv[c]);
            const u4 o = g1_tile(acc[2 * v + c], __builtin_amdgcn_rcpf(sd), sd);
            char* dst = hp + c * HH_T + v * 1024;
            asm volatile("global_store_dwordx4 %0, %1, off sc1\n\ts_nop 1" :: "v"(dst), "v"(o) : "memory");
          }
        }
      }
      {
        asm volatile("s_waitcnt vmcnt(0)" ::: "memory");
        if (lane == 0 && __hip_atomic_fetch_add((LAS u32*)fl, 1u, __ATOMIC_RELAXED, __HIP_MEMORY_SCOPE_WORKGROUP) == 3u)
          __hip_atomic_fetch_add((u32*)(ws + WS_CNT) + 64 * rt, 1u, __ATOMIC_RELAXED, __HIP_MEMORY_SCOPE_AGENT);
      }
    }
  }
  if (w12 >= 8) {
    asm volatile("s_nop 4");
    fill2(G2_RES, 3, 0);
    fill2(3, G2_RES - 3, 3);
    vmwait<2 * (G2_RES - 3)>();
    rawbar();
    rawbar();
    fill2(0, 3, 0);
    vmwait<0>();
    rawbar();
    rawbar();
    rawbar();
  } else {
    const int wl = w12 & 1, kq = w12 >> 1, dc = 64 * dt + 16 * kq + 4 * hh;
    f4 gb[2], xr[2];
#pragma unroll
    for (int e = 0; e < 2; ++e) {
      gb[e] = *(const f4*)((const float*)(ws + WS_MOD) + 5 * D + dc + 8 * e);
      xr[e] = *(const f4*)((const float*)(ws + WS_INV) + dc + 8 * e);
#pragma unroll
      for (int i = 0; i < 4; ++i) xr[e][i] *= b2f(((const u16*)(ws + WS_X1T))[(dc + 8 * e + i) * L + 64 * lt + 32 * wl + (lane & 31)]);
    }
    u32 pa[2];
#pragma unroll
    for (int e = 0; e < 2; ++e)
      pa[e] = lb0 + G2_RES * 2048 * kq + 128 * (4 * hh + q) + 16 * ((((4 * e + 2 * (g & 1)) ^ (2 * kq)) + (p >> 1)) ^ (4 * (q >> 1))) + 8 * (p & 1);
    f16v acc[2];
#pragma unroll
    for (int e = 0; e < 16; ++e) acc[0][e] = acc[1][e] = 0.f;
    asm volatile("" : "+v"(xr[0]), "+v"(xr[1]), "+v"(gb[0]), "+v"(gb[1]), "+v"(acc[0]), "+v"(acc[1]));
    rawbar();
    u4 A[2][2], AR[3][2];
#pragma unroll
    for (int i = 0; i < 6; ++i) AR[i >> 1][i & 1] = cat(trd(pa[i & 1] + (i >> 1) * 2048), trd(pa[i & 1] + (i >> 1) * 2048 + 1024));
    asm volatile("s_waitcnt lgkmcnt(0)\n\ts_barrier" : "+v"(AR[0][0]), "+v"(AR[0][1]), "+v"(AR[1][0]), "+v"(AR[1][1]), "+v"(AR[2][0]), "+v"(AR[2][1]) :: "memory");
    if (w12 == 0) {
      const u32* cnt = (const u32*)(ws + WS_CNT) + 64 * (lt >> 1);
#pragma unroll 1
      for (int spin = 0; spin < (1 << 18); ++spin)
        if (__hip_atomic_load(cnt, __ATOMIC_RELAXED, __HIP_MEMORY_SCOPE_AGENT) >= 15u) break;
    }
    rawbar();
    const rsrc_t rh = mkrsrc(ws + WS_HH + (2 * lt + wl) * HH_T, HH_T);
    constexpr int LEAD = G2_LEAD, NH = LEAD + 2;
    u4 H[NH];
    u32 kh = kfirst(kq);
    auto ldh = [&](auto i) { if constexpr (i < 22) { bld16_sc1<0>(H[i % NH], lane * 16, rh, kh * 1024); kh = knext(kh); } };
    sfor<0, LEAD>(ldh);
    auto rda = [&](auto i) {
#pragma unroll
      for (int e = 0; e < 2; ++e) A[i & 1][e] = cat(trd(pa[e] + i * 2048), trd(pa[e] + i * 2048 + 1024));
    };
    rda(std::integral_constant<int, 0>{});
    sfor<0, 22>([&](auto i) {
      vmwait<(i < 22 - LEAD ? LEAD - 1 : 21 - i)>(H[i % NH]);
      SB();
      if constexpr (i + 1 < G2_RES) rda(std::integral_constant<int, i + 1>{});
      if (i < 21 || kq < 2) {
#pragma unroll
        for (int e = 0; e < 2; ++e) acc[e] = mfma32(i < G2_RES ? A[i & 1][e] : AR[i < G2_RES ? 0 : i - G2_RES][e], H[i % NH], acc[e]);
      }
      if constexpr (i + 1 < G2_RES) { SGB(0x8, 1); SGB(0x100, 2); SGB(0x8, 1); SGB(0x100, 2); }
      SB();
      ldh(std::integral_constant<int, i + LEAD>{});
      if constexpr (i == G2_RES - 1) rawbar();
    });
    f4* red = (f4*)sm + wl * 1536 + lane;
#pragma unroll
    for (int k = 1; k < 4; ++k)
#pragma unroll
      for (int e = 0; e < 2; ++e) {
        const f16v& a = acc[k >> 1];
        const int r = 8 * (k & 1) + 4 * e;
        red[(kq ^ k) * 384 + (k - 1) * 128 + 64 * e] = (f4){a[r], a[r + 1], a[r + 2], a[r + 3]};
      }
    bar();
    u2 o[2];
#pragma unroll
    for (int e = 0; e < 2; ++e) {
      const f4* rp = red + kq * 384 + 64 * e;
      const f4 y = ((f4){acc[0][4 * e], acc[0][4 * e + 1], acc[0][4 * e + 2], acc[0][4 * e + 3]} + rp[0] + rp[128] + rp[256]) * gb[e] + xr[e];
      o[e] = (u2){pk(y[0], y[1]), pk(y[2], y[3])};
    }
    const auto r0 = __builtin_amdgcn_permlane32_swap(o[0].x, o[1].x, false, false);
    const auto r1 = __builtin_amdgcn_permlane32_swap(o[0].y, o[1].y, false, false);
    *(u4*)(out + (64 * lt + 32 * wl + (lane & 31)) * D + 64 * dt + 16 * kq + 8 * hh) = (u4){r0[0], r1[0], r0[1], r1[1]};
  }
}

extern "C" void kernel_launch(void* const* d_in, const int* in_sizes, int n_in, void* d_out, int out_size, void* d_ws, size_t ws_size,
                              hipStream_t stream) {
  const u16* x = (const u16*)d_in[0];
  const u16 *wg = (const u16*)d_in[8], *wu = (const u16*)d_in[9], *wd = (const u16*)d_in[10];
  char* ws = (char*)d_ws;
  (void)hipFuncSetAttribute((const void*)k2_conv_prep, hipFuncAttributeMaxDynamicSharedMemorySize, K2_LDS);
  (void)hipFuncSetAttribute((const void*)k3_mlp, hipFuncAttributeMaxDynamicSharedMemorySize, K3_LDS);
  k1_prep<<<496, 256, 0, stream>>>(x, (const u16*)d_in[1], (const u16*)d_in[2], (const u16*)d_in[3], (const u16*)d_in[4], (const u16*)d_in[5],
                                   (const u16*)d_in[6], (const u16*)d_in[11], (const u16*)d_in[12], ws);
  k2_conv_prep<<<256, 512, K2_LDS, stream>>>(wg, wu, wd, ws);
  k3_mlp<<<256, 768, K3_LDS, stream>>>(wg, wu, wd, ws, (u16*)d_out);
}
```

```cpp
#include <hip/hip_runtime.h>
#include <hip/hip_bf16.h>
#include <stdint.h>
#include <type_traits>

typedef unsigned short u16;
typedef unsigned u32;
typedef float f2 __attribute__((ext_vector_type(2)));
typedef float f4 __attribute__((ext_vector_type(4)));
typedef float f16v __attribute__((ext_vector_type(16)));
typedef u32 u2 __attribute__((ext_vector_type(2)));
typedef u32 u4 __attribute__((ext_vector_type(4)));
typedef u4 u4a8 __attribute__((aligned(8)));
typedef short s4 __attribute__((ext_vector_type(4)));
typedef __bf16 bf2 __attribute__((ext_vector_type(2)));
typedef __bf16 bf8 __attribute__((ext_vector_type(8)));
typedef _Float16 h2 __attribute__((ext_vector_type(2)));
typedef _Float16 h8 __attribute__((ext_vector_type(8)));
#define LAS __attribute__((address_space(3)))
#define DI __device__ __forceinline__

constexpr int L = 2048, D = 512, NM = 128, DF = 1376, NU = 86;

constexpr size_t WS_GB = 0, WS_INV = 2 << 10, WS_EPOCH = 4 << 10, WS_ST2 = 8 << 10, WS_CNT = 16 << 10, WS_ST1 = 32 << 10, WS_EX = 64 << 10,
                 WS_FX = 20 << 10, WS_MG = 160 << 10, WS_X1T = 256 << 10, WS_HH = WS_X1T + (2 << 20), WS_XT = WS_HH + (5504 << 10);

template <class T> DI T ldnt(const T* p) { return __builtin_nontemporal_load(p); }
DI float blo(u32 v) { return __uint_as_float(v << 16); }
DI float bhi(u32 v) { return __uint_as_float(v & 0xffff0000u); }
DI float b2f(u16 v) { return __uint_as_float(((u32)v) << 16); }
DI u32 pk(float a, float b) { return __builtin_bit_cast(u32, __builtin_convertvector((f2){a, b}, bf2)); }
DI u32 pkh(f2 v) { return __builtin_bit_cast(u32, __builtin_convertvector(v, h2)); }
DI float rb(float a) { return blo(pk(a, a)); }
DI u2 trd(const char* p) { return __builtin_bit_cast(u2, __builtin_amdgcn_ds_read_tr16_b64_v4i16((LAS s4*)p)); }
DI u2 trd(u32 a) { return __builtin_bit_cast(u2, __builtin_amdgcn_ds_read_tr16_b64_v4i16((LAS s4*)(size_t)a)); }
DI u4 cat(u2 a, u2 b) { return (u4){a.x, a.y, b.x, b.y}; }
DI float dot2(u32 a, u32 b, float c) { return __builtin_amdgcn_fdot2_f32_bf16(__builtin_bit_cast(bf2, a), __builtin_bit_cast(bf2, b), c, false); }
DI f4 mfma16(u4 a, u4 b, f4 c) { return __builtin_amdgcn_mfma_f32_16x16x32_bf16(__builtin_bit_cast(bf8, a), __builtin_bit_cast(bf8, b), c, 0, 0, 0); }
DI f4 mfma16h(u4 a, u4 b, f4 c) { return __builtin_amdgcn_mfma_f32_16x16x32_f16(__builtin_bit_cast(h8, a), __builtin_bit_cast(h8, b), c, 0, 0, 0); }
DI f16v mfma32(u4 a, u4 b, f16v c) { return __builtin_amdgcn_mfma_f32_32x32x16_bf16(__builtin_bit_cast(bf8, a), __builtin_bit_cast(bf8, b), c, 0, 0, 0); }
#define CM_MUL(d, a, b) "v_pk_mul_f32 " d ", " a ", " b " op_sel_hi:[0,1]\n\t"
#define CM_FMA(d, a, b) "v_pk_fma_f32 " d ", " a ", " b ", " d " op_sel:[1,1,0] op_sel_hi:[1,0,1] neg_lo:[0,1,0]\n\t"
DI void cmul2(f2& r0, f2 a0, f2 b0, f2& r1, f2 a1, f2 b1) {
  asm(CM_MUL("%0", "%2", "%3") CM_MUL("%1", "%4", "%5") CM_FMA("%0", "%2", "%3") CM_FMA("%1", "%4", "%5")
      : "=&v"(r0), "=&v"(r1) : "v"(a0), "v"(b0), "v"(a1), "v"(b1));
}
DI void cmul3(f2& r0, f2 a0, f2 b0, f2& r1, f2 a1, f2 b1, f2& r2, f2 a2, f2 b2) {
  asm(CM_MUL("%0", "%3", "%4") CM_MUL("%1", "%5", "%6") CM_MUL("%2", "%7", "%8") CM_FMA("%0", "%3", "%4") CM_FMA("%1", "%5", "%6") CM_FMA("%2", "%7", "%8")
      : "=&v"(r0), "=&v"(r1), "=&v"(r2) : "v"(a0), "v"(b0), "v"(a1), "v"(b1), "v"(a2), "v"(b2));
}
DI void cmul4(f2& r0, f2& r1, f2& r2, f2& r3, f2 a0, f2 a1, f2 a2, f2 a3, f2 b) {
  asm(CM_MUL("%0", "%4", "%8") CM_MUL("%1", "%5", "%8") CM_MUL("%2", "%6", "%8") CM_MUL("%3", "%7", "%8")
      CM_FMA("%0", "%4", "%8") CM_FMA("%1", "%5", "%8") CM_FMA("%2", "%6", "%8") CM_FMA("%3", "%7", "%8")
      : "=&v"(r0), "=&v"(r1), "=&v"(r2), "=&v"(r3) : "v"(a0), "v"(a1), "v"(a2), "v"(a3), "v"(b));
}
DI float exp_acc(float x) {
  const float t = x * 1.44269502f, r = fmaf(x, 1.44269502f, -t) + x * 1.92596299e-8f, e = __builtin_amdgcn_exp2f(t);
  return fmaf(e, r * 0.693147182f, e);
}
template <int CTRL> DI float dppf(float v) { return __int_as_float(__builtin_amdgcn_update_dpp(0, __float_as_int(v), CTRL, 0xf, 0xf, true)); }
DI float sum32(float v) {
  auto r = __builtin_amdgcn_permlane32_swap(__float_as_uint(v), __float_as_uint(v), false, false);
  return __uint_as_float(r[0]) + __uint_as_float(r[1]);
}
DI f2 ln_stats(float s1, float s2) {
  const float mean = s1 * (1.f / D);
  const float var = fmaxf(fmaf(-mean, mean, s2 * (1.f / D)), 0.f);
  return (f2){rb(mean), rb(__builtin_amdgcn_sqrtf(rb(rb(var) + 1e-5f)))};
}
typedef __amdgpu_buffer_rsrc_t rsrc_t;
DI rsrc_t mkrsrc(const void* p, u32 bytes) { return __builtin_amdgcn_make_buffer_rsrc((void*)p, 0, bytes, 0x00020000); }
DI u32 ldsaddr(const char* p) { return (u32)(size_t)(LAS const char*)p; }
template <int OFF> DI void glds16(u32 m0v, u32 voff, rsrc_t rs, u32 soff) {
  asm volatile("s_mov_b32 m0, %0\n\ts_nop 0\n\tbuffer_load_dwordx4 %1, %2, %3 offen offset:%4 lds" :: "s"(m0v), "v"(voff), "s"(rs), "s"(soff), "n"(OFF) : "memory");
}
DI void glds16x4(u32 m0v, u32 voff, rsrc_t rs, u32 soff) {
  asm volatile("s_mov_b32 m0, %0\n\ts_nop 0\n\tbuffer_load_dwordx4 %1, %2, %3 offen lds\n\tbuffer_load_dwordx4 %1, %2, %3 offen offset:1024 lds\n\t"
               "buffer_load_dwordx4 %1, %2, %3 offen offset:2048 lds\n\tbuffer_load_dwordx4 %1, %2, %3 offen offset:3072 lds"
               :: "s"(m0v), "v"(voff), "s"(rs), "s"(soff) : "memory");
}
template <int OFF> DI void bld16(u4& d, u32 voff, rsrc_t rs, u32 soff) {
  asm volatile("buffer_load_dwordx4 %0, %1, %2, %3 offen offset:%4" : "=v"(d) : "v"(voff), "s"(rs), "s"(soff), "n"(OFF));
}
template <int OFF> DI void bld16_sc1(u4& d, u32 voff, rsrc_t rs, u32 soff) {
  asm volatile("buffer_load_dwordx4 %0, %1, %2, %3 offen offset:%4 sc1" : "=v"(d) : "v"(voff), "s"(rs), "s"(soff), "n"(OFF));
}
template <int OFF, class T> DI void gld16(T& d, const void* p) { asm volatile("global_load_dwordx4 %0, %1, off offset:%2" : "=v"(d) : "v"(p), "n"(OFF)); }
DI void pin(u32& v) { asm("" : "+v"(v)); }
DI void rawbar() { asm volatile("s_barrier" ::: "memory"); }
template <int N> DI void vmwait() { asm volatile("s_waitcnt vmcnt(%0)" :: "n"(N) : "memory"); }
template <int N> DI void vmwait(u4& d) { asm volatile("s_waitcnt vmcnt(%1)" : "+v"(d) : "n"(N)); }
DI void bar() { asm volatile("s_waitcnt lgkmcnt(0)\n\ts_barrier" ::: "memory"); }
#define SB() __builtin_amdgcn_sched_barrier(0)
#define SGB(m, n) __builtin_amdgcn_sched_group_barrier(m, n, 0)
template <int I, int N, class F> DI void sfor(F&& f) { if constexpr (I < N) { f(std::integral_constant<int, I>{}); sfor<I + 1, N>(f); } }

typedef unsigned long long u64;
constexpr int KG_ROW = 544, KG_CH = 57 * KG_ROW, KG_T = 2 * KG_CH, KG_TS = 4160;
constexpr int CV_EX = 0, CV_K = KG_T + 2 * KG_TS, CV_KC = 4128, CV_H = CV_K + 8 * CV_KC, CV_HP = 1152, CV_X = CV_H + 8 * CV_HP, CV_ST = CV_X + 8192,
              CV_W = CV_ST + 8192, CV_V = CV_W + 16384, CV_M = CV_V + 1024, CV_FL = CV_M + 256, K1_LDS = CV_FL + 64;
enum { FL_EPOCH = 0, FL_EPOK = 2, FL_KG = 3, FL_MOD = 7, FL_MAIN = 8, FL_XT = 9, FL_XD = 10, FL_GO = 11, FL_EXCH = 12 };
typedef volatile LAS u32* ldsw;
DI void lds_arrive(ldsw w) {
  asm volatile("s_waitcnt lgkmcnt(0)" ::: "memory");
  if ((threadIdx.x & 63) == 0) __hip_atomic_fetch_add((LAS u32*)w, 1u, __ATOMIC_RELAXED, __HIP_MEMORY_SCOPE_WORKGROUP);
}
DI void lds_wait(ldsw w, u32 n) { while (*w < n) {} asm volatile("" ::: "memory"); }
DI u64 get_epoch(ldsw fl) { lds_wait(fl + FL_EPOK, 1u); return ((u64)fl[FL_EPOCH + 1] << 32) | fl[FL_EPOCH]; }
DI u4 ld16_sc1(const void* p) { u4 d; asm volatile("global_load_dwordx4 %0, %1, off sc1\n\ts_waitcnt vmcnt(0)" : "=v"(d) : "v"(p) : "memory"); return d; }
template <int OFF> DI void gld16_sc1(u4& d, const void* p) { asm volatile("global_load_dwordx4 %0, %1, off offset:%2 sc1" : "=v"(d) : "v"(p), "n"(OFF) : "memory"); }
DI bool mg_ok(u4 g, u64 ep) { const u32 h = (u32)(ep >> 8), l = (u32)ep << 24; return g.y == h && g.w == h && (g.x & 0xff000000u) == l && (g.z & 0xff000000u) == l; }
DI float mg_sum(u4 g) { return __uint_as_float(g.x << 8) + __uint_as_float(g.z << 8); }
DI float scale_c(float v) { v = rb(1.f + rb(v)); return v == 0.f ? 0x1p-60f : v; }

DI f2 cexp_pow2(float a, float fh, float fl, float p) {
  const float e = __builtin_amdgcn_exp2f(a * p);
  const float r = fmaf(fl, p, __builtin_amdgcn_fractf(fh * p));
  return (f2){e * __builtin_amdgcn_cosf(r), e * __builtin_amdgcn_sinf(r)};
}

DI void k1_help(char* sm, int b, const u16* lar, const u16* aim, const u16* cre, const u16* cim, const u16* ldt, const u16* dsk,
                const u16* wg, const u16* wu, const u16* wd, char* ws) {
  const int t = threadIdx.x - 256, w = __builtin_amdgcn_readfirstlane(t >> 6), lane = t & 63, ch = w >> 1, half = w & 1;
  const int d0 = 64 * (b & 7) + 2 * (b >> 3), d = d0 + ch, n = 64 * half + lane;
  const ldsw fl = (ldsw)(size_t)(ldsaddr(sm) + CV_FL);
  const u32 v_lar = ldnt(lar + d * NM + n), v_aim = ldnt(aim + d * NM + n);
  const u32 cr = ldnt(cre + d * NM + n), ci = ldnt(cim + d * NM + n), cr2 = ldnt(cre + d * NM + (n ^ 64)), ci2 = ldnt(cim + d * NM + (n ^ 64));
  const u16 v_ldt = ldt[d], v_dsk = dsk[d];
  rawbar();
  const float dt = rb(exp_acc(b2f(v_ldt)));
  const float ds = b2f(v_dsk);
  const float zr = -rb(exp_acc(blo(v_lar))) * dt, zi = blo(v_aim) * dt;
  const float a = zr * 1.44269502f;
  const float fh = zi * 0.159154937f, fq = fmaf(zi, 0.159154937f, -fh) + zi * 6.42063833e-9f;
  const f2 e1 = cexp_pow2(a, fh, fq, 1.f), e8 = cexp_pow2(a, -fh, -fq, 8.f), e128 = cexp_pow2(a, fh, fq, 128.f), e1024 = cexp_pow2(a, fh, fq, 1024.f);
  int mx = (int)max(max(cr & 0x7fffu, ci & 0x7fffu), max(cr2 & 0x7fffu, ci2 & 0x7fffu));
  mx = max(mx, __builtin_amdgcn_update_dpp(0, mx, 0x111, 0xf, 0xf, true));
  mx = max(mx, __builtin_amdgcn_update_dpp(0, mx, 0x112, 0xf, 0xf, true));
  mx = max(mx, __builtin_amdgcn_update_dpp(0, mx, 0x114, 0xf, 0xf, true));
  mx = max(mx, __builtin_amdgcn_update_dpp(0, mx, 0x118, 0xf, 0xf, true));
  mx = max(max(__builtin_amdgcn_readlane(mx, 15), __builtin_amdgcn_readlane(mx, 31)), max(__builtin_amdgcn_readlane(mx, 47), __builtin_amdgcn_readlane(mx, 63)));
  const int ex = min(max(mx >> 7, 1), 253);
  const float cs = __int_as_float((254 - ex) << 23), kf = 2.f * dt * __int_as_float(ex << 23);
  char* base = sm + ch * KG_CH + n * 4;
  auto put = [&](int row, f2 v) { *(u32*)(base + row * KG_ROW) = pkh(v); };
  f2 lo[8], e[8], m[8], x0, x1, x2, x3, e16, e32, e64;
  lo[0] = (f2){blo(cr) * cs, -blo(ci) * cs};
  e[1] = e1; m[1] = e128;
  put(0, lo[0]); put(16, (f2){1.f, 0.f}); put(17, e1); put(24, e128); put(48, e1024);
  cmul3(e16, e8, e8, e[2], e1, e1, m[2], e128, e128);
  cmul2(lo[1], lo[0], e8, x0, e1024, e1024);
  put(18, e[2]); put(32, m[2]); put(1, lo[1]); put(56, x0);
  cmul3(e32, e16, e16, e[4], e[2], e[2], m[4], m[2], m[2]);
  cmul2(e[3], e[2], e1, m[3], m[2], e128);
  cmul2(lo[2], lo[0], e16, lo[3], lo[1], e16);
  put(20, e[4]); put(40, m[4]); put(19, e[3]); put(36, m[3]); put(2, lo[2]); put(3, lo[3]);
  cmul4(e[5], e[6], e[7], x0, e1, e[2], e[3], m[4], e[4]);
  put(21, e[5]); put(22, e[6]); put(23, e[7]); put(41, x0);
  cmul3(m[5], m[4], e128, m[6], m[4], m[2], m[7], m[4], m[3]);
  put(42, m[5]); put(44, m[6]); put(46, m[7]);
  cmul4(lo[4], lo[5], lo[6], lo[7], lo[0], lo[1], lo[2], lo[3], e32);
  put(4, lo[4]); put(5, lo[5]); put(6, lo[6]); put(7, lo[7]);
  cmul2(e64, e32, e32, x0, e128, e1);
  put(25, x0);
  cmul3(x0, m[5], e[4], x1, m[6], e[4], x2, m[7], e[4]);
  put(43, x0); put(45, x1); put(47, x2);
  cmul4(x0, x1, x2, x3, lo[0], lo[1], lo[2], lo[3], e64);
  put(8, x0); put(9, x1); put(10, x2); put(11, x3);
  cmul4(x0, x1, x2, x3, lo[4], lo[5], lo[6], lo[7], e64);
  put(12, x0); put(13, x1); put(14, x2); put(15, x3);
  cmul4(x0, x1, x2, x3, e[2], e[3], e[4], e[5], e128);
  put(26, x0); put(27, x1); put(28, x2); put(29, x3);
  cmul2(x0, e[6], e128, x1, e[7], e128);
  put(30, x0); put(31, x1);
  cmul3(x0, e[2], m[2], x1, e[4], m[2], x2, e[6], m[2]);
  put(33, x0); put(34, x1); put(35, x2);
  cmul3(x0, e[2], m[3], x1, e[4], m[3], x2, e[6], m[3]);
  put(37, x0); put(38, x1); put(39, x2);
  cmul4(x0, x1, x2, x3, m[1], m[2], m[3], m[4], e1024);
  put(49, x0); put(50, x1); put(51, x2); put(52, x3);
  cmul3(x0, m[5], e1024, x1, m[6], e1024, x2, m[7], e1024);
  put(53, x0); put(54, x1); put(55, x2);
  lds_arrive(fl + FL_KG + ch);
  lds_wait(fl + FL_KG + ch, 2u);
  {
    const int c = lane & 15, g = lane >> 4;
    const char* fb = sm + ch * KG_CH + c * KG_ROW + g * 16;
    u16* T = (u16*)(sm + KG_T + ch * KG_TS);
    f4 acc0 = {0.f, 0.f, 0.f, 0.f}, acc1 = acc0;
    u4 B[8], A[8];
#pragma unroll
    for (int s = 0; s < 8; ++s) { B[s] = *(const u4*)(fb + s * 64); A[s] = *(const u4*)(fb + (16 + 16 * half) * KG_ROW + s * 64); }
    if (!half) {
      u4 A2[8];
#pragma unroll
      for (int s = 0; s < 8; ++s) A2[s] = *(const u4*)(fb + 48 * KG_ROW + s * 64);
      SB();
#pragma unroll
      for (int s = 0; s < 8; ++s) acc0 = mfma16h(A[s], B[s], acc0);
#pragma unroll
      for (int s = 0; s < 8; ++s) acc1 = mfma16h(A2[s], B[s], acc1);
      acc0 *= kf; acc1 *= kf;
      if (lane == 0) acc0[0] += ds;
      *(u2*)(T + 8 * c + 4 * (g & 1) + 128 * (g >> 1)) = (u2){pk(acc0[0], acc0[1]), pk(acc0[2], acc0[3])};
      if (g < 2) {
#pragma unroll
        for (int e = 0; e < 4; ++e) T[1024 + 128 * (4 * g + e) + 8 * c] = (u16)pk(acc1[e], 0.f);
      } else if (lane == 32) T[2048] = (u16)pk(acc1[0], 0.f);
    } else {
      SB();
#pragma unroll
      for (int s = 0; s < 8; ++s) acc0 = mfma16h(A[s], B[s], acc0);
      acc0 *= kf;
#pragma unroll
      for (int e = 0; e < 4; ++e) {
        const int p = g < 2 ? 256 + 128 * g + 2 * e : 512 + 128 * (2 * (g - 2) + (e >> 1)) + 4 * (e & 1);
        T[p + 8 * c] = (u16)pk(acc0[e], 0.f);
      }
    }
  }
  lds_arrive(fl + FL_KG + 2 + ch);
  constexpr u32 KEEP = 0x03020100u, LL = 0x01000100u, LH = 0x05040100u, HH = 0x05040504u;
  const u32 sel0 = n < 16 ? KEEP : LL, sel1 = n < 16 ? KEEP : n < 64 ? LH : LL, sel2 = n < 16 ? KEEP : n < 64 ? LL : LH, sel2b = n < 64 ? sel2 : HH;
  const u32 sel3 = n < 16 ? KEEP : n < 32 ? LH : HH, selp2 = n <= 16 ? KEEP : n <= 64 ? LL : HH, selp3 = n <= 16 ? KEEP : n <= 32 ? LH : HH;
  if (t < 16) *(u4*)(sm + CV_K + (t >> 1) * CV_KC + 16 * (t & 1)) = (u4){0u, 0u, 0u, 0u};
  lds_wait(fl + FL_KG + 2 + ch, 2u);
  {
    const u16* T = (const u16*)(sm + KG_T + ch * KG_TS) + 16 * n;
    const u4 v0 = *(const u4*)T, v1 = *(const u4*)(T + 8);
    const u2 vp = *(const u2*)(T - 4);
    const u32 e16 = T[16];
    u32 k[10];
    k[0] = n ? __builtin_amdgcn_perm(v0.x, vp.x, selp2) : 0u;
    k[1] = n ? __builtin_amdgcn_perm(v0.x, vp.y, selp3) : 0u;
    k[2] = __builtin_amdgcn_perm(v0.x, v0.x, sel0); k[3] = __builtin_amdgcn_perm(v0.z, n < 32 ? v0.y : v0.x, sel1);
    k[4] = __builtin_amdgcn_perm(v1.x, n < 64 ? v0.z : v0.x, sel2); k[5] = __builtin_amdgcn_perm(v1.x, v0.w, sel3);
    k[6] = __builtin_amdgcn_perm(v1.x, v1.x, sel0); k[7] = __builtin_amdgcn_perm(v1.z, n < 32 ? v1.y : v1.x, sel1);
    k[8] = __builtin_amdgcn_perm(e16, v1.z, sel2b); k[9] = __builtin_amdgcn_perm(e16, v1.w, sel3);
    u32 o[9];
#pragma unroll
    for (int i = 0; i < 9; ++i) o[i] = __builtin_amdgcn_alignbit(k[i + 1], k[i], 16);
    char* kd = sm + CV_K + 4 * ch * CV_KC + 32 + 32 * n;
#pragma unroll
    for (int i = 0; i < 2; ++i) {
      *(u4*)(kd + 16 * i) = (u4){k[4 * i + 2], k[4 * i + 3], k[4 * i + 4], k[4 * i + 5]};
      *(u4*)(kd + 16 * i + CV_KC) = (u4){o[4 * i + 1], o[4 * i + 2], o[4 * i + 3], o[4 * i + 4]};
      *(u4*)(kd + 16 * i + 2 * CV_KC) = (u4){k[4 * i + 1], k[4 * i + 2], k[4 * i + 3], k[4 * i + 4]};
      *(u4*)(kd + 16 * i + 3 * CV_KC) = (u4){o[4 * i], o[4 * i + 1], o[4 * i + 2], o[4 * i + 3]};
    }
  }
  bar();
  const int task = b + 256 * w, kq = task & 3, blk = task >> 2, mat = blk >= 43, jb = blk - 43 * mat;
  const bool work = task < 344;
  if (work) {
    const rsrc_t rw = mkrsrc(mat ? wu : wg, D * DF * 2);
    const u32 vo = (128 * kq + (lane >> 2)) * (DF * 2) + 64 * jb + 16 * (lane & 3);
    asm volatile("s_nop 4");
#pragma unroll
    for (int i = 0; i < 8; ++i) glds16<0>(ldsaddr(sm) + CV_W + 8192 * w + 1024 * i, vo, rw, i * 16 * DF * 2);
  }
  u32 dummy[4];
  {
    const int row = 32 * (b >> 3) + 8 * w + (lane >> 3);
    const char* base = (const char*)(row >= D ? wu : wg) + (row & (D - 1)) * (DF * 2);
#pragma unroll
    for (int i = 0; i < 2; ++i) {
      const int off = min((b & 1) * 1344 + 128 * ((lane & 7) + 8 * i), (b & 1) ? DF * 2 - 4 : 1340);
      asm volatile("global_load_dword %0, %1, off" : "=v"(dummy[i]) : "v"(base + off));
      asm volatile("global_load_dword %0, %1, off" : "=v"(dummy[2 + i]) : "v"((const char*)wd + (b >> 3) * 44032 + w * 11008 + min(lane + 64 * i, 85) * 128));
    }
  }
  if (work) {
    const u64 ep = get_epoch(fl);
    const char* gp = ws + WS_MG + (3 * D + 128 * kq + 2 * lane) * 32;
    u4 gs[4], gc[4];
#pragma unroll 1
    for (int spin = 0; spin < (1 << 16); ++spin) {
      sfor<0, 4>([&](auto i) { gld16_sc1<16 * i>(gs[i], gp); gld16_sc1<16 * i>(gc[i], gp + D * 32); });
      asm volatile("s_waitcnt vmcnt(0)" : "+v"(gs[0]), "+v"(gs[1]), "+v"(gs[2]), "+v"(gs[3]), "+v"(gc[0]), "+v"(gc[1]), "+v"(gc[2]), "+v"(gc[3]) :: "memory");
      bool ok = true;
#pragma unroll
      for (int i = 0; i < 4; ++i) ok = ok && mg_ok(gs[i], ep) && mg_ok(gc[i], ep);
      if (__all(ok)) break;
    }
    u32* vv = (u32*)(sm + CV_V + 512 * w);
    vv[lane] = pk(scale_c(mg_sum(gc[0]) + mg_sum(gc[1])), scale_c(mg_sum(gc[2]) + mg_sum(gc[3])));
    vv[64 + lane] = pk(mg_sum(gs[0]) + mg_sum(gs[1]), mg_sum(gs[2]) + mg_sum(gs[3]));
    const int hh = lane >> 5;
    const u32 ra = ldsaddr(sm) + CV_W + 8192 * w + 64 * (8 * hh + ((lane & 15) >> 2)) + 32 * ((lane >> 4) & 1) + 8 * (lane & 3);
    f16v sums;
#pragma unroll
    for (int e = 0; e < 16; ++e) sums[e] = 0.f;
#pragma unroll
    for (int i = 0; i < 8; ++i) sums = mfma32(*(const u4*)(vv + 64 * (lane & 1) + 8 * i + 4 * hh), cat(trd(ra + 1024 * i), trd(ra + 1024 * i + 256)), sums);
    if (lane < 32) {
      u32* exf = (u32*)(ws + WS_EX) + ((2 * jb + (lane >> 4)) * 64 + 16 * mat + (lane & 15)) * 4 + kq;
      const float h0 = rb(sums[0]), h1 = rb(sums[1]);
      exf[0] = pk(h0, sums[0] - h0);
      exf[128] = pk(h1, sums[1] - h1);
    }
  }
  asm volatile("s_waitcnt vmcnt(0)" : "+v"(dummy[0]), "+v"(dummy[1]), "+v"(dummy[2]), "+v"(dummy[3]) :: "memory");
}

DI float red2(float a, float b, bool rows16) {
  if (rows16) { const auto r = __builtin_amdgcn_permlane16_swap(__float_as_uint(a), __float_as_uint(b), false, false); return __uint_as_float(r[0]) + __uint_as_float(r[1]); }
  const auto r = __builtin_amdgcn_permlane32_swap(__float_as_uint(a), __float_as_uint(b), false, false);
  return __uint_as_float(r[0]) + __uint_as_float(r[1]);
}

DI void k1_main(char* sm, int b, const u16* x, const u16* c, const u16* wm, char* ws) {
  const int t = threadIdx.x, w = __builtin_amdgcn_readfirstlane(t >> 6), lane = t & 63, ch = w >> 1, half = w & 1;
  const int d0 = 64 * (b & 7) + 2 * (b >> 3), d = d0 + ch;
  const ldsw fl = (ldsw)(size_t)(ldsaddr(sm) + CV_FL);
  const int c16 = lane & 15, g = lane >> 4;
  __builtin_amdgcn_s_setprio(2);
  if (t < 16) fl[t] = 0u;
  if (w == 3) *(u4*)(sm + CV_H + (lane >> 3) * CV_HP + 16 * (t & 7)) = (u4){0u, 0u, 0u, 0u};
  if (w < 2) {
    const int row = 8 * b + 4 * w + g, mq = b / 48, mc = b - 48 * mq, rg = t >> 3;
    u4 xs[4], wv[8], cv;
    u64 ep0;
    if (w == 0) ep0 = *(const u64*)(ws + WS_EPOCH);
#pragma unroll
    for (int i = 0; i < 4; ++i) xs[i] = ldnt((const u4*)(x + row * D + 128 * i + 8 * c16));
    if (b < 192) {
      cv = *(const u4*)(c + 128 * mq + 8 * rg);
#pragma unroll
      for (int i = 0; i < 8; ++i) wv[i] = ldnt((const u4*)(wm + (128 * mq + 8 * rg + i) * (6 * D) + 64 * mc + 8 * (t & 7)));
    }
    bar();
    if (w == 0) {
      fl[FL_EPOCH] = (u32)(ep0 + 1); fl[FL_EPOCH + 1] = (u32)((ep0 + 1) >> 32);
      asm volatile("s_waitcnt lgkmcnt(0)" ::: "memory");
      fl[FL_EPOK] = 1u;
    }
    float s1 = 0.f, s2 = 0.f, s3 = 0.f, s4 = 0.f;
#pragma unroll
    for (int i = 0; i < 4; ++i)
#pragma unroll
      for (int j = 0; j < 4; j += 2) {
        s1 = dot2(xs[i][j], 0x3f803f80u, s1); s2 = dot2(xs[i][j], xs[i][j], s2);
        s3 = dot2(xs[i][j + 1], 0x3f803f80u, s3); s4 = dot2(xs[i][j + 1], xs[i][j + 1], s4);
      }
    s1 += s3; s2 += s4;
    s1 += dppf<0xb1>(s1); s2 += dppf<0xb1>(s2); s1 += dppf<0x4e>(s1); s2 += dppf<0x4e>(s2);
    s1 += dppf<0x141>(s1); s2 += dppf<0x141>(s2); s1 += dppf<0x140>(s1); s2 += dppf<0x140>(s2);
    const f2 st = ln_stats(s1, s2);
    const u64 ep = get_epoch(fl);
    if (c16 == 0) {
      __hip_atomic_store((u64*)(ws + WS_ST1) + row, (ep << 32) | (__float_as_uint(st.x) >> 16) | __float_as_uint(st.y), __ATOMIC_RELAXED, __HIP_MEMORY_SCOPE_AGENT);
      ((u32*)(ws + WS_ST2))[row] = 0u;
    }
    if (t == 0 && b < 16) ((u32*)(ws + WS_CNT))[64 * b] = 0u;
    if (b < 192) {
      float acc[8] = {0.f, 0.f, 0.f, 0.f, 0.f, 0.f, 0.f, 0.f};
#pragma unroll
      for (int i = 0; i < 4; ++i) {
        const float c0 = blo(cv[i]), c1 = bhi(cv[i]);
        const u32 sp = pk(c0 * __builtin_amdgcn_rcpf(1.f + __expf(-c0)), c1 * __builtin_amdgcn_rcpf(1.f + __expf(-c1)));
#pragma unroll
        for (int j = 0; j < 4; ++j) {
          acc[2 * j] = dot2(__builtin_amdgcn_perm(wv[2 * i + 1][j], wv[2 * i][j], 0x05040100u), sp, acc[2 * j]);
          acc[2 * j + 1] = dot2(__builtin_amdgcn_perm(wv[2 * i + 1][j], wv[2 * i][j], 0x07060302u), sp, acc[2 * j + 1]);
        }
      }
      const float p0 = red2(red2(acc[0], acc[4], false), red2(acc[2], acc[6], false), true), p1 = red2(red2(acc[1], acc[5], false), red2(acc[3], acc[7], false), true);
      const float q0 = p0 + dppf<0x128>(p0), q1 = p1 + dppf<0x128>(p1);
      float v = (lane & 8) ? q1 : q0;
      float* mx = (float*)(sm + CV_M);
      if (w == 1) { mx[lane] = v; lds_arrive(fl + FL_MOD); }
      else {
        lds_wait(fl + FL_MOD, 1u);
        v += mx[lane];
        const u32 bits = __float_as_uint(v) + 0x80u;
        __hip_atomic_store((u64*)(ws + WS_MG) + (64 * mc + 8 * (lane & 7) + (lane >> 3)) * 4 + mq, (ep << 24) | (bits >> 8), __ATOMIC_RELAXED, __HIP_MEMORY_SCOPE_AGENT);
      }
    }
  } else {
    const int tg = t - 128, r0 = 64 * (b >> 3), c0 = 64 * (b & 7), q = c16 >> 2, p = lane & 3;
    u4 v[4];
#pragma unroll
    for (int i = 0; i < 4; ++i) v[i] = ldnt((const u4*)(x + (r0 + 16 * i + (tg >> 3)) * D + c0 + 8 * (tg & 7)));
    bar();
    char* tb = sm + CV_X;
#pragma unroll
    for (int i = 0; i < 4; ++i) *(u4*)(tb + (16 * i + (tg >> 3)) * 144 + 16 * (tg & 7)) = v[i];
    lds_arrive(fl + FL_XT);
    lds_wait(fl + FL_XT, 2u);
#pragma unroll
    for (int i = 0; i < 4; ++i) {
      const char* a = tb + (32 * (i & 1) + 8 * g + q) * 144 + 64 * half + 32 * (i >> 1) + 8 * p;
      const u4 o = cat(trd(a), trd(a + 4 * 144));
      const u16* dst = (const u16*)(ws + WS_XT) + (c0 + 32 * half + 16 * (i >> 1) + c16) * L + r0 + 32 * (i & 1) + 8 * g;
      asm volatile("global_store_dwordx4 %0, %1, off sc1\n\ts_nop 1" :: "v"(dst), "v"(o) : "memory");
    }
    const u64 ep = get_epoch(fl);
    asm volatile("s_waitcnt vmcnt(0)" ::: "memory");
    if (lane == 0 && __hip_atomic_fetch_add((LAS u32*)fl + FL_XD, 1u, __ATOMIC_RELAXED, __HIP_MEMORY_SCOPE_WORKGROUP) == 1u)
      __hip_atomic_store((u32*)(ws + WS_FX) + 32 * (b & 7) + (b >> 3), (u32)ep, __ATOMIC_RELAXED, __HIP_MEMORY_SCOPE_AGENT);
  }
  const u64 ep = get_epoch(fl);
  float sa[2], c1[2], ga, c2;
  u4 xv[2];
  if (w == 0) {
    const u32* fx = (const u32*)(ws + WS_FX) + 32 * (b & 7) + (lane & 31);
#pragma unroll 1
    for (int spin = 0; spin < (1 << 16); ++spin) {
      if (__all(__hip_atomic_load(fx, __ATOMIC_RELAXED, __HIP_MEMORY_SCOPE_AGENT) == (u32)ep)) break;
      __builtin_amdgcn_s_sleep(2);
    }
    fl[FL_GO] = 1u;
  } else {
    while (fl[FL_GO] == 0u) __builtin_amdgcn_s_sleep(1);
  }
  {
    const char* xp = ws + WS_XT + d0 * (L * 2) + 16 * t;
    gld16_sc1<0>(xv[0], xp); gld16_sc1<0>(xv[1], xp + L * 2);
    const char* sp = ws + WS_ST1 + 16 * t;
    const int ml = lane < 20 ? lane : 19, mch = ml >> 2;
    const char* mp = ws + WS_MG + (((mch + (mch > 2)) * D + d0) * 4 + 2 * (ml & 3)) * 8;
    u4 gs[4], gm;
#pragma unroll 1
    for (int spin = 0; spin < (1 << 16); ++spin) {
      gld16_sc1<0>(gs[0], sp); gld16_sc1<0>(gs[1], sp + 4096); gld16_sc1<0>(gs[2], sp + 8192); gld16_sc1<0>(gs[3], sp + 12288); gld16_sc1<0>(gm, mp);
      asm volatile("s_waitcnt vmcnt(0)" : "+v"(gs[0]), "+v"(gs[1]), "+v"(gs[2]), "+v"(gs[3]), "+v"(gm) :: "memory");
      bool ok = mg_ok(gm, ep);
#pragma unroll
      for (int i = 0; i < 4; ++i) ok = ok && gs[i].y == (u32)ep && gs[i].w == (u32)ep;
      if (__all(ok)) break;
    }
    asm volatile("" : "+v"(xv[0]), "+v"(xv[1]));
#pragma unroll
    for (int i = 0; i < 4; ++i) *(u2*)(sm + CV_ST + 8 * t + 2048 * i) = (u2){gs[i].x, gs[i].z};
    lds_arrive(fl + FL_MAIN);
    float v = mg_sum(gm);
    v += dppf<0xb1>(v);
    const float vr = rb(v), vs = scale_c(v), vp = rb(1.f + vr);
    auto pick = [](float z, int l) { return __int_as_float(__builtin_amdgcn_readlane(__float_as_int(z), l)); };
    sa[0] = pick(vr, 0); sa[1] = pick(vr, 2); c1[0] = pick(vp, 4); c1[1] = pick(vp, 6);
    ga = ch ? pick(vr, 10) : pick(vr, 8); c2 = ch ? pick(vs, 14) : pick(vs, 12);
    if (w == 0) {
      if (lane == 12 || lane == 14) ((float*)(ws + WS_INV))[d0 + ((lane >> 1) & 1)] = 1.f / vs;
      if (lane == 16 || lane == 18) ((float*)(ws + WS_GB))[d0 + ((lane >> 1) & 1)] = vr;
    }
    lds_wait(fl + FL_MAIN, 4u);
    if (b == 0 && t == 0) *(u64*)(ws + WS_EPOCH) = ep;
  }
  {
    const u4 m0 = *(const u4*)(sm + CV_ST + 32 * t), m1 = *(const u4*)(sm + CV_ST + 32 * t + 16);
    float rs[8], mm[8];
#pragma unroll
    for (int j = 0; j < 8; ++j) { const u32 z = j < 4 ? m0[j & 3] : m1[j & 3]; rs[j] = __builtin_amdgcn_rcpf(bhi(z)); mm[j] = blo(z); }
#pragma unroll
    for (int i = 0; i < 2; ++i) {
      *(u4*)(sm + CV_X + 4096 * i + 16 * t) = xv[i];
      float h[8];
#pragma unroll
      for (int j = 0; j < 8; ++j) h[j] = fmaf((((j & 1) ? bhi(xv[i][j >> 1]) : blo(xv[i][j >> 1])) - mm[j]) * rs[j], c1[i], sa[i]);
#pragma unroll
      for (int r = 0; r < 4; ++r) *(u32*)(sm + CV_H + (4 * i + r) * CV_HP + 128 + 4 * t) = pk(h[r], h[r + 4]);
    }
  }
  f4 acc[8];
#pragma unroll
  for (int i = 0; i < 8; ++i) acc[i] = (f4){0.f, 0.f, 0.f, 0.f};
  u32 ka = ldsaddr(sm) + CV_K + (4 * ch + 2 * half) * CV_KC + 32 + 8 * (8 * g + (lane & 3) - ((lane & 15) >> 2));
  u32 hb = ldsaddr(sm) + CV_H + (4 * ch + 2 * half) * CV_HP + 128 + 8 * c16 - 16 * g - 8 - 64;
  bar();
  {
    u32 cnt;
    asm volatile(
      "ds_read_b64_tr_b16 v[64:65], %8 offset:32\n"
      "ds_read_b64_tr_b16 v[66:67], %8 offset:0\n"
      "ds_read_b64 v[120:121], %9 offset:832\n"
      "ds_read_b64 v[122:123], %9 offset:840\n"
      "ds_read_b64 v[124:125], %9 offset:960\n"
      "ds_read_b64 v[126:127], %9 offset:968\n"
      "ds_read_b64 v[112:113], %9 offset:576\n"
      "ds_read_b64 v[114:115], %9 offset:584\n"
      "ds_read_b64 v[116:117], %9 offset:704\n"
      "ds_read_b64 v[118:119], %9 offset:712\n"
      "ds_read_b64_tr_b16 v[68:69], %8 offset:544\n"
      "ds_read_b64_tr_b16 v[70:71], %8 offset:512\n"
      "ds_read_b64 v[108:109], %9 offset:448\n"
      "ds_read_b64 v[110:111], %9 offset:456\n"
      "ds_read_b64 v[104:105], %9 offset:320\n"
      "ds_read_b64 v[106:107], %9 offset:328\n"
      "ds_read_b64_tr_b16 v[72:73], %8 offset:1056\n"
      "ds_read_b64_tr_b16 v[74:75], %8 offset:1024\n"
      "ds_read_b64_tr_b16 v[76:77], %8 offset:1568\n"
      "ds_read_b64_tr_b16 v[78:79], %8 offset:1536\n"
      "ds_read_b64 v[100:101], %9 offset:192\n"
      "ds_read_b64 v[102:103], %9 offset:200\n"
      "ds_read_b64_tr_b16 v[80:81], %8 offset:2080\n"
      "ds_read_b64_tr_b16 v[82:83], %8 offset:2048\n"
      "ds_read_b64 v[96:97], %9 offset:64\n"
      "ds_read_b64 v[98:99], %9 offset:72\n"
      "ds_read_b64_tr_b16 v[84:85], %8 offset:2592\n"
      "ds_read_b64_tr_b16 v[86:87], %8 offset:2560\n"
      "ds_read_b64_tr_b16 v[88:89], %8 offset:3104\n"
      "ds_read_b64_tr_b16 v[90:91], %8 offset:3072\n"
      "ds_read_b64_tr_b16 v[92:93], %8 offset:3616\n"
      "ds_read_b64_tr_b16 v[94:95], %8 offset:3584\n"
      "s_mov_b32 %10, 2\n"
      "s_waitcnt lgkmcnt(0)\n"
      "Lconv%=:\n"
      "v_mfma_f32_16x16x32_bf16 %6, v[64:67], v[120:123], %6\n"
      "ds_read_b64_tr_b16 v[128:129], %8 offset:288\n"
      "v_mfma_f32_16x16x32_bf16 %7, v[64:67], v[124:127], %7\n"
      "ds_read_b64_tr_b16 v[130:131], %8 offset:256\n"
      "v_mfma_f32_16x16x32_bf16 %4, v[64:67], v[112:115], %4\n"
      "ds_read_b64 v[184:185], %9 offset:768\n"
      "v_mfma_f32_16x16x32_bf16 %5, v[64:67], v[116:119], %5\n"
      "ds_read_b64 v[186:187], %9 offset:776\n"
      "v_mfma_f32_16x16x32_bf16 %6, v[68:71], v[116:119], %6\n"
      "ds_read_b64 v[188:189], %9 offset:896\n"
      "v_mfma_f32_16x16x32_bf16 %7, v[68:71], v[120:123], %7\n"
      "ds_read_b64 v[190:191], %9 offset:904\n"
      "v_mfma_f32_16x16x32_bf16 %4, v[68:71], v[108:111], %4\n"
      "ds_read_b64 v[176:177], %9 offset:512\n"
      "v_mfma_f32_16x16x32_bf16 %2, v[64:67], v[104:107], %2\n"
      "ds_read_b64 v[178:179], %9 offset:520\n"
      "v_mfma_f32_16x16x32_bf16 %6, v[72:75], v[112:115], %6\n"
      "ds_read_b64 v[180:181], %9 offset:640\n"
      "v_mfma_f32_16x16x32_bf16 %3, v[64:67], v[108:111], %3\n"
      "ds_read_b64 v[182:183], %9 offset:648\n"
      "v_mfma_f32_16x16x32_bf16 %7, v[72:75], v[116:119], %7\n"
      "ds_read_b64_tr_b16 v[132:133], %8 offset:800\n"
      "v_mfma_f32_16x16x32_bf16 %4, v[72:75], v[104:107], %4\n"
      "ds_read_b64_tr_b16 v[134:135], %8 offset:768\n"
      "v_mfma_f32_16x16x32_bf16 %5, v[68:71], v[112:115], %5\n"
      "ds_read_b64 v[172:173], %9 offset:384\n"
      "v_mfma_f32_16x16x32_bf16 %6, v[76:79], v[108:111], %6\n"
      "ds_read_b64 v[174:175], %9 offset:392\n"
      "v_mfma_f32_16x16x32_bf16 %7, v[76:79], v[112:115], %7\n"
      "ds_read_b64 v[168:169], %9 offset:256\n"
      "v_mfma_f32_16x16x32_bf16 %4, v[76:79], v[100:103], %4\n"
      "ds_read_b64 v[170:171], %9 offset:264\n"
      "v_mfma_f32_16x16x32_bf16 %1, v[64:67], v[100:103], %1\n"
      "ds_read_b64_tr_b16 v[136:137], %8 offset:1312\n"
      "v_mfma_f32_16x16x32_bf16 %5, v[72:75], v[108:111], %5\n"
      "ds_read_b64_tr_b16 v[138:139], %8 offset:1280\n"
      "v_mfma_f32_16x16x32_bf16 %6, v[80:83], v[104:107], %6\n"
      "ds_read_b64_tr_b16 v[140:141], %8 offset:1824\n"
      "v_mfma_f32_16x16x32_bf16 %0, v[64:67], v[96:99], %0\n"
      "ds_read_b64_tr_b16 v[142:143], %8 offset:1792\n"
      "v_mfma_f32_16x16x32_bf16 %7, v[80:83], v[108:111], %7\n"
      "ds_read_b64 v[164:165], %9 offset:128\n"
      "v_mfma_f32_16x16x32_bf16 %5, v[76:79], v[104:107], %5\n"
      "ds_read_b64 v[166:167], %9 offset:136\n"
      "v_mfma_f32_16x16x32_bf16 %2, v[68:71], v[100:103], %2\n"
      "ds_read_b64_tr_b16 v[144:145], %8 offset:2336\n"
      "v_mfma_f32_16x16x32_bf16 %6, v[84:87], v[100:103], %6\n"
      "ds_read_b64_tr_b16 v[146:147], %8 offset:2304\n"
      "v_mfma_f32_16x16x32_bf16 %7, v[84:87], v[104:107], %7\n"
      "ds_read_b64 v[160:161], %9 offset:0\n"
      "v_mfma_f32_16x16x32_bf16 %3, v[68:71], v[104:107], %3\n"
      "ds_read_b64 v[162:163], %9 offset:8\n"
      "v_mfma_f32_16x16x32_bf16 %2, v[72:75], v[96:99], %2\n"
      "ds_read_b64_tr_b16 v[148:149], %8 offset:2848\n"
      "v_mfma_f32_16x16x32_bf16 %5, v[80:83], v[100:103], %5\n"
      "ds_read_b64_tr_b16 v[150:151], %8 offset:2816\n"
      "v_mfma_f32_16x16x32_bf16 %7, v[88:91], v[100:103], %7\n"
      "ds_read_b64_tr_b16 v[152:153], %8 offset:3360\n"
      "v_mfma_f32_16x16x32_bf16 %6, v[88:91], v[96:99], %6\n"
      "ds_read_b64_tr_b16 v[154:155], %8 offset:3328\n"
      "v_mfma_f32_16x16x32_bf16 %3, v[72:75], v[100:103], %3\n"
      "ds_read_b64_tr_b16 v[156:157], %8 offset:3872\n"
      "v_mfma_f32_16x16x32_bf16 %1, v[68:71], v[96:99], %1\n"
      "ds_read_b64_tr_b16 v[158:159], %8 offset:3840\n"
      "v_mfma_f32_16x16x32_bf16 %7, v[92:95], v[96:99], %7\n"
      "v_mfma_f32_16x16x32_bf16 %5, v[84:87], v[96:99], %5\n"
      "v_mfma_f32_16x16x32_bf16 %4, v[80:83], v[96:99], %4\n"
      "v_mfma_f32_16x16x32_bf16 %3, v[76:79], v[96:99], %3\n"
      "s_waitcnt lgkmcnt(0)\n"
      "v_add_u32 %8, 4128, %8\n"
      "v_add_u32 %9, 1152, %9\n"
      "v_mfma_f32_16x16x32_bf16 %6, v[128:131], v[184:187], %6\n"
      "ds_read_b64_tr_b16 v[64:65], %8 offset:32\n"
      "v_mfma_f32_16x16x32_bf16 %7, v[128:131], v[188:191], %7\n"
      "ds_read_b64_tr_b16 v[66:67], %8 offset:0\n"
      "v_mfma_f32_16x16x32_bf16 %4, v[128:131], v[176:179], %4\n"
      "ds_read_b64 v[120:121], %9 offset:832\n"
      "v_mfma_f32_16x16x32_bf16 %5, v[128:131], v[180:183], %5\n"
      "ds_read_b64 v[122:123], %9 offset:840\n"
      "v_mfma_f32_16x16x32_bf16 %6, v[132:135], v[180:183], %6\n"
      "ds_read_b64 v[124:125], %9 offset:960\n"
      "v_mfma_f32_16x16x32_bf16 %7, v[132:135], v[184:187], %7\n"
      "ds_read_b64 v[126:127], %9 offset:968\n"
      "v_mfma_f32_16x16x32_bf16 %4, v[132:135], v[172:175], %4\n"
      "ds_read_b64 v[112:113], %9 offset:576\n"
      "v_mfma_f32_16x16x32_bf16 %2, v[128:131], v[168:171], %2\n"
      "ds_read_b64 v[114:115], %9 offset:584\n"
      "v_mfma_f32_16x16x32_bf16 %6, v[136:139], v[176:179], %6\n"
      "ds_read_b64 v[116:117], %9 offset:704\n"
      "v_mfma_f32_16x16x32_bf16 %3, v[128:131], v[172:175], %3\n"
      "ds_read_b64 v[118:119], %9 offset:712\n"
      "v_mfma_f32_16x16x32_bf16 %7, v[136:139], v[180:183], %7\n"
      "ds_read_b64_tr_b16 v[68:69], %8 offset:544\n"
      "v_mfma_f32_16x16x32_bf16 %4, v[136:139], v[168:171], %4\n"
      "ds_read_b64_tr_b16 v[70:71], %8 offset:512\n"
      "v_mfma_f32_16x16x32_bf16 %5, v[132:135], v[176:179], %5\n"
      "ds_read_b64 v[108:109], %9 offset:448\n"
      "v_mfma_f32_16x16x32_bf16 %6, v[140:143], v[172:175], %6\n"
      "ds_read_b64 v[110:111], %9 offset:456\n"
      "v_mfma_f32_16x16x32_bf16 %7, v[140:143], v[176:179], %7\n"
      "ds_read_b64 v[104:105], %9 offset:320\n"
      "v_mfma_f32_16x16x32_bf16 %4, v[140:143], v[164:167], %4\n"
      "ds_read_b64 v[106:107], %9 offset:328\n"
      "v_mfma_f32_16x16x32_bf16 %1, v[128:131], v[164:167], %1\n"
      "ds_read_b64_tr_b16 v[72:73], %8 offset:1056\n"
      "v_mfma_f32_16x16x32_bf16 %5, v[136:139], v[172:175], %5\n"
      "ds_read_b64_tr_b16 v[74:75], %8 offset:1024\n"
      "v_mfma_f32_16x16x32_bf16 %6, v[144:147], v[168:171], %6\n"
      "ds_read_b64_tr_b16 v[76:77], %8 offset:1568\n"
      "v_mfma_f32_16x16x32_bf16 %0, v[128:131], v[160:163], %0\n"
      "ds_read_b64_tr_b16 v[78:79], %8 offset:1536\n"
      "v_mfma_f32_16x16x32_bf16 %7, v[144:147], v[172:175], %7\n"
      "ds_read_b64 v[100:101], %9 offset:192\n"
      "v_mfma_f32_16x16x32_bf16 %5, v[140:143], v[168:171], %5\n"
      "ds_read_b64 v[102:103], %9 offset:200\n"
      "v_mfma_f32_16x16x32_bf16 %2, v[132:135], v[164:167], %2\n"
      "ds_read_b64_tr_b16 v[80:81], %8 offset:2080\n"
      "v_mfma_f32_16x16x32_bf16 %6, v[148:151], v[164:167], %6\n"
      "ds_read_b64_tr_b16 v[82:83], %8 offset:2048\n"
      "v_mfma_f32_16x16x32_bf16 %7, v[148:151], v[168:171], %7\n"
      "ds_read_b64 v[96:97], %9 offset:64\n"
      "v_mfma_f32_16x16x32_bf16 %3, v[132:135], v[168:171], %3\n"
      "ds_read_b64 v[98:99], %9 offset:72\n"
      "v_mfma_f32_16x16x32_bf16 %2, v[136:139], v[160:163], %2\n"
      "ds_read_b64_tr_b16 v[84:85], %8 offset:2592\n"
      "v_mfma_f32_16x16x32_bf16 %5, v[144:147], v[164:167], %5\n"
      "ds_read_b64_tr_b16 v[86:87], %8 offset:2560\n"
      "v_mfma_f32_16x16x32_bf16 %7, v[152:155], v[164:167], %7\n"
      "ds_read_b64_tr_b16 v[88:89], %8 offset:3104\n"
      "v_mfma_f32_16x16x32_bf16 %6, v[152:155], v[160:163], %6\n"
      "ds_read_b64_tr_b16 v[90:91], %8 offset:3072\n"
      "v_mfma_f32_16x16x32_bf16 %3, v[136:139], v[164:167], %3\n"
      "ds_read_b64_tr_b16 v[92:93], %8 offset:3616\n"
      "v_mfma_f32_16x16x32_bf16 %1, v[132:135], v[160:163], %1\n"
      "ds_read_b64_tr_b16 v[94:95], %8 offset:3584\n"
      "v_mfma_f32_16x16x32_bf16 %7, v[156:159], v[160:163], %7\n"
      "v_mfma_f32_16x16x32_bf16 %5, v[148:151], v[160:163], %5\n"
      "v_mfma_f32_16x16x32_bf16 %4, v[144:147], v[160:163], %4\n"
      "v_mfma_f32_16x16x32_bf16 %3, v[140:143], v[160:163], %3\n"
      "s_waitcnt lgkmcnt(0)\n"
      "s_sub_u32 %10, %10, 1\n"
      "s_cmp_lg_u32 %10, 0\n"
      "s_cbranch_scc1 Lconv%=\n"
      "s_nop 7\n"
      : "+v"(acc[0]), "+v"(acc[1]), "+v"(acc[2]), "+v"(acc[3]), "+v"(acc[4]), "+v"(acc[5]), "+v"(acc[6]), "+v"(acc[7]), "+v"(ka), "+v"(hb), "=&s"(cnt)
      :: "memory", "scc", "v64", "v65", "v66", "v67", "v68", "v69", "v70", "v71", "v72", "v73", "v74", "v75", "v76", "v77", "v78", "v79", "v80", "v81", "v82", "v83", "v84", "v85", "v86", "v87", "v88", "v89", "v90", "v91", "v92", "v93", "v94", "v95", "v96", "v97", "v98", "v99", "v100", "v101", "v102", "v103", "v104", "v105", "v106", "v107", "v108", "v109", "v110", "v111", "v112", "v113", "v114", "v115", "v116", "v117", "v118", "v119", "v120", "v121", "v122", "v123", "v124", "v125", "v126", "v127", "v128", "v129", "v130", "v131", "v132", "v133", "v134", "v135", "v136", "v137", "v138", "v139", "v140", "v141", "v142", "v143", "v144", "v145", "v146", "v147", "v148", "v149", "v150", "v151", "v152", "v153", "v154", "v155", "v156", "v157", "v158", "v159", "v160", "v161", "v162", "v163", "v164", "v165", "v166", "v167", "v168", "v169", "v170", "v171", "v172", "v173", "v174", "v175", "v176", "v177", "v178", "v179", "v180", "v181", "v182", "v183", "v184", "v185", "v186", "v187", "v188", "v189", "v190", "v191");
  }
  f4* ex = (f4*)(sm + CV_EX);
  if (half) {
#pragma unroll
    for (int i = 0; i < 4; ++i) { ex[(w * 4 + i) * 64 + lane] = acc[i]; acc[i] = acc[4 + i]; }
  } else {
#pragma unroll
    for (int i = 0; i < 4; ++i) ex[(w * 4 + i) * 64 + lane] = acc[4 + i];
  }
  lds_arrive(fl + FL_EXCH + w);
  u2 xr[4];
#pragma unroll
  for (int i = 0; i < 4; ++i) xr[i] = *(const u2*)(sm + CV_X + 4096 * ch + 2 * (256 * (4 * half + i) + 16 * c16 + 4 * g));
  lds_wait(fl + FL_EXCH + (w ^ 1), 1u);
  u16* x1t = (u16*)(ws + WS_X1T);
  const float gc = ga * c2;
#pragma unroll
  for (int i = 0; i < 4; ++i) {
    const f4 y = (acc[i] + ex[((w ^ 1) * 4 + i) * 64 + lane]) * gc + (f4){blo(xr[i].x), bhi(xr[i].x), blo(xr[i].y), bhi(xr[i].y)} * c2;
    *(u2*)(x1t + d * L + 256 * (4 * half + i) + 16 * c16 + 4 * g) = (u2){pk(y[0], y[1]), pk(y[2], y[3])};
  }
}

__global__ __launch_bounds__(512, 2) void k1_conv(const u16* x, const u16* lar, const u16* aim, const u16* cre, const u16* cim, const u16* ldt, const u16* dsk,
                                                  const u16* c, const u16* wm, const u16* wg, const u16* wu, const u16* wd, char* ws) {
  extern __shared__ __attribute__((aligned(16))) char sm[];
  const int b = blockIdx.x;
  if (__builtin_amdgcn_readfirstlane(threadIdx.x) < 256) k1_main(sm, b, x, c, wm, ws);
  else k1_help(sm, b, lar, aim, cre, cim, ldt, dsk, wg, wu, wd, ws);
}

constexpr int G1_LEAD = 3, G1_NS = 5, G1_SLOT = 20480;
constexpr int G2_RES = 19, G2_FL = 4 * G2_RES * 2048, G1_SV = G2_FL + 64, K3_LDS = G1_SV + 512;
constexpr int G2_LEAD = 6;
constexpr int HH_T = NU * 1024;

template <int K> DI u32 g1_pair(const f16v& acc, float rs, float sd) {
  const f2 g = (f2){acc[2 * K], acc[2 * K + 1]} * rs;
  const f2 z = g * (g * g * -0.10294324f + -2.30220819f);
  const f2 dn = (f2){__builtin_amdgcn_exp2f(z.x), __builtin_amdgcn_exp2f(z.y)} * sd + sd;
  const f2 r = g * (f2){acc[8 + 2 * K], acc[9 + 2 * K]} * (f2){__builtin_amdgcn_rcpf(dn.x), __builtin_amdgcn_rcpf(dn.y)};
  return pk(r.x, r.y);
}
DI u4 g1_tile(const f16v& acc, float rs, float sd) { return (u4){g1_pair<0>(acc, rs, sd), g1_pair<1>(acc, rs, sd), g1_pair<2>(acc, rs, sd), g1_pair<3>(acc, rs, sd)}; }

DI void g1_stats(char* sm, int rt, char* ws) {
  const int t = threadIdx.x;
  float* red = (float*)sm;
  if (t < 512) {
    const u16* xp = (const u16*)(ws + WS_X1T) + (t >> 4) * L + 128 * rt + 8 * (t & 15);
    u4 v[16];
    float iv[16];
#pragma unroll
    for (int i = 0; i < 16; ++i) { v[i] = *(const u4*)(xp + 32 * i * L); iv[i] = ((const float*)(ws + WS_INV))[(t >> 4) + 32 * i]; }
    f2 s1[4], s2[4];
#pragma unroll
    for (int j = 0; j < 4; ++j) s1[j] = s2[j] = (f2){0.f, 0.f};
#pragma unroll
    for (int i = 0; i < 16; ++i)
#pragma unroll
      for (int j = 0; j < 4; ++j) { const f2 x = (f2){blo(v[i][j]), bhi(v[i][j])} * iv[i]; s1[j] += x; s2[j] += x * x; }
#pragma unroll
    for (int j = 0; j < 4; ++j) *(f4*)(red + (t >> 4) * 260 + 16 * (t & 15) + 4 * j) = (f4){s1[j].x, s2[j].x, s1[j].y, s2[j].y};
  }
  __syncthreads();
  if (t < 512) {
    f2 p = {0.f, 0.f};
#pragma unroll
    for (int i = 0; i < 8; ++i) p += *(const f2*)(red + (8 * (t & 3) + i) * 260 + 2 * (t >> 2));
    p.x += dppf<0xb1>(p.x); p.y += dppf<0xb1>(p.y); p.x += dppf<0x4e>(p.x); p.y += dppf<0x4e>(p.y);
    const f2 ms = ln_stats(p.x, p.y);
    if ((t & 3) == 0)
      __hip_atomic_store((u32*)(ws + WS_ST2) + 128 * rt + (t >> 2), (__float_as_uint(ms.x) >> 16) | __float_as_uint(ms.y), __ATOMIC_RELAXED, __HIP_MEMORY_SCOPE_AGENT);
  }
  __syncthreads();
}

__global__ __launch_bounds__(768, 3) void k3_mlp(const u16* wg, const u16* wu, const u16* wd, char* ws, u16* out) {
  extern __shared__ __attribute__((aligned(16))) char sm[];
  const int b = blockIdx.x, j = b >> 3;
  const int t = threadIdx.x, w12 = __builtin_amdgcn_readfirstlane(t >> 6), w = w12 & 3, lane = t & 63, hh = lane >> 5;
  const u32 lb0 = ldsaddr(sm);
  const int q = (lane & 15) >> 2, p = lane & 3, g = lane >> 4;
  const int lt = 4 * (b & 7) + (j & 3), dt = j >> 2;
  auto kfirst = [&](int kq) { const u32 k = 22 * kq - (kq == 3) + 3 * dt; return min(k, k - NU); };
  auto knext = [](u32 k) { return min(k + 1, k + 1 - NU); };
  const rsrc_t rd = mkrsrc(wd, DF * D * 2);
  const u32 vd = (lane >> 3) * 1024 + 128 * dt + 16 * ((lane & 7) ^ (4 * ((lane >> 4) & 1)));
  volatile LAS u32* fl = (volatile LAS u32*)(size_t)(lb0 + G2_FL);
  auto fill2 = [&](int i0, int n, int slot0) {
    u32 kd = kfirst(w) + i0;
    kd = min(kd, kd - NU);
#pragma unroll 1
    for (int i = 0; i < n; ++i) {
      glds16<0>(lb0 + (G2_RES * w + slot0 + i) * 2048, vd, rd, kd * 16384);
      glds16<0>(lb0 + (G2_RES * w + slot0 + i) * 2048 + 1024, vd, rd, kd * 16384 + 8192);
      kd = knext(kd);
    }
  };
  {
    const int rt = 4 * ((b & 7) >> 1) + (j & 3), cg = (j >> 2) + 8 * (b & 1) - 1;
    if (t == 0) fl[0] = 0u;
    if (cg < 0) {
      g1_stats(sm, rt, ws);
    } else if (w12 >= 8) {
      const rsrc_t rx = mkrsrc(ws + WS_X1T, D * L * 2), rg = mkrsrc(wg, D * DF * 2), ru = mkrsrc(wu, D * DF * 2);
      const u32 vx = (4 * w + g) * 4096 + 256 * rt + 16 * ((lane & 15) ^ ((g << 2) | w));
      u32 vw[3];
#pragma unroll
      for (int i = 0; i < 3; ++i) {
        const int pc = (3 * (w & 1) + i) * 64 + lane, row = pc / 12, chk = pc - 12 * row;
        vw[i] = row * (DF * 2) + min(192 * cg + 16 * (chk ^ (w & 2)), DF * 2 - 16);
      }
      const rsrc_t rwm = w < 2 ? rg : ru;
      auto stage = [&](int i, int slot) {
        const u32 m = lb0 + slot * G1_SLOT;
        glds16<0>(m + 1024 * w, vx, rx, i * 131072);
        glds16<0>(m + 4096 + 1024 * w, vx, rx, i * 131072 + 65536);
#pragma unroll
        for (int k = 0; k < 3; ++k) glds16<0>(m + 8192 + 3072 * w + 1024 * k, vw[k], rwm, i * (32 * DF * 2));
      };
      asm volatile("s_nop 4");
      static_assert(G1_LEAD + 2 <= G1_NS, "ring too short");
#pragma unroll 1
      for (int i = 0; i < G1_LEAD - 1; ++i) stage(i, i);
      int slot = G1_LEAD - 1;
#pragma unroll 1
      for (int i = 0; i < 17 - G1_LEAD; ++i) {
        stage(i + G1_LEAD - 1, slot);
        slot = slot == G1_NS - 1 ? 0 : slot + 1;
        vmwait<5 * (G1_LEAD - 1)>();
        rawbar();
      }
      const u32* st2 = (const u32*)(ws + WS_ST2) + 128 * rt + 32 * w + (lane & 31);
      u32 v;
      asm volatile("global_load_dword %0, %1, off sc1" : "=v"(v) : "v"(st2));
      sfor<17 - G1_LEAD, 16>([&](auto i) { vmwait<5 * (15 - i) + 1>(); rawbar(); });
      asm volatile("s_waitcnt vmcnt(0)" : "+v"(v));
#pragma unroll 1
      for (int spin = 0; spin < (1 << 16) && !__all(v != 0u); ++spin) {
        __builtin_amdgcn_s_sleep(2);
        v = __hip_atomic_load(st2, __ATOMIC_RELAXED, __HIP_MEMORY_SCOPE_AGENT);
      }
      ((u32*)(sm + G1_SV))[32 * w + (lane & 31)] = v;
      bar();
      bar();
    } else if (w12 >= 4) {
      const char* pf = (const char*)wd + j * (43 * 1024) + w * (43 * 256);
#pragma unroll 1
      for (int i = 0; i < 18; ++i) {
        rawbar();
      }
    } else {
      const int wl = w & 1, wu3 = 3 * (w >> 1), u0 = 6 * cg + wu3;
      u32 xa[2][2], wa[3];
#pragma unroll
      for (int c = 0; c < 2; ++c)
#pragma unroll
        for (int tt = 0; tt < 2; ++tt)
          xa[c][tt] = lb0 + 256 * (8 * hh + 4 * tt + q) + 8 * (p & 1) + 16 * ((4 * (2 * wl + c) + 2 * (g & 1) + (p >> 1)) ^ ((q << 2) | (2 * hh + tt)));
#pragma unroll
      for (int v = 0; v < 3; ++v) wa[v] = lb0 + 8192 + 6144 * (g & 1) + 192 * (8 * hh + q) + 16 * ((2 * (wu3 + v) + (p >> 1)) ^ (2 * (g & 1))) + 8 * (p & 1);
      u4 ex[3];
#pragma unroll
      for (int v = 0; v < 3; ++v) ex[v] = *(const u4*)(ws + WS_EX + min(u0 + v, NU - 1) * 1024 + lane * 16);
      f16v acc[6];
#pragma unroll
      for (int i = 0; i < 6; ++i)
#pragma unroll
        for (int e = 0; e < 16; ++e) acc[i][e] = 0.f;
#pragma unroll
      for (int i = 0; i < 6; ++i) asm volatile("" : "+v"(acc[i]));
      rawbar();
      {
        u32 c0, c1, c2;
        asm volatile(
          "ds_read_b64_tr_b16 v[112:113], %6 offset:0\n"
          "ds_read_b64_tr_b16 v[114:115], %7 offset:0\n"
          "ds_read_b64_tr_b16 v[120:121], %10 offset:0\n"
          "ds_read_b64_tr_b16 v[122:123], %10 offset:768\n"
          "ds_read_b64_tr_b16 v[116:117], %8 offset:0\n"
          "ds_read_b64_tr_b16 v[118:119], %9 offset:0\n"
          "ds_read_b64_tr_b16 v[124:125], %11 offset:0\n"
          "ds_read_b64_tr_b16 v[126:127], %11 offset:768\n"
          "ds_read_b64_tr_b16 v[128:129], %12 offset:0\n"
          "ds_read_b64_tr_b16 v[130:131], %12 offset:768\n"
          "s_mov_b32 %13, 16\n"
          "s_mov_b32 %14, 0\n"
          "Lg1%=:\n"
          "s_add_u32 %14, %14, 1\n"
          "s_cmp_eq_u32 %14, 5\n"
          "s_cselect_b32 %15, -81920, %16\n"
          "s_cselect_b32 %14, 0, %14\n"
          "s_waitcnt lgkmcnt(6)\n"
          "v_mfma_f32_32x32x16_bf16 %0, v[120:123], v[112:115], %0\n"
          "ds_read_b64_tr_b16 v[132:133], %6 offset:4096\n"
          "ds_read_b64_tr_b16 v[134:135], %7 offset:4096\n"
          "s_waitcnt lgkmcnt(6)\n"
          "v_mfma_f32_32x32x16_bf16 %1, v[120:123], v[116:119], %1\n"
          "ds_read_b64_tr_b16 v[140:141], %10 offset:3072\n"
          "ds_read_b64_tr_b16 v[142:143], %10 offset:3840\n"
          "s_waitcnt lgkmcnt(6)\n"
          "v_mfma_f32_32x32x16_bf16 %2, v[124:127], v[112:115], %2\n"
          "ds_read_b64_tr_b16 v[136:137], %8 offset:4096\n"
          "ds_read_b64_tr_b16 v[138:139], %9 offset:4096\n"
          "v_mfma_f32_32x32x16_bf16 %3, v[124:127], v[116:119], %3\n"
          "ds_read_b64_tr_b16 v[144:145], %11 offset:3072\n"
          "ds_read_b64_tr_b16 v[146:147], %11 offset:3840\n"
          "s_waitcnt lgkmcnt(8)\n"
          "v_mfma_f32_32x32x16_bf16 %4, v[128:131], v[112:115], %4\n"
          "ds_read_b64_tr_b16 v[148:149], %12 offset:3072\n"
          "ds_read_b64_tr_b16 v[150:151], %12 offset:3840\n"
          "v_add_u32 %6, %15, %6\n"
          "v_add_u32 %7, %15, %7\n"
          "v_add_u32 %8, %15, %8\n"
          "v_add_u32 %9, %15, %9\n"
          "v_mfma_f32_32x32x16_bf16 %5, v[128:131], v[116:119], %5\n"
          "v_add_u32 %10, %15, %10\n"
          "v_add_u32 %11, %15, %11\n"
          "v_add_u32 %12, %15, %12\n"
          "s_barrier\n"
          "s_waitcnt lgkmcnt(6)\n"
          "v_mfma_f32_32x32x16_bf16 %0, v[140:143], v[132:135], %0\n"
          "ds_read_b64_tr_b16 v[112:113], %6 offset:0\n"
          "ds_read_b64_tr_b16 v[114:115], %7 offset:0\n"
          "s_waitcnt lgkmcnt(6)\n"
          "v_mfma_f32_32x32x16_bf16 %1, v[140:143], v[136:139], %1\n"
          "ds_read_b64_tr_b16 v[120:121], %10 offset:0\n"
          "ds_read_b64_tr_b16 v[122:123], %10 offset:768\n"
          "s_waitcnt lgkmcnt(6)\n"
          "v_mfma_f32_32x32x16_bf16 %2, v[144:147], v[132:135], %2\n"
          "ds_read_b64_tr_b16 v[116:117], %8 offset:0\n"
          "ds_read_b64_tr_b16 v[118:119], %9 offset:0\n"
          "v_mfma_f32_32x32x16_bf16 %3, v[144:147], v[136:139], %3\n"
          "ds_read_b64_tr_b16 v[124:125], %11 offset:0\n"
          "ds_read_b64_tr_b16 v[126:127], %11 offset:768\n"
          "s_waitcnt lgkmcnt(8)\n"
          "v_mfma_f32_32x32x16_bf16 %4, v[148:151], v[132:135], %4\n"
          "ds_read_b64_tr_b16 v[128:129], %12 offset:0\n"
          "ds_read_b64_tr_b16 v[130:131], %12 offset:768\n"
          "s_sub_u32 %13, %13, 1\n"
          "s_cmp_lg_u32 %13, 0\n"
          "v_mfma_f32_32x32x16_bf16 %5, v[148:151], v[136:139], %5\n"
          "s_cbranch_scc1 Lg1%=\n"
          "s_waitcnt lgkmcnt(0)\n"
          : "+v"(acc[0]), "+v"(acc[1]), "+v"(acc[2]), "+v"(acc[3]), "+v"(acc[4]), "+v"(acc[5]), "+v"(xa[0][0]), "+v"(xa[0][1]), "+v"(xa[1][0]), "+v"(xa[1][1]),
            "+v"(wa[0]), "+v"(wa[1]), "+v"(wa[2]), "=&s"(c0), "=&s"(c1), "=&s"(c2)
          : "s"(G1_SLOT) : "memory", "scc", "v112", "v113", "v114", "v115", "v116", "v117", "v118", "v119", "v120", "v121", "v122", "v123", "v124", "v125", "v126", "v127", "v128", "v129", "v130", "v131", "v132", "v133", "v134", "v135", "v136", "v137", "v138", "v139", "v140", "v141", "v142", "v143", "v144", "v145", "v146", "v147", "v148", "v149", "v150", "v151");
      }
      u32 sv[2];
#pragma unroll
      for (int c = 0; c < 2; ++c) sv[c] = ((const u32*)(sm + G1_SV))[64 * wl + 32 * c + (lane & 31)];
      bar();
#pragma unroll
      for (int c = 0; c < 2; ++c) {
        const u32 bv = hh ? __builtin_amdgcn_perm(sv[c], sv[c], 0x03020302u) : (__builtin_amdgcn_perm(sv[c], sv[c], 0x01000100u) ^ 0x80008000u);
#pragma unroll
        for (int v = 0; v < 3; ++v) acc[2 * v + c] = mfma32(ex[v], (u4){bv, bv, bv, bv}, acc[2 * v + c]);
      }
      char* hp = ws + WS_HH + (4 * rt + 2 * wl) * HH_T + u0 * 1024 + 16 * lane;
#pragma unroll
      for (int v = 0; v < 3; ++v) {
        if (u0 + v < NU) {
#pragma unroll
          for (int c = 0; c < 2; ++c) {
            const float sd = bhi(sv[c]);
            const u4 o = g1_tile(acc[2 * v + c], __builtin_amdgcn_rcpf(sd), sd);
            char* dst = hp + c * HH_T + v * 1024;
            asm volatile("global_store_dwordx4 %0, %1, off sc1\n\ts_nop 1" :: "v"(dst), "v"(o) : "memory");
          }
        }
      }
      {
        asm volatile("s_waitcnt vmcnt(0)" ::: "memory");
        if (lane == 0 && __hip_atomic_fetch_add((LAS u32*)fl, 1u, __ATOMIC_RELAXED, __HIP_MEMORY_SCOPE_WORKGROUP) == 3u)
          __hip_atomic_fetch_add((u32*)(ws + WS_CNT) + 64 * rt, 1u, __ATOMIC_RELAXED, __HIP_MEMORY_SCOPE_AGENT);
      }
    }
  }
  if (w12 >= 8) {
    asm volatile("s_nop 4");
    fill2(G2_RES, 3, 0);
    fill2(3, G2_RES - 3, 3);
    vmwait<2 * (G2_RES - 3)>();
    rawbar();
    rawbar();
    fill2(0, 3, 0);
    vmwait<0>();
    rawbar();
    rawbar();
    rawbar();
  } else {
    const int wl = w12 & 1, kq = w12 >> 1, dc = 64 * dt + 16 * kq + 4 * hh;
    f4 gb[2], xr[2];
#pragma unroll
    for (int e = 0; e < 2; ++e) {
      gb[e] = *(const f4*)((const float*)(ws + WS_GB) + dc + 8 * e);
      xr[e] = *(const f4*)((const float*)(ws + WS_INV) + dc + 8 * e);
#pragma unroll
      for (int i = 0; i < 4; ++i) xr[e][i] *= b2f(((const u16*)(ws + WS_X1T))[(dc + 8 * e + i) * L + 64 * lt + 32 * wl + (lane & 31)]);
    }
    u32 pa[2];
#pragma unroll
    for (int e = 0; e < 2; ++e)
      pa[e] = lb0 + G2_RES * 2048 * kq + 128 * (4 * hh + q) + 16 * ((((4 * e + 2 * (g & 1)) ^ (2 * kq)) + (p >> 1)) ^ (4 * (q >> 1))) + 8 * (p & 1);
    f16v acc[2];
#pragma unroll
    for (int e = 0; e < 16; ++e) acc[0][e] = acc[1][e] = 0.f;
    asm volatile("" : "+v"(xr[0]), "+v"(xr[1]), "+v"(gb[0]), "+v"(gb[1]), "+v"(acc[0]), "+v"(acc[1]));
    rawbar();
    u4 A[2][2], AR[3][2];
#pragma unroll
    for (int i = 0; i < 6; ++i) AR[i >> 1][i & 1] = cat(trd(pa[i & 1] + (i >> 1) * 2048), trd(pa[i & 1] + (i >> 1) * 2048 + 1024));
    asm volatile("s_waitcnt lgkmcnt(0)\n\ts_barrier" : "+v"(AR[0][0]), "+v"(AR[0][1]), "+v"(AR[1][0]), "+v"(AR[1][1]), "+v"(AR[2][0]), "+v"(AR[2][1]) :: "memory");
    if (w12 == 0) {
      const u32* cnt = (const u32*)(ws + WS_CNT) + 64 * (lt >> 1);
#pragma unroll 1
      for (int spin = 0; spin < (1 << 18); ++spin)
        if (__hip_atomic_load(cnt, __ATOMIC_RELAXED, __HIP_MEMORY_SCOPE_AGENT) >= 15u) break;
    }
    rawbar();
    const rsrc_t rh = mkrsrc(ws + WS_HH + (2 * lt + wl) * HH_T, HH_T);
    constexpr int LEAD = G2_LEAD, NH = LEAD + 2;
    u4 H[NH];
    u32 kh = kfirst(kq);
    auto ldh = [&](auto i) { if constexpr (i < 22) { bld16_sc1<0>(H[i % NH], lane * 16, rh, kh * 1024); kh = knext(kh); } };
    sfor<0, LEAD>(ldh);
    auto rda = [&](auto i) {
#pragma unroll
      for (int e = 0; e < 2; ++e) A[i & 1][e] = cat(trd(pa[e] + i * 2048), trd(pa[e] + i * 2048 + 1024));
    };
    rda(std::integral_constant<int, 0>{});
    sfor<0, 22>([&](auto i) {
      vmwait<(i < 22 - LEAD ? LEAD - 1 : 21 - i)>(H[i % NH]);
      SB();
      if constexpr (i + 1 < G2_RES) rda(std::integral_constant<int, i + 1>{});
      if (i < 21 || kq < 2) {
#pragma unroll
        for (int e = 0; e < 2; ++e) acc[e] = mfma32(i < G2_RES ? A[i & 1][e] : AR[i < G2_RES ? 0 : i - G2_RES][e], H[i % NH], acc[e]);
      }
      if constexpr (i + 1 < G2_RES) { SGB(0x8, 1); SGB(0x100, 2); SGB(0x8, 1); SGB(0x100, 2); }
      SB();
      ldh(std::integral_constant<int, i + LEAD>{});
      if constexpr (i == G2_RES - 1) rawbar();
    });
    f4* red = (f4*)sm + wl * 1536 + lane;
#pragma unroll
    for (int k = 1; k < 4; ++k)
#pragma unroll
      for (int e = 0; e < 2; ++e) {
        const f16v& a = acc[k >> 1];
        const int r = 8 * (k & 1) + 4 * e;
        red[(kq ^ k) * 384 + (k - 1) * 128 + 64 * e] = (f4){a[r], a[r + 1], a[r + 2], a[r + 3]};
      }
    bar();
    u2 o[2];
#pragma unroll
    for (int e = 0; e < 2; ++e) {
      const f4* rp = red + kq * 384 + 64 * e;
      const f4 y = ((f4){acc[0][4 * e], acc[0][4 * e + 1], acc[0][4 * e + 2], acc[0][4 * e + 3]} + rp[0] + rp[128] + rp[256]) * gb[e] + xr[e];
      o[e] = (u2){pk(y[0], y[1]), pk(y[2], y[3])};
    }
    const auto r0 = __builtin_amdgcn_permlane32_swap(o[0].x, o[1].x, false, false);
    const auto r1 = __builtin_amdgcn_permlane32_swap(o[0].y, o[1].y, false, false);
    *(u4*)(out + (64 * lt + 32 * wl + (lane & 31)) * D + 64 * dt + 16 * kq + 8 * hh) = (u4){r0[0], r1[0], r0[1], r1[1]};
  }
}

extern "C" void kernel_launch(void* const* d_in, const int* in_sizes, int n_in, void* d_out, int out_size, void* d_ws, size_t ws_size,
                              hipStream_t stream) {
  const u16 *wg = (const u16*)d_in[8], *wu = (const u16*)d_in[9], *wd = (const u16*)d_in[10];
  char* ws = (char*)d_ws;
  (void)hipFuncSetAttribute((const void*)k1_conv, hipFuncAttributeMaxDynamicSharedMemorySize, K1_LDS);
  (void)hipFuncSetAttribute((const void*)k3_mlp, hipFuncAttributeMaxDynamicSharedMemorySize, K3_LDS);
  k1_conv<<<256, 512, K1_LDS, stream>>>((const u16*)d_in[0], (const u16*)d_in[1], (const u16*)d_in[2], (const u16*)d_in[3], (const u16*)d_in[4], (const u16*)d_in[5],
                                        (const u16*)d_in[6], (const u16*)d_in[11], (const u16*)d_in[12], wg, wu, wd, ws);
  k3_mlp<<<256, 768, K3_LDS, stream>>>(wg, wu, wd, ws, (u16*)d_out);
}
```

```cpp
#include <hip/hip_runtime.h>
#include <hip/hip_bf16.h>
#include <stdint.h>

__device__ unsigned long long g_stamps[2 * 4 * 512 * 8 * 8];
struct Stamps { unsigned long long r[8], c[8]; };
#define STAMP(kid, idx) do { __builtin_amdgcn_sched_barrier(0); asm volatile("s_memrealtime %0\n s_memtime %1\n s_waitcnt lgkmcnt(0)" : "=s"(ts_.r[idx]), "=s"(ts_.c[idx]) :: "memory"); __builtin_amdgcn_sched_barrier(0); } while (0)
#define STAMPV(kid, idx) do { asm volatile("s_waitcnt vmcnt(0)" ::: "memory"); STAMP(kid, idx); \
  if ((threadIdx.x & 63) == 0 && (blockDim.x < 768 || (threadIdx.x >> 6) < 4 || (threadIdx.x >> 6) >= 8)) { for (int i_ = 0; i_ < 8; ++i_) { g_stamps[(((kid) * 512 + blockIdx.x) * 8 + ((threadIdx.x >> 6) >= 8 ? (threadIdx.x >> 6) - 4 : (threadIdx.x >> 6))) * 8 + i_] = ts_.r[i_]; \
     g_stamps[4 * 512 * 64 + (((kid) * 512 + blockIdx.x) * 8 + ((threadIdx.x >> 6) >= 8 ? (threadIdx.x >> 6) - 4 : (threadIdx.x >> 6))) * 8 + i_] = ts_.c[i_]; } } } while (0)
#include <type_traits>

typedef unsigned short u16;
typedef unsigned u32;
typedef float f2 __attribute__((ext_vector_type(2)));
typedef float f4 __attribute__((ext_vector_type(4)));
typedef float f16v __attribute__((ext_vector_type(16)));
typedef u32 u2 __attribute__((ext_vector_type(2)));
typedef u32 u4 __attribute__((ext_vector_type(4)));
typedef u4 u4a8 __attribute__((aligned(8)));
typedef short s4 __attribute__((ext_vector_type(4)));
typedef __bf16 bf2 __attribute__((ext_vector_type(2)));
typedef __bf16 bf8 __attribute__((ext_vector_type(8)));
typedef _Float16 h2 __attribute__((ext_vector_type(2)));
typedef _Float16 h8 __attribute__((ext_vector_type(8)));
#define LAS __attribute__((address_space(3)))
#define DI __device__ __forceinline__

constexpr int L = 2048, D = 512, NM = 128, DF = 1376, NU = 86;

constexpr size_t WS_GB = 0, WS_INV = 2 << 10, WS_EPOCH = 4 << 10, WS_ST2 = 8 << 10, WS_CNT = 16 << 10, WS_ST1 = 32 << 10, WS_EX = 64 << 10,
                 WS_FX = 20 << 10, WS_MG = 160 << 10, WS_X1T = 256 << 10, WS_HH = WS_X1T + (2 << 20), WS_XT = WS_HH + (5504 << 10);

template <class T> DI T ldnt(const T* p) { return __builtin_nontemporal_load(p); }
DI float blo(u32 v) { return __uint_as_float(v << 16); }
DI float bhi(u32 v) { return __uint_as_float(v & 0xffff0000u); }
DI float b2f(u16 v) { return __uint_as_float(((u32)v) << 16); }
DI u32 pk(float a, float b) { return __builtin_bit_cast(u32, __builtin_convertvector((f2){a, b}, bf2)); }
DI u32 pkh(f2 v) { return __builtin_bit_cast(u32, __builtin_convertvector(v, h2)); }
DI float rb(float a) { return blo(pk(a, a)); }
DI u2 trd(const char* p) { return __builtin_bit_cast(u2, __builtin_amdgcn_ds_read_tr16_b64_v4i16((LAS s4*)p)); }
DI u2 trd(u32 a) { return __builtin_bit_cast(u2, __builtin_amdgcn_ds_read_tr16_b64_v4i16((LAS s4*)(size_t)a)); }
DI u4 cat(u2 a, u2 b) { return (u4){a.x, a.y, b.x, b.y}; }
DI float dot2(u32 a, u32 b, float c) { return __builtin_amdgcn_fdot2_f32_bf16(__builtin_bit_cast(bf2, a), __builtin_bit_cast(bf2, b), c, false); }
DI f4 mfma16(u4 a, u4 b, f4 c) { return __builtin_amdgcn_mfma_f32_16x16x32_bf16(__builtin_bit_cast(bf8, a), __builtin_bit_cast(bf8, b), c, 0, 0, 0); }
DI f4 mfma16h(u4 a, u4 b, f4 c) { return __builtin_amdgcn_mfma_f32_16x16x32_f16(__builtin_bit_cast(h8, a), __builtin_bit_cast(h8, b), c, 0, 0, 0); }
DI f16v mfma32(u4 a, u4 b, f16v c) { return __builtin_amdgcn_mfma_f32_32x32x16_bf16(__builtin_bit_cast(bf8, a), __builtin_bit_cast(bf8, b), c, 0, 0, 0); }
#define CM_MUL(d, a, b) "v_pk_mul_f32 " d ", " a ", " b " op_sel_hi:[0,1]\n\t"
#define CM_FMA(d, a, b) "v_pk_fma_f32 " d ", " a ", " b ", " d " op_sel:[1,1,0] op_sel_hi:[1,0,1] neg_lo:[0,1,0]\n\t"
DI void cmul2(f2& r0, f2 a0, f2 b0, f2& r1, f2 a1, f2 b1) {
  asm(CM_MUL("%0", "%2", "%3") CM_MUL("%1", "%4", "%5") CM_FMA("%0", "%2", "%3") CM_FMA("%1", "%4", "%5")
      : "=&v"(r0), "=&v"(r1) : "v"(a0), "v"(b0), "v"(a1), "v"(b1));
}
DI void cmul3(f2& r0, f2 a0, f2 b0, f2& r1, f2 a1, f2 b1, f2& r2, f2 a2, f2 b2) {
  asm(CM_MUL("%0", "%3", "%4") CM_MUL("%1", "%5", "%6") CM_MUL("%2", "%7", "%8") CM_FMA("%0", "%3", "%4") CM_FMA("%1", "%5", "%6") CM_FMA("%2", "%7", "%8")
      : "=&v"(r0), "=&v"(r1), "=&v"(r2) : "v"(a0), "v"(b0), "v"(a1), "v"(b1), "v"(a2), "v"(b2));
}
DI void cmul4(f2& r0, f2& r1, f2& r2, f2& r3, f2 a0, f2 a1, f2 a2, f2 a3, f2 b) {
  asm(CM_MUL("%0", "%4", "%8") CM_MUL("%1", "%5", "%8") CM_MUL("%2", "%6", "%8") CM_MUL("%3", "%7", "%8")
      CM_FMA("%0", "%4", "%8") CM_FMA("%1", "%5", "%8") CM_FMA("%2", "%6", "%8") CM_FMA("%3", "%7", "%8")
      : "=&v"(r0), "=&v"(r1), "=&v"(r2), "=&v"(r3) : "v"(a0), "v"(a1), "v"(a2), "v"(a3), "v"(b));
}
DI float exp_acc(float x) {
  const float t = x * 1.44269502f, r = fmaf(x, 1.44269502f, -t) + x * 1.92596299e-8f, e = __builtin_amdgcn_exp2f(t);
  return fmaf(e, r * 0.693147182f, e);
}
template <int CTRL> DI float dppf(float v) { return __int_as_float(__builtin_amdgcn_update_dpp(0, __float_as_int(v), CTRL, 0xf, 0xf, true)); }
DI float sum32(float v) {
  auto r = __builtin_amdgcn_permlane32_swap(__float_as_uint(v), __float_as_uint(v), false, false);
  return __uint_as_float(r[0]) + __uint_as_float(r[1]);
}
DI f2 ln_stats(float s1, float s2) {
  const float mean = s1 * (1.f / D);
  const float var = fmaxf(fmaf(-mean, mean, s2 * (1.f / D)), 0.f);
  return (f2){rb(mean), rb(__builtin_amdgcn_sqrtf(rb(rb(var) + 1e-5f)))};
}
typedef __amdgpu_buffer_rsrc_t rsrc_t;
DI rsrc_t mkrsrc(const void* p, u32 bytes) { return __builtin_amdgcn_make_buffer_rsrc((void*)p, 0, bytes, 0x00020000); }
DI u32 ldsaddr(const char* p) { return (u32)(size_t)(LAS const char*)p; }
template <int OFF> DI void glds16(u32 m0v, u32 voff, rsrc_t rs, u32 soff) {
  asm volatile("s_mov_b32 m0, %0\n\ts_nop 0\n\tbuffer_load_dwordx4 %1, %2, %3 offen offset:%4 lds" :: "s"(m0v), "v"(voff), "s"(rs), "s"(soff), "n"(OFF) : "memory");
}
DI void glds16x4(u32 m0v, u32 voff, rsrc_t rs, u32 soff) {
  asm volatile("s_mov_b32 m0, %0\n\ts_nop 0\n\tbuffer_load_dwordx4 %1, %2, %3 offen lds\n\tbuffer_load_dwordx4 %1, %2, %3 offen offset:1024 lds\n\t"
               "buffer_load_dwordx4 %1, %2, %3 offen offset:2048 lds\n\tbuffer_load_dwordx4 %1, %2, %3 offen offset:3072 lds"
               :: "s"(m0v), "v"(voff), "s"(rs), "s"(soff) : "memory");
}
template <int OFF> DI void bld16(u4& d, u32 voff, rsrc_t rs, u32 soff) {
  asm volatile("buffer_load_dwordx4 %0, %1, %2, %3 offen offset:%4" : "=v"(d) : "v"(voff), "s"(rs), "s"(soff), "n"(OFF));
}
template <int OFF> DI void bld16_sc1(u4& d, u32 voff, rsrc_t rs, u32 soff) {
  asm volatile("buffer_load_dwordx4 %0, %1, %2, %3 offen offset:%4 sc1" : "=v"(d) : "v"(voff), "s"(rs), "s"(soff), "n"(OFF));
}
template <int OFF, class T> DI void gld16(T& d, const void* p) { asm volatile("global_load_dwordx4 %0, %1, off offset:%2" : "=v"(d) : "v"(p), "n"(OFF)); }
DI void pin(u32& v) { asm("" : "+v"(v)); }
DI void rawbar() { asm volatile("s_barrier" ::: "memory"); }
template <int N> DI void vmwait() { asm volatile("s_waitcnt vmcnt(%0)" :: "n"(N) : "memory"); }
template <int N> DI void vmwait(u4& d) { asm volatile("s_waitcnt vmcnt(%1)" : "+v"(d) : "n"(N)); }
DI void bar() { asm volatile("s_waitcnt lgkmcnt(0)\n\ts_barrier" ::: "memory"); }
#define SB() __builtin_amdgcn_sched_barrier(0)
#define SGB(m, n) __builtin_amdgcn_sched_group_barrier(m, n, 0)
template <int I, int N, class F> DI void sfor(F&& f) { if constexpr (I < N) { f(std::integral_constant<int, I>{}); sfor<I + 1, N>(f); } }

typedef unsigned long long u64;
constexpr int KG_ROW = 544, KG_CH = 57 * KG_ROW, KG_T = 2 * KG_CH, KG_TS = 4160;
constexpr int CV_EX = 0, CV_K = KG_T + 2 * KG_TS, CV_KC = 4128, CV_H = CV_K + 8 * CV_KC, CV_HP = 1152, CV_X = CV_H + 8 * CV_HP, CV_ST = CV_X + 8192,
              CV_W = CV_ST + 8192, CV_V = CV_W + 16384, CV_M = CV_V + 1024, CV_FL = CV_M + 256, K1_LDS = CV_FL + 64;
enum { FL_EPOCH = 0, FL_EPOK = 2, FL_KG = 3, FL_MOD = 7, FL_MAIN = 8, FL_XT = 9, FL_XD = 10, FL_GO = 11, FL_EXCH = 12 };
typedef volatile LAS u32* ldsw;
DI void lds_arrive(ldsw w) {
  asm volatile("s_waitcnt lgkmcnt(0)" ::: "memory");
  if ((threadIdx.x & 63) == 0) __hip_atomic_fetch_add((LAS u32*)w, 1u, __ATOMIC_RELAXED, __HIP_MEMORY_SCOPE_WORKGROUP);
}
DI void lds_wait(ldsw w, u32 n) { while (*w < n) {} asm volatile("" ::: "memory"); }
DI u64 get_epoch(ldsw fl) { lds_wait(fl + FL_EPOK, 1u); return ((u64)fl[FL_EPOCH + 1] << 32) | fl[FL_EPOCH]; }
DI u4 ld16_sc1(const void* p) { u4 d; asm volatile("global_load_dwordx4 %0, %1, off sc1\n\ts_waitcnt vmcnt(0)" : "=v"(d) : "v"(p) : "memory"); return d; }
template <int OFF> DI void gld16_sc1(u4& d, const void* p) { asm volatile("global_load_dwordx4 %0, %1, off offset:%2 sc1" : "=v"(d) : "v"(p), "n"(OFF) : "memory"); }
DI bool mg_ok(u4 g, u64 ep) { const u32 h = (u32)(ep >> 8), l = (u32)ep << 24; return g.y == h && g.w == h && (g.x & 0xff000000u) == l && (g.z & 0xff000000u) == l; }
DI float mg_sum(u4 g) { return __uint_as_float(g.x << 8) + __uint_as_float(g.z << 8); }
DI float scale_c(float v) { v = rb(1.f + rb(v)); return v == 0.f ? 0x1p-60f : v; }

DI f2 cexp_pow2(float a, float fh, float fl, float p) {
  const float e = __builtin_amdgcn_exp2f(a * p);
  const float r = fmaf(fl, p, __builtin_amdgcn_fractf(fh * p));
  return (f2){e * __builtin_amdgcn_cosf(r), e * __builtin_amdgcn_sinf(r)};
}

DI void k1_help(Stamps& ts_, char* sm, int b, const u16* lar, const u16* aim, const u16* cre, const u16* cim, const u16* ldt, const u16* dsk,
                const u16* wg, const u16* wu, const u16* wd, char* ws) {
  const int t = threadIdx.x - 256, w = __builtin_amdgcn_readfirstlane(t >> 6), lane = t & 63, ch = w >> 1, half = w & 1;
  const int d0 = 64 * (b & 7) + 2 * (b >> 3), d = d0 + ch, n = 64 * half + lane;
  const ldsw fl = (ldsw)(size_t)(ldsaddr(sm) + CV_FL);
  const u32 v_lar = ldnt(lar + d * NM + n), v_aim = ldnt(aim + d * NM + n);
  const u32 cr = ldnt(cre + d * NM + n), ci = ldnt(cim + d * NM + n), cr2 = ldnt(cre + d * NM + (n ^ 64)), ci2 = ldnt(cim + d * NM + (n ^ 64));
  const u16 v_ldt = ldt[d], v_dsk = dsk[d];
  rawbar();
  const float dt = rb(exp_acc(b2f(v_ldt)));
  const float ds = b2f(v_dsk);
  const float zr = -rb(exp_acc(blo(v_lar))) * dt, zi = blo(v_aim) * dt;
  const float a = zr * 1.44269502f;
  const float fh = zi * 0.159154937f, fq = fmaf(zi, 0.159154937f, -fh) + zi * 6.42063833e-9f;
  const f2 e1 = cexp_pow2(a, fh, fq, 1.f), e8 = cexp_pow2(a, -fh, -fq, 8.f), e128 = cexp_pow2(a, fh, fq, 128.f), e1024 = cexp_pow2(a, fh, fq, 1024.f);
  int mx = (int)max(max(cr & 0x7fffu, ci & 0x7fffu), max(cr2 & 0x7fffu, ci2 & 0x7fffu));
  mx = max(mx, __builtin_amdgcn_update_dpp(0, mx, 0x111, 0xf, 0xf, true));
  mx = max(mx, __builtin_amdgcn_update_dpp(0, mx, 0x112, 0xf, 0xf, true));
  mx = max(mx, __builtin_amdgcn_update_dpp(0, mx, 0x114, 0xf, 0xf, true));
  mx = max(mx, __builtin_amdgcn_update_dpp(0, mx, 0x118, 0xf, 0xf, true));
  mx = max(max(__builtin_amdgcn_readlane(mx, 15), __builtin_amdgcn_readlane(mx, 31)), max(__builtin_amdgcn_readlane(mx, 47), __builtin_amdgcn_readlane(mx, 63)));
  const int ex = min(max(mx >> 7, 1), 253);
  const float cs = __int_as_float((254 - ex) << 23), kf = 2.f * dt * __int_as_float(ex << 23);
  char* base = sm + ch * KG_CH + n * 4;
  auto put = [&](int row, f2 v) { *(u32*)(base + row * KG_ROW) = pkh(v); };
  f2 lo[8], e[8], m[8], x0, x1, x2, x3, e16, e32, e64;
  lo[0] = (f2){blo(cr) * cs, -blo(ci) * cs};
  e[1] = e1; m[1] = e128;
  put(0, lo[0]); put(16, (f2){1.f, 0.f}); put(17, e1); put(24, e128); put(48, e1024);
  cmul3(e16, e8, e8, e[2], e1, e1, m[2], e128, e128);
  cmul2(lo[1], lo[0], e8, x0, e1024, e1024);
  put(18, e[2]); put(32, m[2]); put(1, lo[1]); put(56, x0);
  cmul3(e32, e16, e16, e[4], e[2], e[2], m[4], m[2], m[2]);
  cmul2(e[3], e[2], e1, m[3], m[2], e128);
  cmul2(lo[2], lo[0], e16, lo[3], lo[1], e16);
  put(20, e[4]); put(40, m[4]); put(19, e[3]); put(36, m[3]); put(2, lo[2]); put(3, lo[3]);
  cmul4(e[5], e[6], e[7], x0, e1, e[2], e[3], m[4], e[4]);
  put(21, e[5]); put(22, e[6]); put(23, e[7]); put(41, x0);
  cmul3(m[5], m[4], e128, m[6], m[4], m[2], m[7], m[4], m[3]);
  put(42, m[5]); put(44, m[6]); put(46, m[7]);
  cmul4(lo[4], lo[5], lo[6], lo[7], lo[0], lo[1], lo[2], lo[3], e32);
  put(4, lo[4]); put(5, lo[5]); put(6, lo[6]); put(7, lo[7]);
  cmul2(e64, e32, e32, x0, e128, e1);
  put(25, x0);
  cmul3(x0, m[5], e[4], x1, m[6], e[4], x2, m[7], e[4]);
  put(43, x0); put(45, x1); put(47, x2);
  cmul4(x0, x1, x2, x3, lo[0], lo[1], lo[2], lo[3], e64);
  put(8, x0); put(9, x1); put(10, x2); put(11, x3);
  cmul4(x0, x1, x2, x3, lo[4], lo[5], lo[6], lo[7], e64);
  put(12, x0); put(13, x1); put(14, x2); put(15, x3);
  cmul4(x0, x1, x2, x3, e[2], e[3], e[4], e[5], e128);
  put(26, x0); put(27, x1); put(28, x2); put(29, x3);
  cmul2(x0, e[6], e128, x1, e[7], e128);
  put(30, x0); put(31, x1);
  cmul3(x0, e[2], m[2], x1, e[4], m[2], x2, e[6], m[2]);
  put(33, x0); put(34, x1); put(35, x2);
  cmul3(x0, e[2], m[3], x1, e[4], m[3], x2, e[6], m[3]);
  put(37, x0); put(38, x1); put(39, x2);
  cmul4(x0, x1, x2, x3, m[1], m[2], m[3], m[4], e1024);
  put(49, x0); put(50, x1); put(51, x2); put(52, x3);
  cmul3(x0, m[5], e1024, x1, m[6], e1024, x2, m[7], e1024);
  put(53, x0); put(54, x1); put(55, x2);

  lds_arrive(fl + FL_KG + ch);
  lds_wait(fl + FL_KG + ch, 2u);
  {
    const int c = lane & 15, g = lane >> 4;
    const char* fb = sm + ch * KG_CH + c * KG_ROW + g * 16;
    u16* T = (u16*)(sm + KG_T + ch * KG_TS);
    f4 acc0 = {0.f, 0.f, 0.f, 0.f}, acc1 = acc0;
    u4 B[8], A[8];
#pragma unroll
    for (int s = 0; s < 8; ++s) { B[s] = *(const u4*)(fb + s * 64); A[s] = *(const u4*)(fb + (16 + 16 * half) * KG_ROW + s * 64); }
    if (!half) {
      u4 A2[8];
#pragma unroll
      for (int s = 0; s < 8; ++s) A2[s] = *(const u4*)(fb + 48 * KG_ROW + s * 64);
      SB();
#pragma unroll
      for (int s = 0; s < 8; ++s) acc0 = mfma16h(A[s], B[s], acc0);
#pragma unroll
      for (int s = 0; s < 8; ++s) acc1 = mfma16h(A2[s], B[s], acc1);
      acc0 *= kf; acc1 *= kf;
      if (lane == 0) acc0[0] += ds;
      *(u2*)(T + 8 * c + 4 * (g & 1) + 128 * (g >> 1)) = (u2){pk(acc0[0], acc0[1]), pk(acc0[2], acc0[3])};
      if (g < 2) {
#pragma unroll
        for (int e = 0; e < 4; ++e) T[1024 + 128 * (4 * g + e) + 8 * c] = (u16)pk(acc1[e], 0.f);
      } else if (lane == 32) T[2048] = (u16)pk(acc1[0], 0.f);
    } else {
      SB();
#pragma unroll
      for (int s = 0; s < 8; ++s) acc0 = mfma16h(A[s], B[s], acc0);
      acc0 *= kf;
#pragma unroll
      for (int e = 0; e < 4; ++e) {
        const int p = g < 2 ? 256 + 128 * g + 2 * e : 512 + 128 * (2 * (g - 2) + (e >> 1)) + 4 * (e & 1);
        T[p + 8 * c] = (u16)pk(acc0[e], 0.f);
      }
    }
  }
  lds_arrive(fl + FL_KG + 2 + ch);
  constexpr u32 KEEP = 0x03020100u, LL = 0x01000100u, LH = 0x05040100u, HH = 0x05040504u;
  const u32 sel0 = n < 16 ? KEEP : LL, sel1 = n < 16 ? KEEP : n < 64 ? LH : LL, sel2 = n < 16 ? KEEP : n < 64 ? LL : LH, sel2b = n < 64 ? sel2 : HH;
  const u32 sel3 = n < 16 ? KEEP : n < 32 ? LH : HH, selp2 = n <= 16 ? KEEP : n <= 64 ? LL : HH, selp3 = n <= 16 ? KEEP : n <= 32 ? LH : HH;
  if (t < 16) *(u4*)(sm + CV_K + (t >> 1) * CV_KC + 16 * (t & 1)) = (u4){0u, 0u, 0u, 0u};
  lds_wait(fl + FL_KG + 2 + ch, 2u);
  {
    const u16* T = (const u16*)(sm + KG_T + ch * KG_TS) + 16 * n;
    const u4 v0 = *(const u4*)T, v1 = *(const u4*)(T + 8);
    const u2 vp = *(const u2*)(T - 4);
    const u32 e16 = T[16];
    u32 k[10];
    k[0] = n ? __builtin_amdgcn_perm(v0.x, vp.x, selp2) : 0u;
    k[1] = n ? __builtin_amdgcn_perm(v0.x, vp.y, selp3) : 0u;
    k[2] = __builtin_amdgcn_perm(v0.x, v0.x, sel0); k[3] = __builtin_amdgcn_perm(v0.z, n < 32 ? v0.y : v0.x, sel1);
    k[4] = __builtin_amdgcn_perm(v1.x, n < 64 ? v0.z : v0.x, sel2); k[5] = __builtin_amdgcn_perm(v1.x, v0.w, sel3);
    k[6] = __builtin_amdgcn_perm(v1.x, v1.x, sel0); k[7] = __builtin_amdgcn_perm(v1.z, n < 32 ? v1.y : v1.x, sel1);
    k[8] = __builtin_amdgcn_perm(e16, v1.z, sel2b); k[9] = __builtin_amdgcn_perm(e16, v1.w, sel3);
    u32 o[9];
#pragma unroll
    for (int i = 0; i < 9; ++i) o[i] = __builtin_amdgcn_alignbit(k[i + 1], k[i], 16);
    char* kd = sm + CV_K + 4 * ch * CV_KC + 32 + 32 * n;
#pragma unroll
    for (int i = 0; i < 2; ++i) {
      *(u4*)(kd + 16 * i) = (u4){k[4 * i + 2], k[4 * i + 3], k[4 * i + 4], k[4 * i + 5]};
      *(u4*)(kd + 16 * i + CV_KC) = (u4){o[4 * i + 1], o[4 * i + 2], o[4 * i + 3], o[4 * i + 4]};
      *(u4*)(kd + 16 * i + 2 * CV_KC) = (u4){k[4 * i + 1], k[4 * i + 2], k[4 * i + 3], k[4 * i + 4]};
      *(u4*)(kd + 16 * i + 3 * CV_KC) = (u4){o[4 * i], o[4 * i + 1], o[4 * i + 2], o[4 * i + 3]};
    }
  }
  STAMP(0,4);
  bar();
  const int task = b + 256 * w, kq = task & 3, blk = task >> 2, mat = blk >= 43, jb = blk - 43 * mat;
  const bool work = task < 344;
  if (work) {
    const rsrc_t rw = mkrsrc(mat ? wu : wg, D * DF * 2);
    const u32 vo = (128 * kq + (lane >> 2)) * (DF * 2) + 64 * jb + 16 * (lane & 3);
    asm volatile("s_nop 4");
#pragma unroll
    for (int i = 0; i < 8; ++i) glds16<0>(ldsaddr(sm) + CV_W + 8192 * w + 1024 * i, vo, rw, i * 16 * DF * 2);
  }
  u32 dummy[4];
  {
    const int row = 32 * (b >> 3) + 8 * w + (lane >> 3);
    const char* base = (const char*)(row >= D ? wu : wg) + (row & (D - 1)) * (DF * 2);
#pragma unroll
    for (int i = 0; i < 2; ++i) {
      const int off = min((b & 1) * 1344 + 128 * ((lane & 7) + 8 * i), (b & 1) ? DF * 2 - 4 : 1340);
      asm volatile("global_load_dword %0, %1, off" : "=v"(dummy[i]) : "v"(base + off));
      asm volatile("global_load_dword %0, %1, off" : "=v"(dummy[2 + i]) : "v"((const char*)wd + (b >> 3) * 44032 + w * 11008 + min(lane + 64 * i, 85) * 128));
    }
  }
  if (work) {
    const u64 ep = get_epoch(fl);
    const char* gp = ws + WS_MG + (3 * D + 128 * kq + 2 * lane) * 32;
    u4 gs[4], gc[4];
#pragma unroll 1
    for (int spin = 0; spin < (1 << 16); ++spin) {
      sfor<0, 4>([&](auto i) { gld16_sc1<16 * i>(gs[i], gp); gld16_sc1<16 * i>(gc[i], gp + D * 32); });
      asm volatile("s_waitcnt vmcnt(0)" : "+v"(gs[0]), "+v"(gs[1]), "+v"(gs[2]), "+v"(gs[3]), "+v"(gc[0]), "+v"(gc[1]), "+v"(gc[2]), "+v"(gc[3]) :: "memory");
      bool ok = true;
#pragma unroll
      for (int i = 0; i < 4; ++i) ok = ok && mg_ok(gs[i], ep) && mg_ok(gc[i], ep);
      if (__all(ok)) break;
    }
    u32* vv = (u32*)(sm + CV_V + 512 * w);
    vv[lane] = pk(scale_c(mg_sum(gc[0]) + mg_sum(gc[1])), scale_c(mg_sum(gc[2]) + mg_sum(gc[3])));
    vv[64 + lane] = pk(mg_sum(gs[0]) + mg_sum(gs[1]), mg_sum(gs[2]) + mg_sum(gs[3]));
    const int hh = lane >> 5;
    const u32 ra = ldsaddr(sm) + CV_W + 8192 * w + 64 * (8 * hh + ((lane & 15) >> 2)) + 32 * ((lane >> 4) & 1) + 8 * (lane & 3);
    f16v sums;
#pragma unroll
    for (int e = 0; e < 16; ++e) sums[e] = 0.f;
#pragma unroll
    for (int i = 0; i < 8; ++i) sums = mfma32(*(const u4*)(vv + 64 * (lane & 1) + 8 * i + 4 * hh), cat(trd(ra + 1024 * i), trd(ra + 1024 * i + 256)), sums);
    if (lane < 32) {
      u32* exf = (u32*)(ws + WS_EX) + ((2 * jb + (lane >> 4)) * 64 + 16 * mat + (lane & 15)) * 4 + kq;
      const float h0 = rb(sums[0]), h1 = rb(sums[1]);
      exf[0] = pk(h0, sums[0] - h0);
      exf[128] = pk(h1, sums[1] - h1);
    }
  }
  asm volatile("s_waitcnt vmcnt(0)" : "+v"(dummy[0]), "+v"(dummy[1]), "+v"(dummy[2]), "+v"(dummy[3]) :: "memory");
}

DI float red2(float a, float b, bool rows16) {
  if (rows16) { const auto r = __builtin_amdgcn_permlane16_swap(__float_as_uint(a), __float_as_uint(b), false, false); return __uint_as_float(r[0]) + __uint_as_float(r[1]); }
  const auto r = __builtin_amdgcn_permlane32_swap(__float_as_uint(a), __float_as_uint(b), false, false);
  return __uint_as_float(r[0]) + __uint_as_float(r[1]);
}

DI void k1_main(Stamps& ts_, char* sm, int b, const u16* x, const u16* c, const u16* wm, char* ws) {
  const int t = threadIdx.x, w = __builtin_amdgcn_readfirstlane(t >> 6), lane = t & 63, ch = w >> 1, half = w & 1;
  const int d0 = 64 * (b & 7) + 2 * (b >> 3), d = d0 + ch;
  const ldsw fl = (ldsw)(size_t)(ldsaddr(sm) + CV_FL);
  const int c16 = lane & 15, g = lane >> 4;
  __builtin_amdgcn_s_setprio(2);
  if (t < 16) fl[t] = 0u;
  if (w == 3) *(u4*)(sm + CV_H + (lane >> 3) * CV_HP + 16 * (t & 7)) = (u4){0u, 0u, 0u, 0u};
  if (w < 2) {
    const int row = 8 * b + 4 * w + g, mq = b / 48, mc = b - 48 * mq, rg = t >> 3;
    u4 xs[4], wv[8], cv;
    u64 ep0;
    if (w == 0) ep0 = *(const u64*)(ws + WS_EPOCH);
#pragma unroll
    for (int i = 0; i < 4; ++i) xs[i] = ldnt((const u4*)(x + row * D + 128 * i + 8 * c16));
    if (b < 192) {
      cv = *(const u4*)(c + 128 * mq + 8 * rg);
#pragma unroll
      for (int i = 0; i < 8; ++i) wv[i] = ldnt((const u4*)(wm + (128 * mq + 8 * rg + i) * (6 * D) + 64 * mc + 8 * (t & 7)));
    }
    bar();
    if (w == 0) {
      fl[FL_EPOCH] = (u32)(ep0 + 1); fl[FL_EPOCH + 1] = (u32)((ep0 + 1) >> 32);
      asm volatile("s_waitcnt lgkmcnt(0)" ::: "memory");
      fl[FL_EPOK] = 1u;
    }
    float s1 = 0.f, s2 = 0.f, s3 = 0.f, s4 = 0.f;
#pragma unroll
    for (int i = 0; i < 4; ++i)
#pragma unroll
      for (int j = 0; j < 4; j += 2) {
        s1 = dot2(xs[i][j], 0x3f803f80u, s1); s2 = dot2(xs[i][j], xs[i][j], s2);
        s3 = dot2(xs[i][j + 1], 0x3f803f80u, s3); s4 = dot2(xs[i][j + 1], xs[i][j + 1], s4);
      }
    s1 += s3; s2 += s4;
    s1 += dppf<0xb1>(s1); s2 += dppf<0xb1>(s2); s1 += dppf<0x4e>(s1); s2 += dppf<0x4e>(s2);
    s1 += dppf<0x141>(s1); s2 += dppf<0x141>(s2); s1 += dppf<0x140>(s1); s2 += dppf<0x140>(s2);
    const f2 st = ln_stats(s1, s2);
    const u64 ep = get_epoch(fl);
    if (c16 == 0) {
      __hip_atomic_store((u64*)(ws + WS_ST1) + row, (ep << 32) | (__float_as_uint(st.x) >> 16) | __float_as_uint(st.y), __ATOMIC_RELAXED, __HIP_MEMORY_SCOPE_AGENT);
      ((u32*)(ws + WS_ST2))[row] = 0u;
    }
    if (t == 0 && b < 16) ((u32*)(ws + WS_CNT))[64 * b] = 0u;
    if (b < 192) {
      float acc[8] = {0.f, 0.f, 0.f, 0.f, 0.f, 0.f, 0.f, 0.f};
#pragma unroll
      for (int i = 0; i < 4; ++i) {
        const float c0 = blo(cv[i]), c1 = bhi(cv[i]);
        const u32 sp = pk(c0 * __builtin_amdgcn_rcpf(1.f + __expf(-c0)), c1 * __builtin_amdgcn_rcpf(1.f + __expf(-c1)));
#pragma unroll
        for (int j = 0; j < 4; ++j) {
          acc[2 * j] = dot2(__builtin_amdgcn_perm(wv[2 * i + 1][j], wv[2 * i][j], 0x05040100u), sp, acc[2 * j]);
          acc[2 * j + 1] = dot2(__builtin_amdgcn_perm(wv[2 * i + 1][j], wv[2 * i][j], 0x07060302u), sp, acc[2 * j + 1]);
        }
      }
      const float p0 = red2(red2(acc[0], acc[4], false), red2(acc[2], acc[6], false), true), p1 = red2(red2(acc[1], acc[5], false), red2(acc[3], acc[7], false), true);
      const float q0 = p0 + dppf<0x128>(p0), q1 = p1 + dppf<0x128>(p1);
      float v = (lane & 8) ? q1 : q0;
      float* mx = (float*)(sm + CV_M);
      if (w == 1) { mx[lane] = v; lds_arrive(fl + FL_MOD); }
      else {
        lds_wait(fl + FL_MOD, 1u);
        v += mx[lane];
        const u32 bits = __float_as_uint(v) + 0x80u;
        __hip_atomic_store((u64*)(ws + WS_MG) + (64 * mc + 8 * (lane & 7) + (lane >> 3)) * 4 + mq, (ep << 24) | (bits >> 8), __ATOMIC_RELAXED, __HIP_MEMORY_SCOPE_AGENT);
      }
    }
  } else {
    const int tg = t - 128, r0 = 64 * (b >> 3), c0 = 64 * (b & 7), q = c16 >> 2, p = lane & 3;
    u4 v[4];
#pragma unroll
    for (int i = 0; i < 4; ++i) v[i] = ldnt((const u4*)(x + (r0 + 16 * i + (tg >> 3)) * D + c0 + 8 * (tg & 7)));
    bar();
    char* tb = sm + CV_X;
#pragma unroll
    for (int i = 0; i < 4; ++i) *(u4*)(tb + (16 * i + (tg >> 3)) * 144 + 16 * (tg & 7)) = v[i];
    lds_arrive(fl + FL_XT);
    lds_wait(fl + FL_XT, 2u);
#pragma unroll
    for (int i = 0; i < 4; ++i) {
      const char* a = tb + (32 * (i & 1) + 8 * g + q) * 144 + 64 * half + 32 * (i >> 1) + 8 * p;
      const u4 o = cat(trd(a), trd(a + 4 * 144));
      const u16* dst = (const u16*)(ws + WS_XT) + (c0 + 32 * half + 16 * (i >> 1) + c16) * L + r0 + 32 * (i & 1) + 8 * g;
      asm volatile("global_store_dwordx4 %0, %1, off sc1\n\ts_nop 1" :: "v"(dst), "v"(o) : "memory");
    }
    const u64 ep = get_epoch(fl);
    asm volatile("s_waitcnt vmcnt(0)" ::: "memory");
    if (lane == 0 && __hip_atomic_fetch_add((LAS u32*)fl + FL_XD, 1u, __ATOMIC_RELAXED, __HIP_MEMORY_SCOPE_WORKGROUP) == 1u)
      __hip_atomic_store((u32*)(ws + WS_FX) + 32 * (b & 7) + (b >> 3), (u32)ep, __ATOMIC_RELAXED, __HIP_MEMORY_SCOPE_AGENT);
  }
  STAMP(0,1);
  const u64 ep = get_epoch(fl);
  float sa[2], c1[2], ga, c2;
  u4 xv[2];
  if (w == 0) {
    const u32* fx = (const u32*)(ws + WS_FX) + 32 * (b & 7) + (lane & 31);
#pragma unroll 1
    for (int spin = 0; spin < (1 << 16); ++spin) {
      if (__all(__hip_atomic_load(fx, __ATOMIC_RELAXED, __HIP_MEMORY_SCOPE_AGENT) == (u32)ep)) break;
      __builtin_amdgcn_s_sleep(2);
    }
    fl[FL_GO] = 1u;
  } else {
    while (fl[FL_GO] == 0u) __builtin_amdgcn_s_sleep(1);
  }
  STAMP(0,5);
  {
    const char* xp = ws + WS_XT + d0 * (L * 2) + 16 * t;
    gld16_sc1<0>(xv[0], xp); gld16_sc1<0>(xv[1], xp + L * 2);
    const char* sp = ws + WS_ST1 + 16 * t;
    const int ml = lane < 20 ? lane : 19, mch = ml >> 2;
    const char* mp = ws + WS_MG + (((mch + (mch > 2)) * D + d0) * 4 + 2 * (ml & 3)) * 8;
    u4 gs[4], gm;
#pragma unroll 1
    for (int spin = 0; spin < (1 << 16); ++spin) {
      gld16_sc1<0>(gs[0], sp); gld16_sc1<0>(gs[1], sp + 4096); gld16_sc1<0>(gs[2], sp + 8192); gld16_sc1<0>(gs[3], sp + 12288); gld16_sc1<0>(gm, mp);
      asm volatile("s_waitcnt vmcnt(0)" : "+v"(gs[0]), "+v"(gs[1]), "+v"(gs[2]), "+v"(gs[3]), "+v"(gm) :: "memory");
      bool ok = mg_ok(gm, ep);
#pragma unroll
      for (int i = 0; i < 4; ++i) ok = ok && gs[i].y == (u32)ep && gs[i].w == (u32)ep;
      if (__all(ok)) break;
    }
    asm volatile("" : "+v"(xv[0]), "+v"(xv[1]));
#pragma unroll
    for (int i = 0; i < 4; ++i) *(u2*)(sm + CV_ST + 8 * t + 2048 * i) = (u2){gs[i].x, gs[i].z};
    lds_arrive(fl + FL_MAIN);
    float v = mg_sum(gm);
    v += dppf<0xb1>(v);
    const float vr = rb(v), vs = scale_c(v), vp = rb(1.f + vr);
    auto pick = [](float z, int l) { return __int_as_float(__builtin_amdgcn_readlane(__float_as_int(z), l)); };
    sa[0] = pick(vr, 0); sa[1] = pick(vr, 2); c1[0] = pick(vp, 4); c1[1] = pick(vp, 6);
    ga = ch ? pick(vr, 10) : pick(vr, 8); c2 = ch ? pick(vs, 14) : pick(vs, 12);
    if (w == 0) {
      if (lane == 12 || lane == 14) ((float*)(ws + WS_INV))[d0 + ((lane >> 1) & 1)] = 1.f / vs;
      if (lane == 16 || lane == 18) ((float*)(ws + WS_GB))[d0 + ((lane >> 1) & 1)] = vr;
    }
    lds_wait(fl + FL_MAIN, 4u);
    if (b == 0 && t == 0) *(u64*)(ws + WS_EPOCH) = ep;
  }
  STAMP(0,2);
  {
    const u4 m0 = *(const u4*)(sm + CV_ST + 32 * t), m1 = *(const u4*)(sm + CV_ST + 32 * t + 16);
    float rs[8], mm[8];
#pragma unroll
    for (int j = 0; j < 8; ++j) { const u32 z = j < 4 ? m0[j & 3] : m1[j & 3]; rs[j] = __builtin_amdgcn_rcpf(bhi(z)); mm[j] = blo(z); }
#pragma unroll
    for (int i = 0; i < 2; ++i) {
      *(u4*)(sm + CV_X + 4096 * i + 16 * t) = xv[i];
      float h[8];
#pragma unroll
      for (int j = 0; j < 8; ++j) h[j] = fmaf((((j & 1) ? bhi(xv[i][j >> 1]) : blo(xv[i][j >> 1])) - mm[j]) * rs[j], c1[i], sa[i]);
#pragma unroll
      for (int r = 0; r < 4; ++r) *(u32*)(sm + CV_H + (4 * i + r) * CV_HP + 128 + 4 * t) = pk(h[r], h[r + 4]);
    }
  }
  f4 acc[8];
#pragma unroll
  for (int i = 0; i < 8; ++i) acc[i] = (f4){0.f, 0.f, 0.f, 0.f};
  u32 ka = ldsaddr(sm) + CV_K + (4 * ch + 2 * half) * CV_KC + 32 + 8 * (8 * g + (lane & 3) - ((lane & 15) >> 2));
  u32 hb = ldsaddr(sm) + CV_H + (4 * ch + 2 * half) * CV_HP + 128 + 8 * c16 - 16 * g - 8 - 64;

  bar();
  {
    u32 cnt;
    asm volatile(
      "ds_read_b64_tr_b16 v[64:65], %8 offset:32\n"
      "ds_read_b64_tr_b16 v[66:67], %8 offset:0\n"
      "ds_read_b64 v[120:121], %9 offset:832\n"
      "ds_read_b64 v[122:123], %9 offset:840\n"
      "ds_read_b64 v[124:125], %9 offset:960\n"
      "ds_read_b64 v[126:127], %9 offset:968\n"
      "ds_read_b64 v[112:113], %9 offset:576\n"
      "ds_read_b64 v[114:115], %9 offset:584\n"
      "ds_read_b64 v[116:117], %9 offset:704\n"
      "ds_read_b64 v[118:119], %9 offset:712\n"
      "ds_read_b64_tr_b16 v[68:69], %8 offset:544\n"
      "ds_read_b64_tr_b16 v[70:71], %8 offset:512\n"
      "ds_read_b64 v[108:109], %9 offset:448\n"
      "ds_read_b64 v[110:111], %9 offset:456\n"
      "ds_read_b64 v[104:105], %9 offset:320\n"
      "ds_read_b64 v[106:107], %9 offset:328\n"
      "ds_read_b64_tr_b16 v[72:73], %8 offset:1056\n"
      "ds_read_b64_tr_b16 v[74:75], %8 offset:1024\n"
      "ds_read_b64_tr_b16 v[76:77], %8 offset:1568\n"
      "ds_read_b64_tr_b16 v[78:79], %8 offset:1536\n"
      "ds_read_b64 v[100:101], %9 offset:192\n"
      "ds_read_b64 v[102:103], %9 offset:200\n"
      "ds_read_b64_tr_b16 v[80:81], %8 offset:2080\n"
      "ds_read_b64_tr_b16 v[82:83], %8 offset:2048\n"
      "ds_read_b64 v[96:97], %9 offset:64\n"
      "ds_read_b64 v[98:99], %9 offset:72\n"
      "ds_read_b64_tr_b16 v[84:85], %8 offset:2592\n"
      "ds_read_b64_tr_b16 v[86:87], %8 offset:2560\n"
      "ds_read_b64_tr_b16 v[88:89], %8 offset:3104\n"
      "ds_read_b64_tr_b16 v[90:91], %8 offset:3072\n"
      "ds_read_b64_tr_b16 v[92:93], %8 offset:3616\n"
      "ds_read_b64_tr_b16 v[94:95], %8 offset:3584\n"
      "s_mov_b32 %10, 2\n"
      "s_waitcnt lgkmcnt(0)\n"
      "Lconv%=:\n"
      "v_mfma_f32_16x16x32_bf16 %6, v[64:67], v[120:123], %6\n"
      "ds_read_b64_tr_b16 v[128:129], %8 offset:288\n"
      "v_mfma_f32_16x16x32_bf16 %7, v[64:67], v[124:127], %7\n"
      "ds_read_b64_tr_b16 v[130:131], %8 offset:256\n"
      "v_mfma_f32_16x16x32_bf16 %4, v[64:67], v[112:115], %4\n"
      "ds_read_b64 v[184:185], %9 offset:768\n"
      "v_mfma_f32_16x16x32_bf16 %5, v[64:67], v[116:119], %5\n"
      "ds_read_b64 v[186:187], %9 offset:776\n"
      "v_mfma_f32_16x16x32_bf16 %6, v[68:71], v[116:119], %6\n"
      "ds_read_b64 v[188:189], %9 offset:896\n"
      "v_mfma_f32_16x16x32_bf16 %7, v[68:71], v[120:123], %7\n"
      "ds_read_b64 v[190:191], %9 offset:904\n"
      "v_mfma_f32_16x16x32_bf16 %4, v[68:71], v[108:111], %4\n"
      "ds_read_b64 v[176:177], %9 offset:512\n"
      "v_mfma_f32_16x16x32_bf16 %2, v[64:67], v[104:107], %2\n"
      "ds_read_b64 v[178:179], %9 offset:520\n"
      "v_mfma_f32_16x16x32_bf16 %6, v[72:75], v[112:115], %6\n"
      "ds_read_b64 v[180:181], %9 offset:640\n"
      "v_mfma_f32_16x16x32_bf16 %3, v[64:67], v[108:111], %3\n"
      "ds_read_b64 v[182:183], %9 offset:648\n"
      "v_mfma_f32_16x16x32_bf16 %7, v[72:75], v[116:119], %7\n"
      "ds_read_b64_tr_b16 v[132:133], %8 offset:800\n"
      "v_mfma_f32_16x16x32_bf16 %4, v[72:75], v[104:107], %4\n"
      "ds_read_b64_tr_b16 v[134:135], %8 offset:768\n"
      "v_mfma_f32_16x16x32_bf16 %5, v[68:71], v[112:115], %5\n"
      "ds_read_b64 v[172:173], %9 offset:384\n"
      "v_mfma_f32_16x16x32_bf16 %6, v[76:79], v[108:111], %6\n"
      "ds_read_b64 v[174:175], %9 offset:392\n"
      "v_mfma_f32_16x16x32_bf16 %7, v[76:79], v[112:115], %7\n"
      "ds_read_b64 v[168:169], %9 offset:256\n"
      "v_mfma_f32_16x16x32_bf16 %4, v[76:79], v[100:103], %4\n"
      "ds_read_b64 v[170:171], %9 offset:264\n"
      "v_mfma_f32_16x16x32_bf16 %1, v[64:67], v[100:103], %1\n"
      "ds_read_b64_tr_b16 v[136:137], %8 offset:1312\n"
      "v_mfma_f32_16x16x32_bf16 %5, v[72:75], v[108:111], %5\n"
      "ds_read_b64_tr_b16 v[138:139], %8 offset:1280\n"
      "v_mfma_f32_16x16x32_bf16 %6, v[80:83], v[104:107], %6\n"
      "ds_read_b64_tr_b16 v[140:141], %8 offset:1824\n"
      "v_mfma_f32_16x16x32_bf16 %0, v[64:67], v[96:99], %0\n"
      "ds_read_b64_tr_b16 v[142:143], %8 offset:1792\n"
      "v_mfma_f32_16x16x32_bf16 %7, v[80:83], v[108:111], %7\n"
      "ds_read_b64 v[164:165], %9 offset:128\n"
      "v_mfma_f32_16x16x32_bf16 %5, v[76:79], v[104:107], %5\n"
      "ds_read_b64 v[166:167], %9 offset:136\n"
      "v_mfma_f32_16x16x32_bf16 %2, v[68:71], v[100:103], %2\n"
      "ds_read_b64_tr_b16 v[144:145], %8 offset:2336\n"
      "v_mfma_f32_16x16x32_bf16 %6, v[84:87], v[100:103], %6\n"
      "ds_read_b64_tr_b16 v[146:147], %8 offset:2304\n"
      "v_mfma_f32_16x16x32_bf16 %7, v[84:87], v[104:107], %7\n"
      "ds_read_b64 v[160:161], %9 offset:0\n"
      "v_mfma_f32_16x16x32_bf16 %3, v[68:71], v[104:107], %3\n"
      "ds_read_b64 v[162:163], %9 offset:8\n"
      "v_mfma_f32_16x16x32_bf16 %2, v[72:75], v[96:99], %2\n"
      "ds_read_b64_tr_b16 v[148:149], %8 offset:2848\n"
      "v_mfma_f32_16x16x32_bf16 %5, v[80:83], v[100:103], %5\n"
      "ds_read_b64_tr_b16 v[150:151], %8 offset:2816\n"
      "v_mfma_f32_16x16x32_bf16 %7, v[88:91], v[100:103], %7\n"
      "ds_read_b64_tr_b16 v[152:153], %8 offset:3360\n"
      "v_mfma_f32_16x16x32_bf16 %6, v[88:91], v[96:99], %6\n"
      "ds_read_b64_tr_b16 v[154:155], %8 offset:3328\n"
      "v_mfma_f32_16x16x32_bf16 %3, v[72:75], v[100:103], %3\n"
      "ds_read_b64_tr_b16 v[156:157], %8 offset:3872\n"
      "v_mfma_f32_16x16x32_bf16 %1, v[68:71], v[96:99], %1\n"
      "ds_read_b64_tr_b16 v[158:159], %8 offset:3840\n"
      "v_mfma_f32_16x16x32_bf16 %7, v[92:95], v[96:99], %7\n"
      "v_mfma_f32_16x16x32_bf16 %5, v[84:87], v[96:99], %5\n"
      "v_mfma_f32_16x16x32_bf16 %4, v[80:83], v[96:99], %4\n"
      "v_mfma_f32_16x16x32_bf16 %3, v[76:79], v[96:99], %3\n"
      "s_waitcnt lgkmcnt(0)\n"
      "v_add_u32 %8, 4128, %8\n"
      "v_add_u32 %9, 1152, %9\n"
      "v_mfma_f32_16x16x32_bf16 %6, v[128:131], v[184:187], %6\n"
      "ds_read_b64_tr_b16 v[64:65], %8 offset:32\n"
      "v_mfma_f32_16x16x32_bf16 %7, v[128:131], v[188:191], %7\n"
      "ds_read_b64_tr_b16 v[66:67], %8 offset:0\n"
      "v_mfma_f32_16x16x32_bf16 %4, v[128:131], v[176:179], %4\n"
      "ds_read_b64 v[120:121], %9 offset:832\n"
      "v_mfma_f32_16x16x32_bf16 %5, v[128:131], v[180:183], %5\n"
      "ds_read_b64 v[122:123], %9 offset:840\n"
      "v_mfma_f32_16x16x32_bf16 %6, v[132:135], v[180:183], %6\n"
      "ds_read_b64 v[124:125], %9 offset:960\n"
      "v_mfma_f32_16x16x32_bf16 %7, v[132:135], v[184:187], %7\n"
      "ds_read_b64 v[126:127], %9 offset:968\n"
      "v_mfma_f32_16x16x32_bf16 %4, v[132:135], v[172:175], %4\n"
      "ds_read_b64 v[112:113], %9 offset:576\n"
      "v_mfma_f32_16x16x32_bf16 %2, v[128:131], v[168:171], %2\n"
      "ds_read_b64 v[114:115], %9 offset:584\n"
      "v_mfma_f32_16x16x32_bf16 %6, v[136:139], v[176:179], %6\n"
      "ds_read_b64 v[116:117], %9 offset:704\n"
      "v_mfma_f32_16x16x32_bf16 %3, v[128:131], v[172:175], %3\n"
      "ds_read_b64 v[118:119], %9 offset:712\n"
      "v_mfma_f32_16x16x32_bf16 %7, v[136:139], v[180:183], %7\n"
      "ds_read_b64_tr_b16 v[68:69], %8 offset:544\n"
      "v_mfma_f32_16x16x32_bf16 %4, v[136:139], v[168:171], %4\n"
      "ds_read_b64_tr_b16 v[70:71], %8 offset:512\n"
      "v_mfma_f32_16x16x32_bf16 %5, v[132:135], v[176:179], %5\n"
      "ds_read_b64 v[108:109], %9 offset:448\n"
      "v_mfma_f32_16x16x32_bf16 %6, v[140:143], v[172:175], %6\n"
      "ds_read_b64 v[110:111], %9 offset:456\n"
      "v_mfma_f32_16x16x32_bf16 %7, v[140:143], v[176:179], %7\n"
      "ds_read_b64 v[104:105], %9 offset:320\n"
      "v_mfma_f32_16x16x32_bf16 %4, v[140:143], v[164:167], %4\n"
      "ds_read_b64 v[106:107], %9 offset:328\n"
      "v_mfma_f32_16x16x32_bf16 %1, v[128:131], v[164:167], %1\n"
      "ds_read_b64_tr_b16 v[72:73], %8 offset:1056\n"
      "v_mfma_f32_16x16x32_bf16 %5, v[136:139], v[172:175], %5\n"
      "ds_read_b64_tr_b16 v[74:75], %8 offset:1024\n"
      "v_mfma_f32_16x16x32_bf16 %6, v[144:147], v[168:171], %6\n"
      "ds_read_b64_tr_b16 v[76:77], %8 offset:1568\n"
      "v_mfma_f32_16x16x32_bf16 %0, v[128:131], v[160:163], %0\n"
      "ds_read_b64_tr_b16 v[78:79], %8 offset:1536\n"
      "v_mfma_f32_16x16x32_bf16 %7, v[144:147], v[172:175], %7\n"
      "ds_read_b64 v[100:101], %9 offset:192\n"
      "v_mfma_f32_16x16x32_bf16 %5, v[140:143], v[168:171], %5\n"
      "ds_read_b64 v[102:103], %9 offset:200\n"
      "v_mfma_f32_16x16x32_bf16 %2, v[132:135], v[164:167], %2\n"
      "ds_read_b64_tr_b16 v[80:81], %8 offset:2080\n"
      "v_mfma_f32_16x16x32_bf16 %6, v[148:151], v[164:167], %6\n"
      "ds_read_b64_tr_b16 v[82:83], %8 offset:2048\n"
      "v_mfma_f32_16x16x32_bf16 %7, v[148:151], v[168:171], %7\n"
      "ds_read_b64 v[96:97], %9 offset:64\n"
      "v_mfma_f32_16x16x32_bf16 %3, v[132:135], v[168:171], %3\n"
      "ds_read_b64 v[98:99], %9 offset:72\n"
      "v_mfma_f32_16x16x32_bf16 %2, v[136:139], v[160:163], %2\n"
      "ds_read_b64_tr_b16 v[84:85], %8 offset:2592\n"
      "v_mfma_f32_16x16x32_bf16 %5, v[144:147], v[164:167], %5\n"
      "ds_read_b64_tr_b16 v[86:87], %8 offset:2560\n"
      "v_mfma_f32_16x16x32_bf16 %7, v[152:155], v[164:167], %7\n"
      "ds_read_b64_tr_b16 v[88:89], %8 offset:3104\n"
      "v_mfma_f32_16x16x32_bf16 %6, v[152:155], v[160:163], %6\n"
      "ds_read_b64_tr_b16 v[90:91], %8 offset:3072\n"
      "v_mfma_f32_16x16x32_bf16 %3, v[136:139], v[164:167], %3\n"
      "ds_read_b64_tr_b16 v[92:93], %8 offset:3616\n"
      "v_mfma_f32_16x16x32_bf16 %1, v[132:135], v[160:163], %1\n"
      "ds_read_b64_tr_b16 v[94:95], %8 offset:3584\n"
      "v_mfma_f32_16x16x32_bf16 %7, v[156:159], v[160:163], %7\n"
      "v_mfma_f32_16x16x32_bf16 %5, v[148:151], v[160:163], %5\n"
      "v_mfma_f32_16x16x32_bf16 %4, v[144:147], v[160:163], %4\n"
      "v_mfma_f32_16x16x32_bf16 %3, v[140:143], v[160:163], %3\n"
      "s_waitcnt lgkmcnt(0)\n"
      "s_sub_u32 %10, %10, 1\n"
      "s_cmp_lg_u32 %10, 0\n"
      "s_cbranch_scc1 Lconv%=\n"
      "s_nop 7\n"
      : "+v"(acc[0]), "+v"(acc[1]), "+v"(acc[2]), "+v"(acc[3]), "+v"(acc[4]), "+v"(acc[5]), "+v"(acc[6]), "+v"(acc[7]), "+v"(ka), "+v"(hb), "=&s"(cnt)
      :: "memory", "scc", "v64", "v65", "v66", "v67", "v68", "v69", "v70", "v71", "v72", "v73", "v74", "v75", "v76", "v77", "v78", "v79", "v80", "v81", "v82", "v83", "v84", "v85", "v86", "v87", "v88", "v89", "v90", "v91", "v92", "v93", "v94", "v95", "v96", "v97", "v98", "v99", "v100", "v101", "v102", "v103", "v104", "v105", "v106", "v107", "v108", "v109", "v110", "v111", "v112", "v113", "v114", "v115", "v116", "v117", "v118", "v119", "v120", "v121", "v122", "v123", "v124", "v125", "v126", "v127", "v128", "v129", "v130", "v131", "v132", "v133", "v134", "v135", "v136", "v137", "v138", "v139", "v140", "v141", "v142", "v143", "v144", "v145", "v146", "v147", "v148", "v149", "v150", "v151", "v152", "v153", "v154", "v155", "v156", "v157", "v158", "v159", "v160", "v161", "v162", "v163", "v164", "v165", "v166", "v167", "v168", "v169", "v170", "v171", "v172", "v173", "v174", "v175", "v176", "v177", "v178", "v179", "v180", "v181", "v182", "v183", "v184", "v185", "v186", "v187", "v188", "v189", "v190", "v191");
  }

  f4* ex = (f4*)(sm + CV_EX);
  if (half) {
#pragma unroll
    for (int i = 0; i < 4; ++i) { ex[(w * 4 + i) * 64 + lane] = acc[i]; acc[i] = acc[4 + i]; }
  } else {
#pragma unroll
    for (int i = 0; i < 4; ++i) ex[(w * 4 + i) * 64 + lane] = acc[4 + i];
  }
  lds_arrive(fl + FL_EXCH + w);
  u2 xr[4];
#pragma unroll
  for (int i = 0; i < 4; ++i) xr[i] = *(const u2*)(sm + CV_X + 4096 * ch + 2 * (256 * (4 * half + i) + 16 * c16 + 4 * g));
  lds_wait(fl + FL_EXCH + (w ^ 1), 1u);
  u16* x1t = (u16*)(ws + WS_X1T);
  const float gc = ga * c2;
#pragma unroll
  for (int i = 0; i < 4; ++i) {
    const f4 y = (acc[i] + ex[((w ^ 1) * 4 + i) * 64 + lane]) * gc + (f4){blo(xr[i].x), bhi(xr[i].x), blo(xr[i].y), bhi(xr[i].y)} * c2;
    *(u2*)(x1t + d * L + 256 * (4 * half + i) + 16 * c16 + 4 * g) = (u2){pk(y[0], y[1]), pk(y[2], y[3])};
  }
}

__global__ __launch_bounds__(512, 2) void k1_conv(const u16* x, const u16* lar, const u16* aim, const u16* cre, const u16* cim, const u16* ldt, const u16* dsk,
                                                  const u16* c, const u16* wm, const u16* wg, const u16* wu, const u16* wd, char* ws) {
  extern __shared__ __attribute__((aligned(16))) char sm[];
  const int b = blockIdx.x;
  Stamps ts_ = {}; STAMP(0,0);
  if (__builtin_amdgcn_readfirstlane(threadIdx.x) < 256) k1_main(ts_, sm, b, x, c, wm, ws);
  else k1_help(ts_, sm, b, lar, aim, cre, cim, ldt, dsk, wg, wu, wd, ws);
  STAMP(0,6);
  STAMPV(0,7);
}

constexpr int G1_LEAD = 3, G1_NS = 5, G1_SLOT = 20480;
constexpr int G2_RES = 19, G2_FL = 4 * G2_RES * 2048, G1_SV = G2_FL + 64, K3_LDS = G1_SV + 512;
constexpr int G2_LEAD = 6;
constexpr int HH_T = NU * 1024;

template <int K> DI u32 g1_pair(const f16v& acc, float rs, float sd) {
  const f2 g = (f2){acc[2 * K], acc[2 * K + 1]} * rs;
  const f2 z = g * (g * g * -0.10294324f + -2.30220819f);
  const f2 dn = (f2){__builtin_amdgcn_exp2f(z.x), __builtin_amdgcn_exp2f(z.y)} * sd + sd;
  const f2 r = g * (f2){acc[8 + 2 * K], acc[9 + 2 * K]} * (f2){__builtin_amdgcn_rcpf(dn.x), __builtin_amdgcn_rcpf(dn.y)};
  return pk(r.x, r.y);
}
DI u4 g1_tile(const f16v& acc, float rs, float sd) { return (u4){g1_pair<0>(acc, rs, sd), g1_pair<1>(acc, rs, sd), g1_pair<2>(acc, rs, sd), g1_pair<3>(acc, rs, sd)}; }

DI void g1_stats(char* sm, int rt, char* ws) {
  const int t = threadIdx.x;
  float* red = (float*)sm;
  if (t < 512) {
    const u16* xp = (const u16*)(ws + WS_X1T) + (t >> 4) * L + 128 * rt + 8 * (t & 15);
    u4 v[16];
    float iv[16];
#pragma unroll
    for (int i = 0; i < 16; ++i) { v[i] = *(const u4*)(xp + 32 * i * L); iv[i] = ((const float*)(ws + WS_INV))[(t >> 4) + 32 * i]; }
    f2 s1[4], s2[4];
#pragma unroll
    for (int j = 0; j < 4; ++j) s1[j] = s2[j] = (f2){0.f, 0.f};
#pragma unroll
    for (int i = 0; i < 16; ++i)
#pragma unroll
      for (int j = 0; j < 4; ++j) { const f2 x = (f2){blo(v[i][j]), bhi(v[i][j])} * iv[i]; s1[j] += x; s2[j] += x * x; }
#pragma unroll
    for (int j = 0; j < 4; ++j) *(f4*)(red + (t >> 4) * 260 + 16 * (t & 15) + 4 * j) = (f4){s1[j].x, s2[j].x, s1[j].y, s2[j].y};
  }
  __syncthreads();
  if (t < 512) {
    f2 p = {0.f, 0.f};
#pragma unroll
    for (int i = 0; i < 8; ++i) p += *(const f2*)(red + (8 * (t & 3) + i) * 260 + 2 * (t >> 2));
    p.x += dppf<0xb1>(p.x); p.y += dppf<0xb1>(p.y); p.x += dppf<0x4e>(p.x); p.y += dppf<0x4e>(p.y);
    const f2 ms = ln_stats(p.x, p.y);
    if ((t & 3) == 0)
      __hip_atomic_store((u32*)(ws + WS_ST2) + 128 * rt + (t >> 2), (__float_as_uint(ms.x) >> 16) | __float_as_uint(ms.y), __ATOMIC_RELAXED, __HIP_MEMORY_SCOPE_AGENT);
  }
  __syncthreads();
}

__global__ __launch_bounds__(768, 3) void k3_mlp(const u16* wg, const u16* wu, const u16* wd, char* ws, u16* out) {
  extern __shared__ __attribute__((aligned(16))) char sm[];
  const int b = blockIdx.x, j = b >> 3;
  Stamps ts_ = {}; STAMP(2,0);
  const int t = threadIdx.x, w12 = __builtin_amdgcn_readfirstlane(t >> 6), w = w12 & 3, lane = t & 63, hh = lane >> 5;
  const u32 lb0 = ldsaddr(sm);
  const int q = (lane & 15) >> 2, p = lane & 3, g = lane >> 4;
  const int lt = 4 * (b & 7) + (j & 3), dt = j >> 2;
  auto kfirst = [&](int kq) { const u32 k = 22 * kq - (kq == 3) + 3 * dt; return min(k, k - NU); };
  auto knext = [](u32 k) { return min(k + 1, k + 1 - NU); };
  const rsrc_t rd = mkrsrc(wd, DF * D * 2);
  const u32 vd = (lane >> 3) * 1024 + 128 * dt + 16 * ((lane & 7) ^ (4 * ((lane >> 4) & 1)));
  volatile LAS u32* fl = (volatile LAS u32*)(size_t)(lb0 + G2_FL);
  auto fill2 = [&](int i0, int n, int slot0) {
    u32 kd = kfirst(w) + i0;
    kd = min(kd, kd - NU);
#pragma unroll 1
    for (int i = 0; i < n; ++i) {
      glds16<0>(lb0 + (G2_RES * w + slot0 + i) * 2048, vd, rd, kd * 16384);
      glds16<0>(lb0 + (G2_RES * w + slot0 + i) * 2048 + 1024, vd, rd, kd * 16384 + 8192);
      kd = knext(kd);
    }
  };
  {
    const int rt = 4 * ((b & 7) >> 1) + (j & 3), cg = (j >> 2) + 8 * (b & 1) - 1;
    if (t == 0) fl[0] = 0u;
    if (cg < 0) {
      g1_stats(sm, rt, ws);
    } else if (w12 >= 8) {
      const rsrc_t rx = mkrsrc(ws + WS_X1T, D * L * 2), rg = mkrsrc(wg, D * DF * 2), ru = mkrsrc(wu, D * DF * 2);
      const u32 vx = (4 * w + g) * 4096 + 256 * rt + 16 * ((lane & 15) ^ ((g << 2) | w));
      u32 vw[3];
#pragma unroll
      for (int i = 0; i < 3; ++i) {
        const int pc = (3 * (w & 1) + i) * 64 + lane, row = pc / 12, chk = pc - 12 * row;
        vw[i] = row * (DF * 2) + min(192 * cg + 16 * (chk ^ (w & 2)), DF * 2 - 16);
      }
      const rsrc_t rwm = w < 2 ? rg : ru;
      auto stage = [&](int i, int slot) {
        const u32 m = lb0 + slot * G1_SLOT;
        glds16<0>(m + 1024 * w, vx, rx, i * 131072);
        glds16<0>(m + 4096 + 1024 * w, vx, rx, i * 131072 + 65536);
#pragma unroll
        for (int k = 0; k < 3; ++k) glds16<0>(m + 8192 + 3072 * w + 1024 * k, vw[k], rwm, i * (32 * DF * 2));
      };
      asm volatile("s_nop 4");
      static_assert(G1_LEAD + 2 <= G1_NS, "ring too short");
#pragma unroll 1
      for (int i = 0; i < G1_LEAD - 1; ++i) stage(i, i);
      int slot = G1_LEAD - 1;
#pragma unroll 1
      for (int i = 0; i < 17 - G1_LEAD; ++i) {
        stage(i + G1_LEAD - 1, slot);
        slot = slot == G1_NS - 1 ? 0 : slot + 1;
        vmwait<5 * (G1_LEAD - 1)>();
        rawbar();
      }
      const u32* st2 = (const u32*)(ws + WS_ST2) + 128 * rt + 32 * w + (lane & 31);
      u32 v;
      asm volatile("global_load_dword %0, %1, off sc1" : "=v"(v) : "v"(st2));
      sfor<17 - G1_LEAD, 16>([&](auto i) { vmwait<5 * (15 - i) + 1>(); rawbar(); });
      asm volatile("s_waitcnt vmcnt(0)" : "+v"(v));
#pragma unroll 1
      for (int spin = 0; spin < (1 << 16) && !__all(v != 0u); ++spin) {
        __builtin_amdgcn_s_sleep(2);
        v = __hip_atomic_load(st2, __ATOMIC_RELAXED, __HIP_MEMORY_SCOPE_AGENT);
      }
      ((u32*)(sm + G1_SV))[32 * w + (lane & 31)] = v;
      bar();
      bar();
    } else if (w12 >= 4) {
      const char* pf = (const char*)wd + j * (43 * 1024) + w * (43 * 256);
#pragma unroll 1
      for (int i = 0; i < 18; ++i) {
        rawbar();
      }
    } else {
      const int wl = w & 1, wu3 = 3 * (w >> 1), u0 = 6 * cg + wu3;
      u32 xa[2][2], wa[3];
#pragma unroll
      for (int c = 0; c < 2; ++c)
#pragma unroll
        for (int tt = 0; tt < 2; ++tt)
          xa[c][tt] = lb0 + 256 * (8 * hh + 4 * tt + q) + 8 * (p & 1) + 16 * ((4 * (2 * wl + c) + 2 * (g & 1) + (p >> 1)) ^ ((q << 2) | (2 * hh + tt)));
#pragma unroll
      for (int v = 0; v < 3; ++v) wa[v] = lb0 + 8192 + 6144 * (g & 1) + 192 * (8 * hh + q) + 16 * ((2 * (wu3 + v) + (p >> 1)) ^ (2 * (g & 1))) + 8 * (p & 1);
      u4 ex[3];
#pragma unroll
      for (int v = 0; v < 3; ++v) ex[v] = *(const u4*)(ws + WS_EX + min(u0 + v, NU - 1) * 1024 + lane * 16);
      f16v acc[6];
#pragma unroll
      for (int i = 0; i < 6; ++i)
#pragma unroll
        for (int e = 0; e < 16; ++e) acc[i][e] = 0.f;
#pragma unroll
      for (int i = 0; i < 6; ++i) asm volatile("" : "+v"(acc[i]));
      rawbar();

      {
        u32 c0, c1, c2;
        asm volatile(
          "ds_read_b64_tr_b16 v[112:113], %6 offset:0\n"
          "ds_read_b64_tr_b16 v[114:115], %7 offset:0\n"
          "ds_read_b64_tr_b16 v[120:121], %10 offset:0\n"
          "ds_read_b64_tr_b16 v[122:123], %10 offset:768\n"
          "ds_read_b64_tr_b16 v[116:117], %8 offset:0\n"
          "ds_read_b64_tr_b16 v[118:119], %9 offset:0\n"
          "ds_read_b64_tr_b16 v[124:125], %11 offset:0\n"
          "ds_read_b64_tr_b16 v[126:127], %11 offset:768\n"
          "ds_read_b64_tr_b16 v[128:129], %12 offset:0\n"
          "ds_read_b64_tr_b16 v[130:131], %12 offset:768\n"
          "s_mov_b32 %13, 16\n"
          "s_mov_b32 %14, 0\n"
          "Lg1%=:\n"
          "s_add_u32 %14, %14, 1\n"
          "s_cmp_eq_u32 %14, 5\n"
          "s_cselect_b32 %15, -81920, %16\n"
          "s_cselect_b32 %14, 0, %14\n"
          "s_waitcnt lgkmcnt(6)\n"
          "v_mfma_f32_32x32x16_bf16 %0, v[120:123], v[112:115], %0\n"
          "ds_read_b64_tr_b16 v[132:133], %6 offset:4096\n"
          "ds_read_b64_tr_b16 v[134:135], %7 offset:4096\n"
          "s_waitcnt lgkmcnt(6)\n"
          "v_mfma_f32_32x32x16_bf16 %1, v[120:123], v[116:119], %1\n"
          "ds_read_b64_tr_b16 v[140:141], %10 offset:3072\n"
          "ds_read_b64_tr_b16 v[142:143], %10 offset:3840\n"
          "s_waitcnt lgkmcnt(6)\n"
          "v_mfma_f32_32x32x16_bf16 %2, v[124:127], v[112:115], %2\n"
          "ds_read_b64_tr_b16 v[136:137], %8 offset:4096\n"
          "ds_read_b64_tr_b16 v[138:139], %9 offset:4096\n"
          "v_mfma_f32_32x32x16_bf16 %3, v[124:127], v[116:119], %3\n"
          "ds_read_b64_tr_b16 v[144:145], %11 offset:3072\n"
          "ds_read_b64_tr_b16 v[146:147], %11 offset:3840\n"
          "s_waitcnt lgkmcnt(8)\n"
          "v_mfma_f32_32x32x16_bf16 %4, v[128:131], v[112:115], %4\n"
          "ds_read_b64_tr_b16 v[148:149], %12 offset:3072\n"
          "ds_read_b64_tr_b16 v[150:151], %12 offset:3840\n"
          "v_add_u32 %6, %15, %6\n"
          "v_add_u32 %7, %15, %7\n"
          "v_add_u32 %8, %15, %8\n"
          "v_add_u32 %9, %15, %9\n"
          "v_mfma_f32_32x32x16_bf16 %5, v[128:131], v[116:119], %5\n"
          "v_add_u32 %10, %15, %10\n"
          "v_add_u32 %11, %15, %11\n"
          "v_add_u32 %12, %15, %12\n"
          "s_barrier\n"
          "s_waitcnt lgkmcnt(6)\n"
          "v_mfma_f32_32x32x16_bf16 %0, v[140:143], v[132:135], %0\n"
          "ds_read_b64_tr_b16 v[112:113], %6 offset:0\n"
          "ds_read_b64_tr_b16 v[114:115], %7 offset:0\n"
          "s_waitcnt lgkmcnt(6)\n"
          "v_mfma_f32_32x32x16_bf16 %1, v[140:143], v[136:139], %1\n"
          "ds_read_b64_tr_b16 v[120:121], %10 offset:0\n"
          "ds_read_b64_tr_b16 v[122:123], %10 offset:768\n"
          "s_waitcnt lgkmcnt(6)\n"
          "v_mfma_f32_32x32x16_bf16 %2, v[144:147], v[132:135], %2\n"
          "ds_read_b64_tr_b16 v[116:117], %8 offset:0\n"
          "ds_read_b64_tr_b16 v[118:119], %9 offset:0\n"
          "v_mfma_f32_32x32x16_bf16 %3, v[144:147], v[136:139], %3\n"
          "ds_read_b64_tr_b16 v[124:125], %11 offset:0\n"
          "ds_read_b64_tr_b16 v[126:127], %11 offset:768\n"
          "s_waitcnt lgkmcnt(8)\n"
          "v_mfma_f32_32x32x16_bf16 %4, v[148:151], v[132:135], %4\n"
          "ds_read_b64_tr_b16 v[128:129], %12 offset:0\n"
          "ds_read_b64_tr_b16 v[130:131], %12 offset:768\n"
          "s_sub_u32 %13, %13, 1\n"
          "s_cmp_lg_u32 %13, 0\n"
          "v_mfma_f32_32x32x16_bf16 %5, v[148:151], v[136:139], %5\n"
          "s_cbranch_scc1 Lg1%=\n"
          "s_waitcnt lgkmcnt(0)\n"
          : "+v"(acc[0]), "+v"(acc[1]), "+v"(acc[2]), "+v"(acc[3]), "+v"(acc[4]), "+v"(acc[5]), "+v"(xa[0][0]), "+v"(xa[0][1]), "+v"(xa[1][0]), "+v"(xa[1][1]),
            "+v"(wa[0]), "+v"(wa[1]), "+v"(wa[2]), "=&s"(c0), "=&s"(c1), "=&s"(c2)
          : "s"(G1_SLOT) : "memory", "scc", "v112", "v113", "v114", "v115", "v116", "v117", "v118", "v119", "v120", "v121", "v122", "v123", "v124", "v125", "v126", "v127", "v128", "v129", "v130", "v131", "v132", "v133", "v134", "v135", "v136", "v137", "v138", "v139", "v140", "v141", "v142", "v143", "v144", "v145", "v146", "v147", "v148", "v149", "v150", "v151");
      }

      u32 sv[2];
#pragma unroll
      for (int c = 0; c < 2; ++c) sv[c] = ((const u32*)(sm + G1_SV))[64 * wl + 32 * c + (lane & 31)];
      bar();
#pragma unroll
      for (int c = 0; c < 2; ++c) {
        const u32 bv = hh ? __builtin_amdgcn_perm(sv[c], sv[c], 0x03020302u) : (__builtin_amdgcn_perm(sv[c], sv[c], 0x01000100u) ^ 0x80008000u);
#pragma unroll
        for (int v = 0; v < 3; ++v) acc[2 * v + c] = mfma32(ex[v], (u4){bv, bv, bv, bv}, acc[2 * v + c]);
      }

      char* hp = ws + WS_HH + (4 * rt + 2 * wl) * HH_T + u0 * 1024 + 16 * lane;
#pragma unroll
      for (int v = 0; v < 3; ++v) {
        if (u0 + v < NU) {
#pragma unroll
          for (int c = 0; c < 2; ++c) {
            const float sd = bhi(sv[c]);
            const u4 o = g1_tile(acc[2 * v + c], __builtin_amdgcn_rcpf(sd), sd);
            char* dst = hp + c * HH_T + v * 1024;
            asm volatile("global_store_dwordx4 %0, %1, off sc1\n\ts_nop 1" :: "v"(dst), "v"(o) : "memory");
          }
        }
      }
      STAMP(2,4);
      {
        asm volatile("s_waitcnt vmcnt(0)" ::: "memory");
        if (lane == 0 && __hip_atomic_fetch_add((LAS u32*)fl, 1u, __ATOMIC_RELAXED, __HIP_MEMORY_SCOPE_WORKGROUP) == 3u)
          __hip_atomic_fetch_add((u32*)(ws + WS_CNT) + 64 * rt, 1u, __ATOMIC_RELAXED, __HIP_MEMORY_SCOPE_AGENT);
      }
    }
  }
  if (w12 >= 8) {
    asm volatile("s_nop 4");
    fill2(G2_RES, 3, 0);
    fill2(3, G2_RES - 3, 3);
    vmwait<2 * (G2_RES - 3)>();
    rawbar();
    rawbar();
    fill2(0, 3, 0);
    vmwait<0>();
    rawbar();
    rawbar();
    rawbar();
  } else {
    const int wl = w12 & 1, kq = w12 >> 1, dc = 64 * dt + 16 * kq + 4 * hh;
    f4 gb[2], xr[2];
#pragma unroll
    for (int e = 0; e < 2; ++e) {
      gb[e] = *(const f4*)((const float*)(ws + WS_GB) + dc + 8 * e);
      xr[e] = *(const f4*)((const float*)(ws + WS_INV) + dc + 8 * e);
#pragma unroll
      for (int i = 0; i < 4; ++i) xr[e][i] *= b2f(((const u16*)(ws + WS_X1T))[(dc + 8 * e + i) * L + 64 * lt + 32 * wl + (lane & 31)]);
    }
    u32 pa[2];
#pragma unroll
    for (int e = 0; e < 2; ++e)
      pa[e] = lb0 + G2_RES * 2048 * kq + 128 * (4 * hh + q) + 16 * ((((4 * e + 2 * (g & 1)) ^ (2 * kq)) + (p >> 1)) ^ (4 * (q >> 1))) + 8 * (p & 1);
    f16v acc[2];
#pragma unroll
    for (int e = 0; e < 16; ++e) acc[0][e] = acc[1][e] = 0.f;
    asm volatile("" : "+v"(xr[0]), "+v"(xr[1]), "+v"(gb[0]), "+v"(gb[1]), "+v"(acc[0]), "+v"(acc[1]));
    rawbar();
    u4 A[2][2], AR[3][2];
#pragma unroll
    for (int i = 0; i < 6; ++i) AR[i >> 1][i & 1] = cat(trd(pa[i & 1] + (i >> 1) * 2048), trd(pa[i & 1] + (i >> 1) * 2048 + 1024));
    asm volatile("s_waitcnt lgkmcnt(0)\n\ts_barrier" : "+v"(AR[0][0]), "+v"(AR[0][1]), "+v"(AR[1][0]), "+v"(AR[1][1]), "+v"(AR[2][0]), "+v"(AR[2][1]) :: "memory");
    if (w12 == 0) {
      const u32* cnt = (const u32*)(ws + WS_CNT) + 64 * (lt >> 1);
#pragma unroll 1
      for (int spin = 0; spin < (1 << 18); ++spin)
        if (__hip_atomic_load(cnt, __ATOMIC_RELAXED, __HIP_MEMORY_SCOPE_AGENT) >= 15u) break;
    }
    rawbar();
    STAMP(2,5);
    const rsrc_t rh = mkrsrc(ws + WS_HH + (2 * lt + wl) * HH_T, HH_T);
    constexpr int LEAD = G2_LEAD, NH = LEAD + 2;
    u4 H[NH];
    u32 kh = kfirst(kq);
    auto ldh = [&](auto i) { if constexpr (i < 22) { bld16_sc1<0>(H[i % NH], lane * 16, rh, kh * 1024); kh = knext(kh); } };
    sfor<0, LEAD>(ldh);
    auto rda = [&](auto i) {
#pragma unroll
      for (int e = 0; e < 2; ++e) A[i & 1][e] = cat(trd(pa[e] + i * 2048), trd(pa[e] + i * 2048 + 1024));
    };
    rda(std::integral_constant<int, 0>{});
    sfor<0, 22>([&](auto i) {
      vmwait<(i < 22 - LEAD ? LEAD - 1 : 21 - i)>(H[i % NH]);
      SB();
      if constexpr (i + 1 < G2_RES) rda(std::integral_constant<int, i + 1>{});
      if (i < 21 || kq < 2) {
#pragma unroll
        for (int e = 0; e < 2; ++e) acc[e] = mfma32(i < G2_RES ? A[i & 1][e] : AR[i < G2_RES ? 0 : i - G2_RES][e], H[i % NH], acc[e]);
      }
      if constexpr (i + 1 < G2_RES) { SGB(0x8, 1); SGB(0x100, 2); SGB(0x8, 1); SGB(0x100, 2); }
      SB();
      ldh(std::integral_constant<int, i + LEAD>{});
      if constexpr (i == G2_RES - 1) rawbar();
    });

    f4* red = (f4*)sm + wl * 1536 + lane;
#pragma unroll
    for (int k = 1; k < 4; ++k)
#pragma unroll
      for (int e = 0; e < 2; ++e) {
        const f16v& a = acc[k >> 1];
        const int r = 8 * (k & 1) + 4 * e;
        red[(kq ^ k) * 384 + (k - 1) * 128 + 64 * e] = (f4){a[r], a[r + 1], a[r + 2], a[r + 3]};
      }
    bar();
    u2 o[2];
#pragma unroll
    for (int e = 0; e < 2; ++e) {
      const f4* rp = red + kq * 384 + 64 * e;
      const f4 y = ((f4){acc[0][4 * e], acc[0][4 * e + 1], acc[0][4 * e + 2], acc[0][4 * e + 3]} + rp[0] + rp[128] + rp[256]) * gb[e] + xr[e];
      o[e] = (u2){pk(y[0], y[1]), pk(y[2], y[3])};
    }
    const auto r0 = __builtin_amdgcn_permlane32_swap(o[0].x, o[1].x, false, false);
    const auto r1 = __builtin_amdgcn_permlane32_swap(o[0].y, o[1].y, false, false);
    *(u4*)(out + (64 * lt + 32 * wl + (lane & 31)) * D + 64 * dt + 16 * kq + 8 * hh) = (u4){r0[0], r1[0], r0[1], r1[1]};
  }
  STAMPV(2,7);
}


__global__ void k_amp() {
  __shared__ unsigned long long r[2][256];
  const int t = threadIdx.x;
  const int kk[2] = {0, 0}, ii[2] = {0, 0}, mm[2] = {0, 0};
  for (int z = 0; z < 2; ++z) {
    unsigned long long v = mm[z] ? 0ull : ~0ull;
    for (int i = t; i < 512 * 8; i += 256) { unsigned long long x = g_stamps[(kk[z] * 512 * 8 + i) * 8 + ii[z]]; if (x) v = mm[z] ? (x > v ? x : v) : (x < v ? x : v); }
    r[z][t] = v;
  }
  __syncthreads();
  if (t == 0) {
    unsigned long long a = r[0][0], b = r[1][0];
    for (int i = 1; i < 256; ++i) { a = mm[0] ? (r[0][i] > a ? r[0][i] : a) : (r[0][i] < a ? r[0][i] : a); b = mm[1] ? (r[1][i] > b ? r[1][i] : b) : (r[1][i] < b ? r[1][i] : b); }
    unsigned long long d = b > a ? b - a : 0; if (d > 5000) d = 5000;
    const unsigned long long t0 = __builtin_amdgcn_s_memrealtime();
    while (__builtin_amdgcn_s_memrealtime() - t0 < 10 * d) {}
  }
}
static const int g_mask = 15;
extern "C" void kernel_launch(void* const* d_in, const int* in_sizes, int n_in, void* d_out, int out_size, void* d_ws, size_t ws_size,
                              hipStream_t stream) {
  const u16 *wg = (const u16*)d_in[8], *wu = (const u16*)d_in[9], *wd = (const u16*)d_in[10];
  char* ws = (char*)d_ws;
  (void)hipFuncSetAttribute((const void*)k1_conv, hipFuncAttributeMaxDynamicSharedMemorySize, K1_LDS);
  (void)hipFuncSetAttribute((const void*)k3_mlp, hipFuncAttributeMaxDynamicSharedMemorySize, K3_LDS);
  if (g_mask & (1 << 0)) k1_conv<<<256, 512, K1_LDS, stream>>>((const u16*)d_in[0], (const u16*)d_in[1], (const u16*)d_in[2], (const u16*)d_in[3], (const u16*)d_in[4], (const u16*)d_in[5],
                                        (const u16*)d_in[6], (const u16*)d_in[11], (const u16*)d_in[12], wg, wu, wd, ws);
  if (g_mask & (1 << 1)) k3_mlp<<<256, 768, K3_LDS, stream>>>(wg, wu, wd, ws, (u16*)d_out);
  k_amp<<<1, 256, 0, stream>>>();
}
```

```cpp
#include <hip/hip_runtime.h>
#include <hip/hip_bf16.h>
#include <stdint.h>
#include <type_traits>

typedef unsigned short u16;
typedef unsigned u32;
typedef float f2 __attribute__((ext_vector_type(2)));
typedef float f4 __attribute__((ext_vector_type(4)));
typedef float f16v __attribute__((ext_vector_type(16)));
typedef u32 u2 __attribute__((ext_vector_type(2)));
typedef u32 u4 __attribute__((ext_vector_type(4)));
typedef u4 u4a8 __attribute__((aligned(8)));
typedef short s4 __attribute__((ext_vector_type(4)));
typedef __bf16 bf2 __attribute__((ext_vector_type(2)));
typedef __bf16 bf8 __attribute__((ext_vector_type(8)));
typedef _Float16 h2 __attribute__((ext_vector_type(2)));
typedef _Float16 h8 __attribute__((ext_vector_type(8)));
#define LAS __attribute__((address_space(3)))
#define DI __device__ __forceinline__

constexpr int L = 2048, D = 512, NM = 128, DF = 1376, NU = 86;

constexpr size_t WS_GB = 0, WS_INV = 2 << 10, WS_EPOCH = 4 << 10, WS_ST2 = 8 << 10, WS_CNT = 16 << 10, WS_ST1 = 32 << 10, WS_EX = 64 << 10,
                 WS_FX = 20 << 10, WS_MG = 160 << 10, WS_X1T = 256 << 10, WS_HH = WS_X1T + (2 << 20), WS_XT = WS_HH + (5504 << 10);

template <class T> DI T ldnt(const T* p) { return *p; }
DI float blo(u32 v) { return __uint_as_float(v << 16); }
DI float bhi(u32 v) { return __uint_as_float(v & 0xffff0000u); }
DI float b2f(u16 v) { return __uint_as_float(((u32)v) << 16); }
DI u32 pk(float a, float b) { return __builtin_bit_cast(u32, __builtin_convertvector((f2){a, b}, bf2)); }
DI u32 pkh(f2 v) { return __builtin_bit_cast(u32, __builtin_convertvector(v, h2)); }
DI float rb(float a) { return blo(pk(a, a)); }
DI u2 trd(const char* p) { return __builtin_bit_cast(u2, __builtin_amdgcn_ds_read_tr16_b64_v4i16((LAS s4*)p)); }
DI u2 trd(u32 a) { return __builtin_bit_cast(u2, __builtin_amdgcn_ds_read_tr16_b64_v4i16((LAS s4*)(size_t)a)); }
DI u4 cat(u2 a, u2 b) { return (u4){a.x, a.y, b.x, b.y}; }
DI float dot2(u32 a, u32 b, float c) { return __builtin_amdgcn_fdot2_f32_bf16(__builtin_bit_cast(bf2, a), __builtin_bit_cast(bf2, b), c, false); }
DI f4 mfma16(u4 a, u4 b, f4 c) { return __builtin_amdgcn_mfma_f32_16x16x32_bf16(__builtin_bit_cast(bf8, a), __builtin_bit_cast(bf8, b), c, 0, 0, 0); }
DI f4 mfma16h(u4 a, u4 b, f4 c) { return __builtin_amdgcn_mfma_f32_16x16x32_f16(__builtin_bit_cast(h8, a), __builtin_bit_cast(h8, b), c, 0, 0, 0); }
DI f16v mfma32(u4 a, u4 b, f16v c) { return __builtin_amdgcn_mfma_f32_32x32x16_bf16(__builtin_bit_cast(bf8, a), __builtin_bit_cast(bf8, b), c, 0, 0, 0); }
#define CM_MUL(d, a, b) "v_pk_mul_f32 " d ", " a ", " b " op_sel_hi:[0,1]\n\t"
#define CM_FMA(d, a, b) "v_pk_fma_f32 " d ", " a ", " b ", " d " op_sel:[1,1,0] op_sel_hi:[1,0,1] neg_lo:[0,1,0]\n\t"
DI void cmul2(f2& r0, f2 a0, f2 b0, f2& r1, f2 a1, f2 b1) {
  asm(CM_MUL("%0", "%2", "%3") CM_MUL("%1", "%4", "%5") CM_FMA("%0", "%2", "%3") CM_FMA("%1", "%4", "%5")
      : "=&v"(r0), "=&v"(r1) : "v"(a0), "v"(b0), "v"(a1), "v"(b1));
}
DI void cmul3(f2& r0, f2 a0, f2 b0, f2& r1, f2 a1, f2 b1, f2& r2, f2 a2, f2 b2) {
  asm(CM_MUL("%0", "%3", "%4") CM_MUL("%1", "%5", "%6") CM_MUL("%2", "%7", "%8") CM_FMA("%0", "%3", "%4") CM_FMA("%1", "%5", "%6") CM_FMA("%2", "%7", "%8")
      : "=&v"(r0), "=&v"(r1), "=&v"(r2) : "v"(a0), "v"(b0), "v"(a1), "v"(b1), "v"(a2), "v"(b2));
}
DI void cmul4(f2& r0, f2& r1, f2& r2, f2& r3, f2 a0, f2 a1, f2 a2, f2 a3, f2 b) {
  asm(CM_MUL("%0", "%4", "%8") CM_MUL("%1", "%5", "%8") CM_MUL("%2", "%6", "%8") CM_MUL("%3", "%7", "%8")
      CM_FMA("%0", "%4", "%8") CM_FMA("%1", "%5", "%8") CM_FMA("%2", "%6", "%8") CM_FMA("%3", "%7", "%8")
      : "=&v"(r0), "=&v"(r1), "=&v"(r2), "=&v"(r3) : "v"(a0), "v"(a1), "v"(a2), "v"(a3), "v"(b));
}
DI float exp_acc(float x) {
  const float t = x * 1.44269502f, r = fmaf(x, 1.44269502f, -t) + x * 1.92596299e-8f, e = __builtin_amdgcn_exp2f(t);
  return fmaf(e, r * 0.693147182f, e);
}
template <int CTRL> DI float dppf(float v) { return __int_as_float(__builtin_amdgcn_update_dpp(0, __float_as_int(v), CTRL, 0xf, 0xf, true)); }
DI float sum32(float v) {
  auto r = __builtin_amdgcn_permlane32_swap(__float_as_uint(v), __float_as_uint(v), false, false);
  return __uint_as_float(r[0]) + __uint_as_float(r[1]);
}
DI f2 ln_stats(float s1, float s2) {
  const float mean = s1 * (1.f / D);
  const float var = fmaxf(fmaf(-mean, mean, s2 * (1.f / D)), 0.f);
  return (f2){rb(mean), rb(__builtin_amdgcn_sqrtf(rb(rb(var) + 1e-5f)))};
}
typedef __amdgpu_buffer_rsrc_t rsrc_t;
DI rsrc_t mkrsrc(const void* p, u32 bytes) { return __builtin_amdgcn_make_buffer_rsrc((void*)p, 0, bytes, 0x00020000); }
DI u32 ldsaddr(const char* p) { return (u32)(size_t)(LAS const char*)p; }
template <int OFF> DI void glds16(u32 m0v, u32 voff, rsrc_t rs, u32 soff) {
  asm volatile("s_mov_b32 m0, %0\n\ts_nop 0\n\tbuffer_load_dwordx4 %1, %2, %3 offen offset:%4 lds" :: "s"(m0v), "v"(voff), "s"(rs), "s"(soff), "n"(OFF) : "memory");
}
DI void glds16x4(u32 m0v, u32 voff, rsrc_t rs, u32 soff) {
  asm volatile("s_mov_b32 m0, %0\n\ts_nop 0\n\tbuffer_load_dwordx4 %1, %2, %3 offen lds\n\tbuffer_load_dwordx4 %1, %2, %3 offen offset:1024 lds\n\t"
               "buffer_load_dwordx4 %1, %2, %3 offen offset:2048 lds\n\tbuffer_load_dwordx4 %1, %2, %3 offen offset:3072 lds"
               :: "s"(m0v), "v"(voff), "s"(rs), "s"(soff) : "memory");
}
template <int OFF> DI void bld16(u4& d, u32 voff, rsrc_t rs, u32 soff) {
  asm volatile("buffer_load_dwordx4 %0, %1, %2, %3 offen offset:%4" : "=v"(d) : "v"(voff), "s"(rs), "s"(soff), "n"(OFF));
}
template <int OFF> DI void bld16_sc1(u4& d, u32 voff, rsrc_t rs, u32 soff) {
  asm volatile("buffer_load_dwordx4 %0, %1, %2, %3 offen offset:%4 sc1" : "=v"(d) : "v"(voff), "s"(rs), "s"(soff), "n"(OFF));
}
template <int OFF, class T> DI void gld16(T& d, const void* p) { asm volatile("global_load_dwordx4 %0, %1, off offset:%2" : "=v"(d) : "v"(p), "n"(OFF)); }
DI void pin(u32& v) { asm("" : "+v"(v)); }
DI void rawbar() { asm volatile("s_barrier" ::: "memory"); }
template <int N> DI void vmwait() { asm volatile("s_waitcnt vmcnt(%0)" :: "n"(N) : "memory"); }
template <int N> DI void vmwait(u4& d) { asm volatile("s_waitcnt vmcnt(%1)" : "+v"(d) : "n"(N)); }
DI void bar() { asm volatile("s_waitcnt lgkmcnt(0)\n\ts_barrier" ::: "memory"); }
#define SB() __builtin_amdgcn_sched_barrier(0)
#define SGB(m, n) __builtin_amdgcn_sched_group_barrier(m, n, 0)
template <int I, int N, class F> DI void sfor(F&& f) { if constexpr (I < N) { f(std::integral_constant<int, I>{}); sfor<I + 1, N>(f); } }

typedef unsigned long long u64;
constexpr int KG_ROW = 544, KG_CH = 57 * KG_ROW, KG_T = 2 * KG_CH, KG_TS = 4160;
constexpr int CV_EX = 0, CV_K = KG_T + 2 * KG_TS, CV_KC = 4128, CV_H = CV_K + 8 * CV_KC, CV_HP = 1152, CV_X = CV_H + 8 * CV_HP, CV_ST = CV_X + 8192,
              CV_W = CV_ST + 8192, CV_TB = CV_W + 16384, CV_V = CV_TB + 9216, CV_M = CV_V + 1024, CV_FL = CV_M + 256, K1_LDS = CV_FL + 64;
enum { FL_EPOCH = 0, FL_EPOK = 2, FL_KG = 3, FL_MOD = 7, FL_MAIN = 8, FL_XT = 9, FL_XD = 10, FL_GO = 11, FL_EXCH = 12 };
typedef volatile LAS u32* ldsw;
DI void lds_arrive(ldsw w) {
  asm volatile("s_waitcnt lgkmcnt(0)" ::: "memory");
  if ((threadIdx.x & 63) == 0) __hip_atomic_fetch_add((LAS u32*)w, 1u, __ATOMIC_RELAXED, __HIP_MEMORY_SCOPE_WORKGROUP);
}
DI void lds_wait(ldsw w, u32 n) { while (*w < n) {} asm volatile("" ::: "memory"); }
DI u64 get_epoch(ldsw fl) { lds_wait(fl + FL_EPOK, 1u); return ((u64)fl[FL_EPOCH + 1] << 32) | fl[FL_EPOCH]; }
DI u4 ld16_sc1(const void* p) { u4 d; asm volatile("global_load_dwordx4 %0, %1, off sc1\n\ts_waitcnt vmcnt(0)" : "=v"(d) : "v"(p) : "memory"); return d; }
template <int OFF> DI void gld16_sc1(u4& d, const void* p) { asm volatile("global_load_dwordx4 %0, %1, off offset:%2 sc1" : "=v"(d) : "v"(p), "n"(OFF) : "memory"); }
DI bool mg_ok(u4 g, u64 ep) { const u32 h = (u32)(ep >> 8), l = (u32)ep << 24; return g.y == h && g.w == h && (g.x & 0xff000000u) == l && (g.z & 0xff000000u) == l; }
DI float mg_sum(u4 g) { return __uint_as_float(g.x << 8) + __uint_as_float(g.z << 8); }
DI float scale_c(float v) { v = rb(1.f + rb(v)); return v == 0.f ? 0x1p-60f : v; }

DI f2 cexp_pow2(float a, float fh, float fl, float p) {
  const float e = __builtin_amdgcn_exp2f(a * p);
  const float r = fmaf(fl, p, __builtin_amdgcn_fractf(fh * p));
  return (f2){e * __builtin_amdgcn_cosf(r), e * __builtin_amdgcn_sinf(r)};
}

DI void k1_help(char* sm, int b, const u16* lar, const u16* aim, const u16* cre, const u16* cim, const u16* ldt, const u16* dsk,
                const u16* wg, const u16* wu, const u16* wd, char* ws) {
  const int t = threadIdx.x - 256, w = __builtin_amdgcn_readfirstlane(t >> 6), lane = t & 63, ch = w >> 1, half = w & 1;
  const int d0 = 64 * (b & 7) + 2 * (b >> 3), d = d0 + ch, n = 64 * half + lane;
  const ldsw fl = (ldsw)(size_t)(ldsaddr(sm) + CV_FL);
  const u32 v_lar = ldnt(lar + d * NM + n), v_aim = ldnt(aim + d * NM + n);
  const u32 cr = ldnt(cre + d * NM + n), ci = ldnt(cim + d * NM + n), cr2 = ldnt(cre + d * NM + (n ^ 64)), ci2 = ldnt(cim + d * NM + (n ^ 64));
  const u16 v_ldt = ldt[d], v_dsk = dsk[d];
  rawbar();
  const float dt = rb(exp_acc(b2f(v_ldt)));
  const float ds = b2f(v_dsk);
  const float zr = -rb(exp_acc(blo(v_lar))) * dt, zi = blo(v_aim) * dt;
  const float a = zr * 1.44269502f;
  const float fh = zi * 0.159154937f, fq = fmaf(zi, 0.159154937f, -fh) + zi * 6.42063833e-9f;
  const f2 e1 = cexp_pow2(a, fh, fq, 1.f), e8 = cexp_pow2(a, -fh, -fq, 8.f), e128 = cexp_pow2(a, fh, fq, 128.f), e1024 = cexp_pow2(a, fh, fq, 1024.f);
  int mx = (int)max(max(cr & 0x7fffu, ci & 0x7fffu), max(cr2 & 0x7fffu, ci2 & 0x7fffu));
  mx = max(mx, __builtin_amdgcn_update_dpp(0, mx, 0x111, 0xf, 0xf, true));
  mx = max(mx, __builtin_amdgcn_update_dpp(0, mx, 0x112, 0xf, 0xf, true));
  mx = max(mx, __builtin_amdgcn_update_dpp(0, mx, 0x114, 0xf, 0xf, true));
  mx = max(mx, __builtin_amdgcn_update_dpp(0, mx, 0x118, 0xf, 0xf, true));
  mx = max(max(__builtin_amdgcn_readlane(mx, 15), __builtin_amdgcn_readlane(mx, 31)), max(__builtin_amdgcn_readlane(mx, 47), __builtin_amdgcn_readlane(mx, 63)));
  const int ex = min(max(mx >> 7, 1), 253);
  const float cs = __int_as_float((254 - ex) << 23), kf = 2.f * dt * __int_as_float(ex << 23);
  float dsv = ds, kfv = kf, fhv = fh, av = a, crv = blo(cr) * cs, civ = -blo(ci) * cs;
  asm volatile("" : "+v"(dsv), "+v"(kfv), "+v"(fhv), "+v"(av), "+v"(crv), "+v"(civ));
  const int task = b + 256 * w, kq = task & 3, blk = task >> 2, mat = blk >= 43, jb = blk - 43 * mat;
  const bool work = task < 344;
  if (work) {
    const rsrc_t rw = mkrsrc(mat ? wu : wg, D * DF * 2);
    const u32 vo = (128 * kq + (lane >> 2)) * (DF * 2) + 64 * jb + 16 * (lane & 3);
    asm volatile("s_nop 4");
#pragma unroll
    for (int i = 0; i < 8; ++i) glds16<0>(ldsaddr(sm) + CV_W + 8192 * w + 1024 * i, vo, rw, i * 16 * DF * 2);
  }
  u32 early = 0;
  if (w == 3) asm volatile("global_load_dword %0, %1, off" : "=v"(early) : "v"((const char*)wd + b * 5504 + min(lane, 42) * 128));
  char* base = sm + ch * KG_CH + n * 4;
  auto put = [&](int row, f2 v) { *(u32*)(base + row * KG_ROW) = pkh(v); };
  f2 lo[8], e[8], m[8], x0, x1, x2, x3, e16, e32, e64;
  lo[0] = (f2){crv, civ};
  e[1] = e1; m[1] = e128;
  put(0, lo[0]); put(16, (f2){1.f, 0.f}); put(17, e1); put(24, e128); put(48, e1024);
  cmul3(e16, e8, e8, e[2], e1, e1, m[2], e128, e128);
  cmul2(lo[1], lo[0], e8, x0, e1024, e1024);
  put(18, e[2]); put(32, m[2]); put(1, lo[1]); put(56, x0);
  cmul3(e32, e16, e16, e[4], e[2], e[2], m[4], m[2], m[2]);
  cmul2(e[3], e[2], e1, m[3], m[2], e128);
  cmul2(lo[2], lo[0], e16, lo[3], lo[1], e16);
  put(20, e[4]); put(40, m[4]); put(19, e[3]); put(36, m[3]); put(2, lo[2]); put(3, lo[3]);
  cmul4(e[5], e[6], e[7], x0, e1, e[2], e[3], m[4], e[4]);
  put(21, e[5]); put(22, e[6]); put(23, e[7]); put(41, x0);
  cmul3(m[5], m[4], e128, m[6], m[4], m[2], m[7], m[4], m[3]);
  put(42, m[5]); put(44, m[6]); put(46, m[7]);
  cmul4(lo[4], lo[5], lo[6], lo[7], lo[0], lo[1], lo[2], lo[3], e32);
  put(4, lo[4]); put(5, lo[5]); put(6, lo[6]); put(7, lo[7]);
  cmul2(e64, e32, e32, x0, e128, e1);
  put(25, x0);
  cmul3(x0, m[5], e[4], x1, m[6], e[4], x2, m[7], e[4]);
  put(43, x0); put(45, x1); put(47, x2);
  cmul4(x0, x1, x2, x3, lo[0], lo[1], lo[2], lo[3], e64);
  put(8, x0); put(9, x1); put(10, x2); put(11, x3);
  cmul4(x0, x1, x2, x3, lo[4], lo[5], lo[6], lo[7], e64);
  put(12, x0); put(13, x1); put(14, x2); put(15, x3);
  cmul4(x0, x1, x2, x3, e[2], e[3], e[4], e[5], e128);
  put(26, x0); put(27, x1); put(28, x2); put(29, x3);
  cmul2(x0, e[6], e128, x1, e[7], e128);
  put(30, x0); put(31, x1);
  cmul3(x0, e[2], m[2], x1, e[4], m[2], x2, e[6], m[2]);
  put(33, x0); put(34, x1); put(35, x2);
  cmul3(x0, e[2], m[3], x1, e[4], m[3], x2, e[6], m[3]);
  put(37, x0); put(38, x1); put(39, x2);
  cmul4(x0, x1, x2, x3, m[1], m[2], m[3], m[4], e1024);
  put(49, x0); put(50, x1); put(51, x2); put(52, x3);
  cmul3(x0, m[5], e1024, x1, m[6], e1024, x2, m[7], e1024);
  put(53, x0); put(54, x1); put(55, x2);
  lds_arrive(fl + FL_KG + ch);
  lds_wait(fl + FL_KG + ch, 2u);
  {
    const int c = lane & 15, g = lane >> 4;
    const char* fb = sm + ch * KG_CH + c * KG_ROW + g * 16;
    u16* T = (u16*)(sm + KG_T + ch * KG_TS);
    f4 acc0 = {0.f, 0.f, 0.f, 0.f}, acc1 = acc0;
    u4 B[8], A[8];
#pragma unroll
    for (int s = 0; s < 8; ++s) { B[s] = *(const u4*)(fb + s * 64); A[s] = *(const u4*)(fb + (16 + 16 * half) * KG_ROW + s * 64); }
    if (!half) {
      u4 A2[8];
#pragma unroll
      for (int s = 0; s < 8; ++s) A2[s] = *(const u4*)(fb + 48 * KG_ROW + s * 64);
      SB();
#pragma unroll
      for (int s = 0; s < 8; ++s) acc0 = mfma16h(A[s], B[s], acc0);
#pragma unroll
      for (int s = 0; s < 8; ++s) acc1 = mfma16h(A2[s], B[s], acc1);
      acc0 *= kfv; acc1 *= kfv;
      if (lane == 0) acc0[0] += dsv;
      *(u2*)(T + 8 * c + 4 * (g & 1) + 128 * (g >> 1)) = (u2){pk(acc0[0], acc0[1]), pk(acc0[2], acc0[3])};
      if (g < 2) {
#pragma unroll
        for (int e = 0; e < 4; ++e) T[1024 + 128 * (4 * g + e) + 8 * c] = (u16)pk(acc1[e], 0.f);
      } else if (lane == 32) T[2048] = (u16)pk(acc1[0], 0.f);
    } else {
      SB();
#pragma unroll
      for (int s = 0; s < 8; ++s) acc0 = mfma16h(A[s], B[s], acc0);
      acc0 *= kfv;
#pragma unroll
      for (int e = 0; e < 4; ++e) {
        const int p = g < 2 ? 256 + 128 * g + 2 * e : 512 + 128 * (2 * (g - 2) + (e >> 1)) + 4 * (e & 1);
        T[p + 8 * c] = (u16)pk(acc0[e], 0.f);
      }
    }
  }
  lds_arrive(fl + FL_KG + 2 + ch);
  constexpr u32 KEEP = 0x03020100u, LL = 0x01000100u, LH = 0x05040100u, HH = 0x05040504u;
  const u32 sel0 = n < 16 ? KEEP : LL, sel1 = n < 16 ? KEEP : n < 64 ? LH : LL, sel2 = n < 16 ? KEEP : n < 64 ? LL : LH, sel2b = n < 64 ? sel2 : HH;
  const u32 sel3 = n < 16 ? KEEP : n < 32 ? LH : HH, selp2 = n <= 16 ? KEEP : n <= 64 ? LL : HH, selp3 = n <= 16 ? KEEP : n <= 32 ? LH : HH;
  if (t < 16) *(u4*)(sm + CV_K + (t >> 1) * CV_KC + 16 * (t & 1)) = (u4){0u, 0u, 0u, 0u};
  lds_wait(fl + FL_KG + 2 + ch, 2u);
  {
    const u16* T = (const u16*)(sm + KG_T + ch * KG_TS) + 16 * n;
    const u4 v0 = *(const u4*)T, v1 = *(const u4*)(T + 8);
    const u2 vp = *(const u2*)(T - 4);
    const u32 e16 = T[16];
    u32 k[10];
    k[0] = n ? __builtin_amdgcn_perm(v0.x, vp.x, selp2) : 0u;
    k[1] = n ? __builtin_amdgcn_perm(v0.x, vp.y, selp3) : 0u;
    k[2] = __builtin_amdgcn_perm(v0.x, v0.x, sel0); k[3] = __builtin_amdgcn_perm(v0.z, n < 32 ? v0.y : v0.x, sel1);
    k[4] = __builtin_amdgcn_perm(v1.x, n < 64 ? v0.z : v0.x, sel2); k[5] = __builtin_amdgcn_perm(v1.x, v0.w, sel3);
    k[6] = __builtin_amdgcn_perm(v1.x, v1.x, sel0); k[7] = __builtin_amdgcn_perm(v1.z, n < 32 ? v1.y : v1.x, sel1);
    k[8] = __builtin_amdgcn_perm(e16, v1.z, sel2b); k[9] = __builtin_amdgcn_perm(e16, v1.w, sel3);
    u32 o[9];
#pragma unroll
    for (int i = 0; i < 9; ++i) o[i] = __builtin_amdgcn_alignbit(k[i + 1], k[i], 16);
    char* kd = sm + CV_K + 4 * ch * CV_KC + 32 + 32 * n;
#pragma unroll
    for (int i = 0; i < 2; ++i) {
      *(u4*)(kd + 16 * i) = (u4){k[4 * i + 2], k[4 * i + 3], k[4 * i + 4], k[4 * i + 5]};
      *(u4*)(kd + 16 * i + CV_KC) = (u4){o[4 * i + 1], o[4 * i + 2], o[4 * i + 3], o[4 * i + 4]};
      *(u4*)(kd + 16 * i + 2 * CV_KC) = (u4){k[4 * i + 1], k[4 * i + 2], k[4 * i + 3], k[4 * i + 4]};
      *(u4*)(kd + 16 * i + 3 * CV_KC) = (u4){o[4 * i], o[4 * i + 1], o[4 * i + 2], o[4 * i + 3]};
    }
  }
  asm volatile("s_waitcnt vmcnt(0)" : "+v"(early));
  bar();
  if (w >= 2) {
    u32 dummy[8];
#pragma unroll
    for (int k = 0; k < 2; ++k) {
      const int wk = 2 * (w - 2) + k, row = 32 * (b >> 3) + 8 * wk + (lane >> 3);
      const char* base = (const char*)(row >= D ? wu : wg) + (row & (D - 1)) * (DF * 2);
#pragma unroll
      for (int i = 0; i < 2; ++i) {
        const int off = min((b & 1) * 1344 + 128 * ((lane & 7) + 8 * i), (b & 1) ? DF * 2 - 4 : 1340);
        asm volatile("global_load_dword %0, %1, off" : "=v"(dummy[4 * k + i]) : "v"(base + off));
      }
    }
#pragma unroll
    for (int k = 0; k < 2; ++k)
#pragma unroll
      for (int i = 0; i < 2; ++i)
        asm volatile("global_load_dword %0, %1, off" : "=v"(dummy[4 * k + 2 + i]) : "v"((const char*)wd + (b >> 3) * 44032 + (2 * (w - 2) + k) * 11008 + min(lane + 64 * i, 85) * 128));
    asm volatile("s_endpgm" :: "v"(dummy[0]), "v"(dummy[1]), "v"(dummy[2]), "v"(dummy[3]), "v"(dummy[4]), "v"(dummy[5]), "v"(dummy[6]), "v"(dummy[7]));
  }
  if (work) {
    const u64 ep = get_epoch(fl);
    const char* gp = ws + WS_MG + (3 * D + 128 * kq + 2 * lane) * 32;
    u4 gs[4], gc[4];
#pragma unroll 1
    for (int spin = 0; spin < (1 << 16); ++spin) {
      sfor<0, 4>([&](auto i) { gld16_sc1<16 * i>(gs[i], gp); gld16_sc1<16 * i>(gc[i], gp + D * 32); });
      asm volatile("s_waitcnt vmcnt(0)" : "+v"(gs[0]), "+v"(gs[1]), "+v"(gs[2]), "+v"(gs[3]), "+v"(gc[0]), "+v"(gc[1]), "+v"(gc[2]), "+v"(gc[3]) :: "memory");
      bool ok = true;
#pragma unroll
      for (int i = 0; i < 4; ++i) ok = ok && mg_ok(gs[i], ep) && mg_ok(gc[i], ep);
      if (__all(ok)) break;
    }
    u32* vv = (u32*)(sm + CV_V + 512 * w);
    vv[lane] = pk(scale_c(mg_sum(gc[0]) + mg_sum(gc[1])), scale_c(mg_sum(gc[2]) + mg_sum(gc[3])));
    vv[64 + lane] = pk(mg_sum(gs[0]) + mg_sum(gs[1]), mg_sum(gs[2]) + mg_sum(gs[3]));
    const int hh = lane >> 5;
    const u32 ra = ldsaddr(sm) + CV_W + 8192 * w + 64 * (8 * hh + ((lane & 15) >> 2)) + 32 * ((lane >> 4) & 1) + 8 * (lane & 3);
    f16v sums;
#pragma unroll
    for (int e = 0; e < 16; ++e) sums[e] = 0.f;
#pragma unroll
    for (int i = 0; i < 8; ++i) sums = mfma32(*(const u4*)(vv + 64 * (lane & 1) + 8 * i + 4 * hh), cat(trd(ra + 1024 * i), trd(ra + 1024 * i + 256)), sums);
    if (lane < 32) {
      u32* exf = (u32*)(ws + WS_EX) + ((2 * jb + (lane >> 4)) * 64 + 16 * mat + (lane & 15)) * 4 + kq;
      const float h0 = rb(sums[0]), h1 = rb(sums[1]);
      exf[0] = pk(h0, sums[0] - h0);
      exf[128] = pk(h1, sums[1] - h1);
    }
  }
}

DI float red2(float a, float b, bool rows16) {
  if (rows16) { const auto r = __builtin_amdgcn_permlane16_swap(__float_as_uint(a), __float_as_uint(b), false, false); return __uint_as_float(r[0]) + __uint_as_float(r[1]); }
  const auto r = __builtin_amdgcn_permlane32_swap(__float_as_uint(a), __float_as_uint(b), false, false);
  return __uint_as_float(r[0]) + __uint_as_float(r[1]);
}

DI void k1_main(char* sm, int b, const u16* x, const u16* c, const u16* wm, char* ws) {
  const int t = threadIdx.x, w = __builtin_amdgcn_readfirstlane(t >> 6), lane = t & 63, ch = w >> 1, half = w & 1;
  const int d0 = 64 * (b & 7) + 2 * (b >> 3), d = d0 + ch;
  const ldsw fl = (ldsw)(size_t)(ldsaddr(sm) + CV_FL);
  const int c16 = lane & 15, g = lane >> 4;
  __builtin_amdgcn_s_setprio(2);
  if (t < 16) fl[t] = 0u;
  if (w == 3) *(u4*)(sm + CV_H + (lane >> 3) * CV_HP + 16 * (t & 7)) = (u4){0u, 0u, 0u, 0u};
  asm volatile("s_waitcnt lgkmcnt(0)" ::: "memory");
  if (w < 2) {
    const int row = 8 * b + 4 * w + g, mq = (b / 48) & 3, mc = b % 48, rg = t >> 3;
    u4 xs[4], wv[8], cv;
    u64 ep0;
    asm volatile("global_load_dwordx2 %0, %1, %2 sc1" : "=v"(ep0) : "v"(0), "s"(ws + WS_EPOCH));
#pragma unroll
    for (int i = 0; i < 4; ++i) xs[i] = ldnt((const u4*)(x + row * D + 128 * i + 8 * c16));
    SB();
    cv = *(const u4*)(c + 128 * mq + 8 * rg);
#pragma unroll
    for (int i = 0; i < 8; ++i) wv[i] = ldnt((const u4*)(wm + (128 * mq + 8 * rg + i) * (6 * D) + 64 * mc + 8 * (t & 7)));
    SB();
    rawbar();
    SB();
    float s1 = 0.f, s2 = 0.f, s3 = 0.f, s4 = 0.f;
#pragma unroll
    for (int i = 0; i < 4; ++i)
#pragma unroll
      for (int j = 0; j < 4; j += 2) {
        s1 = dot2(xs[i][j], 0x3f803f80u, s1); s2 = dot2(xs[i][j], xs[i][j], s2);
        s3 = dot2(xs[i][j + 1], 0x3f803f80u, s3); s4 = dot2(xs[i][j + 1], xs[i][j + 1], s4);
      }
    s1 += s3; s2 += s4;
    asm volatile("" : "+v"(ep0), "+v"(s1));
    if (w == 0) {
      fl[FL_EPOCH] = (u32)(ep0 + 1); fl[FL_EPOCH + 1] = (u32)((ep0 + 1) >> 32);
      asm volatile("s_waitcnt lgkmcnt(0)" ::: "memory");
      fl[FL_EPOK] = 1u;
    }
    s1 += dppf<0xb1>(s1); s2 += dppf<0xb1>(s2); s1 += dppf<0x4e>(s1); s2 += dppf<0x4e>(s2);
    s1 += dppf<0x141>(s1); s2 += dppf<0x141>(s2); s1 += dppf<0x140>(s1); s2 += dppf<0x140>(s2);
    const f2 st = ln_stats(s1, s2);
    const u64 ep = ep0 + 1;
    if (c16 == 0) {
      __hip_atomic_store((u64*)(ws + WS_ST1) + row, (ep << 32) | (__float_as_uint(st.x) >> 16) | __float_as_uint(st.y), __ATOMIC_RELAXED, __HIP_MEMORY_SCOPE_AGENT);
      ((u32*)(ws + WS_ST2))[row] = 0u;
    }
    if (t == 0 && b < 16) ((u32*)(ws + WS_CNT))[64 * b] = 0u;
    if (b < 192) {
      float acc[8] = {0.f, 0.f, 0.f, 0.f, 0.f, 0.f, 0.f, 0.f};
#pragma unroll
      for (int i = 0; i < 4; ++i) {
        const float c0 = blo(cv[i]), c1 = bhi(cv[i]);
        const u32 sp = pk(c0 * __builtin_amdgcn_rcpf(1.f + __expf(-c0)), c1 * __builtin_amdgcn_rcpf(1.f + __expf(-c1)));
#pragma unroll
        for (int j = 0; j < 4; ++j) {
          acc[2 * j] = dot2(__builtin_amdgcn_perm(wv[2 * i + 1][j], wv[2 * i][j], 0x05040100u), sp, acc[2 * j]);
          acc[2 * j + 1] = dot2(__builtin_amdgcn_perm(wv[2 * i + 1][j], wv[2 * i][j], 0x07060302u), sp, acc[2 * j + 1]);
        }
      }
      const float p0 = red2(red2(acc[0], acc[4], false), red2(acc[2], acc[6], false), true), p1 = red2(red2(acc[1], acc[5], false), red2(acc[3], acc[7], false), true);
      const float q0 = p0 + dppf<0x128>(p0), q1 = p1 + dppf<0x128>(p1);
      float v = (lane & 8) ? q1 : q0;
      float* mx = (float*)(sm + CV_M);
      if (w == 1) { mx[lane] = v; lds_arrive(fl + FL_MOD); }
      else {
        lds_wait(fl + FL_MOD, 1u);
        v += mx[lane];
        const u32 bits = __float_as_uint(v) + 0x80u;
        __hip_atomic_store((u64*)(ws + WS_MG) + (64 * mc + 8 * (lane & 7) + (lane >> 3)) * 4 + mq, (ep << 24) | (bits >> 8), __ATOMIC_RELAXED, __HIP_MEMORY_SCOPE_AGENT);
      }
    }
  } else {
    const int tg = t - 128, r0 = 64 * (b >> 3), c0 = 64 * (b & 7), q = c16 >> 2, p = lane & 3;
    u4 v[4];
#pragma unroll
    for (int i = 0; i < 4; ++i) v[i] = ldnt((const u4*)(x + (r0 + 16 * i + (tg >> 3)) * D + c0 + 8 * (tg & 7)));
    rawbar();
    char* tb = sm + CV_TB;
#pragma unroll
    for (int i = 0; i < 4; ++i) *(u4*)(tb + (16 * i + (tg >> 3)) * 144 + 16 * (tg & 7)) = v[i];
    lds_arrive(fl + FL_XT);
    lds_wait(fl + FL_XT, 2u);
#pragma unroll
    for (int i = 0; i < 4; ++i) {
      const char* a = tb + (32 * (i & 1) + 8 * g + q) * 144 + 64 * half + 32 * (i >> 1) + 8 * p;
      const u4 o = cat(trd(a), trd(a + 4 * 144));
      const u16* dst = (const u16*)(ws + WS_XT) + (c0 + 32 * half + 16 * (i >> 1) + c16) * L + r0 + 32 * (i & 1) + 8 * g;
      asm volatile("global_store_dwordx4 %0, %1, off sc1\n\ts_nop 1" :: "v"(dst), "v"(o) : "memory");
    }
    const u64 ep = get_epoch(fl);
    asm volatile("s_waitcnt vmcnt(0)" ::: "memory");
    if (lane == 0 && __hip_atomic_fetch_add((LAS u32*)fl + FL_XD, 1u, __ATOMIC_RELAXED, __HIP_MEMORY_SCOPE_WORKGROUP) == 1u)
      __hip_atomic_store((u32*)(ws + WS_FX) + 32 * (b & 7) + (b >> 3), (u32)ep, __ATOMIC_RELAXED, __HIP_MEMORY_SCOPE_AGENT);
  }
  const u64 ep = get_epoch(fl);
  float sa[2], c1[2], ga, c2;
  u4 xv[2];
  {
    const char* sp = ws + WS_ST1 + 16 * t;
    const int ml = lane < 20 ? lane : 19, mch = ml >> 2;
    const char* mp = ws + WS_MG + (((mch + (mch > 2)) * D + d0) * 4 + 2 * (ml & 3)) * 8;
    u4 gs[4], gm;
#pragma unroll 1
    for (int spin = 0; spin < (1 << 16); ++spin) {
      gld16_sc1<0>(gs[0], sp); gld16_sc1<0>(gs[1], sp + 4096); gld16_sc1<0>(gs[2], sp + 8192); gld16_sc1<0>(gs[3], sp + 12288); gld16_sc1<0>(gm, mp);
      asm volatile("s_waitcnt vmcnt(0)" : "+v"(gs[0]), "+v"(gs[1]), "+v"(gs[2]), "+v"(gs[3]), "+v"(gm) :: "memory");
      bool ok = mg_ok(gm, ep);
#pragma unroll
      for (int i = 0; i < 4; ++i) ok = ok && gs[i].y == (u32)ep && gs[i].w == (u32)ep;
      if (__all(ok)) break;
      __builtin_amdgcn_s_sleep(8);
    }
#pragma unroll
    for (int i = 0; i < 4; ++i) *(u2*)(sm + CV_ST + 8 * t + 2048 * i) = (u2){gs[i].x, gs[i].z};
    lds_arrive(fl + FL_MAIN);
    float v = mg_sum(gm);
    v += dppf<0xb1>(v);
    const float vr = rb(v), vs = scale_c(v), vp = rb(1.f + vr);
    auto pick = [](float z, int l) { return __int_as_float(__builtin_amdgcn_readlane(__float_as_int(z), l)); };
    sa[0] = pick(vr, 0); sa[1] = pick(vr, 2); c1[0] = pick(vp, 4); c1[1] = pick(vp, 6);
    ga = ch ? pick(vr, 10) : pick(vr, 8); c2 = ch ? pick(vs, 14) : pick(vs, 12);
    if (w == 0) {
      if (lane == 12 || lane == 14) ((float*)(ws + WS_INV))[d0 + ((lane >> 1) & 1)] = 1.f / vs;
      if (lane == 16 || lane == 18) ((float*)(ws + WS_GB))[d0 + ((lane >> 1) & 1)] = vr;
    }
    if (w == 0) {
      const u32* fx = (const u32*)(ws + WS_FX) + 32 * (b & 7) + (lane & 31);
#pragma unroll 1
      for (int spin = 0; spin < (1 << 16); ++spin) {
        if (__all(__hip_atomic_load(fx, __ATOMIC_RELAXED, __HIP_MEMORY_SCOPE_AGENT) == (u32)ep)) break;
        __builtin_amdgcn_s_sleep(2);
      }
      fl[FL_GO] = 1u;
    } else {
      while (fl[FL_GO] == 0u) __builtin_amdgcn_s_sleep(1);
    }
    const char* xp = ws + WS_XT + d0 * (L * 2) + 16 * t;
    gld16_sc1<0>(xv[0], xp); gld16_sc1<0>(xv[1], xp + L * 2);
    lds_wait(fl + FL_MAIN, 4u);
    if (b == 0 && t == 0) *(u64*)(ws + WS_EPOCH) = ep;
  }
  {
    const u4 m0 = *(const u4*)(sm + CV_ST + 32 * t), m1 = *(const u4*)(sm + CV_ST + 32 * t + 16);
    float rs[8], mm[8];
#pragma unroll
    for (int j = 0; j < 8; ++j) { const u32 z = j < 4 ? m0[j & 3] : m1[j & 3]; rs[j] = __builtin_amdgcn_rcpf(bhi(z)); mm[j] = blo(z); }
    asm volatile("s_waitcnt vmcnt(0)" : "+v"(xv[0]), "+v"(xv[1]), "+v"(rs[0]), "+v"(rs[7]) :: "memory");
#pragma unroll
    for (int i = 0; i < 2; ++i) {
      *(u4*)(sm + CV_X + 4096 * i + 16 * t) = xv[i];
      float h[8];
#pragma unroll
      for (int j = 0; j < 8; ++j) h[j] = fmaf((((j & 1) ? bhi(xv[i][j >> 1]) : blo(xv[i][j >> 1])) - mm[j]) * rs[j], c1[i], sa[i]);
#pragma unroll
      for (int r = 0; r < 4; ++r) *(u32*)(sm + CV_H + (4 * i + r) * CV_HP + 128 + 4 * t) = pk(h[r], h[r + 4]);
    }
  }
  f4 acc[8];
#pragma unroll
  for (int i = 0; i < 8; ++i) acc[i] = (f4){0.f, 0.f, 0.f, 0.f};
  u32 ka = ldsaddr(sm) + CV_K + (4 * ch + 2 * half) * CV_KC + 32 + 8 * (8 * g + (lane & 3) - ((lane & 15) >> 2));
  u32 hb = ldsaddr(sm) + CV_H + (4 * ch + 2 * half) * CV_HP + 128 + 8 * c16 - 16 * g - 8 - 64;
  bar();
  {
    u32 cnt;
    asm volatile(
      "ds_read_b64_tr_b16 v[64:65], %8 offset:32\n"
      "ds_read_b64_tr_b16 v[66:67], %8 offset:0\n"
      "ds_read_b64 v[120:121], %9 offset:832\n"
      "ds_read_b64 v[122:123], %9 offset:840\n"
      "ds_read_b64 v[124:125], %9 offset:960\n"
      "ds_read_b64 v[126:127], %9 offset:968\n"
      "ds_read_b64 v[112:113], %9 offset:576\n"
      "ds_read_b64 v[114:115], %9 offset:584\n"
      "ds_read_b64 v[116:117], %9 offset:704\n"
      "ds_read_b64 v[118:119], %9 offset:712\n"
      "ds_read_b64_tr_b16 v[68:69], %8 offset:544\n"
      "ds_read_b64_tr_b16 v[70:71], %8 offset:512\n"
      "ds_read_b64 v[108:109], %9 offset:448\n"
      "ds_read_b64 v[110:111], %9 offset:456\n"
      "ds_read_b64 v[104:105], %9 offset:320\n"
      "ds_read_b64 v[106:107], %9 offset:328\n"
      "ds_read_b64_tr_b16 v[72:73], %8 offset:1056\n"
      "ds_read_b64_tr_b16 v[74:75], %8 offset:1024\n"
      "ds_read_b64_tr_b16 v[76:77], %8 offset:1568\n"
      "ds_read_b64_tr_b16 v[78:79], %8 offset:1536\n"
      "ds_read_b64 v[100:101], %9 offset:192\n"
      "ds_read_b64 v[102:103], %9 offset:200\n"
      "ds_read_b64_tr_b16 v[80:81], %8 offset:2080\n"
      "ds_read_b64_tr_b16 v[82:83], %8 offset:2048\n"
      "ds_read_b64 v[96:97], %9 offset:64\n"
      "ds_read_b64 v[98:99], %9 offset:72\n"
      "ds_read_b64_tr_b16 v[84:85], %8 offset:2592\n"
      "ds_read_b64_tr_b16 v[86:87], %8 offset:2560\n"
      "ds_read_b64_tr_b16 v[88:89], %8 offset:3104\n"
      "ds_read_b64_tr_b16 v[90:91], %8 offset:3072\n"
      "ds_read_b64_tr_b16 v[92:93], %8 offset:3616\n"
      "ds_read_b64_tr_b16 v[94:95], %8 offset:3584\n"
      "s_mov_b32 %10, 2\n"
      "s_waitcnt lgkmcnt(0)\n"
      "Lconv%=:\n"
      "v_mfma_f32_16x16x32_bf16 %6, v[64:67], v[120:123], %6\n"
      "ds_read_b64_tr_b16 v[128:129], %8 offset:288\n"
      "v_mfma_f32_16x16x32_bf16 %7, v[64:67], v[124:127], %7\n"
      "ds_read_b64_tr_b16 v[130:131], %8 offset:256\n"
      "v_mfma_f32_16x16x32_bf16 %4, v[64:67], v[112:115], %4\n"
      "ds_read_b64 v[184:185], %9 offset:768\n"
      "v_mfma_f32_16x16x32_bf16 %5, v[64:67], v[116:119], %5\n"
      "ds_read_b64 v[186:187], %9 offset:776\n"
      "v_mfma_f32_16x16x32_bf16 %6, v[68:71], v[116:119], %6\n"
      "ds_read_b64 v[188:189], %9 offset:896\n"
      "v_mfma_f32_16x16x32_bf16 %7, v[68:71], v[120:123], %7\n"
      "ds_read_b64 v[190:191], %9 offset:904\n"
      "v_mfma_f32_16x16x32_bf16 %4, v[68:71], v[108:111], %4\n"
      "ds_read_b64 v[176:177], %9 offset:512\n"
      "v_mfma_f32_16x16x32_bf16 %2, v[64:67], v[104:107], %2\n"
      "ds_read_b64 v[178:179], %9 offset:520\n"
      "v_mfma_f32_16x16x32_bf16 %6, v[72:75], v[112:115], %6\n"
      "ds_read_b64 v[180:181], %9 offset:640\n"
      "v_mfma_f32_16x16x32_bf16 %3, v[64:67], v[108:111], %3\n"
      "ds_read_b64 v[182:183], %9 offset:648\n"
      "v_mfma_f32_16x16x32_bf16 %7, v[72:75], v[116:119], %7\n"
      "ds_read_b64_tr_b16 v[132:133], %8 offset:800\n"
      "v_mfma_f32_16x16x32_bf16 %4, v[72:75], v[104:107], %4\n"
      "ds_read_b64_tr_b16 v[134:135], %8 offset:768\n"
      "v_mfma_f32_16x16x32_bf16 %5, v[68:71], v[112:115], %5\n"
      "ds_read_b64 v[172:173], %9 offset:384\n"
      "v_mfma_f32_16x16x32_bf16 %6, v[76:79], v[108:111], %6\n"
      "ds_read_b64 v[174:175], %9 offset:392\n"
      "v_mfma_f32_16x16x32_bf16 %7, v[76:79], v[112:115], %7\n"
      "ds_read_b64 v[168:169], %9 offset:256\n"
      "v_mfma_f32_16x16x32_bf16 %4, v[76:79], v[100:103], %4\n"
      "ds_read_b64 v[170:171], %9 offset:264\n"
      "v_mfma_f32_16x16x32_bf16 %1, v[64:67], v[100:103], %1\n"
      "ds_read_b64_tr_b16 v[136:137], %8 offset:1312\n"
      "v_mfma_f32_16x16x32_bf16 %5, v[72:75], v[108:111], %5\n"
      "ds_read_b64_tr_b16 v[138:139], %8 offset:1280\n"
      "v_mfma_f32_16x16x32_bf16 %6, v[80:83], v[104:107], %6\n"
      "ds_read_b64_tr_b16 v[140:141], %8 offset:1824\n"
      "v_mfma_f32_16x16x32_bf16 %0, v[64:67], v[96:99], %0\n"
      "ds_read_b64_tr_b16 v[142:143], %8 offset:1792\n"
      "v_mfma_f32_16x16x32_bf16 %7, v[80:83], v[108:111], %7\n"
      "ds_read_b64 v[164:165], %9 offset:128\n"
      "v_mfma_f32_16x16x32_bf16 %5, v[76:79], v[104:107], %5\n"
      "ds_read_b64 v[166:167], %9 offset:136\n"
      "v_mfma_f32_16x16x32_bf16 %2, v[68:71], v[100:103], %2\n"
      "ds_read_b64_tr_b16 v[144:145], %8 offset:2336\n"
      "v_mfma_f32_16x16x32_bf16 %6, v[84:87], v[100:103], %6\n"
      "ds_read_b64_tr_b16 v[146:147], %8 offset:2304\n"
      "v_mfma_f32_16x16x32_bf16 %7, v[84:87], v[104:107], %7\n"
      "ds_read_b64 v[160:161], %9 offset:0\n"
      "v_mfma_f32_16x16x32_bf16 %3, v[68:71], v[104:107], %3\n"
      "ds_read_b64 v[162:163], %9 offset:8\n"
      "v_mfma_f32_16x16x32_bf16 %2, v[72:75], v[96:99], %2\n"
      "ds_read_b64_tr_b16 v[148:149], %8 offset:2848\n"
      "v_mfma_f32_16x16x32_bf16 %5, v[80:83], v[100:103], %5\n"
      "ds_read_b64_tr_b16 v[150:151], %8 offset:2816\n"
      "v_mfma_f32_16x16x32_bf16 %7, v[88:91], v[100:103], %7\n"
      "ds_read_b64_tr_b16 v[152:153], %8 offset:3360\n"
      "v_mfma_f32_16x16x32_bf16 %6, v[88:91], v[96:99], %6\n"
      "ds_read_b64_tr_b16 v[154:155], %8 offset:3328\n"
      "v_mfma_f32_16x16x32_bf16 %3, v[72:75], v[100:103], %3\n"
      "ds_read_b64_tr_b16 v[156:157], %8 offset:3872\n"
      "v_mfma_f32_16x16x32_bf16 %1, v[68:71], v[96:99], %1\n"
      "ds_read_b64_tr_b16 v[158:159], %8 offset:3840\n"
      "v_mfma_f32_16x16x32_bf16 %7, v[92:95], v[96:99], %7\n"
      "v_mfma_f32_16x16x32_bf16 %5, v[84:87], v[96:99], %5\n"
      "v_mfma_f32_16x16x32_bf16 %4, v[80:83], v[96:99], %4\n"
      "v_mfma_f32_16x16x32_bf16 %3, v[76:79], v[96:99], %3\n"
      "s_waitcnt lgkmcnt(0)\n"
      "v_add_u32 %8, 4128, %8\n"
      "v_add_u32 %9, 1152, %9\n"
      "v_mfma_f32_16x16x32_bf16 %6, v[128:131], v[184:187], %6\n"
      "ds_read_b64_tr_b16 v[64:65], %8 offset:32\n"
      "v_mfma_f32_16x16x32_bf16 %7, v[128:131], v[188:191], %7\n"
      "ds_read_b64_tr_b16 v[66:67], %8 offset:0\n"
      "v_mfma_f32_16x16x32_bf16 %4, v[128:131], v[176:179], %4\n"
      "ds_read_b64 v[120:121], %9 offset:832\n"
      "v_mfma_f32_16x16x32_bf16 %5, v[128:131], v[180:183], %5\n"
      "ds_read_b64 v[122:123], %9 offset:840\n"
      "v_mfma_f32_16x16x32_bf16 %6, v[132:135], v[180:183], %6\n"
      "ds_read_b64 v[124:125], %9 offset:960\n"
      "v_mfma_f32_16x16x32_bf16 %7, v[132:135], v[184:187], %7\n"
      "ds_read_b64 v[126:127], %9 offset:968\n"
      "v_mfma_f32_16x16x32_bf16 %4, v[132:135], v[172:175], %4\n"
      "ds_read_b64 v[112:113], %9 offset:576\n"
      "v_mfma_f32_16x16x32_bf16 %2, v[128:131], v[168:171], %2\n"
      "ds_read_b64 v[114:115], %9 offset:584\n"
      "v_mfma_f32_16x16x32_bf16 %6, v[136:139], v[176:179], %6\n"
      "ds_read_b64 v[116:117], %9 offset:704\n"
      "v_mfma_f32_16x16x32_bf16 %3, v[128:131], v[172:175], %3\n"
      "ds_read_b64 v[118:119], %9 offset:712\n"
      "v_mfma_f32_16x16x32_bf16 %7, v[136:139], v[180:183], %7\n"
      "ds_read_b64_tr_b16 v[68:69], %8 offset:544\n"
      "v_mfma_f32_16x16x32_bf16 %4, v[136:139], v[168:171], %4\n"
      "ds_read_b64_tr_b16 v[70:71], %8 offset:512\n"
      "v_mfma_f32_16x16x32_bf16 %5, v[132:135], v[176:179], %5\n"
      "ds_read_b64 v[108:109], %9 offset:448\n"
      "v_mfma_f32_16x16x32_bf16 %6, v[140:143], v[172:175], %6\n"
      "ds_read_b64 v[110:111], %9 offset:456\n"
      "v_mfma_f32_16x16x32_bf16 %7, v[140:143], v[176:179], %7\n"
      "ds_read_b64 v[104:105], %9 offset:320\n"
      "v_mfma_f32_16x16x32_bf16 %4, v[140:143], v[164:167], %4\n"
      "ds_read_b64 v[106:107], %9 offset:328\n"
      "v_mfma_f32_16x16x32_bf16 %1, v[128:131], v[164:167], %1\n"
      "ds_read_b64_tr_b16 v[72:73], %8 offset:1056\n"
      "v_mfma_f32_16x16x32_bf16 %5, v[136:139], v[172:175], %5\n"
      "ds_read_b64_tr_b16 v[74:75], %8 offset:1024\n"
      "v_mfma_f32_16x16x32_bf16 %6, v[144:147], v[168:171], %6\n"
      "ds_read_b64_tr_b16 v[76:77], %8 offset:1568\n"
      "v_mfma_f32_16x16x32_bf16 %0, v[128:131], v[160:163], %0\n"
      "ds_read_b64_tr_b16 v[78:79], %8 offset:1536\n"
      "v_mfma_f32_16x16x32_bf16 %7, v[144:147], v[172:175], %7\n"
      "ds_read_b64 v[100:101], %9 offset:192\n"
      "v_mfma_f32_16x16x32_bf16 %5, v[140:143], v[168:171], %5\n"
      "ds_read_b64 v[102:103], %9 offset:200\n"
      "v_mfma_f32_16x16x32_bf16 %2, v[132:135], v[164:167], %2\n"
      "ds_read_b64_tr_b16 v[80:81], %8 offset:2080\n"
      "v_mfma_f32_16x16x32_bf16 %6, v[148:151], v[164:167], %6\n"
      "ds_read_b64_tr_b16 v[82:83], %8 offset:2048\n"
      "v_mfma_f32_16x16x32_bf16 %7, v[148:151], v[168:171], %7\n"
      "ds_read_b64 v[96:97], %9 offset:64\n"
      "v_mfma_f32_16x16x32_bf16 %3, v[132:135], v[168:171], %3\n"
      "ds_read_b64 v[98:99], %9 offset:72\n"
      "v_mfma_f32_16x16x32_bf16 %2, v[136:139], v[160:163], %2\n"
      "ds_read_b64_tr_b16 v[84:85], %8 offset:2592\n"
      "v_mfma_f32_16x16x32_bf16 %5, v[144:147], v[164:167], %5\n"
      "ds_read_b64_tr_b16 v[86:87], %8 offset:2560\n"
      "v_mfma_f32_16x16x32_bf16 %7, v[152:155], v[164:167], %7\n"
      "ds_read_b64_tr_b16 v[88:89], %8 offset:3104\n"
      "v_mfma_f32_16x16x32_bf16 %6, v[152:155], v[160:163], %6\n"
      "ds_read_b64_tr_b16 v[90:91], %8 offset:3072\n"
      "v_mfma_f32_16x16x32_bf16 %3, v[136:139], v[164:167], %3\n"
      "ds_read_b64_tr_b16 v[92:93], %8 offset:3616\n"
      "v_mfma_f32_16x16x32_bf16 %1, v[132:135], v[160:163], %1\n"
      "ds_read_b64_tr_b16 v[94:95], %8 offset:3584\n"
      "v_mfma_f32_16x16x32_bf16 %7, v[156:159], v[160:163], %7\n"
      "v_mfma_f32_16x16x32_bf16 %5, v[148:151], v[160:163], %5\n"
      "v_mfma_f32_16x16x32_bf16 %4, v[144:147], v[160:163], %4\n"
      "v_mfma_f32_16x16x32_bf16 %3, v[140:143], v[160:163], %3\n"
      "s_waitcnt lgkmcnt(0)\n"
      "s_sub_u32 %10, %10, 1\n"
      "s_cmp_lg_u32 %10, 0\n"
      "s_cbranch_scc1 Lconv%=\n"
      "s_nop 7\n"
      : "+v"(acc[0]), "+v"(acc[1]), "+v"(acc[2]), "+v"(acc[3]), "+v"(acc[4]), "+v"(acc[5]), "+v"(acc[6]), "+v"(acc[7]), "+v"(ka), "+v"(hb), "=&s"(cnt)
      :: "memory", "scc", "v64", "v65", "v66", "v67", "v68", "v69", "v70", "v71", "v72", "v73", "v74", "v75", "v76", "v77", "v78", "v79", "v80", "v81", "v82", "v83", "v84", "v85", "v86", "v87", "v88", "v89", "v90", "v91", "v92", "v93", "v94", "v95", "v96", "v97", "v98", "v99", "v100", "v101", "v102", "v103", "v104", "v105", "v106", "v107", "v108", "v109", "v110", "v111", "v112", "v113", "v114", "v115", "v116", "v117", "v118", "v119", "v120", "v121", "v122", "v123", "v124", "v125", "v126", "v127", "v128", "v129", "v130", "v131", "v132", "v133", "v134", "v135", "v136", "v137", "v138", "v139", "v140", "v141", "v142", "v143", "v144", "v145", "v146", "v147", "v148", "v149", "v150", "v151", "v152", "v153", "v154", "v155", "v156", "v157", "v158", "v159", "v160", "v161", "v162", "v163", "v164", "v165", "v166", "v167", "v168", "v169", "v170", "v171", "v172", "v173", "v174", "v175", "v176", "v177", "v178", "v179", "v180", "v181", "v182", "v183", "v184", "v185", "v186", "v187", "v188", "v189", "v190", "v191");
  }
  f4* ex = (f4*)(sm + CV_EX);
  if (half) {
#pragma unroll
    for (int i = 0; i < 4; ++i) { ex[(w * 4 + i) * 64 + lane] = acc[i]; acc[i] = acc[4 + i]; }
  } else {
#pragma unroll
    for (int i = 0; i < 4; ++i) ex[(w * 4 + i) * 64 + lane] = acc[4 + i];
  }
  lds_arrive(fl + FL_EXCH + w);
  u2 xr[4];
#pragma unroll
  for (int i = 0; i < 4; ++i) xr[i] = *(const u2*)(sm + CV_X + 4096 * ch + 2 * (256 * (4 * half + i) + 16 * c16 + 4 * g));
  lds_wait(fl + FL_EXCH + (w ^ 1), 1u);
  u16* x1t = (u16*)(ws + WS_X1T);
  const float gc = ga * c2;
#pragma unroll
  for (int i = 0; i < 4; ++i) {
    const f4 y = (acc[i] + ex[((w ^ 1) * 4 + i) * 64 + lane]) * gc + (f4){blo(xr[i].x), bhi(xr[i].x), blo(xr[i].y), bhi(xr[i].y)} * c2;
    *(u2*)(x1t + d * L + 256 * (4 * half + i) + 16 * c16 + 4 * g) = (u2){pk(y[0], y[1]), pk(y[2], y[3])};
  }
}

__global__ __launch_bounds__(512, 2) void k1_conv(const u16* x, const u16* lar, const u16* aim, const u16* cre, const u16* cim, const u16* ldt, const u16* dsk,
                                                  const u16* c, const u16* wm, const u16* wg, const u16* wu, const u16* wd, char* ws) {
  extern __shared__ __attribute__((aligned(16))) char sm[];
  const int b = blockIdx.x;
  if (__builtin_amdgcn_readfirstlane(threadIdx.x) < 256) k1_main(sm, b, x, c, wm, ws);
  else k1_help(sm, b, lar, aim, cre, cim, ldt, dsk, wg, wu, wd, ws);
}

constexpr int G1_LEAD = 3, G1_NS = 5, G1_SLOT = 20480;
constexpr int G2_RES = 19, G2_FL = 4 * G2_RES * 2048, G1_SV = G2_FL + 64, K3_LDS = G1_SV + 512;
constexpr int G2_LEAD = 6;
constexpr int HH_T = NU * 1024;

template <int K> DI u32 g1_pair(const f16v& acc, float rs, float sd) {
  const f2 g = (f2){acc[2 * K], acc[2 * K + 1]} * rs;
  const f2 z = g * (g * g * -0.10294324f + -2.30220819f);
  const f2 dn = (f2){__builtin_amdgcn_exp2f(z.x), __builtin_amdgcn_exp2f(z.y)} * sd + sd;
  const f2 r = g * (f2){acc[8 + 2 * K], acc[9 + 2 * K]} * (f2){__builtin_amdgcn_rcpf(dn.x), __builtin_amdgcn_rcpf(dn.y)};
  return pk(r.x, r.y);
}
DI u4 g1_tile(const f16v& acc, float rs, float sd) { return (u4){g1_pair<0>(acc, rs, sd), g1_pair<1>(acc, rs, sd), g1_pair<2>(acc, rs, sd), g1_pair<3>(acc, rs, sd)}; }

DI void g1_stats(char* sm, int rt, char* ws) {
  const int t = threadIdx.x;
  float* red = (float*)sm;
  if (t < 512) {
    const u16* xp = (const u16*)(ws + WS_X1T) + (t >> 4) * L + 128 * rt + 8 * (t & 15);
    u4 v[16];
    float iv[16];
#pragma unroll
    for (int i = 0; i < 16; ++i) { v[i] = *(const u4*)(xp + 32 * i * L); iv[i] = ((const float*)(ws + WS_INV))[(t >> 4) + 32 * i]; }
    f2 s1[4], s2[4];
#pragma unroll
    for (int j = 0; j < 4; ++j) s1[j] = s2[j] = (f2){0.f, 0.f};
#pragma unroll
    for (int i = 0; i < 16; ++i)
#pragma unroll
      for (int j = 0; j < 4; ++j) { const f2 x = (f2){blo(v[i][j]), bhi(v[i][j])} * iv[i]; s1[j] += x; s2[j] += x * x; }
#pragma unroll
    for (int j = 0; j < 4; ++j) *(f4*)(red + (t >> 4) * 260 + 16 * (t & 15) + 4 * j) = (f4){s1[j].x, s2[j].x, s1[j].y, s2[j].y};
  }
  __syncthreads();
  if (t < 512) {
    f2 p = {0.f, 0.f};
#pragma unroll
    for (int i = 0; i < 8; ++i) p += *(const f2*)(red + (8 * (t & 3) + i) * 260 + 2 * (t >> 2));
    p.x += dppf<0xb1>(p.x); p.y += dppf<0xb1>(p.y); p.x += dppf<0x4e>(p.x); p.y += dppf<0x4e>(p.y);
    const f2 ms = ln_stats(p.x, p.y);
    if ((t & 3) == 0)
      __hip_atomic_store((u32*)(ws + WS_ST2) + 128 * rt + (t >> 2), (__float_as_uint(ms.x) >> 16) | __float_as_uint(ms.y), __ATOMIC_RELAXED, __HIP_MEMORY_SCOPE_AGENT);
  }
  __syncthreads();
}

__global__ __launch_bounds__(768, 3) void k3_mlp(const u16* wg, const u16* wu, const u16* wd, char* ws, u16* out) {
  extern __shared__ __attribute__((aligned(16))) char sm[];
  const int b = blockIdx.x, j = b >> 3;
  const int t = threadIdx.x, w12 = __builtin_amdgcn_readfirstlane(t >> 6), w = w12 & 3, lane = t & 63, hh = lane >> 5;
  const u32 lb0 = ldsaddr(sm);
  const int q = (lane & 15) >> 2, p = lane & 3, g = lane >> 4;
  const int lt = 4 * (b & 7) + (j & 3), dt = j >> 2;
  auto kfirst = [&](int kq) { const u32 k = 22 * kq - (kq == 3) + 3 * dt; return min(k, k - NU); };
  auto knext = [](u32 k) { return min(k + 1, k + 1 - NU); };
  const rsrc_t rd = mkrsrc(wd, DF * D * 2);
  const u32 vd = (lane >> 3) * 1024 + 128 * dt + 16 * ((lane & 7) ^ (4 * ((lane >> 4) & 1)));
  volatile LAS u32* fl = (volatile LAS u32*)(size_t)(lb0 + G2_FL);
  auto fill2 = [&](int i0, int n, int slot0) {
    u32 kd = kfirst(w) + i0;
    kd = min(kd, kd - NU);
#pragma unroll 1
    for (int i = 0; i < n; ++i) {
      glds16<0>(lb0 + (G2_RES * w + slot0 + i) * 2048, vd, rd, kd * 16384);
      glds16<0>(lb0 + (G2_RES * w + slot0 + i) * 2048 + 1024, vd, rd, kd * 16384 + 8192);
      kd = knext(kd);
    }
  };
  {
    const int rt = 4 * ((b & 7) >> 1) + (j & 3), cg = (j >> 2) + 8 * (b & 1) - 1;
    if (t == 0) fl[0] = 0u;
    if (cg < 0) {
      g1_stats(sm, rt, ws);
    } else if (w12 >= 8) {
      const rsrc_t rx = mkrsrc(ws + WS_X1T, D * L * 2), rg = mkrsrc(wg, D * DF * 2), ru = mkrsrc(wu, D * DF * 2);
      const u32 vx = (4 * w + g) * 4096 + 256 * rt + 16 * ((lane & 15) ^ ((g << 2) | w));
      u32 vw[3];
#pragma unroll
      for (int i = 0; i < 3; ++i) {
        const int pc = (3 * (w & 1) + i) * 64 + lane, row = pc / 12, chk = pc - 12 * row;
        vw[i] = row * (DF * 2) + min(192 * cg + 16 * (chk ^ (w & 2)), DF * 2 - 16);
      }
      const rsrc_t rwm = w < 2 ? rg : ru;
      auto stage = [&](int i, int slot) {
        const u32 m = lb0 + slot * G1_SLOT;
        glds16<0>(m + 1024 * w, vx, rx, i * 131072);
        glds16<0>(m + 4096 + 1024 * w, vx, rx, i * 131072 + 65536);
#pragma unroll
        for (int k = 0; k < 3; ++k) glds16<0>(m + 8192 + 3072 * w + 1024 * k, vw[k], rwm, i * (32 * DF * 2));
      };
      asm volatile("s_nop 4");
      static_assert(G1_LEAD + 2 <= G1_NS, "ring too short");
#pragma unroll 1
      for (int i = 0; i < G1_LEAD - 1; ++i) stage(i, i);
      int slot = G1_LEAD - 1;
#pragma unroll 1
      for (int i = 0; i < 17 - G1_LEAD; ++i) {
        stage(i + G1_LEAD - 1, slot);
        slot = slot == G1_NS - 1 ? 0 : slot + 1;
        vmwait<5 * (G1_LEAD - 1)>();
        rawbar();
      }
      const u32* st2 = (const u32*)(ws + WS_ST2) + 128 * rt + 32 * w + (lane & 31);
      u32 v;
      asm volatile("global_load_dword %0, %1, off sc1" : "=v"(v) : "v"(st2));
      sfor<17 - G1_LEAD, 16>([&](auto i) { vmwait<5 * (15 - i) + 1>(); rawbar(); });
      asm volatile("s_waitcnt vmcnt(0)" : "+v"(v));
#pragma unroll 1
      for (int spin = 0; spin < (1 << 16) && !__all(v != 0u); ++spin) {
        __builtin_amdgcn_s_sleep(2);
        v = __hip_atomic_load(st2, __ATOMIC_RELAXED, __HIP_MEMORY_SCOPE_AGENT);
      }
      ((u32*)(sm + G1_SV))[32 * w + (lane & 31)] = v;
      bar();
      bar();
    } else if (w12 >= 4) {
      const char* pf = (const char*)wd + j * (43 * 1024) + w * (43 * 256);
#pragma unroll 1
      for (int i = 0; i < 18; ++i) {
        rawbar();
      }
    } else {
      const int wl = w & 1, wu3 = 3 * (w >> 1), u0 = 6 * cg + wu3;
      u32 xa[2][2], wa[3];
#pragma unroll
      for (int c = 0; c < 2; ++c)
#pragma unroll
        for (int tt = 0; tt < 2; ++tt)
          xa[c][tt] = lb0 + 256 * (8 * hh + 4 * tt + q) + 8 * (p & 1) + 16 * ((4 * (2 * wl + c) + 2 * (g & 1) + (p >> 1)) ^ ((q << 2) | (2 * hh + tt)));
#pragma unroll
      for (int v = 0; v < 3; ++v) wa[v] = lb0 + 8192 + 6144 * (g & 1) + 192 * (8 * hh + q) + 16 * ((2 * (wu3 + v) + (p >> 1)) ^ (2 * (g & 1))) + 8 * (p & 1);
      u4 ex[3];
#pragma unroll
      for (int v = 0; v < 3; ++v) ex[v] = *(const u4*)(ws + WS_EX + min(u0 + v, NU - 1) * 1024 + lane * 16);
      f16v acc[6];
#pragma unroll
      for (int i = 0; i < 6; ++i)
#pragma unroll
        for (int e = 0; e < 16; ++e) acc[i][e] = 0.f;
#pragma unroll
      for (int i = 0; i < 6; ++i) asm volatile("" : "+v"(acc[i]));
      rawbar();
      {
        u32 c0, c1, c2;
        asm volatile(
          "ds_read_b64_tr_b16 v[112:113], %6 offset:0\n"
          "ds_read_b64_tr_b16 v[114:115], %7 offset:0\n"
          "ds_read_b64_tr_b16 v[120:121], %10 offset:0\n"
          "ds_read_b64_tr_b16 v[122:123], %10 offset:768\n"
          "ds_read_b64_tr_b16 v[116:117], %8 offset:0\n"
          "ds_read_b64_tr_b16 v[118:119], %9 offset:0\n"
          "ds_read_b64_tr_b16 v[124:125], %11 offset:0\n"
          "ds_read_b64_tr_b16 v[126:127], %11 offset:768\n"
          "ds_read_b64_tr_b16 v[128:129], %12 offset:0\n"
          "ds_read_b64_tr_b16 v[130:131], %12 offset:768\n"
          "s_mov_b32 %13, 16\n"
          "s_mov_b32 %14, 0\n"
          "Lg1%=:\n"
          "s_add_u32 %14, %14, 1\n"
          "s_cmp_eq_u32 %14, 5\n"
          "s_cselect_b32 %15, -81920, %16\n"
          "s_cselect_b32 %14, 0, %14\n"
          "s_waitcnt lgkmcnt(6)\n"
          "v_mfma_f32_32x32x16_bf16 %0, v[120:123], v[112:115], %0\n"
          "ds_read_b64_tr_b16 v[132:133], %6 offset:4096\n"
          "ds_read_b64_tr_b16 v[134:135], %7 offset:4096\n"
          "s_waitcnt lgkmcnt(6)\n"
          "v_mfma_f32_32x32x16_bf16 %1, v[120:123], v[116:119], %1\n"
          "ds_read_b64_tr_b16 v[140:141], %10 offset:3072\n"
          "ds_read_b64_tr_b16 v[142:143], %10 offset:3840\n"
          "s_waitcnt lgkmcnt(6)\n"
          "v_mfma_f32_32x32x16_bf16 %2, v[124:127], v[112:115], %2\n"
          "ds_read_b64_tr_b16 v[136:137], %8 offset:4096\n"
          "ds_read_b64_tr_b16 v[138:139], %9 offset:4096\n"
          "v_mfma_f32_32x32x16_bf16 %3, v[124:127], v[116:119], %3\n"
          "ds_read_b64_tr_b16 v[144:145], %11 offset:3072\n"
          "ds_read_b64_tr_b16 v[146:147], %11 offset:3840\n"
          "s_waitcnt lgkmcnt(8)\n"
          "v_mfma_f32_32x32x16_bf16 %4, v[128:131], v[112:115], %4\n"
          "ds_read_b64_tr_b16 v[148:149], %12 offset:3072\n"
          "ds_read_b64_tr_b16 v[150:151], %12 offset:3840\n"
          "v_add_u32 %6, %15, %6\n"
          "v_add_u32 %7, %15, %7\n"
          "v_add_u32 %8, %15, %8\n"
          "v_add_u32 %9, %15, %9\n"
          "v_mfma_f32_32x32x16_bf16 %5, v[128:131], v[116:119], %5\n"
          "v_add_u32 %10, %15, %10\n"
          "v_add_u32 %11, %15, %11\n"
          "v_add_u32 %12, %15, %12\n"
          "s_barrier\n"
          "s_waitcnt lgkmcnt(6)\n"
          "v_mfma_f32_32x32x16_bf16 %0, v[140:143], v[132:135], %0\n"
          "ds_read_b64_tr_b16 v[112:113], %6 offset:0\n"
          "ds_read_b64_tr_b16 v[114:115], %7 offset:0\n"
          "s_waitcnt lgkmcnt(6)\n"
          "v_mfma_f32_32x32x16_bf16 %1, v[140:143], v[136:139], %1\n"
          "ds_read_b64_tr_b16 v[120:121], %10 offset:0\n"
          "ds_read_b64_tr_b16 v[122:123], %10 offset:768\n"
          "s_waitcnt lgkmcnt(6)\n"
          "v_mfma_f32_32x32x16_bf16 %2, v[144:147], v[132:135], %2\n"
          "ds_read_b64_tr_b16 v[116:117], %8 offset:0\n"
          "ds_read_b64_tr_b16 v[118:119], %9 offset:0\n"
          "v_mfma_f32_32x32x16_bf16 %3, v[144:147], v[136:139], %3\n"
          "ds_read_b64_tr_b16 v[124:125], %11 offset:0\n"
          "ds_read_b64_tr_b16 v[126:127], %11 offset:768\n"
          "s_waitcnt lgkmcnt(8)\n"
          "v_mfma_f32_32x32x16_bf16 %4, v[148:151], v[132:135], %4\n"
          "ds_read_b64_tr_b16 v[128:129], %12 offset:0\n"
          "ds_read_b64_tr_b16 v[130:131], %12 offset:768\n"
          "s_sub_u32 %13, %13, 1\n"
          "s_cmp_lg_u32 %13, 0\n"
          "v_mfma_f32_32x32x16_bf16 %5, v[148:151], v[136:139], %5\n"
          "s_cbranch_scc1 Lg1%=\n"
          "s_waitcnt lgkmcnt(0)\n"
          : "+v"(acc[0]), "+v"(acc[1]), "+v"(acc[2]), "+v"(acc[3]), "+v"(acc[4]), "+v"(acc[5]), "+v"(xa[0][0]), "+v"(xa[0][1]), "+v"(xa[1][0]), "+v"(xa[1][1]),
            "+v"(wa[0]), "+v"(wa[1]), "+v"(wa[2]), "=&s"(c0), "=&s"(c1), "=&s"(c2)
          : "s"(G1_SLOT) : "memory", "scc", "v112", "v113", "v114", "v115", "v116", "v117", "v118", "v119", "v120", "v121", "v122", "v123", "v124", "v125", "v126", "v127", "v128", "v129", "v130", "v131", "v132", "v133", "v134", "v135", "v136", "v137", "v138", "v139", "v140", "v141", "v142", "v143", "v144", "v145", "v146", "v147", "v148", "v149", "v150", "v151");
      }
      u32 sv[2];
#pragma unroll
      for (int c = 0; c < 2; ++c) sv[c] = ((const u32*)(sm + G1_SV))[64 * wl + 32 * c + (lane & 31)];
      bar();
#pragma unroll
      for (int c = 0; c < 2; ++c) {
        const u32 bv = hh ? __builtin_amdgcn_perm(sv[c], sv[c], 0x03020302u) : (__builtin_amdgcn_perm(sv[c], sv[c], 0x01000100u) ^ 0x80008000u);
#pragma unroll
        for (int v = 0; v < 3; ++v) acc[2 * v + c] = mfma32(ex[v], (u4){bv, bv, bv, bv}, acc[2 * v + c]);
      }
      char* hp = ws + WS_HH + (4 * rt + 2 * wl) * HH_T + u0 * 1024 + 16 * lane;
#pragma unroll
      for (int v = 0; v < 3; ++v) {
        if (u0 + v < NU) {
#pragma unroll
          for (int c = 0; c < 2; ++c) {
            const float sd = bhi(sv[c]);
            const u4 o = g1_tile(acc[2 * v + c], __builtin_amdgcn_rcpf(sd), sd);
            char* dst = hp + c * HH_T + v * 1024;
            asm volatile("global_store_dwordx4 %0, %1, off sc1\n\ts_nop 1" :: "v"(dst), "v"(o) : "memory");
          }
        }
      }
      {
        asm volatile("s_waitcnt vmcnt(0)" ::: "memory");
        if (lane == 0 && __hip_atomic_fetch_add((LAS u32*)fl, 1u, __ATOMIC_RELAXED, __HIP_MEMORY_SCOPE_WORKGROUP) == 3u)
          __hip_atomic_fetch_add((u32*)(ws + WS_CNT) + 64 * rt, 1u, __ATOMIC_RELAXED, __HIP_MEMORY_SCOPE_AGENT);
      }
    }
  }
  if (w12 >= 8) {
    asm volatile("s_nop 4");
    fill2(G2_RES, 3, 0);
    fill2(3, G2_RES - 3, 3);
    vmwait<2 * (G2_RES - 3)>();
    rawbar();
    rawbar();
    fill2(0, 3, 0);
    vmwait<0>();
    rawbar();
    rawbar();
    rawbar();
  } else {
    const int wl = w12 & 1, kq = w12 >> 1, dc = 64 * dt + 16 * kq + 4 * hh;
    f4 gb[2], xr[2];
#pragma unroll
    for (int e = 0; e < 2; ++e) {
      gb[e] = *(const f4*)((const float*)(ws + WS_GB) + dc + 8 * e);
      xr[e] = *(const f4*)((const float*)(ws + WS_INV) + dc + 8 * e);
#pragma unroll
      for (int i = 0; i < 4; ++i) xr[e][i] *= b2f(((const u16*)(ws + WS_X1T))[(dc + 8 * e + i) * L + 64 * lt + 32 * wl + (lane & 31)]);
    }
    u32 pa[2];
#pragma unroll
    for (int e = 0; e < 2; ++e)
      pa[e] = lb0 + G2_RES * 2048 * kq + 128 * (4 * hh + q) + 16 * ((((4 * e + 2 * (g & 1)) ^ (2 * kq)) + (p >> 1)) ^ (4 * (q >> 1))) + 8 * (p & 1);
    f16v acc[2];
#pragma unroll
    for (int e = 0; e < 16; ++e) acc[0][e] = acc[1][e] = 0.f;
    asm volatile("" : "+v"(xr[0]), "+v"(xr[1]), "+v"(gb[0]), "+v"(gb[1]), "+v"(acc[0]), "+v"(acc[1]));
    rawbar();
    u4 A[2][2], AR[3][2];
#pragma unroll
    for (int i = 0; i < 6; ++i) AR[i >> 1][i & 1] = cat(trd(pa[i & 1] + (i >> 1) * 2048), trd(pa[i & 1] + (i >> 1) * 2048 + 1024));
    asm volatile("s_waitcnt lgkmcnt(0)\n\ts_barrier" : "+v"(AR[0][0]), "+v"(AR[0][1]), "+v"(AR[1][0]), "+v"(AR[1][1]), "+v"(AR[2][0]), "+v"(AR[2][1]) :: "memory");
    if (w12 == 0) {
      const u32* cnt = (const u32*)(ws + WS_CNT) + 64 * (lt >> 1);
#pragma unroll 1
      for (int spin = 0; spin < (1 << 18); ++spin)
        if (__hip_atomic_load(cnt, __ATOMIC_RELAXED, __HIP_MEMORY_SCOPE_AGENT) >= 15u) break;
    }
    rawbar();
    const rsrc_t rh = mkrsrc(ws + WS_HH + (2 * lt + wl) * HH_T, HH_T);
    constexpr int LEAD = G2_LEAD, NH = LEAD + 2;
    u4 H[NH];
    u32 kh = kfirst(kq);
    auto ldh = [&](auto i) { if constexpr (i < 22) { bld16_sc1<0>(H[i % NH], lane * 16, rh, kh * 1024); kh = knext(kh); } };
    sfor<0, LEAD>(ldh);
    auto rda = [&](auto i) {
#pragma unroll
      for (int e = 0; e < 2; ++e) A[i & 1][e] = cat(trd(pa[e] + i * 2048), trd(pa[e] + i * 2048 + 1024));
    };
    rda(std::integral_constant<int, 0>{});
    sfor<0, 22>([&](auto i) {
      vmwait<(i < 22 - LEAD ? LEAD - 1 : 21 - i)>(H[i % NH]);
      SB();
      if constexpr (i + 1 < G2_RES) rda(std::integral_constant<int, i + 1>{});
      if (i < 21 || kq < 2) {
#pragma unroll
        for (int e = 0; e < 2; ++e) acc[e] = mfma32(i < G2_RES ? A[i & 1][e] : AR[i < G2_RES ? 0 : i - G2_RES][e], H[i % NH], acc[e]);
      }
      if constexpr (i + 1 < G2_RES) { SGB(0x8, 1); SGB(0x100, 2); SGB(0x8, 1); SGB(0x100, 2); }
      SB();
      ldh(std::integral_constant<int, i + LEAD>{});
      if constexpr (i == G2_RES - 1) rawbar();
    });
    f4* red = (f4*)sm + wl * 1536 + lane;
#pragma unroll
    for (int k = 1; k < 4; ++k)
#pragma unroll
      for (int e = 0; e < 2; ++e) {
        const f16v& a = acc[k >> 1];
        const int r = 8 * (k & 1) + 4 * e;
        red[(kq ^ k) * 384 + (k - 1) * 128 + 64 * e] = (f4){a[r], a[r + 1], a[r + 2], a[r + 3]};
      }
    bar();
    u2 o[2];
#pragma unroll
    for (int e = 0; e < 2; ++e) {
      const f4* rp = red + kq * 384 + 64 * e;
      const f4 y = ((f4){acc[0][4 * e], acc[0][4 * e + 1], acc[0][4 * e + 2], acc[0][4 * e + 3]} + rp[0] + rp[128] + rp[256]) * gb[e] + xr[e];
      o[e] = (u2){pk(y[0], y[1]), pk(y[2], y[3])};
    }
    const auto r0 = __builtin_amdgcn_permlane32_swap(o[0].x, o[1].x, false, false);
    const auto r1 = __builtin_amdgcn_permlane32_swap(o[0].y, o[1].y, false, false);
    *(u4*)(out + (64 * lt + 32 * wl + (lane & 31)) * D + 64 * dt + 16 * kq + 8 * hh) = (u4){r0[0], r1[0], r0[1], r1[1]};
  }
}

extern "C" void kernel_launch(void* const* d_in, const int* in_sizes, int n_in, void* d_out, int out_size, void* d_ws, size_t ws_size,
                              hipStream_t stream) {
  const u16 *wg = (const u16*)d_in[8], *wu = (const u16*)d_in[9], *wd = (const u16*)d_in[10];
  char* ws = (char*)d_ws;
  (void)hipFuncSetAttribute((const void*)k1_conv, hipFuncAttributeMaxDynamicSharedMemorySize, K1_LDS);
  (void)hipFuncSetAttribute((const void*)k3_mlp, hipFuncAttributeMaxDynamicSharedMemorySize, K3_LDS);
  k1_conv<<<256, 512, K1_LDS, stream>>>((const u16*)d_in[0], (const u16*)d_in[1], (const u16*)d_in[2], (const u16*)d_in[3], (const u16*)d_in[4], (const u16*)d_in[5],
                                        (const u16*)d_in[6], (const u16*)d_in[11], (const u16*)d_in[12], wg, wu, wd, ws);
  k3_mlp<<<256, 768, K3_LDS, stream>>>(wg, wu, wd, ws, (u16*)d_out);
}
```

```cpp
#include <hip/hip_runtime.h>
#include <hip/hip_bf16.h>
#include <stdint.h>
#include <type_traits>

typedef unsigned short u16;
typedef unsigned u32;
typedef float f2 __attribute__((ext_vector_type(2)));
typedef float f4 __attribute__((ext_vector_type(4)));
typedef float f16v __attribute__((ext_vector_type(16)));
typedef u32 u2 __attribute__((ext_vector_type(2)));
typedef u32 u4 __attribute__((ext_vector_type(4)));
typedef u4 u4a8 __attribute__((aligned(8)));
typedef short s4 __attribute__((ext_vector_type(4)));
typedef __bf16 bf2 __attribute__((ext_vector_type(2)));
typedef __bf16 bf8 __attribute__((ext_vector_type(8)));
typedef _Float16 h2 __attribute__((ext_vector_type(2)));
typedef _Float16 h8 __attribute__((ext_vector_type(8)));
#define LAS __attribute__((address_space(3)))
#define DI __device__ __forceinline__

constexpr int L = 2048, D = 512, NM = 128, DF = 1376, NU = 86;

constexpr size_t WS_GB = 0, WS_INV = 2 << 10, WS_EPOCH = 4 << 10, WS_ST2 = 8 << 10, WS_CNT = 16 << 10, WS_ST1 = 32 << 10, WS_EX = 64 << 10,
                 WS_FX = 20 << 10, WS_MG = 160 << 10, WS_X1T = 256 << 10, WS_HH = WS_X1T + (2 << 20), WS_XT = WS_HH + (5504 << 10);

template <class T> DI T ldnt(const T* p) { return __builtin_nontemporal_load(p); }
DI float blo(u32 v) { return __uint_as_float(v << 16); }
DI float bhi(u32 v) { return __uint_as_float(v & 0xffff0000u); }
DI float b2f(u16 v) { return __uint_as_float(((u32)v) << 16); }
DI u32 pk(float a, float b) { return __builtin_bit_cast(u32, __builtin_convertvector((f2){a, b}, bf2)); }
DI u32 pkh(f2 v) { return __builtin_bit_cast(u32, __builtin_convertvector(v, h2)); }
DI float rb(float a) { return blo(pk(a, a)); }
DI u2 trd(const char* p) { return __builtin_bit_cast(u2, __builtin_amdgcn_ds_read_tr16_b64_v4i16((LAS s4*)p)); }
DI u2 trd(u32 a) { return __builtin_bit_cast(u2, __builtin_amdgcn_ds_read_tr16_b64_v4i16((LAS s4*)(size_t)a)); }
DI u4 cat(u2 a, u2 b) { return (u4){a.x, a.y, b.x, b.y}; }
DI float dot2(u32 a, u32 b, float c) { return __builtin_amdgcn_fdot2_f32_bf16(__builtin_bit_cast(bf2, a), __builtin_bit_cast(bf2, b), c, false); }
DI f4 mfma16(u4 a, u4 b, f4 c) { return __builtin_amdgcn_mfma_f32_16x16x32_bf16(__builtin_bit_cast(bf8, a), __builtin_bit_cast(bf8, b), c, 0, 0, 0); }
DI f4 mfma16h(u4 a, u4 b, f4 c) { return __builtin_amdgcn_mfma_f32_16x16x32_f16(__builtin_bit_cast(h8, a), __builtin_bit_cast(h8, b), c, 0, 0, 0); }
DI f16v mfma32(u4 a, u4 b, f16v c) { return __builtin_amdgcn_mfma_f32_32x32x16_bf16(__builtin_bit_cast(bf8, a), __builtin_bit_cast(bf8, b), c, 0, 0, 0); }
#define CM_MUL(d, a, b) "v_pk_mul_f32 " d ", " a ", " b " op_sel_hi:[0,1]\n\t"
#define CM_FMA(d, a, b) "v_pk_fma_f32 " d ", " a ", " b ", " d " op_sel:[1,1,0] op_sel_hi:[1,0,1] neg_lo:[0,1,0]\n\t"
DI void cmul2(f2& r0, f2 a0, f2 b0, f2& r1, f2 a1, f2 b1) {
  asm(CM_MUL("%0", "%2", "%3") CM_MUL("%1", "%4", "%5") CM_FMA("%0", "%2", "%3") CM_FMA("%1", "%4", "%5")
      : "=&v"(r0), "=&v"(r1) : "v"(a0), "v"(b0), "v"(a1), "v"(b1));
}
DI void cmul3(f2& r0, f2 a0, f2 b0, f2& r1, f2 a1, f2 b1, f2& r2, f2 a2, f2 b2) {
  asm(CM_MUL("%0", "%3", "%4") CM_MUL("%1", "%5", "%6") CM_MUL("%2", "%7", "%8") CM_FMA("%0", "%3", "%4") CM_FMA("%1", "%5", "%6") CM_FMA("%2", "%7", "%8")
      : "=&v"(r0), "=&v"(r1), "=&v"(r2) : "v"(a0), "v"(b0), "v"(a1), "v"(b1), "v"(a2), "v"(b2));
}
DI void cmul4(f2& r0, f2& r1, f2& r2, f2& r3, f2 a0, f2 a1, f2 a2, f2 a3, f2 b) {
  asm(CM_MUL("%0", "%4", "%8") CM_MUL("%1", "%5", "%8") CM_MUL("%2", "%6", "%8") CM_MUL("%3", "%7", "%8")
      CM_FMA("%0", "%4", "%8") CM_FMA("%1", "%5", "%8") CM_FMA("%2", "%6", "%8") CM_FMA("%3", "%7", "%8")
      : "=&v"(r0), "=&v"(r1), "=&v"(r2), "=&v"(r3) : "v"(a0), "v"(a1), "v"(a2), "v"(a3), "v"(b));
}
DI float exp_acc(float x) {
  const float t = x * 1.44269502f, r = fmaf(x, 1.44269502f, -t) + x * 1.92596299e-8f, e = __builtin_amdgcn_exp2f(t);
  return fmaf(e, r * 0.693147182f, e);
}
template <int CTRL> DI float dppf(float v) { return __int_as_float(__builtin_amdgcn_update_dpp(0, __float_as_int(v), CTRL, 0xf, 0xf, true)); }
DI float sum32(float v) {
  auto r = __builtin_amdgcn_permlane32_swap(__float_as_uint(v), __float_as_uint(v), false, false);
  return __uint_as_float(r[0]) + __uint_as_float(r[1]);
}
DI f2 ln_stats(float s1, float s2) {
  const float mean = s1 * (1.f / D);
  const float var = fmaxf(fmaf(-mean, mean, s2 * (1.f / D)), 0.f);
  return (f2){rb(mean), rb(__builtin_amdgcn_sqrtf(rb(rb(var) + 1e-5f)))};
}
typedef __amdgpu_buffer_rsrc_t rsrc_t;
DI rsrc_t mkrsrc(const void* p, u32 bytes) { return __builtin_amdgcn_make_buffer_rsrc((void*)p, 0, bytes, 0x00020000); }
DI u32 ldsaddr(const char* p) { return (u32)(size_t)(LAS const char*)p; }
template <int OFF> DI void glds16(u32 m0v, u32 voff, rsrc_t rs, u32 soff) {
  asm volatile("s_mov_b32 m0, %0\n\ts_nop 0\n\tbuffer_load_dwordx4 %1, %2, %3 offen offset:%4 lds" :: "s"(m0v), "v"(voff), "s"(rs), "s"(soff), "n"(OFF) : "memory");
}
DI void glds16x4(u32 m0v, u32 voff, rsrc_t rs, u32 soff) {
  asm volatile("s_mov_b32 m0, %0\n\ts_nop 0\n\tbuffer_load_dwordx4 %1, %2, %3 offen lds\n\tbuffer_load_dwordx4 %1, %2, %3 offen offset:1024 lds\n\t"
               "buffer_load_dwordx4 %1, %2, %3 offen offset:2048 lds\n\tbuffer_load_dwordx4 %1, %2, %3 offen offset:3072 lds"
               :: "s"(m0v), "v"(voff), "s"(rs), "s"(soff) : "memory");
}
template <int OFF> DI void bld16(u4& d, u32 voff, rsrc_t rs, u32 soff) {
  asm volatile("buffer_load_dwordx4 %0, %1, %2, %3 offen offset:%4" : "=v"(d) : "v"(voff), "s"(rs), "s"(soff), "n"(OFF));
}
template <int OFF> DI void bld16_sc1(u4& d, u32 voff, rsrc_t rs, u32 soff) {
  asm volatile("buffer_load_dwordx4 %0, %1, %2, %3 offen offset:%4 sc1" : "=v"(d) : "v"(voff), "s"(rs), "s"(soff), "n"(OFF));
}
template <int OFF, class T> DI void gld16(T& d, const void* p) { asm volatile("global_load_dwordx4 %0, %1, off offset:%2" : "=v"(d) : "v"(p), "n"(OFF)); }
DI void pin(u32& v) { asm("" : "+v"(v)); }
DI void rawbar() { asm volatile("s_barrier" ::: "memory"); }
template <int N> DI void vmwait() { asm volatile("s_waitcnt vmcnt(%0)" :: "n"(N) : "memory"); }
template <int N> DI void vmwait(u4& d) { asm volatile("s_waitcnt vmcnt(%1)" : "+v"(d) : "n"(N)); }
DI void bar() { asm volatile("s_waitcnt lgkmcnt(0)\n\ts_barrier" ::: "memory"); }
#define SB() __builtin_amdgcn_sched_barrier(0)
#define SGB(m, n) __builtin_amdgcn_sched_group_barrier(m, n, 0)
template <int I, int N, class F> DI void sfor(F&& f) { if constexpr (I < N) { f(std::integral_constant<int, I>{}); sfor<I + 1, N>(f); } }

typedef unsigned long long u64;
constexpr int KG_ROW = 544, KG_CH = 57 * KG_ROW, KG_T = 2 * KG_CH, KG_TS = 4160;
constexpr int CV_EX = 0, CV_K = KG_T + 2 * KG_TS, CV_KC = 4128, CV_H = CV_K + 8 * CV_KC, CV_HP = 1152, CV_X = CV_H + 8 * CV_HP, CV_ST = CV_X + 8192,
              CV_W = CV_ST + 8192, CV_TB = CV_W + 16384, CV_V = CV_TB + 9216, CV_M = CV_V + 1024, CV_FL = CV_M + 256, K1_LDS = CV_FL + 128;
enum { FL_EPOCH = 0, FL_EPOK = 2, FL_KG = 3, FL_MOD = 7, FL_MAIN = 8, FL_XT = 9, FL_XD = 10, FL_GO = 11, FL_EXCH = 12, FL_RDY = 16 };
typedef volatile LAS u32* ldsw;
DI void lds_arrive(ldsw w) {
  asm volatile("s_waitcnt lgkmcnt(0)" ::: "memory");
  if ((threadIdx.x & 63) == 0) __hip_atomic_fetch_add((LAS u32*)w, 1u, __ATOMIC_RELAXED, __HIP_MEMORY_SCOPE_WORKGROUP);
}
DI void lds_wait(ldsw w, u32 n) { while (*w < n) {} asm volatile("" ::: "memory"); }
DI u64 get_epoch(ldsw fl) { lds_wait(fl + FL_EPOK, 1u); return ((u64)fl[FL_EPOCH + 1] << 32) | fl[FL_EPOCH]; }
DI u4 ld16_sc1(const void* p) { u4 d; asm volatile("global_load_dwordx4 %0, %1, off sc1\n\ts_waitcnt vmcnt(0)" : "=v"(d) : "v"(p) : "memory"); return d; }
template <int OFF> DI void gld16_sc1(u4& d, const void* p) { asm volatile("global_load_dwordx4 %0, %1, off offset:%2 sc1" : "=v"(d) : "v"(p), "n"(OFF) : "memory"); }
DI bool mg_ok(u4 g, u64 ep) { const u32 h = (u32)(ep >> 8), l = (u32)ep << 24; return g.y == h && g.w == h && (g.x & 0xff000000u) == l && (g.z & 0xff000000u) == l; }
DI float mg_sum(u4 g) { return __uint_as_float(g.x << 8) + __uint_as_float(g.z << 8); }
DI float scale_c(float v) { v = rb(1.f + rb(v)); return v == 0.f ? 0x1p-60f : v; }

DI f2 cexp_pow2(float a, float fh, float fl, float p) {
  const float e = __builtin_amdgcn_exp2f(a * p);
  const float r = fmaf(fl, p, __builtin_amdgcn_fractf(fh * p));
  return (f2){e * __builtin_amdgcn_cosf(r), e * __builtin_amdgcn_sinf(r)};
}

DI void k1_help(char* sm, int b, const u16* lar, const u16* aim, const u16* cre, const u16* cim, const u16* ldt, const u16* dsk,
                const u16* wg, const u16* wu, const u16* wd, char* ws) {
  const int t = threadIdx.x - 256, w = __builtin_amdgcn_readfirstlane(t >> 6), lane = t & 63, ch = w >> 1, half = w & 1;
  const int d0 = 64 * (b & 7) + 2 * (b >> 3), d = d0 + ch, n = 64 * half + lane;
  const ldsw fl = (ldsw)(size_t)(ldsaddr(sm) + CV_FL);
  const u32 v_lar = ldnt(lar + d * NM + n), v_aim = ldnt(aim + d * NM + n);
  const u32 cr = ldnt(cre + d * NM + n), ci = ldnt(cim + d * NM + n), cr2 = ldnt(cre + d * NM + (n ^ 64)), ci2 = ldnt(cim + d * NM + (n ^ 64));
  const u16 v_ldt = ldt[d], v_dsk = dsk[d];
  __builtin_amdgcn_s_setprio(1);
  rawbar();
  const float dt = rb(exp_acc(b2f(v_ldt)));
  const float ds = b2f(v_dsk);
  const float zr = -rb(exp_acc(blo(v_lar))) * dt, zi = blo(v_aim) * dt;
  const float a = zr * 1.44269502f;
  const float fh = zi * 0.159154937f, fq = fmaf(zi, 0.159154937f, -fh) + zi * 6.42063833e-9f;
  const f2 e1 = cexp_pow2(a, fh, fq, 1.f), e8 = cexp_pow2(a, -fh, -fq, 8.f), e128 = cexp_pow2(a, fh, fq, 128.f), e1024 = cexp_pow2(a, fh, fq, 1024.f);
  int mx = (int)max(max(cr & 0x7fffu, ci & 0x7fffu), max(cr2 & 0x7fffu, ci2 & 0x7fffu));
  mx = max(mx, __builtin_amdgcn_update_dpp(0, mx, 0x111, 0xf, 0xf, true));
  mx = max(mx, __builtin_amdgcn_update_dpp(0, mx, 0x112, 0xf, 0xf, true));
  mx = max(mx, __builtin_amdgcn_update_dpp(0, mx, 0x114, 0xf, 0xf, true));
  mx = max(mx, __builtin_amdgcn_update_dpp(0, mx, 0x118, 0xf, 0xf, true));
  mx = max(max(__builtin_amdgcn_readlane(mx, 15), __builtin_amdgcn_readlane(mx, 31)), max(__builtin_amdgcn_readlane(mx, 47), __builtin_amdgcn_readlane(mx, 63)));
  const int ex = min(max(mx >> 7, 1), 253);
  const float cs = __int_as_float((254 - ex) << 23), kf = 2.f * dt * __int_as_float(ex << 23);
  float dsv = ds, kfv = kf, fhv = fh, av = a, crv = blo(cr) * cs, civ = -blo(ci) * cs;
  asm volatile("" : "+v"(dsv), "+v"(kfv), "+v"(fhv), "+v"(av), "+v"(crv), "+v"(civ));
  char* base = sm + ch * KG_CH + n * 4;
  auto put = [&](int row, f2 v) { *(u32*)(base + row * KG_ROW) = pkh(v); };
  f2 lo[8], e[8], m[8], x0, x1, x2, x3, e16, e32, e64;
  lo[0] = (f2){crv, civ};
  e[1] = e1; m[1] = e128;
  put(0, lo[0]); put(16, (f2){1.f, 0.f}); put(17, e1); put(24, e128); put(48, e1024);
  cmul3(e16, e8, e8, e[2], e1, e1, m[2], e128, e128);
  cmul2(lo[1], lo[0], e8, x0, e1024, e1024);
  put(18, e[2]); put(32, m[2]); put(1, lo[1]); put(56, x0);
  cmul3(e32, e16, e16, e[4], e[2], e[2], m[4], m[2], m[2]);
  cmul2(e[3], e[2], e1, m[3], m[2], e128);
  cmul2(lo[2], lo[0], e16, lo[3], lo[1], e16);
  put(20, e[4]); put(40, m[4]); put(19, e[3]); put(36, m[3]); put(2, lo[2]); put(3, lo[3]);
  cmul4(e[5], e[6], e[7], x0, e1, e[2], e[3], m[4], e[4]);
  put(21, e[5]); put(22, e[6]); put(23, e[7]); put(41, x0);
  cmul3(m[5], m[4], e128, m[6], m[4], m[2], m[7], m[4], m[3]);
  put(42, m[5]); put(44, m[6]); put(46, m[7]);
  cmul4(lo[4], lo[5], lo[6], lo[7], lo[0], lo[1], lo[2], lo[3], e32);
  put(4, lo[4]); put(5, lo[5]); put(6, lo[6]); put(7, lo[7]);
  cmul2(e64, e32, e32, x0, e128, e1);
  put(25, x0);
  cmul3(x0, m[5], e[4], x1, m[6], e[4], x2, m[7], e[4]);
  put(43, x0); put(45, x1); put(47, x2);
  cmul4(x0, x1, x2, x3, lo[0], lo[1], lo[2], lo[3], e64);
  put(8, x0); put(9, x1); put(10, x2); put(11, x3);
  cmul4(x0, x1, x2, x3, lo[4], lo[5], lo[6], lo[7], e64);
  put(12, x0); put(13, x1); put(14, x2); put(15, x3);
  cmul4(x0, x1, x2, x3, e[2], e[3], e[4], e[5], e128);
  put(26, x0); put(27, x1); put(28, x2); put(29, x3);
  cmul2(x0, e[6], e128, x1, e[7], e128);
  put(30, x0); put(31, x1);
  cmul3(x0, e[2], m[2], x1, e[4], m[2], x2, e[6], m[2]);
  put(33, x0); put(34, x1); put(35, x2);
  cmul3(x0, e[2], m[3], x1, e[4], m[3], x2, e[6], m[3]);
  put(37, x0); put(38, x1); put(39, x2);
  cmul4(x0, x1, x2, x3, m[1], m[2], m[3], m[4], e1024);
  put(49, x0); put(50, x1); put(51, x2); put(52, x3);
  cmul3(x0, m[5], e1024, x1, m[6], e1024, x2, m[7], e1024);
  put(53, x0); put(54, x1); put(55, x2);
  lds_arrive(fl + FL_KG + ch);
  const int task = b + 256 * w, kq = task & 3, blk = task >> 2, mat = blk >= 43, jb = blk - 43 * mat;
  const bool work = task < 344;
  if (work) {
    const rsrc_t rw = mkrsrc(mat ? wu : wg, D * DF * 2);
    const u32 vo = (128 * kq + (lane >> 2)) * (DF * 2) + 64 * jb + 16 * (lane & 3);
    asm volatile("s_nop 4");
#pragma unroll
    for (int i = 0; i < 8; ++i) glds16<0>(ldsaddr(sm) + CV_W + 8192 * w + 1024 * i, vo, rw, i * 16 * DF * 2);
  }
  u32 early = 0;
  if (w == 3) asm volatile("global_load_dword %0, %1, off" : "=v"(early) : "v"((const char*)wd + b * 5504 + min(lane, 42) * 128));

  lds_wait(fl + FL_KG + ch, 2u);
  {
    const int c = lane & 15, g = lane >> 4;
    const char* fb = sm + ch * KG_CH + c * KG_ROW + g * 16;
    u16* T = (u16*)(sm + KG_T + ch * KG_TS);
    f4 acc0 = {0.f, 0.f, 0.f, 0.f}, acc1 = acc0;
    u4 B[8], A[8];
#pragma unroll
    for (int s = 0; s < 8; ++s) { B[s] = *(const u4*)(fb + s * 64); A[s] = *(const u4*)(fb + (16 + 16 * half) * KG_ROW + s * 64); }
    if (!half) {
      u4 A2[8];
#pragma unroll
      for (int s = 0; s < 8; ++s) A2[s] = *(const u4*)(fb + 48 * KG_ROW + s * 64);
      SB();
#pragma unroll
      for (int s = 0; s < 8; ++s) acc0 = mfma16h(A[s], B[s], acc0);
#pragma unroll
      for (int s = 0; s < 8; ++s) acc1 = mfma16h(A2[s], B[s], acc1);
      acc0 *= kfv; acc1 *= kfv;
      if (lane == 0) acc0[0] += dsv;
      *(u2*)(T + 8 * c + 4 * (g & 1) + 128 * (g >> 1)) = (u2){pk(acc0[0], acc0[1]), pk(acc0[2], acc0[3])};
      if (g < 2) {
#pragma unroll
        for (int e = 0; e < 4; ++e) T[1024 + 128 * (4 * g + e) + 8 * c] = (u16)pk(acc1[e], 0.f);
      } else if (lane == 32) T[2048] = (u16)pk(acc1[0], 0.f);
    } else {
      SB();
#pragma unroll
      for (int s = 0; s < 8; ++s) acc0 = mfma16h(A[s], B[s], acc0);
      acc0 *= kfv;
#pragma unroll
      for (int e = 0; e < 4; ++e) {
        const int p = g < 2 ? 256 + 128 * g + 2 * e : 512 + 128 * (2 * (g - 2) + (e >> 1)) + 4 * (e & 1);
        T[p + 8 * c] = (u16)pk(acc0[e], 0.f);
      }
    }
  }
  lds_arrive(fl + FL_KG + 2 + ch);
  constexpr u32 KEEP = 0x03020100u, LL = 0x01000100u, LH = 0x05040100u, HH = 0x05040504u;
  const u32 sel0 = n < 16 ? KEEP : LL, sel1 = n < 16 ? KEEP : n < 64 ? LH : LL, sel2 = n < 16 ? KEEP : n < 64 ? LL : LH, sel2b = n < 64 ? sel2 : HH;
  const u32 sel3 = n < 16 ? KEEP : n < 32 ? LH : HH, selp2 = n <= 16 ? KEEP : n <= 64 ? LL : HH, selp3 = n <= 16 ? KEEP : n <= 32 ? LH : HH;
  if (t < 16) *(u4*)(sm + CV_K + (t >> 1) * CV_KC + 16 * (t & 1)) = (u4){0u, 0u, 0u, 0u};
  lds_wait(fl + FL_KG + 2 + ch, 2u);
  {
    const u16* T = (const u16*)(sm + KG_T + ch * KG_TS) + 16 * n;
    const u4 v0 = *(const u4*)T, v1 = *(const u4*)(T + 8);
    const u2 vp = *(const u2*)(T - 4);
    const u32 e16 = T[16];
    u32 k[10];
    k[0] = n ? __builtin_amdgcn_perm(v0.x, vp.x, selp2) : 0u;
    k[1] = n ? __builtin_amdgcn_perm(v0.x, vp.y, selp3) : 0u;
    k[2] = __builtin_amdgcn_perm(v0.x, v0.x, sel0); k[3] = __builtin_amdgcn_perm(v0.z, n < 32 ? v0.y : v0.x, sel1);
    k[4] = __builtin_amdgcn_perm(v1.x, n < 64 ? v0.z : v0.x, sel2); k[5] = __builtin_amdgcn_perm(v1.x, v0.w, sel3);
    k[6] = __builtin_amdgcn_perm(v1.x, v1.x, sel0); k[7] = __builtin_amdgcn_perm(v1.z, n < 32 ? v1.y : v1.x, sel1);
    k[8] = __builtin_amdgcn_perm(e16, v1.z, sel2b); k[9] = __builtin_amdgcn_perm(e16, v1.w, sel3);
    u32 o[9];
#pragma unroll
    for (int i = 0; i < 9; ++i) o[i] = __builtin_amdgcn_alignbit(k[i + 1], k[i], 16);
    char* kd = sm + CV_K + 4 * ch * CV_KC + 32 + 32 * n;
#pragma unroll
    for (int i = 0; i < 2; ++i) {
      *(u4*)(kd + 16 * i) = (u4){k[4 * i + 2], k[4 * i + 3], k[4 * i + 4], k[4 * i + 5]};
      *(u4*)(kd + 16 * i + CV_KC) = (u4){o[4 * i + 1], o[4 * i + 2], o[4 * i + 3], o[4 * i + 4]};
      *(u4*)(kd + 16 * i + 2 * CV_KC) = (u4){k[4 * i + 1], k[4 * i + 2], k[4 * i + 3], k[4 * i + 4]};
      *(u4*)(kd + 16 * i + 3 * CV_KC) = (u4){o[4 * i], o[4 * i + 1], o[4 * i + 2], o[4 * i + 3]};
    }
  }
  __builtin_amdgcn_s_setprio(0);
  lds_arrive(fl + FL_RDY);
  asm volatile("s_waitcnt vmcnt(0)" : "+v"(early));
  if (w >= 2) {
    while (fl[FL_GO] == 0u) __builtin_amdgcn_s_sleep(2);
    u32 dummy[2];
#pragma unroll
    for (int k = 0; k < 2; ++k) {
      const int wk = 2 * (w - 2) + k, row = 32 * (b >> 3) + 8 * wk + (lane >> 3);
      const char* base = (const char*)(row >= D ? wu : wg) + (row & (D - 1)) * (DF * 2);
#pragma unroll
      for (int i = 0; i < 2; ++i) {
        const int off = min((b & 1) * 1344 + 128 * ((lane & 7) + 8 * i), (b & 1) ? DF * 2 - 4 : 1340);
        asm volatile("global_load_dword %0, %1, off" : "=v"(dummy[i]) : "v"(base + off));
      }
      asm volatile("s_waitcnt vmcnt(0)" : "+v"(dummy[0]), "+v"(dummy[1]));
    }
#pragma unroll
    for (int k = 0; k < 2; ++k) {
#pragma unroll
      for (int i = 0; i < 2; ++i)
        asm volatile("global_load_dword %0, %1, off" : "=v"(dummy[i]) : "v"((const char*)wd + (b >> 3) * 44032 + (2 * (w - 2) + k) * 11008 + min(lane + 64 * i, 85) * 128));
      asm volatile("s_waitcnt vmcnt(0)" : "+v"(dummy[0]), "+v"(dummy[1]));
    }
  }
  if (work) {
    const u64 ep = get_epoch(fl);
    const char* gp = ws + WS_MG + (3 * D + 128 * kq + 2 * lane) * 32;
    u4 gs[4], gc[4];
#pragma unroll 1
    for (int spin = 0; spin < (1 << 16); ++spin) {
      sfor<0, 4>([&](auto i) { gld16_sc1<16 * i>(gs[i], gp); gld16_sc1<16 * i>(gc[i], gp + D * 32); });
      asm volatile("s_waitcnt vmcnt(0)" : "+v"(gs[0]), "+v"(gs[1]), "+v"(gs[2]), "+v"(gs[3]), "+v"(gc[0]), "+v"(gc[1]), "+v"(gc[2]), "+v"(gc[3]) :: "memory");
      bool ok = true;
#pragma unroll
      for (int i = 0; i < 4; ++i) ok = ok && mg_ok(gs[i], ep) && mg_ok(gc[i], ep);
      if (__all(ok)) break;
      __builtin_amdgcn_s_sleep(8);
    }
    u32* vv = (u32*)(sm + CV_V + 512 * w);
    vv[lane] = pk(scale_c(mg_sum(gc[0]) + mg_sum(gc[1])), scale_c(mg_sum(gc[2]) + mg_sum(gc[3])));
    vv[64 + lane] = pk(mg_sum(gs[0]) + mg_sum(gs[1]), mg_sum(gs[2]) + mg_sum(gs[3]));
    const int hh = lane >> 5;
    const u32 ra = ldsaddr(sm) + CV_W + 8192 * w + 64 * (8 * hh + ((lane & 15) >> 2)) + 32 * ((lane >> 4) & 1) + 8 * (lane & 3);
    f16v sums;
#pragma unroll
    for (int e = 0; e < 16; ++e) sums[e] = 0.f;
#pragma unroll
    for (int i = 0; i < 8; ++i) sums = mfma32(*(const u4*)(vv + 64 * (lane & 1) + 8 * i + 4 * hh), cat(trd(ra + 1024 * i), trd(ra + 1024 * i + 256)), sums);
    if (lane < 32) {
      u32* exf = (u32*)(ws + WS_EX) + ((2 * jb + (lane >> 4)) * 64 + 16 * mat + (lane & 15)) * 4 + kq;
      const float h0 = rb(sums[0]), h1 = rb(sums[1]);
      exf[0] = pk(h0, sums[0] - h0);
      exf[128] = pk(h1, sums[1] - h1);
    }
  }
}

DI float red2(float a, float b, bool rows16) {
  if (rows16) { const auto r = __builtin_amdgcn_permlane16_swap(__float_as_uint(a), __float_as_uint(b), false, false); return __uint_as_float(r[0]) + __uint_as_float(r[1]); }
  const auto r = __builtin_amdgcn_permlane32_swap(__float_as_uint(a), __float_as_uint(b), false, false);
  return __uint_as_float(r[0]) + __uint_as_float(r[1]);
}

DI void k1_main(char* sm, int b, const u16* x, const u16* c, const u16* wm, char* ws) {
  const int t = threadIdx.x, w = __builtin_amdgcn_readfirstlane(t >> 6), lane = t & 63, ch = w >> 1, half = w & 1;
  const int d0 = 64 * (b & 7) + 2 * (b >> 3), d = d0 + ch;
  const ldsw fl = (ldsw)(size_t)(ldsaddr(sm) + CV_FL);
  const int c16 = lane & 15, g = lane >> 4;
  __builtin_amdgcn_s_setprio(2);
  if (t < 20) fl[t] = 0u;
  if (w == 3) *(u4*)(sm + CV_H + (lane >> 3) * CV_HP + 16 * (t & 7)) = (u4){0u, 0u, 0u, 0u};
  asm volatile("s_waitcnt lgkmcnt(0)" ::: "memory");
  if (w < 2) {
    const int row = 8 * b + 4 * w + g, mq = (b / 48) & 3, mc = b % 48, rg = t >> 3;
    u4 xs[4], wv[8], cv;
    u64 ep0;
    asm volatile("global_load_dwordx2 %0, %1, %2 sc1" : "=v"(ep0) : "v"(0), "s"(ws + WS_EPOCH));
#pragma unroll
    for (int i = 0; i < 4; ++i) xs[i] = ldnt((const u4*)(x + row * D + 128 * i + 8 * c16));
    SB();
    cv = *(const u4*)(c + 128 * mq + 8 * rg);
#pragma unroll
    for (int i = 0; i < 8; ++i) wv[i] = ldnt((const u4*)(wm + (128 * mq + 8 * rg + i) * (6 * D) + 64 * mc + 8 * (t & 7)));
    SB();
    rawbar();
    SB();
    float s1 = 0.f, s2 = 0.f, s3 = 0.f, s4 = 0.f;
#pragma unroll
    for (int i = 0; i < 4; ++i)
#pragma unroll
      for (int j = 0; j < 4; j += 2) {
        s1 = dot2(xs[i][j], 0x3f803f80u, s1); s2 = dot2(xs[i][j], xs[i][j], s2);
        s3 = dot2(xs[i][j + 1], 0x3f803f80u, s3); s4 = dot2(xs[i][j + 1], xs[i][j + 1], s4);
      }
    s1 += s3; s2 += s4;
    asm volatile("" : "+v"(ep0), "+v"(s1));
    if (w == 0) {
      fl[FL_EPOCH] = (u32)(ep0 + 1); fl[FL_EPOCH + 1] = (u32)((ep0 + 1) >> 32);
      asm volatile("s_waitcnt lgkmcnt(0)" ::: "memory");
      fl[FL_EPOK] = 1u;
    }
    s1 += dppf<0xb1>(s1); s2 += dppf<0xb1>(s2); s1 += dppf<0x4e>(s1); s2 += dppf<0x4e>(s2);
    s1 += dppf<0x141>(s1); s2 += dppf<0x141>(s2); s1 += dppf<0x140>(s1); s2 += dppf<0x140>(s2);
    const f2 st = ln_stats(s1, s2);
    const u64 ep = ep0 + 1;
    if (c16 == 0) {
      __hip_atomic_store((u64*)(ws + WS_ST1) + row, (ep << 32) | (__float_as_uint(st.x) >> 16) | __float_as_uint(st.y), __ATOMIC_RELAXED, __HIP_MEMORY_SCOPE_AGENT);
      ((u32*)(ws + WS_ST2))[row] = 0u;
    }
    if (t == 0 && b < 16) ((u32*)(ws + WS_CNT))[64 * b] = 0u;
    if (b < 192) {
      float acc[8] = {0.f, 0.f, 0.f, 0.f, 0.f, 0.f, 0.f, 0.f};
#pragma unroll
      for (int i = 0; i < 4; ++i) {
        const float c0 = blo(cv[i]), c1 = bhi(cv[i]);
        const u32 sp = pk(c0 * __builtin_amdgcn_rcpf(1.f + __expf(-c0)), c1 * __builtin_amdgcn_rcpf(1.f + __expf(-c1)));
#pragma unroll
        for (int j = 0; j < 4; ++j) {
          acc[2 * j] = dot2(__builtin_amdgcn_perm(wv[2 * i + 1][j], wv[2 * i][j], 0x05040100u), sp, acc[2 * j]);
          acc[2 * j + 1] = dot2(__builtin_amdgcn_perm(wv[2 * i + 1][j], wv[2 * i][j], 0x07060302u), sp, acc[2 * j + 1]);
        }
      }
      const float p0 = red2(red2(acc[0], acc[4], false), red2(acc[2], acc[6], false), true), p1 = red2(red2(acc[1], acc[5], false), red2(acc[3], acc[7], false), true);
      const float q0 = p0 + dppf<0x128>(p0), q1 = p1 + dppf<0x128>(p1);
      float v = (lane & 8) ? q1 : q0;
      float* mx = (float*)(sm + CV_M);
      if (w == 1) { mx[lane] = v; lds_arrive(fl + FL_MOD); }
      else {
        lds_wait(fl + FL_MOD, 1u);
        v += mx[lane];
        const u32 bits = __float_as_uint(v) + 0x80u;
        __hip_atomic_store((u64*)(ws + WS_MG) + (64 * mc + 8 * (lane & 7) + (lane >> 3)) * 4 + mq, (ep << 24) | (bits >> 8), __ATOMIC_RELAXED, __HIP_MEMORY_SCOPE_AGENT);
      }
    }
  } else {
    const int tg = t - 128, r0 = 64 * (b >> 3), c0 = 64 * (b & 7), q = c16 >> 2, p = lane & 3;
    u4 v[4];
#pragma unroll
    for (int i = 0; i < 4; ++i) v[i] = ldnt((const u4*)(x + (r0 + 16 * i + (tg >> 3)) * D + c0 + 8 * (tg & 7)));
    rawbar();
    char* tb = sm + CV_TB;
#pragma unroll
    for (int i = 0; i < 4; ++i) *(u4*)(tb + (16 * i + (tg >> 3)) * 144 + 16 * (tg & 7)) = v[i];
    lds_arrive(fl + FL_XT);
    lds_wait(fl + FL_XT, 2u);
#pragma unroll
    for (int i = 0; i < 4; ++i) {
      const char* a = tb + (32 * (i & 1) + 8 * g + q) * 144 + 64 * half + 32 * (i >> 1) + 8 * p;
      const u4 o = cat(trd(a), trd(a + 4 * 144));
      const u16* dst = (const u16*)(ws + WS_XT) + (c0 + 32 * half + 16 * (i >> 1) + c16) * L + r0 + 32 * (i & 1) + 8 * g;
      asm volatile("global_store_dwordx4 %0, %1, off sc1\n\ts_nop 1" :: "v"(dst), "v"(o) : "memory");
    }
    const u64 ep = get_epoch(fl);
    asm volatile("s_waitcnt vmcnt(0)" ::: "memory");
    if (lane == 0 && __hip_atomic_fetch_add((LAS u32*)fl + FL_XD, 1u, __ATOMIC_RELAXED, __HIP_MEMORY_SCOPE_WORKGROUP) == 1u)
      __hip_atomic_store((u32*)(ws + WS_FX) + 32 * (b & 7) + (b >> 3), (u32)ep, __ATOMIC_RELAXED, __HIP_MEMORY_SCOPE_AGENT);
  }
  __builtin_amdgcn_s_setprio(0);
  const u64 ep = get_epoch(fl);
  float sa[2], c1[2], ga, c2;
  u4 xv[2];
  {
    const char* sp = ws + WS_ST1 + 16 * t;
    const int ml = lane < 20 ? lane : 19, mch = ml >> 2;
    const char* mp = ws + WS_MG + (((mch + (mch > 2)) * D + d0) * 4 + 2 * (ml & 3)) * 8;
    u4 gs[4], gm;
#pragma unroll 1
    for (int spin = 0; spin < (1 << 16); ++spin) {
      gld16_sc1<0>(gs[0], sp); gld16_sc1<0>(gs[1], sp + 4096); gld16_sc1<0>(gs[2], sp + 8192); gld16_sc1<0>(gs[3], sp + 12288); gld16_sc1<0>(gm, mp);
      asm volatile("s_waitcnt vmcnt(0)" : "+v"(gs[0]), "+v"(gs[1]), "+v"(gs[2]), "+v"(gs[3]), "+v"(gm) :: "memory");
      bool ok = mg_ok(gm, ep);
#pragma unroll
      for (int i = 0; i < 4; ++i) ok = ok && gs[i].y == (u32)ep && gs[i].w == (u32)ep;
      if (__all(ok)) break;
      __builtin_amdgcn_s_sleep(2);
    }
#pragma unroll
    for (int i = 0; i < 4; ++i) *(u2*)(sm + CV_ST + 8 * t + 2048 * i) = (u2){gs[i].x, gs[i].z};
    lds_arrive(fl + FL_MAIN);
    float v = mg_sum(gm);
    v += dppf<0xb1>(v);
    const float vr = rb(v), vs = scale_c(v), vp = rb(1.f + vr);
    auto pick = [](float z, int l) { return __int_as_float(__builtin_amdgcn_readlane(__float_as_int(z), l)); };
    sa[0] = pick(vr, 0); sa[1] = pick(vr, 2); c1[0] = pick(vp, 4); c1[1] = pick(vp, 6);
    ga = ch ? pick(vr, 10) : pick(vr, 8); c2 = ch ? pick(vs, 14) : pick(vs, 12);
    if (w == 0) {
      if (lane == 12 || lane == 14) ((float*)(ws + WS_INV))[d0 + ((lane >> 1) & 1)] = 1.f / vs;
      if (lane == 16 || lane == 18) ((float*)(ws + WS_GB))[d0 + ((lane >> 1) & 1)] = vr;
    }
    if (w == 0) {
      const u32* fx = (const u32*)(ws + WS_FX) + 32 * (b & 7) + (lane & 31);
#pragma unroll 1
      for (int spin = 0; spin < (1 << 16); ++spin) {
        if (__all(__hip_atomic_load(fx, __ATOMIC_RELAXED, __HIP_MEMORY_SCOPE_AGENT) == (u32)ep)) break;
        __builtin_amdgcn_s_sleep(2);
      }
      fl[FL_GO] = 1u;
    } else {
      while (fl[FL_GO] == 0u) __builtin_amdgcn_s_sleep(1);
    }
    const char* xp = ws + WS_XT + d0 * (L * 2) + 16 * t;
    gld16_sc1<0>(xv[0], xp); gld16_sc1<0>(xv[1], xp + L * 2);
    lds_wait(fl + FL_MAIN, 4u);
    if (b == 0 && t == 0) *(u64*)(ws + WS_EPOCH) = ep;
  }
  __builtin_amdgcn_s_setprio(2);
  {
    const u4 m0 = *(const u4*)(sm + CV_ST + 32 * t), m1 = *(const u4*)(sm + CV_ST + 32 * t + 16);
    float rs[8], mm[8];
#pragma unroll
    for (int j = 0; j < 8; ++j) { const u32 z = j < 4 ? m0[j & 3] : m1[j & 3]; rs[j] = __builtin_amdgcn_rcpf(bhi(z)); mm[j] = blo(z); }
    asm volatile("s_waitcnt vmcnt(0)" : "+v"(xv[0]), "+v"(xv[1]), "+v"(rs[0]), "+v"(rs[7]) :: "memory");
#pragma unroll
    for (int i = 0; i < 2; ++i) {
      *(u4*)(sm + CV_X + 4096 * i + 16 * t) = xv[i];
      float h[8];
#pragma unroll
      for (int j = 0; j < 8; ++j) h[j] = fmaf((((j & 1) ? bhi(xv[i][j >> 1]) : blo(xv[i][j >> 1])) - mm[j]) * rs[j], c1[i], sa[i]);
#pragma unroll
      for (int r = 0; r < 4; ++r) *(u32*)(sm + CV_H + (4 * i + r) * CV_HP + 128 + 4 * t) = pk(h[r], h[r + 4]);
    }
  }
  f4 acc[8];
#pragma unroll
  for (int i = 0; i < 8; ++i) acc[i] = (f4){0.f, 0.f, 0.f, 0.f};
  u32 ka = ldsaddr(sm) + CV_K + (4 * ch + 2 * half) * CV_KC + 32 + 8 * (8 * g + (lane & 3) - ((lane & 15) >> 2));
  u32 hb = ldsaddr(sm) + CV_H + (4 * ch + 2 * half) * CV_HP + 128 + 8 * c16 - 16 * g - 8 - 64;
  lds_arrive(fl + FL_RDY);
  lds_wait(fl + FL_RDY, 8u);
  {
    u32 cnt;
    asm volatile(
      "ds_read_b64_tr_b16 v[64:65], %8 offset:32\n"
      "ds_read_b64_tr_b16 v[66:67], %8 offset:0\n"
      "ds_read_b64 v[120:121], %9 offset:832\n"
      "ds_read_b64 v[122:123], %9 offset:840\n"
      "ds_read_b64 v[124:125], %9 offset:960\n"
      "ds_read_b64 v[126:127], %9 offset:968\n"
      "ds_read_b64 v[112:113], %9 offset:576\n"
      "ds_read_b64 v[114:115], %9 offset:584\n"
      "ds_read_b64 v[116:117], %9 offset:704\n"
      "ds_read_b64 v[118:119], %9 offset:712\n"
      "ds_read_b64_tr_b16 v[68:69], %8 offset:544\n"
      "ds_read_b64_tr_b16 v[70:71], %8 offset:512\n"
      "ds_read_b64 v[108:109], %9 offset:448\n"
      "ds_read_b64 v[110:111], %9 offset:456\n"
      "ds_read_b64 v[104:105], %9 offset:320\n"
      "ds_read_b64 v[106:107], %9 offset:328\n"
      "ds_read_b64_tr_b16 v[72:73], %8 offset:1056\n"
      "ds_read_b64_tr_b16 v[74:75], %8 offset:1024\n"
      "ds_read_b64_tr_b16 v[76:77], %8 offset:1568\n"
      "ds_read_b64_tr_b16 v[78:79], %8 offset:1536\n"
      "ds_read_b64 v[100:101], %9 offset:192\n"
      "ds_read_b64 v[102:103], %9 offset:200\n"
      "ds_read_b64_tr_b16 v[80:81], %8 offset:2080\n"
      "ds_read_b64_tr_b16 v[82:83], %8 offset:2048\n"
      "ds_read_b64 v[96:97], %9 offset:64\n"
      "ds_read_b64 v[98:99], %9 offset:72\n"
      "ds_read_b64_tr_b16 v[84:85], %8 offset:2592\n"
      "ds_read_b64_tr_b16 v[86:87], %8 offset:2560\n"
      "ds_read_b64_tr_b16 v[88:89], %8 offset:3104\n"
      "ds_read_b64_tr_b16 v[90:91], %8 offset:3072\n"
      "ds_read_b64_tr_b16 v[92:93], %8 offset:3616\n"
      "ds_read_b64_tr_b16 v[94:95], %8 offset:3584\n"
      "s_mov_b32 %10, 2\n"
      "s_waitcnt lgkmcnt(0)\n"
      "Lconv%=:\n"
      "v_mfma_f32_16x16x32_bf16 %6, v[64:67], v[120:123], %6\n"
      "ds_read_b64_tr_b16 v[128:129], %8 offset:288\n"
      "v_mfma_f32_16x16x32_bf16 %7, v[64:67], v[124:127], %7\n"
      "ds_read_b64_tr_b16 v[130:131], %8 offset:256\n"
      "v_mfma_f32_16x16x32_bf16 %4, v[64:67], v[112:115], %4\n"
      "ds_read_b64 v[184:185], %9 offset:768\n"
      "v_mfma_f32_16x16x32_bf16 %5, v[64:67], v[116:119], %5\n"
      "ds_read_b64 v[186:187], %9 offset:776\n"
      "v_mfma_f32_16x16x32_bf16 %6, v[68:71], v[116:119], %6\n"
      "ds_read_b64 v[188:189], %9 offset:896\n"
      "v_mfma_f32_16x16x32_bf16 %7, v[68:71], v[120:123], %7\n"
      "ds_read_b64 v[190:191], %9 offset:904\n"
      "v_mfma_f32_16x16x32_bf16 %4, v[68:71], v[108:111], %4\n"
      "ds_read_b64 v[176:177], %9 offset:512\n"
      "v_mfma_f32_16x16x32_bf16 %2, v[64:67], v[104:107], %2\n"
      "ds_read_b64 v[178:179], %9 offset:520\n"
      "v_mfma_f32_16x16x32_bf16 %6, v[72:75], v[112:115], %6\n"
      "ds_read_b64 v[180:181], %9 offset:640\n"
      "v_mfma_f32_16x16x32_bf16 %3, v[64:67], v[108:111], %3\n"
      "ds_read_b64 v[182:183], %9 offset:648\n"
      "v_mfma_f32_16x16x32_bf16 %7, v[72:75], v[116:119], %7\n"
      "ds_read_b64_tr_b16 v[132:133], %8 offset:800\n"
      "v_mfma_f32_16x16x32_bf16 %4, v[72:75], v[104:107], %4\n"
      "ds_read_b64_tr_b16 v[134:135], %8 offset:768\n"
      "v_mfma_f32_16x16x32_bf16 %5, v[68:71], v[112:115], %5\n"
      "ds_read_b64 v[172:173], %9 offset:384\n"
      "v_mfma_f32_16x16x32_bf16 %6, v[76:79], v[108:111], %6\n"
      "ds_read_b64 v[174:175], %9 offset:392\n"
      "v_mfma_f32_16x16x32_bf16 %7, v[76:79], v[112:115], %7\n"
      "ds_read_b64 v[168:169], %9 offset:256\n"
      "v_mfma_f32_16x16x32_bf16 %4, v[76:79], v[100:103], %4\n"
      "ds_read_b64 v[170:171], %9 offset:264\n"
      "v_mfma_f32_16x16x32_bf16 %1, v[64:67], v[100:103], %1\n"
      "ds_read_b64_tr_b16 v[136:137], %8 offset:1312\n"
      "v_mfma_f32_16x16x32_bf16 %5, v[72:75], v[108:111], %5\n"
      "ds_read_b64_tr_b16 v[138:139], %8 offset:1280\n"
      "v_mfma_f32_16x16x32_bf16 %6, v[80:83], v[104:107], %6\n"
      "ds_read_b64_tr_b16 v[140:141], %8 offset:1824\n"
      "v_mfma_f32_16x16x32_bf16 %0, v[64:67], v[96:99], %0\n"
      "ds_read_b64_tr_b16 v[142:143], %8 offset:1792\n"
      "v_mfma_f32_16x16x32_bf16 %7, v[80:83], v[108:111], %7\n"
      "ds_read_b64 v[164:165], %9 offset:128\n"
      "v_mfma_f32_16x16x32_bf16 %5, v[76:79], v[104:107], %5\n"
      "ds_read_b64 v[166:167], %9 offset:136\n"
      "v_mfma_f32_16x16x32_bf16 %2, v[68:71], v[100:103], %2\n"
      "ds_read_b64_tr_b16 v[144:145], %8 offset:2336\n"
      "v_mfma_f32_16x16x32_bf16 %6, v[84:87], v[100:103], %6\n"
      "ds_read_b64_tr_b16 v[146:147], %8 offset:2304\n"
      "v_mfma_f32_16x16x32_bf16 %7, v[84:87], v[104:107], %7\n"
      "ds_read_b64 v[160:161], %9 offset:0\n"
      "v_mfma_f32_16x16x32_bf16 %3, v[68:71], v[104:107], %3\n"
      "ds_read_b64 v[162:163], %9 offset:8\n"
      "v_mfma_f32_16x16x32_bf16 %2, v[72:75], v[96:99], %2\n"
      "ds_read_b64_tr_b16 v[148:149], %8 offset:2848\n"
      "v_mfma_f32_16x16x32_bf16 %5, v[80:83], v[100:103], %5\n"
      "ds_read_b64_tr_b16 v[150:151], %8 offset:2816\n"
      "v_mfma_f32_16x16x32_bf16 %7, v[88:91], v[100:103], %7\n"
      "ds_read_b64_tr_b16 v[152:153], %8 offset:3360\n"
      "v_mfma_f32_16x16x32_bf16 %6, v[88:91], v[96:99], %6\n"
      "ds_read_b64_tr_b16 v[154:155], %8 offset:3328\n"
      "v_mfma_f32_16x16x32_bf16 %3, v[72:75], v[100:103], %3\n"
      "ds_read_b64_tr_b16 v[156:157], %8 offset:3872\n"
      "v_mfma_f32_16x16x32_bf16 %1, v[68:71], v[96:99], %1\n"
      "ds_read_b64_tr_b16 v[158:159], %8 offset:3840\n"
      "v_mfma_f32_16x16x32_bf16 %7, v[92:95], v[96:99], %7\n"
      "v_mfma_f32_16x16x32_bf16 %5, v[84:87], v[96:99], %5\n"
      "v_mfma_f32_16x16x32_bf16 %4, v[80:83], v[96:99], %4\n"
      "v_mfma_f32_16x16x32_bf16 %3, v[76:79], v[96:99], %3\n"
      "s_waitcnt lgkmcnt(0)\n"
      "v_add_u32 %8, 4128, %8\n"
      "v_add_u32 %9, 1152, %9\n"
      "v_mfma_f32_16x16x32_bf16 %6, v[128:131], v[184:187], %6\n"
      "ds_read_b64_tr_b16 v[64:65], %8 offset:32\n"
      "v_mfma_f32_16x16x32_bf16 %7, v[128:131], v[188:191], %7\n"
      "ds_read_b64_tr_b16 v[66:67], %8 offset:0\n"
      "v_mfma_f32_16x16x32_bf16 %4, v[128:131], v[176:179], %4\n"
      "ds_read_b64 v[120:121], %9 offset:832\n"
      "v_mfma_f32_16x16x32_bf16 %5, v[128:131], v[180:183], %5\n"
      "ds_read_b64 v[122:123], %9 offset:840\n"
      "v_mfma_f32_16x16x32_bf16 %6, v[132:135], v[180:183], %6\n"
      "ds_read_b64 v[124:125], %9 offset:960\n"
      "v_mfma_f32_16x16x32_bf16 %7, v[132:135], v[184:187], %7\n"
      "ds_read_b64 v[126:127], %9 offset:968\n"
      "v_mfma_f32_16x16x32_bf16 %4, v[132:135], v[172:175], %4\n"
      "ds_read_b64 v[112:113], %9 offset:576\n"
      "v_mfma_f32_16x16x32_bf16 %2, v[128:131], v[168:171], %2\n"
      "ds_read_b64 v[114:115], %9 offset:584\n"
      "v_mfma_f32_16x16x32_bf16 %6, v[136:139], v[176:179], %6\n"
      "ds_read_b64 v[116:117], %9 offset:704\n"
      "v_mfma_f32_16x16x32_bf16 %3, v[128:131], v[172:175], %3\n"
      "ds_read_b64 v[118:119], %9 offset:712\n"
      "v_mfma_f32_16x16x32_bf16 %7, v[136:139], v[180:183], %7\n"
      "ds_read_b64_tr_b16 v[68:69], %8 offset:544\n"
      "v_mfma_f32_16x16x32_bf16 %4, v[136:139], v[168:171], %4\n"
      "ds_read_b64_tr_b16 v[70:71], %8 offset:512\n"
      "v_mfma_f32_16x16x32_bf16 %5, v[132:135], v[176:179], %5\n"
      "ds_read_b64 v[108:109], %9 offset:448\n"
      "v_mfma_f32_16x16x32_bf16 %6, v[140:143], v[172:175], %6\n"
      "ds_read_b64 v[110:111], %9 offset:456\n"
      "v_mfma_f32_16x16x32_bf16 %7, v[140:143], v[176:179], %7\n"
      "ds_read_b64 v[104:105], %9 offset:320\n"
      "v_mfma_f32_16x16x32_bf16 %4, v[140:143], v[164:167], %4\n"
      "ds_read_b64 v[106:107], %9 offset:328\n"
      "v_mfma_f32_16x16x32_bf16 %1, v[128:131], v[164:167], %1\n"
      "ds_read_b64_tr_b16 v[72:73], %8 offset:1056\n"
      "v_mfma_f32_16x16x32_bf16 %5, v[136:139], v[172:175], %5\n"
      "ds_read_b64_tr_b16 v[74:75], %8 offset:1024\n"
      "v_mfma_f32_16x16x32_bf16 %6, v[144:147], v[168:171], %6\n"
      "ds_read_b64_tr_b16 v[76:77], %8 offset:1568\n"
      "v_mfma_f32_16x16x32_bf16 %0, v[128:131], v[160:163], %0\n"
      "ds_read_b64_tr_b16 v[78:79], %8 offset:1536\n"
      "v_mfma_f32_16x16x32_bf16 %7, v[144:147], v[172:175], %7\n"
      "ds_read_b64 v[100:101], %9 offset:192\n"
      "v_mfma_f32_16x16x32_bf16 %5, v[140:143], v[168:171], %5\n"
      "ds_read_b64 v[102:103], %9 offset:200\n"
      "v_mfma_f32_16x16x32_bf16 %2, v[132:135], v[164:167], %2\n"
      "ds_read_b64_tr_b16 v[80:81], %8 offset:2080\n"
      "v_mfma_f32_16x16x32_bf16 %6, v[148:151], v[164:167], %6\n"
      "ds_read_b64_tr_b16 v[82:83], %8 offset:2048\n"
      "v_mfma_f32_16x16x32_bf16 %7, v[148:151], v[168:171], %7\n"
      "ds_read_b64 v[96:97], %9 offset:64\n"
      "v_mfma_f32_16x16x32_bf16 %3, v[132:135], v[168:171], %3\n"
      "ds_read_b64 v[98:99], %9 offset:72\n"
      "v_mfma_f32_16x16x32_bf16 %2, v[136:139], v[160:163], %2\n"
      "ds_read_b64_tr_b16 v[84:85], %8 offset:2592\n"
      "v_mfma_f32_16x16x32_bf16 %5, v[144:147], v[164:167], %5\n"
      "ds_read_b64_tr_b16 v[86:87], %8 offset:2560\n"
      "v_mfma_f32_16x16x32_bf16 %7, v[152:155], v[164:167], %7\n"
      "ds_read_b64_tr_b16 v[88:89], %8 offset:3104\n"
      "v_mfma_f32_16x16x32_bf16 %6, v[152:155], v[160:163], %6\n"
      "ds_read_b64_tr_b16 v[90:91], %8 offset:3072\n"
      "v_mfma_f32_16x16x32_bf16 %3, v[136:139], v[164:167], %3\n"
      "ds_read_b64_tr_b16 v[92:93], %8 offset:3616\n"
      "v_mfma_f32_16x16x32_bf16 %1, v[132:135], v[160:163], %1\n"
      "ds_read_b64_tr_b16 v[94:95], %8 offset:3584\n"
      "v_mfma_f32_16x16x32_bf16 %7, v[156:159], v[160:163], %7\n"
      "v_mfma_f32_16x16x32_bf16 %5, v[148:151], v[160:163], %5\n"
      "v_mfma_f32_16x16x32_bf16 %4, v[144:147], v[160:163], %4\n"
      "v_mfma_f32_16x16x32_bf16 %3, v[140:143], v[160:163], %3\n"
      "s_waitcnt lgkmcnt(0)\n"
      "s_sub_u32 %10, %10, 1\n"
      "s_cmp_lg_u32 %10, 0\n"
      "s_cbranch_scc1 Lconv%=\n"
      "s_nop 7\n"
      : "+v"(acc[0]), "+v"(acc[1]), "+v"(acc[2]), "+v"(acc[3]), "+v"(acc[4]), "+v"(acc[5]), "+v"(acc[6]), "+v"(acc[7]), "+v"(ka), "+v"(hb), "=&s"(cnt)
      :: "memory", "scc", "v64", "v65", "v66", "v67", "v68", "v69", "v70", "v71", "v72", "v73", "v74", "v75", "v76", "v77", "v78", "v79", "v80", "v81", "v82", "v83", "v84", "v85", "v86", "v87", "v88", "v89", "v90", "v91", "v92", "v93", "v94", "v95", "v96", "v97", "v98", "v99", "v100", "v101", "v102", "v103", "v104", "v105", "v106", "v107", "v108", "v109", "v110", "v111", "v112", "v113", "v114", "v115", "v116", "v117", "v118", "v119", "v120", "v121", "v122", "v123", "v124", "v125", "v126", "v127", "v128", "v129", "v130", "v131", "v132", "v133", "v134", "v135", "v136", "v137", "v138", "v139", "v140", "v141", "v142", "v143", "v144", "v145", "v146", "v147", "v148", "v149", "v150", "v151", "v152", "v153", "v154", "v155", "v156", "v157", "v158", "v159", "v160", "v161", "v162", "v163", "v164", "v165", "v166", "v167", "v168", "v169", "v170", "v171", "v172", "v173", "v174", "v175", "v176", "v177", "v178", "v179", "v180", "v181", "v182", "v183", "v184", "v185", "v186", "v187", "v188", "v189", "v190", "v191");
  }
  f4* ex = (f4*)(sm + CV_EX);
  if (half) {
#pragma unroll
    for (int i = 0; i < 4; ++i) { ex[(w * 4 + i) * 64 + lane] = acc[i]; acc[i] = acc[4 + i]; }
  } else {
#pragma unroll
    for (int i = 0; i < 4; ++i) ex[(w * 4 + i) * 64 + lane] = acc[4 + i];
  }
  lds_arrive(fl + FL_EXCH + w);
  u2 xr[4];
#pragma unroll
  for (int i = 0; i < 4; ++i) xr[i] = *(const u2*)(sm + CV_X + 4096 * ch + 2 * (256 * (4 * half + i) + 16 * c16 + 4 * g));
  lds_wait(fl + FL_EXCH + (w ^ 1), 1u);
  u16* x1t = (u16*)(ws + WS_X1T);
  const float gc = ga * c2;
#pragma unroll
  for (int i = 0; i < 4; ++i) {
    const f4 y = (acc[i] + ex[((w ^ 1) * 4 + i) * 64 + lane]) * gc + (f4){blo(xr[i].x), bhi(xr[i].x), blo(xr[i].y), bhi(xr[i].y)} * c2;
    *(u2*)(x1t + d * L + 256 * (4 * half + i) + 16 * c16 + 4 * g) = (u2){pk(y[0], y[1]), pk(y[2], y[3])};
  }
}

__global__ __launch_bounds__(512, 2) void k1_conv(const u16* x, const u16* lar, const u16* aim, const u16* cre, const u16* cim, const u16* ldt, const u16* dsk,
                                                  const u16* c, const u16* wm, const u16* wg, const u16* wu, const u16* wd, char* ws) {
  extern __shared__ __attribute__((aligned(16))) char sm[];
  const int b = blockIdx.x;
  if (__builtin_amdgcn_readfirstlane(threadIdx.x) < 256) k1_main(sm, b, x, c, wm, ws);
  else k1_help(sm, b, lar, aim, cre, cim, ldt, dsk, wg, wu, wd, ws);
}

constexpr int G1_LEAD = 3, G1_NS = 5, G1_SLOT = 20480;
constexpr int G2_RES = 19, G2_FL = 4 * G2_RES * 2048, G1_SV = G2_FL + 64, K3_LDS = G1_SV + 512;
constexpr int G2_LEAD = 6;
constexpr int HH_T = NU * 1024;

template <int K> DI u32 g1_pair(const f16v& acc, float rs, float sd) {
  const f2 g = (f2){acc[2 * K], acc[2 * K + 1]} * rs;
  const f2 z = g * (g * g * -0.10294324f + -2.30220819f);
  const f2 dn = (f2){__builtin_amdgcn_exp2f(z.x), __builtin_amdgcn_exp2f(z.y)} * sd + sd;
  const f2 r = g * (f2){acc[8 + 2 * K], acc[9 + 2 * K]} * (f2){__builtin_amdgcn_rcpf(dn.x), __builtin_amdgcn_rcpf(dn.y)};
  return pk(r.x, r.y);
}
DI u4 g1_tile(const f16v& acc, float rs, float sd) { return (u4){g1_pair<0>(acc, rs, sd), g1_pair<1>(acc, rs, sd), g1_pair<2>(acc, rs, sd), g1_pair<3>(acc, rs, sd)}; }

DI void g1_stats(char* sm, int rt, char* ws) {
  const int t = threadIdx.x;
  float* red = (float*)sm;
  if (t < 512) {
    const u16* xp = (const u16*)(ws + WS_X1T) + (t >> 4) * L + 128 * rt + 8 * (t & 15);
    u4 v[16];
    float iv[16];
#pragma unroll
    for (int i = 0; i < 16; ++i) { v[i] = *(const u4*)(xp + 32 * i * L); iv[i] = ((const float*)(ws + WS_INV))[(t >> 4) + 32 * i]; }
    f2 s1[4], s2[4];
#pragma unroll
    for (int j = 0; j < 4; ++j) s1[j] = s2[j] = (f2){0.f, 0.f};
#pragma unroll
    for (int i = 0; i < 16; ++i)
#pragma unroll
      for (int j = 0; j < 4; ++j) { const f2 x = (f2){blo(v[i][j]), bhi(v[i][j])} * iv[i]; s1[j] += x; s2[j] += x * x; }
#pragma unroll
    for (int j = 0; j < 4; ++j) *(f4*)(red + (t >> 4) * 260 + 16 * (t & 15) + 4 * j) = (f4){s1[j].x, s2[j].x, s1[j].y, s2[j].y};
  }
  __syncthreads();
  if (t < 512) {
    f2 p = {0.f, 0.f};
#pragma unroll
    for (int i = 0; i < 8; ++i) p += *(const f2*)(red + (8 * (t & 3) + i) * 260 + 2 * (t >> 2));
    p.x += dppf<0xb1>(p.x); p.y += dppf<0xb1>(p.y); p.x += dppf<0x4e>(p.x); p.y += dppf<0x4e>(p.y);
    const f2 ms = ln_stats(p.x, p.y);
    if ((t & 3) == 0)
      __hip_atomic_store((u32*)(ws + WS_ST2) + 128 * rt + (t >> 2), (__float_as_uint(ms.x) >> 16) | __float_as_uint(ms.y), __ATOMIC_RELAXED, __HIP_MEMORY_SCOPE_AGENT);
  }
  __syncthreads();
}

__global__ __launch_bounds__(768, 3) void k3_mlp(const u16* wg, const u16* wu, const u16* wd, char* ws, u16* out) {
  extern __shared__ __attribute__((aligned(16))) char sm[];
  const int b = blockIdx.x, j = b >> 3;
  const int t = threadIdx.x, w12 = __builtin_amdgcn_readfirstlane(t >> 6), w = w12 & 3, lane = t & 63, hh = lane >> 5;
  const u32 lb0 = ldsaddr(sm);
  const int q = (lane & 15) >> 2, p = lane & 3, g = lane >> 4;
  const int lt = 4 * (b & 7) + (j & 3), dt = j >> 2;
  auto kfirst = [&](int kq) { const u32 k = 22 * kq - (kq == 3) + 3 * dt; return min(k, k - NU); };
  auto knext = [](u32 k) { return min(k + 1, k + 1 - NU); };
  const rsrc_t rd = mkrsrc(wd, DF * D * 2);
  const u32 vd = (lane >> 3) * 1024 + 128 * dt + 16 * ((lane & 7) ^ (4 * ((lane >> 4) & 1)));
  volatile LAS u32* fl = (volatile LAS u32*)(size_t)(lb0 + G2_FL);
  auto fill2 = [&](int i0, int n, int slot0) {
    u32 kd = kfirst(w) + i0;
    kd = min(kd, kd - NU);
#pragma unroll 1
    for (int i = 0; i < n; ++i) {
      glds16<0>(lb0 + (G2_RES * w + slot0 + i) * 2048, vd, rd, kd * 16384);
      glds16<0>(lb0 + (G2_RES * w + slot0 + i) * 2048 + 1024, vd, rd, kd * 16384 + 8192);
      kd = knext(kd);
    }
  };
  {
    const int rt = 4 * ((b & 7) >> 1) + (j & 3), cg = (j >> 2) + 8 * (b & 1) - 1;
    if (t == 0) fl[0] = 0u;
    if (cg < 0) {
      g1_stats(sm, rt, ws);
    } else if (w12 >= 8) {
      const rsrc_t rx = mkrsrc(ws + WS_X1T, D * L * 2), rg = mkrsrc(wg, D * DF * 2), ru = mkrsrc(wu, D * DF * 2);
      const u32 vx = (4 * w + g) * 4096 + 256 * rt + 16 * ((lane & 15) ^ ((g << 2) | w));
      u32 vw[3];
#pragma unroll
      for (int i = 0; i < 3; ++i) {
        const int pc = (3 * (w & 1) + i) * 64 + lane, row = pc / 12, chk = pc - 12 * row;
        vw[i] = row * (DF * 2) + min(192 * cg + 16 * (chk ^ (w & 2)), DF * 2 - 16);
      }
      const rsrc_t rwm = w < 2 ? rg : ru;
      auto stage = [&](int i, int slot) {
        const u32 m = lb0 + slot * G1_SLOT;
        glds16<0>(m + 1024 * w, vx, rx, i * 131072);
        glds16<0>(m + 4096 + 1024 * w, vx, rx, i * 131072 + 65536);
#pragma unroll
        for (int k = 0; k < 3; ++k) glds16<0>(m + 8192 + 3072 * w + 1024 * k, vw[k], rwm, i * (32 * DF * 2));
      };
      asm volatile("s_nop 4");
      static_assert(G1_LEAD + 2 <= G1_NS, "ring too short");
#pragma unroll 1
      for (int i = 0; i < G1_LEAD - 1; ++i) stage(i, i);
      int slot = G1_LEAD - 1;
#pragma unroll 1
      for (int i = 0; i < 17 - G1_LEAD; ++i) {
        stage(i + G1_LEAD - 1, slot);
        slot = slot == G1_NS - 1 ? 0 : slot + 1;
        vmwait<5 * (G1_LEAD - 1)>();
        rawbar();
      }
      const u32* st2 = (const u32*)(ws + WS_ST2) + 128 * rt + 32 * w + (lane & 31);
      u32 v;
      asm volatile("global_load_dword %0, %1, off sc1" : "=v"(v) : "v"(st2));
      sfor<17 - G1_LEAD, 16>([&](auto i) { vmwait<5 * (15 - i) + 1>(); rawbar(); });
      asm volatile("s_waitcnt vmcnt(0)" : "+v"(v));
#pragma unroll 1
      for (int spin = 0; spin < (1 << 16) && !__all(v != 0u); ++spin) {
        __builtin_amdgcn_s_sleep(2);
        v = __hip_atomic_load(st2, __ATOMIC_RELAXED, __HIP_MEMORY_SCOPE_AGENT);
      }
      ((u32*)(sm + G1_SV))[32 * w + (lane & 31)] = v;
      bar();
      bar();
    } else if (w12 >= 4) {
      const char* pf = (const char*)wd + j * (43 * 1024) + w * (43 * 256);
#pragma unroll 1
      for (int i = 0; i < 18; ++i) {
        rawbar();
      }
    } else {
      const int wl = w & 1, wu3 = 3 * (w >> 1), u0 = 6 * cg + wu3;
      u32 xa[2][2], wa[3];
#pragma unroll
      for (int c = 0; c < 2; ++c)
#pragma unroll
        for (int tt = 0; tt < 2; ++tt)
          xa[c][tt] = lb0 + 256 * (8 * hh + 4 * tt + q) + 8 * (p & 1) + 16 * ((4 * (2 * wl + c) + 2 * (g & 1) + (p >> 1)) ^ ((q << 2) | (2 * hh + tt)));
#pragma unroll
      for (int v = 0; v < 3; ++v) wa[v] = lb0 + 8192 + 6144 * (g & 1) + 192 * (8 * hh + q) + 16 * ((2 * (wu3 + v) + (p >> 1)) ^ (2 * (g & 1))) + 8 * (p & 1);
      u4 ex[3];
#pragma unroll
      for (int v = 0; v < 3; ++v) ex[v] = *(const u4*)(ws + WS_EX + min(u0 + v, NU - 1) * 1024 + lane * 16);
      f16v acc[6];
#pragma unroll
      for (int i = 0; i < 6; ++i)
#pragma unroll
        for (int e = 0; e < 16; ++e) acc[i][e] = 0.f;
#pragma unroll
      for (int i = 0; i < 6; ++i) asm volatile("" : "+v"(acc[i]));
      rawbar();
      {
        u32 c0, c1, c2;
        asm volatile(
          "ds_read_b64_tr_b16 v[112:113], %6 offset:0\n"
          "ds_read_b64_tr_b16 v[114:115], %7 offset:0\n"
          "ds_read_b64_tr_b16 v[120:121], %10 offset:0\n"
          "ds_read_b64_tr_b16 v[122:123], %10 offset:768\n"
          "ds_read_b64_tr_b16 v[116:117], %8 offset:0\n"
          "ds_read_b64_tr_b16 v[118:119], %9 offset:0\n"
          "ds_read_b64_tr_b16 v[124:125], %11 offset:0\n"
          "ds_read_b64_tr_b16 v[126:127], %11 offset:768\n"
          "ds_read_b64_tr_b16 v[128:129], %12 offset:0\n"
          "ds_read_b64_tr_b16 v[130:131], %12 offset:768\n"
          "s_mov_b32 %13, 16\n"
          "s_mov_b32 %14, 0\n"
          "Lg1%=:\n"
          "s_add_u32 %14, %14, 1\n"
          "s_cmp_eq_u32 %14, 5\n"
          "s_cselect_b32 %15, -81920, %16\n"
          "s_cselect_b32 %14, 0, %14\n"
          "s_waitcnt lgkmcnt(6)\n"
          "v_mfma_f32_32x32x16_bf16 %0, v[120:123], v[112:115], %0\n"
          "ds_read_b64_tr_b16 v[132:133], %6 offset:4096\n"
          "ds_read_b64_tr_b16 v[134:135], %7 offset:4096\n"
          "s_waitcnt lgkmcnt(6)\n"
          "v_mfma_f32_32x32x16_bf16 %1, v[120:123], v[116:119], %1\n"
          "ds_read_b64_tr_b16 v[140:141], %10 offset:3072\n"
          "ds_read_b64_tr_b16 v[142:143], %10 offset:3840\n"
          "s_waitcnt lgkmcnt(6)\n"
          "v_mfma_f32_32x32x16_bf16 %2, v[124:127], v[112:115], %2\n"
          "ds_read_b64_tr_b16 v[136:137], %8 offset:4096\n"
          "ds_read_b64_tr_b16 v[138:139], %9 offset:4096\n"
          "v_mfma_f32_32x32x16_bf16 %3, v[124:127], v[116:119], %3\n"
          "ds_read_b64_tr_b16 v[144:145], %11 offset:3072\n"
          "ds_read_b64_tr_b16 v[146:147], %11 offset:3840\n"
          "s_waitcnt lgkmcnt(8)\n"
          "v_mfma_f32_32x32x16_bf16 %4, v[128:131], v[112:115], %4\n"
          "ds_read_b64_tr_b16 v[148:149], %12 offset:3072\n"
          "ds_read_b64_tr_b16 v[150:151], %12 offset:3840\n"
          "v_add_u32 %6, %15, %6\n"
          "v_add_u32 %7, %15, %7\n"
          "v_add_u32 %8, %15, %8\n"
          "v_add_u32 %9, %15, %9\n"
          "v_mfma_f32_32x32x16_bf16 %5, v[128:131], v[116:119], %5\n"
          "v_add_u32 %10, %15, %10\n"
          "v_add_u32 %11, %15, %11\n"
          "v_add_u32 %12, %15, %12\n"
          "s_barrier\n"
          "s_waitcnt lgkmcnt(6)\n"
          "v_mfma_f32_32x32x16_bf16 %0, v[140:143], v[132:135], %0\n"
          "ds_read_b64_tr_b16 v[112:113], %6 offset:0\n"
          "ds_read_b64_tr_b16 v[114:115], %7 offset:0\n"
          "s_waitcnt lgkmcnt(6)\n"
          "v_mfma_f32_32x32x16_bf16 %1, v[140:143], v[136:139], %1\n"
          "ds_read_b64_tr_b16 v[120:121], %10 offset:0\n"
          "ds_read_b64_tr_b16 v[122:123], %10 offset:768\n"
          "s_waitcnt lgkmcnt(6)\n"
          "v_mfma_f32_32x32x16_bf16 %2, v[144:147], v[132:135], %2\n"
          "ds_read_b64_tr_b16 v[116:117], %8 offset:0\n"
          "ds_read_b64_tr_b16 v[118:119], %9 offset:0\n"
          "v_mfma_f32_32x32x16_bf16 %3, v[144:147], v[136:139], %3\n"
          "ds_read_b64_tr_b16 v[124:125], %11 offset:0\n"
          "ds_read_b64_tr_b16 v[126:127], %11 offset:768\n"
          "s_waitcnt lgkmcnt(8)\n"
          "v_mfma_f32_32x32x16_bf16 %4, v[148:151], v[132:135], %4\n"
          "ds_read_b64_tr_b16 v[128:129], %12 offset:0\n"
          "ds_read_b64_tr_b16 v[130:131], %12 offset:768\n"
          "s_sub_u32 %13, %13, 1\n"
          "s_cmp_lg_u32 %13, 0\n"
          "v_mfma_f32_32x32x16_bf16 %5, v[148:151], v[136:139], %5\n"
          "s_cbranch_scc1 Lg1%=\n"
          "s_waitcnt lgkmcnt(0)\n"
          : "+v"(acc[0]), "+v"(acc[1]), "+v"(acc[2]), "+v"(acc[3]), "+v"(acc[4]), "+v"(acc[5]), "+v"(xa[0][0]), "+v"(xa[0][1]), "+v"(xa[1][0]), "+v"(xa[1][1]),
            "+v"(wa[0]), "+v"(wa[1]), "+v"(wa[2]), "=&s"(c0), "=&s"(c1), "=&s"(c2)
          : "s"(G1_SLOT) : "memory", "scc", "v112", "v113", "v114", "v115", "v116", "v117", "v118", "v119", "v120", "v121", "v122", "v123", "v124", "v125", "v126", "v127", "v128", "v129", "v130", "v131", "v132", "v133", "v134", "v135", "v136", "v137", "v138", "v139", "v140", "v141", "v142", "v143", "v144", "v145", "v146", "v147", "v148", "v149", "v150", "v151");
      }
      u32 sv[2];
#pragma unroll
      for (int c = 0; c < 2; ++c) sv[c] = ((const u32*)(sm + G1_SV))[64 * wl + 32 * c + (lane & 31)];
      bar();
#pragma unroll
      for (int c = 0; c < 2; ++c) {
        const u32 bv = hh ? __builtin_amdgcn_perm(sv[c], sv[c], 0x03020302u) : (__builtin_amdgcn_perm(sv[c], sv[c], 0x01000100u) ^ 0x80008000u);
#pragma unroll
        for (int v = 0; v < 3; ++v) acc[2 * v + c] = mfma32(ex[v], (u4){bv, bv, bv, bv}, acc[2 * v + c]);
      }
      char* hp = ws + WS_HH + (4 * rt + 2 * wl) * HH_T + u0 * 1024 + 16 * lane;
#pragma unroll
      for (int v = 0; v < 3; ++v) {
        if (u0 + v < NU) {
#pragma unroll
          for (int c = 0; c < 2; ++c) {
            const float sd = bhi(sv[c]);
            const u4 o = g1_tile(acc[2 * v + c], __builtin_amdgcn_rcpf(sd), sd);
            char* dst = hp + c * HH_T + v * 1024;
            asm volatile("global_store_dwordx4 %0, %1, off sc1\n\ts_nop 1" :: "v"(dst), "v"(o) : "memory");
          }
        }
      }
      {
        asm volatile("s_waitcnt vmcnt(0)" ::: "memory");
        if (lane == 0 && __hip_atomic_fetch_add((LAS u32*)fl, 1u, __ATOMIC_RELAXED, __HIP_MEMORY_SCOPE_WORKGROUP) == 3u)
          __hip_atomic_fetch_add((u32*)(ws + WS_CNT) + 64 * rt, 1u, __ATOMIC_RELAXED, __HIP_MEMORY_SCOPE_AGENT);
      }
    }
  }
  if (w12 >= 8) {
    asm volatile("s_nop 4");
    fill2(G2_RES, 3, 0);
    fill2(3, G2_RES - 3, 3);
    vmwait<2 * (G2_RES - 3)>();
    rawbar();
    rawbar();
    fill2(0, 3, 0);
    vmwait<0>();
    rawbar();
    rawbar();
    rawbar();
  } else {
    const int wl = w12 & 1, kq = w12 >> 1, dc = 64 * dt + 16 * kq + 4 * hh;
    f4 gb[2], xr[2];
#pragma unroll
    for (int e = 0; e < 2; ++e) {
      gb[e] = *(const f4*)((const float*)(ws + WS_GB) + dc + 8 * e);
      xr[e] = *(const f4*)((const float*)(ws + WS_INV) + dc + 8 * e);
#pragma unroll
      for (int i = 0; i < 4; ++i) xr[e][i] *= b2f(((const u16*)(ws + WS_X1T))[(dc + 8 * e + i) * L + 64 * lt + 32 * wl + (lane & 31)]);
    }
    u32 pa[2];
#pragma unroll
    for (int e = 0; e < 2; ++e)
      pa[e] = lb0 + G2_RES * 2048 * kq + 128 * (4 * hh + q) + 16 * ((((4 * e + 2 * (g & 1)) ^ (2 * kq)) + (p >> 1)) ^ (4 * (q >> 1))) + 8 * (p & 1);
    f16v acc[2];
#pragma unroll
    for (int e = 0; e < 16; ++e) acc[0][e] = acc[1][e] = 0.f;
    asm volatile("" : "+v"(xr[0]), "+v"(xr[1]), "+v"(gb[0]), "+v"(gb[1]), "+v"(acc[0]), "+v"(acc[1]));
    rawbar();
    u4 A[2][2], AR[3][2];
#pragma unroll
    for (int i = 0; i < 6; ++i) AR[i >> 1][i & 1] = cat(trd(pa[i & 1] + (i >> 1) * 2048), trd(pa[i & 1] + (i >> 1) * 2048 + 1024));
    asm volatile("s_waitcnt lgkmcnt(0)\n\ts_barrier" : "+v"(AR[0][0]), "+v"(AR[0][1]), "+v"(AR[1][0]), "+v"(AR[1][1]), "+v"(AR[2][0]), "+v"(AR[2][1]) :: "memory");
    if (w12 == 0) {
      const u32* cnt = (const u32*)(ws + WS_CNT) + 64 * (lt >> 1);
#pragma unroll 1
      for (int spin = 0; spin < (1 << 18); ++spin)
        if (__hip_atomic_load(cnt, __ATOMIC_RELAXED, __HIP_MEMORY_SCOPE_AGENT) >= 15u) break;
    }
    rawbar();
    const rsrc_t rh = mkrsrc(ws + WS_HH + (2 * lt + wl) * HH_T, HH_T);
    constexpr int LEAD = G2_LEAD, NH = LEAD + 2;
    u4 H[NH];
    u32 kh = kfirst(kq);
    auto ldh = [&](auto i) { if constexpr (i < 22) { bld16_sc1<0>(H[i % NH], lane * 16, rh, kh * 1024); kh = knext(kh); } };
    sfor<0, LEAD>(ldh);
    auto rda = [&](auto i) {
#pragma unroll
      for (int e = 0; e < 2; ++e) A[i & 1][e] = cat(trd(pa[e] + i * 2048), trd(pa[e] + i * 2048 + 1024));
    };
    rda(std::integral_constant<int, 0>{});
    sfor<0, 22>([&](auto i) {
      vmwait<(i < 22 - LEAD ? LEAD - 1 : 21 - i)>(H[i % NH]);
      SB();
      if constexpr (i + 1 < G2_RES) rda(std::integral_constant<int, i + 1>{});
      if (i < 21 || kq < 2) {
#pragma unroll
        for (int e = 0; e < 2; ++e) acc[e] = mfma32(i < G2_RES ? A[i & 1][e] : AR[i < G2_RES ? 0 : i - G2_RES][e], H[i % NH], acc[e]);
      }
      if constexpr (i + 1 < G2_RES) { SGB(0x8, 1); SGB(0x100, 2); SGB(0x8, 1); SGB(0x100, 2); }
      SB();
      ldh(std::integral_constant<int, i + LEAD>{});
      if constexpr (i == G2_RES - 1) rawbar();
    });
    f4* red = (f4*)sm + wl * 1536 + lane;
#pragma unroll
    for (int k = 1; k < 4; ++k)
#pragma unroll
      for (int e = 0; e < 2; ++e) {
        const f16v& a = acc[k >> 1];
        const int r = 8 * (k & 1) + 4 * e;
        red[(kq ^ k) * 384 + (k - 1) * 128 + 64 * e] = (f4){a[r], a[r + 1], a[r + 2], a[r + 3]};
      }
    bar();
    u2 o[2];
#pragma unroll
    for (int e = 0; e < 2; ++e) {
      const f4* rp = red + kq * 384 + 64 * e;
      const f4 y = ((f4){acc[0][4 * e], acc[0][4 * e + 1], acc[0][4 * e + 2], acc[0][4 * e + 3]} + rp[0] + rp[128] + rp[256]) * gb[e] + xr[e];
      o[e] = (u2){pk(y[0], y[1]), pk(y[2], y[3])};
    }
    const auto r0 = __builtin_amdgcn_permlane32_swap(o[0].x, o[1].x, false, false);
    const auto r1 = __builtin_amdgcn_permlane32_swap(o[0].y, o[1].y, false, false);
    *(u4*)(out + (64 * lt + 32 * wl + (lane & 31)) * D + 64 * dt + 16 * kq + 8 * hh) = (u4){r0[0], r1[0], r0[1], r1[1]};
  }
}

extern "C" void kernel_launch(void* const* d_in, const int* in_sizes, int n_in, void* d_out, int out_size, void* d_ws, size_t ws_size,
                              hipStream_t stream) {
  const u16 *wg = (const u16*)d_in[8], *wu = (const u16*)d_in[9], *wd = (const u16*)d_in[10];
  char* ws = (char*)d_ws;
  (void)hipFuncSetAttribute((const void*)k1_conv, hipFuncAttributeMaxDynamicSharedMemorySize, K1_LDS);
  (void)hipFuncSetAttribute((const void*)k3_mlp, hipFuncAttributeMaxDynamicSharedMemorySize, K3_LDS);
  k1_conv<<<256, 512, K1_LDS, stream>>>((const u16*)d_in[0], (const u16*)d_in[1], (const u16*)d_in[2], (const u16*)d_in[3], (const u16*)d_in[4], (const u16*)d_in[5],
                                        (const u16*)d_in[6], (const u16*)d_in[11], (const u16*)d_in[12], wg, wu, wd, ws);
  k3_mlp<<<256, 768, K3_LDS, stream>>>(wg, wu, wd, ws, (u16*)d_out);
}
```

```cpp
#include <hip/hip_runtime.h>
#include <hip/hip_bf16.h>
#include <stdint.h>
#include <type_traits>

typedef unsigned short u16;
typedef unsigned u32;
typedef float f2 __attribute__((ext_vector_type(2)));
typedef float f4 __attribute__((ext_vector_type(4)));
typedef float f16v __attribute__((ext_vector_type(16)));
typedef u32 u2 __attribute__((ext_vector_type(2)));
typedef u32 u4 __attribute__((ext_vector_type(4)));
typedef u4 u4a8 __attribute__((aligned(8)));
typedef short s4 __attribute__((ext_vector_type(4)));
typedef __bf16 bf2 __attribute__((ext_vector_type(2)));
typedef __bf16 bf8 __attribute__((ext_vector_type(8)));
typedef _Float16 h2 __attribute__((ext_vector_type(2)));
typedef _Float16 h8 __attribute__((ext_vector_type(8)));
#define LAS __attribute__((address_space(3)))
#define DI __device__ __forceinline__

constexpr int L = 2048, D = 512, NM = 128, DF = 1376, NU = 86;

constexpr size_t WS_GB = 0, WS_INV = 2 << 10, WS_EPOCH = 4 << 10, WS_ST2 = 8 << 10, WS_CNT = 16 << 10, WS_ST1 = 32 << 10, WS_EX = 64 << 10,
                 WS_MG = 160 << 10, WS_X1T = 256 << 10, WS_HH = WS_X1T + (2 << 20), WS_XT = WS_HH + (5504 << 10), WS_FXX = WS_XT + (2 << 20);

template <class T> DI T ldnt(const T* p) { return __builtin_nontemporal_load(p); }
DI float blo(u32 v) { return __uint_as_float(v << 16); }
DI float bhi(u32 v) { return __uint_as_float(v & 0xffff0000u); }
DI float b2f(u16 v) { return __uint_as_float(((u32)v) << 16); }
DI u32 pk(float a, float b) { return __builtin_bit_cast(u32, __builtin_convertvector((f2){a, b}, bf2)); }
DI u32 pkh(f2 v) { return __builtin_bit_cast(u32, __builtin_convertvector(v, h2)); }
DI float rb(float a) { return blo(pk(a, a)); }
DI u2 trd(const char* p) { return __builtin_bit_cast(u2, __builtin_amdgcn_ds_read_tr16_b64_v4i16((LAS s4*)p)); }
DI u2 trd(u32 a) { return __builtin_bit_cast(u2, __builtin_amdgcn_ds_read_tr16_b64_v4i16((LAS s4*)(size_t)a)); }
DI u4 cat(u2 a, u2 b) { return (u4){a.x, a.y, b.x, b.y}; }
DI float dot2(u32 a, u32 b, float c) { return __builtin_amdgcn_fdot2_f32_bf16(__builtin_bit_cast(bf2, a), __builtin_bit_cast(bf2, b), c, false); }
DI f4 mfma16(u4 a, u4 b, f4 c) { return __builtin_amdgcn_mfma_f32_16x16x32_bf16(__builtin_bit_cast(bf8, a), __builtin_bit_cast(bf8, b), c, 0, 0, 0); }
DI f4 mfma16h(u4 a, u4 b, f4 c) { return __builtin_amdgcn_mfma_f32_16x16x32_f16(__builtin_bit_cast(h8, a), __builtin_bit_cast(h8, b), c, 0, 0, 0); }
DI f16v mfma32(u4 a, u4 b, f16v c) { return __builtin_amdgcn_mfma_f32_32x32x16_bf16(__builtin_bit_cast(bf8, a), __builtin_bit_cast(bf8, b), c, 0, 0, 0); }
#define CM_MUL(d, a, b) "v_pk_mul_f32 " d ", " a ", " b " op_sel_hi:[0,1]\n\t"
#define CM_FMA(d, a, b) "v_pk_fma_f32 " d ", " a ", " b ", " d " op_sel:[1,1,0] op_sel_hi:[1,0,1] neg_lo:[0,1,0]\n\t"
DI void cmul2(f2& r0, f2 a0, f2 b0, f2& r1, f2 a1, f2 b1) {
  asm(CM_MUL("%0", "%2", "%3") CM_MUL("%1", "%4", "%5") CM_FMA("%0", "%2", "%3") CM_FMA("%1", "%4", "%5")
      : "=&v"(r0), "=&v"(r1) : "v"(a0), "v"(b0), "v"(a1), "v"(b1));
}
DI void cmul3(f2& r0, f2 a0, f2 b0, f2& r1, f2 a1, f2 b1, f2& r2, f2 a2, f2 b2) {
  asm(CM_MUL("%0", "%3", "%4") CM_MUL("%1", "%5", "%6") CM_MUL("%2", "%7", "%8") CM_FMA("%0", "%3", "%4") CM_FMA("%1", "%5", "%6") CM_FMA("%2", "%7", "%8")
      : "=&v"(r0), "=&v"(r1), "=&v"(r2) : "v"(a0), "v"(b0), "v"(a1), "v"(b1), "v"(a2), "v"(b2));
}
DI void cmul4(f2& r0, f2& r1, f2& r2, f2& r3, f2 a0, f2 a1, f2 a2, f2 a3, f2 b) {
  asm(CM_MUL("%0", "%4", "%8") CM_MUL("%1", "%5", "%8") CM_MUL("%2", "%6", "%8") CM_MUL("%3", "%7", "%8")
      CM_FMA("%0", "%4", "%8") CM_FMA("%1", "%5", "%8") CM_FMA("%2", "%6", "%8") CM_FMA("%3", "%7", "%8")
      : "=&v"(r0), "=&v"(r1), "=&v"(r2), "=&v"(r3) : "v"(a0), "v"(a1), "v"(a2), "v"(a3), "v"(b));
}
DI float exp_acc(float x) {
  const float t = x * 1.44269502f, r = fmaf(x, 1.44269502f, -t) + x * 1.92596299e-8f, e = __builtin_amdgcn_exp2f(t);
  return fmaf(e, r * 0.693147182f, e);
}
template <int CTRL> DI float dppf(float v) { return __int_as_float(__builtin_amdgcn_update_dpp(0, __float_as_int(v), CTRL, 0xf, 0xf, true)); }
DI float sum32(float v) {
  auto r = __builtin_amdgcn_permlane32_swap(__float_as_uint(v), __float_as_uint(v), false, false);
  return __uint_as_float(r[0]) + __uint_as_float(r[1]);
}
DI f2 ln_stats(float s1, float s2) {
  const float mean = s1 * (1.f / D);
  const float var = fmaxf(fmaf(-mean, mean, s2 * (1.f / D)), 0.f);
  return (f2){rb(mean), rb(__builtin_amdgcn_sqrtf(rb(rb(var) + 1e-5f)))};
}
typedef __amdgpu_buffer_rsrc_t rsrc_t;
DI rsrc_t mkrsrc(const void* p, u32 bytes) { return __builtin_amdgcn_make_buffer_rsrc((void*)p, 0, bytes, 0x00020000); }
DI u32 ldsaddr(const char* p) { return (u32)(size_t)(LAS const char*)p; }
template <int OFF> DI void glds16(u32 m0v, u32 voff, rsrc_t rs, u32 soff) {
  asm volatile("s_mov_b32 m0, %0\n\ts_nop 0\n\tbuffer_load_dwordx4 %1, %2, %3 offen offset:%4 lds" :: "s"(m0v), "v"(voff), "s"(rs), "s"(soff), "n"(OFF) : "memory");
}
DI void glds16x4(u32 m0v, u32 voff, rsrc_t rs, u32 soff) {
  asm volatile("s_mov_b32 m0, %0\n\ts_nop 0\n\tbuffer_load_dwordx4 %1, %2, %3 offen lds\n\tbuffer_load_dwordx4 %1, %2, %3 offen offset:1024 lds\n\t"
               "buffer_load_dwordx4 %1, %2, %3 offen offset:2048 lds\n\tbuffer_load_dwordx4 %1, %2, %3 offen offset:3072 lds"
               :: "s"(m0v), "v"(voff), "s"(rs), "s"(soff) : "memory");
}
template <int OFF> DI void bld16(u4& d, u32 voff, rsrc_t rs, u32 soff) {
  asm volatile("buffer_load_dwordx4 %0, %1, %2, %3 offen offset:%4" : "=v"(d) : "v"(voff), "s"(rs), "s"(soff), "n"(OFF));
}
template <int OFF> DI void bld16_sc1(u4& d, u32 voff, rsrc_t rs, u32 soff) {
  asm volatile("buffer_load_dwordx4 %0, %1, %2, %3 offen offset:%4 sc1" : "=v"(d) : "v"(voff), "s"(rs), "s"(soff), "n"(OFF));
}
template <int OFF, class T> DI void gld16(T& d, const void* p) { asm volatile("global_load_dwordx4 %0, %1, off offset:%2" : "=v"(d) : "v"(p), "n"(OFF)); }
DI void pin(u32& v) { asm("" : "+v"(v)); }
DI void rawbar() { asm volatile("s_barrier" ::: "memory"); }
template <int N> DI void vmwait() { asm volatile("s_waitcnt vmcnt(%0)" :: "n"(N) : "memory"); }
template <int N> DI void vmwait(u4& d) { asm volatile("s_waitcnt vmcnt(%1)" : "+v"(d) : "n"(N)); }
DI void bar() { asm volatile("s_waitcnt lgkmcnt(0)\n\ts_barrier" ::: "memory"); }
#define SB() __builtin_amdgcn_sched_barrier(0)
#define SGB(m, n) __builtin_amdgcn_sched_group_barrier(m, n, 0)
template <int I, int N, class F> DI void sfor(F&& f) { if constexpr (I < N) { f(std::integral_constant<int, I>{}); sfor<I + 1, N>(f); } }

typedef unsigned long long u64;
constexpr int KG_ROW = 544, KG_CH = 57 * KG_ROW, KG_T = 2 * KG_CH, KG_TS = 4160;
constexpr int CV_EX = 0, CV_K = KG_T + 2 * KG_TS, CV_KC = 4128, CV_H = CV_K + 8 * CV_KC, CV_HP = 1152, CV_X = CV_H + 8 * CV_HP, CV_ST = CV_X + 8192,
              CV_W = CV_ST + 8192, CV_TB = CV_W + 16384, CV_V = CV_TB + 9216, CV_M = CV_V + 1024, CV_FL = CV_M + 384, K1_LDS = CV_FL + 128;
enum { FL_EPOCH = 0, FL_EPOK = 2, FL_KG = 3, FL_MOD = 7, FL_MAIN = 8, FL_XT = 9, FL_XD = 10, FL_GO = 11, FL_EXCH = 12, FL_RDY = 16 };
typedef volatile LAS u32* ldsw;
DI void lds_arrive(ldsw w) {
  asm volatile("s_waitcnt lgkmcnt(0)" ::: "memory");
  if ((threadIdx.x & 63) == 0) __hip_atomic_fetch_add((LAS u32*)w, 1u, __ATOMIC_RELAXED, __HIP_MEMORY_SCOPE_WORKGROUP);
}
DI void lds_wait(ldsw w, u32 n) { while (*w < n) {} asm volatile("" ::: "memory"); }
DI u64 get_epoch(ldsw fl) { lds_wait(fl + FL_EPOK, 1u); return ((u64)fl[FL_EPOCH + 1] << 32) | fl[FL_EPOCH]; }
DI u4 ld16_sc1(const void* p) { u4 d; asm volatile("global_load_dwordx4 %0, %1, off sc1\n\ts_waitcnt vmcnt(0)" : "=v"(d) : "v"(p) : "memory"); return d; }
template <int OFF> DI void gld16_sc1(u4& d, const void* p) { asm volatile("global_load_dwordx4 %0, %1, off offset:%2 sc1" : "=v"(d) : "v"(p), "n"(OFF) : "memory"); }
DI bool mg_ok(u4 g, u64 ep) { const u32 h = (u32)(ep >> 8), l = (u32)ep << 24; return g.y == h && g.w == h && (g.x & 0xff000000u) == l && (g.z & 0xff000000u) == l; }
DI f2 mg_val(u4 g) { return (f2){__uint_as_float(g.x << 8), __uint_as_float(g.z << 8)}; }
DI float scale_c(float v) { v = rb(1.f + rb(v)); return v == 0.f ? 0x1p-60f : v; }

DI f2 cexp_pow2(float a, float fh, float fl, float p) {
  const float e = __builtin_amdgcn_exp2f(a * p);
  const float r = fmaf(fl, p, __builtin_amdgcn_fractf(fh * p));
  return (f2){e * __builtin_amdgcn_cosf(r), e * __builtin_amdgcn_sinf(r)};
}

DI void k1_help(char* sm, int b, const u16* lar, const u16* aim, const u16* cre, const u16* cim, const u16* ldt, const u16* dsk,
                const u16* wg, const u16* wu, const u16* wd, char* ws) {
  const int t = threadIdx.x - 256, w = __builtin_amdgcn_readfirstlane(t >> 6), lane = t & 63, ch = w >> 1, half = w & 1;
  const int d0 = 64 * (b & 7) + 2 * (b >> 3), d = d0 + ch, n = 64 * half + lane;
  const ldsw fl = (ldsw)(size_t)(ldsaddr(sm) + CV_FL);
  const u32 v_lar = ldnt(lar + d * NM + n), v_aim = ldnt(aim + d * NM + n);
  const u32 cr = ldnt(cre + d * NM + n), ci = ldnt(cim + d * NM + n), cr2 = ldnt(cre + d * NM + (n ^ 64)), ci2 = ldnt(cim + d * NM + (n ^ 64));
  const u16 v_ldt = ldt[d], v_dsk = dsk[d];
  __builtin_amdgcn_s_setprio(1);
  rawbar();
  const float dt = rb(exp_acc(b2f(v_ldt)));
  const float ds = b2f(v_dsk);
  const float zr = -rb(exp_acc(blo(v_lar))) * dt, zi = blo(v_aim) * dt;
  const float a = zr * 1.44269502f;
  const float fh = zi * 0.159154937f, fq = fmaf(zi, 0.159154937f, -fh) + zi * 6.42063833e-9f;
  const f2 e1 = cexp_pow2(a, fh, fq, 1.f), e8 = cexp_pow2(a, -fh, -fq, 8.f), e128 = cexp_pow2(a, fh, fq, 128.f), e1024 = cexp_pow2(a, fh, fq, 1024.f);
  int mx = (int)max(max(cr & 0x7fffu, ci & 0x7fffu), max(cr2 & 0x7fffu, ci2 & 0x7fffu));
  mx = max(mx, __builtin_amdgcn_update_dpp(0, mx, 0x111, 0xf, 0xf, true));
  mx = max(mx, __builtin_amdgcn_update_dpp(0, mx, 0x112, 0xf, 0xf, true));
  mx = max(mx, __builtin_amdgcn_update_dpp(0, mx, 0x114, 0xf, 0xf, true));
  mx = max(mx, __builtin_amdgcn_update_dpp(0, mx, 0x118, 0xf, 0xf, true));
  mx = max(max(__builtin_amdgcn_readlane(mx, 15), __builtin_amdgcn_readlane(mx, 31)), max(__builtin_amdgcn_readlane(mx, 47), __builtin_amdgcn_readlane(mx, 63)));
  const int ex = min(max(mx >> 7, 1), 253);
  const float cs = __int_as_float((254 - ex) << 23), kf = 2.f * dt * __int_as_float(ex << 23);
  float dsv = ds, kfv = kf, fhv = fh, av = a, crv = blo(cr) * cs, civ = -blo(ci) * cs;
  asm volatile("" : "+v"(dsv), "+v"(kfv), "+v"(fhv), "+v"(av), "+v"(crv), "+v"(civ));
  char* base = sm + ch * KG_CH + n * 4;
  auto put = [&](int row, f2 v) { *(u32*)(base + row * KG_ROW) = pkh(v); };
  f2 lo[8], e[8], m[8], x0, x1, x2, x3, e16, e32, e64;
  lo[0] = (f2){crv, civ};
  e[1] = e1; m[1] = e128;
  put(0, lo[0]); put(16, (f2){1.f, 0.f}); put(17, e1); put(24, e128); put(48, e1024);
  cmul3(e16, e8, e8, e[2], e1, e1, m[2], e128, e128);
  cmul2(lo[1], lo[0], e8, x0, e1024, e1024);
  put(18, e[2]); put(32, m[2]); put(1, lo[1]); put(56, x0);
  cmul3(e32, e16, e16, e[4], e[2], e[2], m[4], m[2], m[2]);
  cmul2(e[3], e[2], e1, m[3], m[2], e128);
  cmul2(lo[2], lo[0], e16, lo[3], lo[1], e16);
  put(20, e[4]); put(40, m[4]); put(19, e[3]); put(36, m[3]); put(2, lo[2]); put(3, lo[3]);
  cmul4(e[5], e[6], e[7], x0, e1, e[2], e[3], m[4], e[4]);
  put(21, e[5]); put(22, e[6]); put(23, e[7]); put(41, x0);
  cmul3(m[5], m[4], e128, m[6], m[4], m[2], m[7], m[4], m[3]);
  put(42, m[5]); put(44, m[6]); put(46, m[7]);
  cmul4(lo[4], lo[5], lo[6], lo[7], lo[0], lo[1], lo[2], lo[3], e32);
  put(4, lo[4]); put(5, lo[5]); put(6, lo[6]); put(7, lo[7]);
  cmul2(e64, e32, e32, x0, e128, e1);
  put(25, x0);
  cmul3(x0, m[5], e[4], x1, m[6], e[4], x2, m[7], e[4]);
  put(43, x0); put(45, x1); put(47, x2);
  cmul4(x0, x1, x2, x3, lo[0], lo[1], lo[2], lo[3], e64);
  put(8, x0); put(9, x1); put(10, x2); put(11, x3);
  cmul4(x0, x1, x2, x3, lo[4], lo[5], lo[6], lo[7], e64);
  put(12, x0); put(13, x1); put(14, x2); put(15, x3);
  cmul4(x0, x1, x2, x3, e[2], e[3], e[4], e[5], e128);
  put(26, x0); put(27, x1); put(28, x2); put(29, x3);
  cmul2(x0, e[6], e128, x1, e[7], e128);
  put(30, x0); put(31, x1);
  cmul3(x0, e[2], m[2], x1, e[4], m[2], x2, e[6], m[2]);
  put(33, x0); put(34, x1); put(35, x2);
  cmul3(x0, e[2], m[3], x1, e[4], m[3], x2, e[6], m[3]);
  put(37, x0); put(38, x1); put(39, x2);
  cmul4(x0, x1, x2, x3, m[1], m[2], m[3], m[4], e1024);
  put(49, x0); put(50, x1); put(51, x2); put(52, x3);
  cmul3(x0, m[5], e1024, x1, m[6], e1024, x2, m[7], e1024);
  put(53, x0); put(54, x1); put(55, x2);
  lds_arrive(fl + FL_KG + ch);
  const int task = b + 256 * w, kq = task & 3, blk = task >> 2, mat = blk >= 43, jb = blk - 43 * mat;
  const bool work = task < 344;
  if (work) {
    const rsrc_t rw = mkrsrc(mat ? wu : wg, D * DF * 2);
    const u32 vo = (128 * kq + (lane >> 2)) * (DF * 2) + 64 * jb + 16 * (lane & 3);
    asm volatile("s_nop 4");
#pragma unroll
    for (int i = 0; i < 8; ++i) glds16<0>(ldsaddr(sm) + CV_W + 8192 * w + 1024 * i, vo, rw, i * 16 * DF * 2);
  }
  u32 early = 0;
  if (w == 3) asm volatile("global_load_dword %0, %1, off" : "=v"(early) : "v"((const char*)wd + b * 5504 + min(lane, 42) * 128));

  lds_wait(fl + FL_KG + ch, 2u);
  {
    const int c = lane & 15, g = lane >> 4;
    const char* fb = sm + ch * KG_CH + c * KG_ROW + g * 16;
    u16* T = (u16*)(sm + KG_T + ch * KG_TS);
    f4 acc0 = {0.f, 0.f, 0.f, 0.f}, acc1 = acc0;
    u4 B[8], A[8];
#pragma unroll
    for (int s = 0; s < 8; ++s) { B[s] = *(const u4*)(fb + s * 64); A[s] = *(const u4*)(fb + (16 + 16 * half) * KG_ROW + s * 64); }
    if (!half) {
      u4 A2[8];
#pragma unroll
      for (int s = 0; s < 8; ++s) A2[s] = *(const u4*)(fb + 48 * KG_ROW + s * 64);
      SB();
#pragma unroll
      for (int s = 0; s < 8; ++s) acc0 = mfma16h(A[s], B[s], acc0);
#pragma unroll
      for (int s = 0; s < 8; ++s) acc1 = mfma16h(A2[s], B[s], acc1);
      acc0 *= kfv; acc1 *= kfv;
      if (lane == 0) acc0[0] += dsv;
      *(u2*)(T + 8 * c + 4 * (g & 1) + 128 * (g >> 1)) = (u2){pk(acc0[0], acc0[1]), pk(acc0[2], acc0[3])};
      if (g < 2) {
#pragma unroll
        for (int e = 0; e < 4; ++e) T[1024 + 128 * (4 * g + e) + 8 * c] = (u16)pk(acc1[e], 0.f);
      } else if (lane == 32) T[2048] = (u16)pk(acc1[0], 0.f);
    } else {
      SB();
#pragma unroll
      for (int s = 0; s < 8; ++s) acc0 = mfma16h(A[s], B[s], acc0);
      acc0 *= kfv;
#pragma unroll
      for (int e = 0; e < 4; ++e) {
        const int p = g < 2 ? 256 + 128 * g + 2 * e : 512 + 128 * (2 * (g - 2) + (e >> 1)) + 4 * (e & 1);
        T[p + 8 * c] = (u16)pk(acc0[e], 0.f);
      }
    }
  }
  lds_arrive(fl + FL_KG + 2 + ch);
  constexpr u32 KEEP = 0x03020100u, LL = 0x01000100u, LH = 0x05040100u, HH = 0x05040504u;
  const u32 sel0 = n < 16 ? KEEP : LL, sel1 = n < 16 ? KEEP : n < 64 ? LH : LL, sel2 = n < 16 ? KEEP : n < 64 ? LL : LH, sel2b = n < 64 ? sel2 : HH;
  const u32 sel3 = n < 16 ? KEEP : n < 32 ? LH : HH, selp2 = n <= 16 ? KEEP : n <= 64 ? LL : HH, selp3 = n <= 16 ? KEEP : n <= 32 ? LH : HH;
  if (t < 16) *(u4*)(sm + CV_K + (t >> 1) * CV_KC + 16 * (t & 1)) = (u4){0u, 0u, 0u, 0u};
  lds_wait(fl + FL_KG + 2 + ch, 2u);
  {
    const u16* T = (const u16*)(sm + KG_T + ch * KG_TS) + 16 * n;
    const u4 v0 = *(const u4*)T, v1 = *(const u4*)(T + 8);
    const u2 vp = *(const u2*)(T - 4);
    const u32 e16 = T[16];
    u32 k[10];
    k[0] = n ? __builtin_amdgcn_perm(v0.x, vp.x, selp2) : 0u;
    k[1] = n ? __builtin_amdgcn_perm(v0.x, vp.y, selp3) : 0u;
    k[2] = __builtin_amdgcn_perm(v0.x, v0.x, sel0); k[3] = __builtin_amdgcn_perm(v0.z, n < 32 ? v0.y : v0.x, sel1);
    k[4] = __builtin_amdgcn_perm(v1.x, n < 64 ? v0.z : v0.x, sel2); k[5] = __builtin_amdgcn_perm(v1.x, v0.w, sel3);
    k[6] = __builtin_amdgcn_perm(v1.x, v1.x, sel0); k[7] = __builtin_amdgcn_perm(v1.z, n < 32 ? v1.y : v1.x, sel1);
    k[8] = __builtin_amdgcn_perm(e16, v1.z, sel2b); k[9] = __builtin_amdgcn_perm(e16, v1.w, sel3);
    u32 o[9];
#pragma unroll
    for (int i = 0; i < 9; ++i) o[i] = __builtin_amdgcn_alignbit(k[i + 1], k[i], 16);
    char* kd = sm + CV_K + 4 * ch * CV_KC + 32 + 32 * n;
#pragma unroll
    for (int i = 0; i < 2; ++i) {
      *(u4*)(kd + 16 * i) = (u4){k[4 * i + 2], k[4 * i + 3], k[4 * i + 4], k[4 * i + 5]};
      *(u4*)(kd + 16 * i + CV_KC) = (u4){o[4 * i + 1], o[4 * i + 2], o[4 * i + 3], o[4 * i + 4]};
      *(u4*)(kd + 16 * i + 2 * CV_KC) = (u4){k[4 * i + 1], k[4 * i + 2], k[4 * i + 3], k[4 * i + 4]};
      *(u4*)(kd + 16 * i + 3 * CV_KC) = (u4){o[4 * i], o[4 * i + 1], o[4 * i + 2], o[4 * i + 3]};
    }
  }
  __builtin_amdgcn_s_setprio(0);
  lds_arrive(fl + FL_RDY);
  asm volatile("s_waitcnt vmcnt(0)" : "+v"(early));
  if (w >= 2) {
    while (fl[FL_MAIN] < 7u) __builtin_amdgcn_s_sleep(2);
    u32 dummy[2];
    if (b < 32 && w == 3) {
      u32 lo, hi;
      asm("s_getpc_b64 s[40:41]\n\ts_add_u32 %0, s40, _Z6k3_mlpPKtS0_S0_PcPt@rel32@lo+4\n\ts_addc_u32 %1, s41, _Z6k3_mlpPKtS0_S0_PcPt@rel32@hi+12" : "=s"(lo), "=s"(hi) :: "s40", "s41", "scc");
      const char* k3 = (const char*)(((u64)hi << 32) | lo);
#pragma unroll
      for (int i = 0; i < 2; ++i) asm volatile("global_load_dword %0, %1, off" : "=v"(dummy[i]) : "v"(k3 + min(128 * (lane + 64 * i), 12160)));
      asm volatile("s_waitcnt vmcnt(0)" : "+v"(dummy[0]), "+v"(dummy[1]));
    }
#pragma unroll
    for (int k = 0; k < 2; ++k) {
      const int wk = 2 * (w - 2) + k, row = 32 * (b >> 3) + 8 * wk + (lane >> 3);
      const char* base = (const char*)(row >= D ? wu : wg) + (row & (D - 1)) * (DF * 2);
#pragma unroll
      for (int i = 0; i < 2; ++i) {
        const int off = min((b & 1) * 1344 + 128 * ((lane & 7) + 8 * i), (b & 1) ? DF * 2 - 4 : 1340);
        asm volatile("global_load_dword %0, %1, off" : "=v"(dummy[i]) : "v"(base + off));
      }
      asm volatile("s_waitcnt vmcnt(0)" : "+v"(dummy[0]), "+v"(dummy[1]));
    }
#pragma unroll
    for (int k = 0; k < 2; ++k) {
#pragma unroll
      for (int i = 0; i < 2; ++i)
        asm volatile("global_load_dword %0, %1, off" : "=v"(dummy[i]) : "v"((const char*)wd + (b >> 3) * 44032 + (2 * (w - 2) + k) * 11008 + min(lane + 64 * i, 85) * 128));
      asm volatile("s_waitcnt vmcnt(0)" : "+v"(dummy[0]), "+v"(dummy[1]));
    }
  }
  if (work) {
    const u64 ep = get_epoch(fl);
    while (fl[FL_MAIN] < 7u) __builtin_amdgcn_s_sleep(2);
    const char* gp = ws + WS_MG + (3 * D + 128 * kq + 2 * lane) * 8;
    u4 gs[4], gc[4];
#pragma unroll 1
    for (int spin = 0; spin < (1 << 16); ++spin) {
#pragma unroll
      for (int i = 0; i < 4; ++i) { gld16_sc1<0>(gs[i], gp + i * (6 * D * 8)); gld16_sc1<0>(gc[i], gp + i * (6 * D * 8) + D * 8); }
      asm volatile("s_waitcnt vmcnt(0)" : "+v"(gs[0]), "+v"(gs[1]), "+v"(gs[2]), "+v"(gs[3]), "+v"(gc[0]), "+v"(gc[1]), "+v"(gc[2]), "+v"(gc[3]) :: "memory");
      bool ok = true;
#pragma unroll
      for (int i = 0; i < 4; ++i) ok = ok && mg_ok(gs[i], ep) && mg_ok(gc[i], ep);
      if (__all(ok)) break;
      __builtin_amdgcn_s_sleep(8);
    }
    u32* vv = (u32*)(sm + CV_V + 512 * w);
    const f2 vc = (mg_val(gc[0]) + mg_val(gc[1])) + (mg_val(gc[2]) + mg_val(gc[3])), vs = (mg_val(gs[0]) + mg_val(gs[1])) + (mg_val(gs[2]) + mg_val(gs[3]));
    vv[lane] = pk(scale_c(vc.x), scale_c(vc.y));
    vv[64 + lane] = pk(vs.x, vs.y);
    const int hh = lane >> 5;
    const u32 ra = ldsaddr(sm) + CV_W + 8192 * w + 64 * (8 * hh + ((lane & 15) >> 2)) + 32 * ((lane >> 4) & 1) + 8 * (lane & 3);
    f16v sums;
#pragma unroll
    for (int e = 0; e < 16; ++e) sums[e] = 0.f;
#pragma unroll
    for (int i = 0; i < 8; ++i) sums = mfma32(*(const u4*)(vv + 64 * (lane & 1) + 8 * i + 4 * hh), cat(trd(ra + 1024 * i), trd(ra + 1024 * i + 256)), sums);
    if (lane < 32) {
      u32* exf = (u32*)(ws + WS_EX) + ((2 * jb + (lane >> 4)) * 64 + 16 * mat + (lane & 15)) * 4 + kq;
      const float h0 = rb(sums[0]), h1 = rb(sums[1]);
      exf[0] = pk(h0, sums[0] - h0);
      exf[128] = pk(h1, sums[1] - h1);
    }
  }
}

DI float red2(float a, float b, bool rows16) {
  if (rows16) { const auto r = __builtin_amdgcn_permlane16_swap(__float_as_uint(a), __float_as_uint(b), false, false); return __uint_as_float(r[0]) + __uint_as_float(r[1]); }
  const auto r = __builtin_amdgcn_permlane32_swap(__float_as_uint(a), __float_as_uint(b), false, false);
  return __uint_as_float(r[0]) + __uint_as_float(r[1]);
}

DI void k1_main(char* sm, int b, const u16* x, const u16* c, const u16* wm, char* ws) {
  const int t = threadIdx.x, w = __builtin_amdgcn_readfirstlane(t >> 6), lane = t & 63, ch = w >> 1, half = w & 1;
  const int d0 = 64 * (b & 7) + 2 * (b >> 3), d = d0 + ch;
  const ldsw fl = (ldsw)(size_t)(ldsaddr(sm) + CV_FL);
  const int c16 = lane & 15, g = lane >> 4;
  __builtin_amdgcn_s_setprio(2);
  if (t < 20) fl[t] = 0u;
  if (w == 2) *(u4*)(sm + CV_H + (lane >> 3) * CV_HP + 16 * (t & 7)) = (u4){0u, 0u, 0u, 0u};
  asm volatile("s_waitcnt lgkmcnt(0)" ::: "memory");
  if (w == 0) {
    const int row = 8 * b + (lane >> 3);
    u4 xs[8];
    u64 ep0;
    asm volatile("global_load_dwordx2 %0, %1, %2 sc1" : "=v"(ep0) : "v"(0), "s"(ws + WS_EPOCH));
#pragma unroll
    for (int i = 0; i < 8; ++i) xs[i] = ldnt((const u4*)(x + row * D + 64 * i + 8 * (lane & 7)));
    SB();
    rawbar();
    SB();
    float s1 = 0.f, s2 = 0.f, s3 = 0.f, s4 = 0.f;
#pragma unroll
    for (int i = 0; i < 8; ++i) {
#pragma unroll
      for (int j = 0; j < 4; j += 2) {
        s1 = dot2(xs[i][j], 0x3f803f80u, s1); s2 = dot2(xs[i][j], xs[i][j], s2);
        s3 = dot2(xs[i][j + 1], 0x3f803f80u, s3); s4 = dot2(xs[i][j + 1], xs[i][j + 1], s4);
      }
      if (i == 0) {
        asm volatile("" : "+v"(ep0), "+v"(s1));
        fl[FL_EPOCH] = (u32)(ep0 + 1); fl[FL_EPOCH + 1] = (u32)((ep0 + 1) >> 32);
        asm volatile("s_waitcnt lgkmcnt(0)" ::: "memory");
        fl[FL_EPOK] = 1u;
      }
    }
    s1 += s3; s2 += s4;
    s1 += dppf<0xb1>(s1); s2 += dppf<0xb1>(s2); s1 += dppf<0x4e>(s1); s2 += dppf<0x4e>(s2); s1 += dppf<0x141>(s1); s2 += dppf<0x141>(s2);
    const f2 st = ln_stats(s1, s2);
    if ((lane & 7) == 0) {
      __hip_atomic_store((u64*)(ws + WS_ST1) + 16 * b + (lane >> 3), ((ep0 + 1) << 32) | (__float_as_uint(st.x) >> 16) | __float_as_uint(st.y), __ATOMIC_RELAXED, __HIP_MEMORY_SCOPE_AGENT);
      ((u32*)(ws + WS_ST2))[row] = 0u;
    }
    if (lane == 0 && b < 16) ((u32*)(ws + WS_CNT))[64 * b] = 0u;
  } else if (w < 3) {
    const int mq = (b / 48) & 3, mc = b % 48, rg = (t - 64) >> 3;
    u4 wv[8];
    const u4 cv = *(const u4*)(c + 128 * mq + 8 * rg);
    if (b < 192) {
#pragma unroll
      for (int i = 0; i < 8; ++i) wv[i] = ldnt((const u4*)(wm + (128 * mq + 8 * rg + i) * (6 * D) + 64 * mc + 8 * (t & 7)));
    }
    SB();
    rawbar();
    SB();
    if (b < 192) {
      u32 sp[4];
#pragma unroll
      for (int i = 0; i < 4; ++i) {
        const float c0 = blo(cv[i]), c1 = bhi(cv[i]);
        sp[i] = pk(c0 * __builtin_amdgcn_rcpf(1.f + __expf(-c0)), c1 * __builtin_amdgcn_rcpf(1.f + __expf(-c1)));
      }
      float acc[8] = {0.f, 0.f, 0.f, 0.f, 0.f, 0.f, 0.f, 0.f};
#pragma unroll
      for (int i = 0; i < 4; ++i)
#pragma unroll
        for (int j = 0; j < 4; ++j) {
          acc[2 * j] = dot2(__builtin_amdgcn_perm(wv[2 * i + 1][j], wv[2 * i][j], 0x05040100u), sp[i], acc[2 * j]);
          acc[2 * j + 1] = dot2(__builtin_amdgcn_perm(wv[2 * i + 1][j], wv[2 * i][j], 0x07060302u), sp[i], acc[2 * j + 1]);
        }
      const float p0 = red2(red2(acc[0], acc[4], false), red2(acc[2], acc[6], false), true), p1 = red2(red2(acc[1], acc[5], false), red2(acc[3], acc[7], false), true);
      const float q0 = p0 + dppf<0x128>(p0), q1 = p1 + dppf<0x128>(p1);
      float v = (lane & 8) ? q1 : q0;
      float* mx = (float*)(sm + CV_M);
      if (w == 2) { mx[lane] = v; lds_arrive(fl + FL_MOD); }
      else {
        const u64 ep = get_epoch(fl);
        lds_wait(fl + FL_MOD, 1u);
        v += mx[lane];
        const u32 bits = __float_as_uint(v) + 0x80u;
        __hip_atomic_store((u64*)(ws + WS_MG) + mq * (6 * D) + 64 * mc + 8 * (lane & 7) + (lane >> 3), (ep << 24) | (bits >> 8), __ATOMIC_RELAXED, __HIP_MEMORY_SCOPE_AGENT);
      }
    }
  } else {
    const int r0 = 64 * (b >> 3), c0 = 64 * (b & 7), q = c16 >> 2, p = lane & 3;
    u4 v[8];
#pragma unroll
    for (int i = 0; i < 8; ++i) v[i] = ldnt((const u4*)(x + (r0 + 8 * i + (lane >> 3)) * D + c0 + 8 * (lane & 7)));
    SB();
    rawbar();
    SB();
    char* tb = sm + CV_TB;
#pragma unroll
    for (int i = 0; i < 8; ++i) *(u4*)(tb + (8 * i + (lane >> 3)) * 144 + 16 * (lane & 7)) = v[i];
#pragma unroll
    for (int i = 0; i < 8; ++i) {
      const char* a = tb + (32 * (i & 1) + 8 * g + q) * 144 + 32 * (i >> 1) + 8 * p;
      const u4 o = cat(trd(a), trd(a + 4 * 144));
      const u16* dst = (const u16*)(ws + WS_XT) + (c0 + 16 * (i >> 1) + c16) * L + r0 + 32 * (i & 1) + 8 * g;
      asm volatile("global_store_dwordx4 %0, %1, off sc1\n\ts_nop 1" :: "v"(dst), "v"(o) : "memory");
    }
    const u64 ep = get_epoch(fl);
    asm volatile("s_waitcnt vmcnt(0)" ::: "memory");
    if (lane == 0) __hip_atomic_store((u32*)(ws + WS_FXX) + 32 * b, (u32)ep, __ATOMIC_RELAXED, __HIP_MEMORY_SCOPE_AGENT);
  }
  __builtin_amdgcn_s_setprio(0);
  const u64 ep = get_epoch(fl);
  u4 xv[2];
  float* mv = (float*)(sm + CV_M + 256);
  if (w == 3) {
    fl[FL_XD] = 1u;
    const u32* fx = (const u32*)(ws + WS_FXX) + 32 * ((b & 7) + 8 * (lane & 31));
    __builtin_amdgcn_s_sleep(4);
#pragma unroll 1
    for (int spin = 0; spin < (1 << 16); ++spin) {
      if (__all(__hip_atomic_load(fx, __ATOMIC_RELAXED, __HIP_MEMORY_SCOPE_AGENT) == (u32)ep)) break;
      __builtin_amdgcn_s_sleep(2);
    }
    fl[FL_GO] = 1u;
  } else {
    while (fl[FL_XD] == 0u) __builtin_amdgcn_s_sleep(1);
    __builtin_amdgcn_s_sleep(12);
    const int ml = lane < 20 ? lane : 19, mch = ml >> 2;
    const char* mp = ws + WS_MG + ((ml & 3) * (6 * D) + (mch + (mch > 2)) * D + d0) * 8;
    u4 gs[6], gm;
#pragma unroll 1
    for (int spin = 0; spin < (1 << 16); ++spin) {
#pragma unroll
      for (int i = 0; i < 6; ++i) { const int pi = min(t + 192 * i, 1023); gld16_sc1<0>(gs[i], ws + WS_ST1 + 128 * (pi >> 2) + 16 * (pi & 3)); }
      gld16_sc1<0>(gm, mp);
      asm volatile("s_waitcnt vmcnt(0)" : "+v"(gs[0]), "+v"(gs[1]), "+v"(gs[2]), "+v"(gs[3]), "+v"(gs[4]), "+v"(gs[5]), "+v"(gm) :: "memory");
      bool ok = true;
#pragma unroll
      for (int i = 0; i < 6; ++i) ok = ok && gs[i].y == (u32)ep && gs[i].w == (u32)ep;
      if (__all(ok)) break;
      __builtin_amdgcn_s_sleep(2);
    }
#pragma unroll
    for (int i = 0; i < 6; ++i) *(u2*)(sm + CV_ST + 8 * min(t + 192 * i, 1023)) = (u2){gs[i].x, gs[i].z};
    if (w == 0) {
#pragma unroll 1
      for (int spin = 0; spin < (1 << 16) && !__all(mg_ok(gm, ep)); ++spin) gm = ld16_sc1(mp);
      f2 v = mg_val(gm);
      v.x += dppf<0xb1>(v.x); v.y += dppf<0xb1>(v.y); v.x += dppf<0x4e>(v.x); v.y += dppf<0x4e>(v.y);
      const float vm = (lane & 1) ? v.y : v.x, vr = rb(vm);
      if (lane < 20) mv[lane] = mch == 1 ? rb(1.f + vr) : mch == 3 ? scale_c(vm) : vr;
    }
    lds_arrive(fl + FL_MAIN);
    while (fl[FL_GO] == 0u) {}
  }
  asm volatile("" ::: "memory");
  {
    const char* xp = ws + WS_XT + d0 * (L * 2) + 16 * t;
    gld16_sc1<0>(xv[0], xp); gld16_sc1<0>(xv[1], xp + L * 2);
  }
  lds_wait(fl + FL_MAIN, 3u);
  const f2 sa = *(const f2*)mv, c1 = *(const f2*)(mv + 4);
  const float ga = mv[8 + ch], c2 = mv[12 + ch];
  __builtin_amdgcn_s_setprio(2);
  {
    const u4 m0 = *(const u4*)(sm + CV_ST + 32 * t), m1 = *(const u4*)(sm + CV_ST + 32 * t + 16);
    float rs[8], mm[8];
#pragma unroll
    for (int j = 0; j < 8; ++j) { const u32 z = j < 4 ? m0[j & 3] : m1[j & 3]; rs[j] = __builtin_amdgcn_rcpf(bhi(z)); mm[j] = blo(z); }
    asm volatile("s_waitcnt vmcnt(0)" : "+v"(xv[0]), "+v"(xv[1]), "+v"(rs[0]), "+v"(rs[7]) :: "memory");
    lds_arrive(fl + FL_MAIN);
#pragma unroll
    for (int i = 0; i < 2; ++i) {
      *(u4*)(sm + CV_X + 4096 * i + 16 * t) = xv[i];
      float h[8];
#pragma unroll
      for (int j = 0; j < 8; ++j) h[j] = fmaf((((j & 1) ? bhi(xv[i][j >> 1]) : blo(xv[i][j >> 1])) - mm[j]) * rs[j], c1[i], sa[i]);
#pragma unroll
      for (int r = 0; r < 4; ++r) *(u32*)(sm + CV_H + (4 * i + r) * CV_HP + 128 + 4 * t) = pk(h[r], h[r + 4]);
    }
  }
  f4 acc[8];
#pragma unroll
  for (int i = 0; i < 8; ++i) acc[i] = (f4){0.f, 0.f, 0.f, 0.f};
  u32 ka = ldsaddr(sm) + CV_K + (4 * ch + 2 * half) * CV_KC + 32 + 8 * (8 * g + (lane & 3) - ((lane & 15) >> 2));
  u32 hb = ldsaddr(sm) + CV_H + (4 * ch + 2 * half) * CV_HP + 128 + 8 * c16 - 16 * g - 8 - 64;
  lds_arrive(fl + FL_RDY);
  lds_wait(fl + FL_RDY, 8u);
  {
    u32 cnt;
    asm volatile(
      "ds_read_b64_tr_b16 v[64:65], %8 offset:32\n"
      "ds_read_b64_tr_b16 v[66:67], %8 offset:0\n"
      "ds_read_b64 v[120:121], %9 offset:832\n"
      "ds_read_b64 v[122:123], %9 offset:840\n"
      "ds_read_b64 v[124:125], %9 offset:960\n"
      "ds_read_b64 v[126:127], %9 offset:968\n"
      "ds_read_b64 v[112:113], %9 offset:576\n"
      "ds_read_b64 v[114:115], %9 offset:584\n"
      "ds_read_b64 v[116:117], %9 offset:704\n"
      "ds_read_b64 v[118:119], %9 offset:712\n"
      "ds_read_b64_tr_b16 v[68:69], %8 offset:544\n"
      "ds_read_b64_tr_b16 v[70:71], %8 offset:512\n"
      "ds_read_b64 v[108:109], %9 offset:448\n"
      "ds_read_b64 v[110:111], %9 offset:456\n"
      "ds_read_b64 v[104:105], %9 offset:320\n"
      "ds_read_b64 v[106:107], %9 offset:328\n"
      "ds_read_b64_tr_b16 v[72:73], %8 offset:1056\n"
      "ds_read_b64_tr_b16 v[74:75], %8 offset:1024\n"
      "ds_read_b64_tr_b16 v[76:77], %8 offset:1568\n"
      "ds_read_b64_tr_b16 v[78:79], %8 offset:1536\n"
      "ds_read_b64 v[100:101], %9 offset:192\n"
      "ds_read_b64 v[102:103], %9 offset:200\n"
      "ds_read_b64_tr_b16 v[80:81], %8 offset:2080\n"
      "ds_read_b64_tr_b16 v[82:83], %8 offset:2048\n"
      "ds_read_b64 v[96:97], %9 offset:64\n"
      "ds_read_b64 v[98:99], %9 offset:72\n"
      "ds_read_b64_tr_b16 v[84:85], %8 offset:2592\n"
      "ds_read_b64_tr_b16 v[86:87], %8 offset:2560\n"
      "ds_read_b64_tr_b16 v[88:89], %8 offset:3104\n"
      "ds_read_b64_tr_b16 v[90:91], %8 offset:3072\n"
      "ds_read_b64_tr_b16 v[92:93], %8 offset:3616\n"
      "ds_read_b64_tr_b16 v[94:95], %8 offset:3584\n"
      "s_mov_b32 %10, 2\n"
      "s_waitcnt lgkmcnt(0)\n"
      "Lconv%=:\n"
      "v_mfma_f32_16x16x32_bf16 %6, v[64:67], v[120:123], %6\n"
      "ds_read_b64_tr_b16 v[128:129], %8 offset:288\n"
      "v_mfma_f32_16x16x32_bf16 %7, v[64:67], v[124:127], %7\n"
      "ds_read_b64_tr_b16 v[130:131], %8 offset:256\n"
      "v_mfma_f32_16x16x32_bf16 %4, v[64:67], v[112:115], %4\n"
      "ds_read_b64 v[184:185], %9 offset:768\n"
      "v_mfma_f32_16x16x32_bf16 %5, v[64:67], v[116:119], %5\n"
      "ds_read_b64 v[186:187], %9 offset:776\n"
      "v_mfma_f32_16x16x32_bf16 %6, v[68:71], v[116:119], %6\n"
      "ds_read_b64 v[188:189], %9 offset:896\n"
      "v_mfma_f32_16x16x32_bf16 %7, v[68:71], v[120:123], %7\n"
      "ds_read_b64 v[190:191], %9 offset:904\n"
      "v_mfma_f32_16x16x32_bf16 %4, v[68:71], v[108:111], %4\n"
      "ds_read_b64 v[176:177], %9 offset:512\n"
      "v_mfma_f32_16x16x32_bf16 %2, v[64:67], v[104:107], %2\n"
      "ds_read_b64 v[178:179], %9 offset:520\n"
      "v_mfma_f32_16x16x32_bf16 %6, v[72:75], v[112:115], %6\n"
      "ds_read_b64 v[180:181], %9 offset:640\n"
      "v_mfma_f32_16x16x32_bf16 %3, v[64:67], v[108:111], %3\n"
      "ds_read_b64 v[182:183], %9 offset:648\n"
      "v_mfma_f32_16x16x32_bf16 %7, v[72:75], v[116:119], %7\n"
      "ds_read_b64_tr_b16 v[132:133], %8 offset:800\n"
      "v_mfma_f32_16x16x32_bf16 %4, v[72:75], v[104:107], %4\n"
      "ds_read_b64_tr_b16 v[134:135], %8 offset:768\n"
      "v_mfma_f32_16x16x32_bf16 %5, v[68:71], v[112:115], %5\n"
      "ds_read_b64 v[172:173], %9 offset:384\n"
      "v_mfma_f32_16x16x32_bf16 %6, v[76:79], v[108:111], %6\n"
      "ds_read_b64 v[174:175], %9 offset:392\n"
      "v_mfma_f32_16x16x32_bf16 %7, v[76:79], v[112:115], %7\n"
      "ds_read_b64 v[168:169], %9 offset:256\n"
      "v_mfma_f32_16x16x32_bf16 %4, v[76:79], v[100:103], %4\n"
      "ds_read_b64 v[170:171], %9 offset:264\n"
      "v_mfma_f32_16x16x32_bf16 %1, v[64:67], v[100:103], %1\n"
      "ds_read_b64_tr_b16 v[136:137], %8 offset:1312\n"
      "v_mfma_f32_16x16x32_bf16 %5, v[72:75], v[108:111], %5\n"
      "ds_read_b64_tr_b16 v[138:139], %8 offset:1280\n"
      "v_mfma_f32_16x16x32_bf16 %6, v[80:83], v[104:107], %6\n"
      "ds_read_b64_tr_b16 v[140:141], %8 offset:1824\n"
      "v_mfma_f32_16x16x32_bf16 %0, v[64:67], v[96:99], %0\n"
      "ds_read_b64_tr_b16 v[142:143], %8 offset:1792\n"
      "v_mfma_f32_16x16x32_bf16 %7, v[80:83], v[108:111], %7\n"
      "ds_read_b64 v[164:165], %9 offset:128\n"
      "v_mfma_f32_16x16x32_bf16 %5, v[76:79], v[104:107], %5\n"
      "ds_read_b64 v[166:167], %9 offset:136\n"
      "v_mfma_f32_16x16x32_bf16 %2, v[68:71], v[100:103], %2\n"
      "ds_read_b64_tr_b16 v[144:145], %8 offset:2336\n"
      "v_mfma_f32_16x16x32_bf16 %6, v[84:87], v[100:103], %6\n"
      "ds_read_b64_tr_b16 v[146:147], %8 offset:2304\n"
      "v_mfma_f32_16x16x32_bf16 %7, v[84:87], v[104:107], %7\n"
      "ds_read_b64 v[160:161], %9 offset:0\n"
      "v_mfma_f32_16x16x32_bf16 %3, v[68:71], v[104:107], %3\n"
      "ds_read_b64 v[162:163], %9 offset:8\n"
      "v_mfma_f32_16x16x32_bf16 %2, v[72:75], v[96:99], %2\n"
      "ds_read_b64_tr_b16 v[148:149], %8 offset:2848\n"
      "v_mfma_f32_16x16x32_bf16 %5, v[80:83], v[100:103], %5\n"
      "ds_read_b64_tr_b16 v[150:151], %8 offset:2816\n"
      "v_mfma_f32_16x16x32_bf16 %7, v[88:91], v[100:103], %7\n"
      "ds_read_b64_tr_b16 v[152:153], %8 offset:3360\n"
      "v_mfma_f32_16x16x32_bf16 %6, v[88:91], v[96:99], %6\n"
      "ds_read_b64_tr_b16 v[154:155], %8 offset:3328\n"
      "v_mfma_f32_16x16x32_bf16 %3, v[72:75], v[100:103], %3\n"
      "ds_read_b64_tr_b16 v[156:157], %8 offset:3872\n"
      "v_mfma_f32_16x16x32_bf16 %1, v[68:71], v[96:99], %1\n"
      "ds_read_b64_tr_b16 v[158:159], %8 offset:3840\n"
      "v_mfma_f32_16x16x32_bf16 %7, v[92:95], v[96:99], %7\n"
      "v_mfma_f32_16x16x32_bf16 %5, v[84:87], v[96:99], %5\n"
      "v_mfma_f32_16x16x32_bf16 %4, v[80:83], v[96:99], %4\n"
      "v_mfma_f32_16x16x32_bf16 %3, v[76:79], v[96:99], %3\n"
      "s_waitcnt lgkmcnt(0)\n"
      "v_add_u32 %8, 4128, %8\n"
      "v_add_u32 %9, 1152, %9\n"
      "v_mfma_f32_16x16x32_bf16 %6, v[128:131], v[184:187], %6\n"
      "ds_read_b64_tr_b16 v[64:65], %8 offset:32\n"
      "v_mfma_f32_16x16x32_bf16 %7, v[128:131], v[188:191], %7\n"
      "ds_read_b64_tr_b16 v[66:67], %8 offset:0\n"
      "v_mfma_f32_16x16x32_bf16 %4, v[128:131], v[176:179], %4\n"
      "ds_read_b64 v[120:121], %9 offset:832\n"
      "v_mfma_f32_16x16x32_bf16 %5, v[128:131], v[180:183], %5\n"
      "ds_read_b64 v[122:123], %9 offset:840\n"
      "v_mfma_f32_16x16x32_bf16 %6, v[132:135], v[180:183], %6\n"
      "ds_read_b64 v[124:125], %9 offset:960\n"
      "v_mfma_f32_16x16x32_bf16 %7, v[132:135], v[184:187], %7\n"
      "ds_read_b64 v[126:127], %9 offset:968\n"
      "v_mfma_f32_16x16x32_bf16 %4, v[132:135], v[172:175], %4\n"
      "ds_read_b64 v[112:113], %9 offset:576\n"
      "v_mfma_f32_16x16x32_bf16 %2, v[128:131], v[168:171], %2\n"
      "ds_read_b64 v[114:115], %9 offset:584\n"
      "v_mfma_f32_16x16x32_bf16 %6, v[136:139], v[176:179], %6\n"
      "ds_read_b64 v[116:117], %9 offset:704\n"
      "v_mfma_f32_16x16x32_bf16 %3, v[128:131], v[172:175], %3\n"
      "ds_read_b64 v[118:119], %9 offset:712\n"
      "v_mfma_f32_16x16x32_bf16 %7, v[136:139], v[180:183], %7\n"
      "ds_read_b64_tr_b16 v[68:69], %8 offset:544\n"
      "v_mfma_f32_16x16x32_bf16 %4, v[136:139], v[168:171], %4\n"
      "ds_read_b64_tr_b16 v[70:71], %8 offset:512\n"
      "v_mfma_f32_16x16x32_bf16 %5, v[132:135], v[176:179], %5\n"
      "ds_read_b64 v[108:109], %9 offset:448\n"
      "v_mfma_f32_16x16x32_bf16 %6, v[140:143], v[172:175], %6\n"
      "ds_read_b64 v[110:111], %9 offset:456\n"
      "v_mfma_f32_16x16x32_bf16 %7, v[140:143], v[176:179], %7\n"
      "ds_read_b64 v[104:105], %9 offset:320\n"
      "v_mfma_f32_16x16x32_bf16 %4, v[140:143], v[164:167], %4\n"
      "ds_read_b64 v[106:107], %9 offset:328\n"
      "v_mfma_f32_16x16x32_bf16 %1, v[128:131], v[164:167], %1\n"
      "ds_read_b64_tr_b16 v[72:73], %8 offset:1056\n"
      "v_mfma_f32_16x16x32_bf16 %5, v[136:139], v[172:175], %5\n"
      "ds_read_b64_tr_b16 v[74:75], %8 offset:1024\n"
      "v_mfma_f32_16x16x32_bf16 %6, v[144:147], v[168:171], %6\n"
      "ds_read_b64_tr_b16 v[76:77], %8 offset:1568\n"
      "v_mfma_f32_16x16x32_bf16 %0, v[128:131], v[160:163], %0\n"
      "ds_read_b64_tr_b16 v[78:79], %8 offset:1536\n"
      "v_mfma_f32_16x16x32_bf16 %7, v[144:147], v[172:175], %7\n"
      "ds_read_b64 v[100:101], %9 offset:192\n"
      "v_mfma_f32_16x16x32_bf16 %5, v[140:143], v[168:171], %5\n"
      "ds_read_b64 v[102:103], %9 offset:200\n"
      "v_mfma_f32_16x16x32_bf16 %2, v[132:135], v[164:167], %2\n"
      "ds_read_b64_tr_b16 v[80:81], %8 offset:2080\n"
      "v_mfma_f32_16x16x32_bf16 %6, v[148:151], v[164:167], %6\n"
      "ds_read_b64_tr_b16 v[82:83], %8 offset:2048\n"
      "v_mfma_f32_16x16x32_bf16 %7, v[148:151], v[168:171], %7\n"
      "ds_read_b64 v[96:97], %9 offset:64\n"
      "v_mfma_f32_16x16x32_bf16 %3, v[132:135], v[168:171], %3\n"
      "ds_read_b64 v[98:99], %9 offset:72\n"
      "v_mfma_f32_16x16x32_bf16 %2, v[136:139], v[160:163], %2\n"
      "ds_read_b64_tr_b16 v[84:85], %8 offset:2592\n"
      "v_mfma_f32_16x16x32_bf16 %5, v[144:147], v[164:167], %5\n"
      "ds_read_b64_tr_b16 v[86:87], %8 offset:2560\n"
      "v_mfma_f32_16x16x32_bf16 %7, v[152:155], v[164:167], %7\n"
      "ds_read_b64_tr_b16 v[88:89], %8 offset:3104\n"
      "v_mfma_f32_16x16x32_bf16 %6, v[152:155], v[160:163], %6\n"
      "ds_read_b64_tr_b16 v[90:91], %8 offset:3072\n"
      "v_mfma_f32_16x16x32_bf16 %3, v[136:139], v[164:167], %3\n"
      "ds_read_b64_tr_b16 v[92:93], %8 offset:3616\n"
      "v_mfma_f32_16x16x32_bf16 %1, v[132:135], v[160:163], %1\n"
      "ds_read_b64_tr_b16 v[94:95], %8 offset:3584\n"
      "v_mfma_f32_16x16x32_bf16 %7, v[156:159], v[160:163], %7\n"
      "v_mfma_f32_16x16x32_bf16 %5, v[148:151], v[160:163], %5\n"
      "v_mfma_f32_16x16x32_bf16 %4, v[144:147], v[160:163], %4\n"
      "v_mfma_f32_16x16x32_bf16 %3, v[140:143], v[160:163], %3\n"
      "s_waitcnt lgkmcnt(0)\n"
      "s_sub_u32 %10, %10, 1\n"
      "s_cmp_lg_u32 %10, 0\n"
      "s_cbranch_scc1 Lconv%=\n"
      "s_nop 7\n"
      : "+v"(acc[0]), "+v"(acc[1]), "+v"(acc[2]), "+v"(acc[3]), "+v"(acc[4]), "+v"(acc[5]), "+v"(acc[6]), "+v"(acc[7]), "+v"(ka), "+v"(hb), "=&s"(cnt)
      :: "memory", "scc", "v64", "v65", "v66", "v67", "v68", "v69", "v70", "v71", "v72", "v73", "v74", "v75", "v76", "v77", "v78", "v79", "v80", "v81", "v82", "v83", "v84", "v85", "v86", "v87", "v88", "v89", "v90", "v91", "v92", "v93", "v94", "v95", "v96", "v97", "v98", "v99", "v100", "v101", "v102", "v103", "v104", "v105", "v106", "v107", "v108", "v109", "v110", "v111", "v112", "v113", "v114", "v115", "v116", "v117", "v118", "v119", "v120", "v121", "v122", "v123", "v124", "v125", "v126", "v127", "v128", "v129", "v130", "v131", "v132", "v133", "v134", "v135", "v136", "v137", "v138", "v139", "v140", "v141", "v142", "v143", "v144", "v145", "v146", "v147", "v148", "v149", "v150", "v151", "v152", "v153", "v154", "v155", "v156", "v157", "v158", "v159", "v160", "v161", "v162", "v163", "v164", "v165", "v166", "v167", "v168", "v169", "v170", "v171", "v172", "v173", "v174", "v175", "v176", "v177", "v178", "v179", "v180", "v181", "v182", "v183", "v184", "v185", "v186", "v187", "v188", "v189", "v190", "v191");
  }
  f4* ex = (f4*)(sm + CV_EX);
  if (half) {
#pragma unroll
    for (int i = 0; i < 4; ++i) { ex[(w * 4 + i) * 64 + lane] = acc[i]; acc[i] = acc[4 + i]; }
  } else {
#pragma unroll
    for (int i = 0; i < 4; ++i) ex[(w * 4 + i) * 64 + lane] = acc[4 + i];
  }
  lds_arrive(fl + FL_EXCH + w);
  u2 xr[4];
#pragma unroll
  for (int i = 0; i < 4; ++i) xr[i] = *(const u2*)(sm + CV_X + 4096 * ch + 2 * (256 * (4 * half + i) + 16 * c16 + 4 * g));
  lds_wait(fl + FL_EXCH + (w ^ 1), 1u);
  u16* x1t = (u16*)(ws + WS_X1T);
  const float gc = ga * c2;
#pragma unroll
  for (int i = 0; i < 4; ++i) {
    const f4 y = (acc[i] + ex[((w ^ 1) * 4 + i) * 64 + lane]) * gc + (f4){blo(xr[i].x), bhi(xr[i].x), blo(xr[i].y), bhi(xr[i].y)} * c2;
    *(u2*)(x1t + d * L + 256 * (4 * half + i) + 16 * c16 + 4 * g) = (u2){pk(y[0], y[1]), pk(y[2], y[3])};
  }
  if (w == 0) {
    if (lane < 2) { ((float*)(ws + WS_INV))[d0 + lane] = 1.f / mv[12 + lane]; ((float*)(ws + WS_GB))[d0 + lane] = mv[16 + lane]; }
    if (b == 0 && lane == 0) *(u64*)(ws + WS_EPOCH) = ep;
  }
}

__global__ __launch_bounds__(512, 2) void k1_conv(const u16* x, const u16* lar, const u16* aim, const u16* cre, const u16* cim, const u16* ldt, const u16* dsk,
                                                  const u16* c, const u16* wm, const u16* wg, const u16* wu, const u16* wd, char* ws) {
  extern __shared__ __attribute__((aligned(16))) char sm[];
  const int b = blockIdx.x;
  if (__builtin_amdgcn_readfirstlane(threadIdx.x) < 256) k1_main(sm, b, x, c, wm, ws);
  else k1_help(sm, b, lar, aim, cre, cim, ldt, dsk, wg, wu, wd, ws);
}

constexpr int G1_LEAD = 3, G1_NS = 5, G1_SLOT = 20480;
constexpr int G2_RES = 19, G2_FL = 4 * G2_RES * 2048, G1_SV = G2_FL + 64, K3_LDS = G1_SV + 512;
constexpr int G2_LEAD = 6;
constexpr int HH_T = NU * 1024;

template <int K> DI u32 g1_pair(const f16v& acc, float rs, float sd) {
  const f2 g = (f2){acc[2 * K], acc[2 * K + 1]} * rs;
  const f2 z = g * (g * g * -0.10294324f + -2.30220819f);
  const f2 dn = (f2){__builtin_amdgcn_exp2f(z.x), __builtin_amdgcn_exp2f(z.y)} * sd + sd;
  const f2 r = g * (f2){acc[8 + 2 * K], acc[9 + 2 * K]} * (f2){__builtin_amdgcn_rcpf(dn.x), __builtin_amdgcn_rcpf(dn.y)};
  return pk(r.x, r.y);
}
DI u4 g1_tile(const f16v& acc, float rs, float sd) { return (u4){g1_pair<0>(acc, rs, sd), g1_pair<1>(acc, rs, sd), g1_pair<2>(acc, rs, sd), g1_pair<3>(acc, rs, sd)}; }

DI void g1_stats(char* sm, int rt, char* ws) {
  const int t = threadIdx.x;
  float* red = (float*)sm;
  if (t < 512) {
    const u16* xp = (const u16*)(ws + WS_X1T) + (t >> 4) * L + 128 * rt + 8 * (t & 15);
    u4 v[16];
    float iv[16];
#pragma unroll
    for (int i = 0; i < 16; ++i) { v[i] = *(const u4*)(xp + 32 * i * L); iv[i] = ((const float*)(ws + WS_INV))[(t >> 4) + 32 * i]; }
    f2 s1[4], s2[4];
#pragma unroll
    for (int j = 0; j < 4; ++j) s1[j] = s2[j] = (f2){0.f, 0.f};
#pragma unroll
    for (int i = 0; i < 16; ++i)
#pragma unroll
      for (int j = 0; j < 4; ++j) { const f2 x = (f2){blo(v[i][j]), bhi(v[i][j])} * iv[i]; s1[j] += x; s2[j] += x * x; }
#pragma unroll
    for (int j = 0; j < 4; ++j) *(f4*)(red + (t >> 4) * 260 + 16 * (t & 15) + 4 * j) = (f4){s1[j].x, s2[j].x, s1[j].y, s2[j].y};
  }
  __syncthreads();
  if (t < 512) {
    f2 p = {0.f, 0.f};
#pragma unroll
    for (int i = 0; i < 8; ++i) p += *(const f2*)(red + (8 * (t & 3) + i) * 260 + 2 * (t >> 2));
    p.x += dppf<0xb1>(p.x); p.y += dppf<0xb1>(p.y); p.x += dppf<0x4e>(p.x); p.y += dppf<0x4e>(p.y);
    const f2 ms = ln_stats(p.x, p.y);
    if ((t & 3) == 0)
      __hip_atomic_store((u32*)(ws + WS_ST2) + 128 * rt + (t >> 2), (__float_as_uint(ms.x) >> 16) | __float_as_uint(ms.y), __ATOMIC_RELAXED, __HIP_MEMORY_SCOPE_AGENT);
  }
  __syncthreads();
}

__global__ __launch_bounds__(768, 3) void k3_mlp(const u16* wg, const u16* wu, const u16* wd, char* ws, u16* out) {
  extern __shared__ __attribute__((aligned(16))) char sm[];
  const int b = blockIdx.x, j = b >> 3;
  const int t = threadIdx.x, w12 = __builtin_amdgcn_readfirstlane(t >> 6), w = w12 & 3, lane = t & 63, hh = lane >> 5;
  const u32 lb0 = ldsaddr(sm);
  const int q = (lane & 15) >> 2, p = lane & 3, g = lane >> 4;
  const int lt = 4 * (b & 7) + (j & 3), dt = j >> 2;
  auto kfirst = [&](int kq) { const u32 k = 22 * kq - (kq == 3) + 3 * dt; return min(k, k - NU); };
  auto knext = [](u32 k) { return min(k + 1, k + 1 - NU); };
  const rsrc_t rd = mkrsrc(wd, DF * D * 2);
  const u32 vd = (lane >> 3) * 1024 + 128 * dt + 16 * ((lane & 7) ^ (4 * ((lane >> 4) & 1)));
  volatile LAS u32* fl = (volatile LAS u32*)(size_t)(lb0 + G2_FL);
  auto fill2 = [&](int i0, int n, int slot0) {
    u32 kd = kfirst(w) + i0;
    kd = min(kd, kd - NU);
#pragma unroll 1
    for (int i = 0; i < n; ++i) {
      glds16<0>(lb0 + (G2_RES * w + slot0 + i) * 2048, vd, rd, kd * 16384);
      glds16<0>(lb0 + (G2_RES * w + slot0 + i) * 2048 + 1024, vd, rd, kd * 16384 + 8192);
      kd = knext(kd);
      if (fl[1] < 4u) __builtin_amdgcn_s_sleep(3);
    }
  };
  {
    const int rt = 4 * ((b & 7) >> 1) + (j & 3), cg = (j >> 2) + 8 * (b & 1) - 1;
    if (t < 2) fl[t] = cg < 0 ? 4u : 0u;
    if (cg < 0) {
      g1_stats(sm, rt, ws);
    } else if (w12 >= 8) {
      const rsrc_t rx = mkrsrc(ws + WS_X1T, D * L * 2), rg = mkrsrc(wg, D * DF * 2), ru = mkrsrc(wu, D * DF * 2);
      const u32 vx = (4 * w + g) * 4096 + 256 * rt + 16 * ((lane & 15) ^ ((g << 2) | w));
      u32 vw[3];
#pragma unroll
      for (int i = 0; i < 3; ++i) {
        const int pc = (3 * (w & 1) + i) * 64 + lane, row = pc / 12, chk = pc - 12 * row;
        vw[i] = row * (DF * 2) + min(192 * cg + 16 * (chk ^ (w & 2)), DF * 2 - 16);
      }
      const rsrc_t rwm = w < 2 ? rg : ru;
      auto stage = [&](int i, int slot) {
        const u32 m = lb0 + slot * G1_SLOT;
        glds16<0>(m + 1024 * w, vx, rx, i * 131072);
        glds16<0>(m + 4096 + 1024 * w, vx, rx, i * 131072 + 65536);
#pragma unroll
        for (int k = 0; k < 3; ++k) glds16<0>(m + 8192 + 3072 * w + 1024 * k, vw[k], rwm, i * (32 * DF * 2));
      };
      asm volatile("s_nop 4");
      static_assert(G1_LEAD + 2 <= G1_NS, "ring too short");
#pragma unroll 1
      for (int i = 0; i < G1_LEAD - 1; ++i) stage(i, i);
      int slot = G1_LEAD - 1;
#pragma unroll 1
      for (int i = 0; i < 17 - G1_LEAD; ++i) {
        stage(i + G1_LEAD - 1, slot);
        slot = slot == G1_NS - 1 ? 0 : slot + 1;
        vmwait<5 * (G1_LEAD - 1)>();
        rawbar();
      }
      const u32* st2 = (const u32*)(ws + WS_ST2) + 128 * rt + 32 * w + (lane & 31);
      u32 v;
      asm volatile("global_load_dword %0, %1, off sc1" : "=v"(v) : "v"(st2));
      sfor<17 - G1_LEAD, 16>([&](auto i) { vmwait<5 * (15 - i) + 1>(); rawbar(); });
      asm volatile("s_waitcnt vmcnt(0)" : "+v"(v));
#pragma unroll 1
      for (int spin = 0; spin < (1 << 16) && !__all(v != 0u); ++spin) {
        __builtin_amdgcn_s_sleep(2);
        v = __hip_atomic_load(st2, __ATOMIC_RELAXED, __HIP_MEMORY_SCOPE_AGENT);
      }
      ((u32*)(sm + G1_SV))[32 * w + (lane & 31)] = v;
      bar();
      bar();
    } else if (w12 >= 4) {
      const char* pf = (const char*)wd + j * (43 * 1024) + w * (43 * 256);
#pragma unroll 1
      for (int i = 0; i < 18; ++i) {
        rawbar();
      }
    } else {
      const int wl = w & 1, wu3 = 3 * (w >> 1), u0 = 6 * cg + wu3;
      u32 xa[2][2], wa[3];
#pragma unroll
      for (int c = 0; c < 2; ++c)
#pragma unroll
        for (int tt = 0; tt < 2; ++tt)
          xa[c][tt] = lb0 + 256 * (8 * hh + 4 * tt + q) + 8 * (p & 1) + 16 * ((4 * (2 * wl + c) + 2 * (g & 1) + (p >> 1)) ^ ((q << 2) | (2 * hh + tt)));
#pragma unroll
      for (int v = 0; v < 3; ++v) wa[v] = lb0 + 8192 + 6144 * (g & 1) + 192 * (8 * hh + q) + 16 * ((2 * (wu3 + v) + (p >> 1)) ^ (2 * (g & 1))) + 8 * (p & 1);
      u4 ex[3];
#pragma unroll
      for (int v = 0; v < 3; ++v) ex[v] = *(const u4*)(ws + WS_EX + min(u0 + v, NU - 1) * 1024 + lane * 16);
      f16v acc[6];
#pragma unroll
      for (int i = 0; i < 6; ++i)
#pragma unroll
        for (int e = 0; e < 16; ++e) acc[i][e] = 0.f;
#pragma unroll
      for (int i = 0; i < 6; ++i) asm volatile("" : "+v"(acc[i]));
      rawbar();
      {
        u32 c0, c1, c2;
        asm volatile(
          "ds_read_b64_tr_b16 v[112:113], %6 offset:0\n"
          "ds_read_b64_tr_b16 v[114:115], %7 offset:0\n"
          "ds_read_b64_tr_b16 v[120:121], %10 offset:0\n"
          "ds_read_b64_tr_b16 v[122:123], %10 offset:768\n"
          "ds_read_b64_tr_b16 v[116:117], %8 offset:0\n"
          "ds_read_b64_tr_b16 v[118:119], %9 offset:0\n"
          "ds_read_b64_tr_b16 v[124:125], %11 offset:0\n"
          "ds_read_b64_tr_b16 v[126:127], %11 offset:768\n"
          "ds_read_b64_tr_b16 v[128:129], %12 offset:0\n"
          "ds_read_b64_tr_b16 v[130:131], %12 offset:768\n"
          "s_mov_b32 %13, 16\n"
          "s_mov_b32 %14, 0\n"
          "Lg1%=:\n"
          "s_add_u32 %14, %14, 1\n"
          "s_cmp_eq_u32 %14, 5\n"
          "s_cselect_b32 %15, -81920, %16\n"
          "s_cselect_b32 %14, 0, %14\n"
          "s_waitcnt lgkmcnt(6)\n"
          "v_mfma_f32_32x32x16_bf16 %0, v[120:123], v[112:115], %0\n"
          "ds_read_b64_tr_b16 v[132:133], %6 offset:4096\n"
          "ds_read_b64_tr_b16 v[134:135], %7 offset:4096\n"
          "s_waitcnt lgkmcnt(6)\n"
          "v_mfma_f32_32x32x16_bf16 %1, v[120:123], v[116:119], %1\n"
          "ds_read_b64_tr_b16 v[140:141], %10 offset:3072\n"
          "ds_read_b64_tr_b16 v[142:143], %10 offset:3840\n"
          "s_waitcnt lgkmcnt(6)\n"
          "v_mfma_f32_32x32x16_bf16 %2, v[124:127], v[112:115], %2\n"
          "ds_read_b64_tr_b16 v[136:137], %8 offset:4096\n"
          "ds_read_b64_tr_b16 v[138:139], %9 offset:4096\n"
          "v_mfma_f32_32x32x16_bf16 %3, v[124:127], v[116:119], %3\n"
          "ds_read_b64_tr_b16 v[144:145], %11 offset:3072\n"
          "ds_read_b64_tr_b16 v[146:147], %11 offset:3840\n"
          "s_waitcnt lgkmcnt(8)\n"
          "v_mfma_f32_32x32x16_bf16 %4, v[128:131], v[112:115], %4\n"
          "ds_read_b64_tr_b16 v[148:149], %12 offset:3072\n"
          "ds_read_b64_tr_b16 v[150:151], %12 offset:3840\n"
          "v_add_u32 %6, %15, %6\n"
          "v_add_u32 %7, %15, %7\n"
          "v_add_u32 %8, %15, %8\n"
          "v_add_u32 %9, %15, %9\n"
          "v_mfma_f32_32x32x16_bf16 %5, v[128:131], v[116:119], %5\n"
          "v_add_u32 %10, %15, %10\n"
          "v_add_u32 %11, %15, %11\n"
          "v_add_u32 %12, %15, %12\n"
          "s_barrier\n"
          "s_waitcnt lgkmcnt(6)\n"
          "v_mfma_f32_32x32x16_bf16 %0, v[140:143], v[132:135], %0\n"
          "ds_read_b64_tr_b16 v[112:113], %6 offset:0\n"
          "ds_read_b64_tr_b16 v[114:115], %7 offset:0\n"
          "s_waitcnt lgkmcnt(6)\n"
          "v_mfma_f32_32x32x16_bf16 %1, v[140:143], v[136:139], %1\n"
          "ds_read_b64_tr_b16 v[120:121], %10 offset:0\n"
          "ds_read_b64_tr_b16 v[122:123], %10 offset:768\n"
          "s_waitcnt lgkmcnt(6)\n"
          "v_mfma_f32_32x32x16_bf16 %2, v[144:147], v[132:135], %2\n"
          "ds_read_b64_tr_b16 v[116:117], %8 offset:0\n"
          "ds_read_b64_tr_b16 v[118:119], %9 offset:0\n"
          "v_mfma_f32_32x32x16_bf16 %3, v[144:147], v[136:139], %3\n"
          "ds_read_b64_tr_b16 v[124:125], %11 offset:0\n"
          "ds_read_b64_tr_b16 v[126:127], %11 offset:768\n"
          "s_waitcnt lgkmcnt(8)\n"
          "v_mfma_f32_32x32x16_bf16 %4, v[148:151], v[132:135], %4\n"
          "ds_read_b64_tr_b16 v[128:129], %12 offset:0\n"
          "ds_read_b64_tr_b16 v[130:131], %12 offset:768\n"
          "s_sub_u32 %13, %13, 1\n"
          "s_cmp_lg_u32 %13, 0\n"
          "v_mfma_f32_32x32x16_bf16 %5, v[148:151], v[136:139], %5\n"
          "s_cbranch_scc1 Lg1%=\n"
          "s_waitcnt lgkmcnt(0)\n"
          : "+v"(acc[0]), "+v"(acc[1]), "+v"(acc[2]), "+v"(acc[3]), "+v"(acc[4]), "+v"(acc[5]), "+v"(xa[0][0]), "+v"(xa[0][1]), "+v"(xa[1][0]), "+v"(xa[1][1]),
            "+v"(wa[0]), "+v"(wa[1]), "+v"(wa[2]), "=&s"(c0), "=&s"(c1), "=&s"(c2)
          : "s"(G1_SLOT) : "memory", "scc", "v112", "v113", "v114", "v115", "v116", "v117", "v118", "v119", "v120", "v121", "v122", "v123", "v124", "v125", "v126", "v127", "v128", "v129", "v130", "v131", "v132", "v133", "v134", "v135", "v136", "v137", "v138", "v139", "v140", "v141", "v142", "v143", "v144", "v145", "v146", "v147", "v148", "v149", "v150", "v151");
      }
      u32 sv[2];
#pragma unroll
      for (int c = 0; c < 2; ++c) sv[c] = ((const u32*)(sm + G1_SV))[64 * wl + 32 * c + (lane & 31)];
      bar();
#pragma unroll
      for (int c = 0; c < 2; ++c) {
        const u32 bv = hh ? __builtin_amdgcn_perm(sv[c], sv[c], 0x03020302u) : (__builtin_amdgcn_perm(sv[c], sv[c], 0x01000100u) ^ 0x80008000u);
#pragma unroll
        for (int v = 0; v < 3; ++v) acc[2 * v + c] = mfma32(ex[v], (u4){bv, bv, bv, bv}, acc[2 * v + c]);
      }
      char* hp = ws + WS_HH + (4 * rt + 2 * wl) * HH_T + u0 * 1024 + 16 * lane;
#pragma unroll
      for (int v = 0; v < 3; ++v) {
        if (u0 + v < NU) {
#pragma unroll
          for (int c = 0; c < 2; ++c) {
            const float sd = bhi(sv[c]);
            const u4 o = g1_tile(acc[2 * v + c], __builtin_amdgcn_rcpf(sd), sd);
            char* dst = hp + c * HH_T + v * 1024;
            asm volatile("global_store_dwordx4 %0, %1, off sc1\n\ts_nop 1" :: "v"(dst), "v"(o) : "memory");
          }
        }
      }
      if (lane == 0) __hip_atomic_fetch_add((LAS u32*)fl + 1, 1u, __ATOMIC_RELAXED, __HIP_MEMORY_SCOPE_WORKGROUP);
      {
        asm volatile("s_waitcnt vmcnt(0)" ::: "memory");
        if (lane == 0 && __hip_atomic_fetch_add((LAS u32*)fl, 1u, __ATOMIC_RELAXED, __HIP_MEMORY_SCOPE_WORKGROUP) == 3u)
          __hip_atomic_fetch_add((u32*)(ws + WS_CNT) + 64 * rt, 1u, __ATOMIC_RELAXED, __HIP_MEMORY_SCOPE_AGENT);
      }
    }
  }
  if (w12 >= 8) {
    asm volatile("s_nop 4");
    fill2(G2_RES, 3, 0);
    fill2(3, G2_RES - 3, 3);
    vmwait<2 * (G2_RES - 3)>();
    rawbar();
    rawbar();
    fill2(0, 3, 0);
    vmwait<0>();
    rawbar();
    rawbar();
    rawbar();
  } else {
    const int wl = w12 & 1, kq = w12 >> 1, dc = 64 * dt + 16 * kq + 4 * hh;
    f4 gb[2], xr[2];
#pragma unroll
    for (int e = 0; e < 2; ++e) {
      gb[e] = *(const f4*)((const float*)(ws + WS_GB) + dc + 8 * e);
      xr[e] = *(const f4*)((const float*)(ws + WS_INV) + dc + 8 * e);
#pragma unroll
      for (int i = 0; i < 4; ++i) xr[e][i] *= b2f(((const u16*)(ws + WS_X1T))[(dc + 8 * e + i) * L + 64 * lt + 32 * wl + (lane & 31)]);
    }
    u32 pa[2];
#pragma unroll
    for (int e = 0; e < 2; ++e)
      pa[e] = lb0 + G2_RES * 2048 * kq + 128 * (4 * hh + q) + 16 * ((((4 * e + 2 * (g & 1)) ^ (2 * kq)) + (p >> 1)) ^ (4 * (q >> 1))) + 8 * (p & 1);
    f16v acc[2];
#pragma unroll
    for (int e = 0; e < 16; ++e) acc[0][e] = acc[1][e] = 0.f;
    asm volatile("" : "+v"(xr[0]), "+v"(xr[1]), "+v"(gb[0]), "+v"(gb[1]), "+v"(acc[0]), "+v"(acc[1]));
    rawbar();
    u4 A[2][2], AR[3][2];
#pragma unroll
    for (int i = 0; i < 6; ++i) AR[i >> 1][i & 1] = cat(trd(pa[i & 1] + (i >> 1) * 2048), trd(pa[i & 1] + (i >> 1) * 2048 + 1024));
    asm volatile("s_waitcnt lgkmcnt(0)\n\ts_barrier" : "+v"(AR[0][0]), "+v"(AR[0][1]), "+v"(AR[1][0]), "+v"(AR[1][1]), "+v"(AR[2][0]), "+v"(AR[2][1]) :: "memory");
    if (w12 == 0) {
      const u32* cnt = (const u32*)(ws + WS_CNT) + 64 * (lt >> 1);
#pragma unroll 1
      for (int spin = 0; spin < (1 << 18); ++spin)
        if (__hip_atomic_load(cnt, __ATOMIC_RELAXED, __HIP_MEMORY_SCOPE_AGENT) >= 15u) break;
    }
    rawbar();
    const rsrc_t rh = mkrsrc(ws + WS_HH + (2 * lt + wl) * HH_T, HH_T);
    constexpr int LEAD = G2_LEAD, NH = LEAD + 2;
    u4 H[NH];
    u32 kh = kfirst(kq);
    auto ldh = [&](auto i) { if constexpr (i < 22) { bld16_sc1<0>(H[i % NH], lane * 16, rh, kh * 1024); kh = knext(kh); } };
    sfor<0, LEAD>(ldh);
    auto rda = [&](auto i) {
#pragma unroll
      for (int e = 0; e < 2; ++e) A[i & 1][e] = cat(trd(pa[e] + i * 2048), trd(pa[e] + i * 2048 + 1024));
    };
    rda(std::integral_constant<int, 0>{});
    sfor<0, 22>([&](auto i) {
      vmwait<(i < 22 - LEAD ? LEAD - 1 : 21 - i)>(H[i % NH]);
      SB();
      if constexpr (i + 1 < G2_RES) rda(std::integral_constant<int, i + 1>{});
      if (i < 21 || kq < 2) {
#pragma unroll
        for (int e = 0; e < 2; ++e) acc[e] = mfma32(i < G2_RES ? A[i & 1][e] : AR[i < G2_RES ? 0 : i - G2_RES][e], H[i % NH], acc[e]);
      }
      if constexpr (i + 1 < G2_RES) { SGB(0x8, 1); SGB(0x100, 2); SGB(0x8, 1); SGB(0x100, 2); }
      SB();
      ldh(std::integral_constant<int, i + LEAD>{});
      if constexpr (i == G2_RES - 1) rawbar();
    });
    f4* red = (f4*)sm + wl * 1536 + lane;
#pragma unroll
    for (int k = 1; k < 4; ++k)
#pragma unroll
      for (int e = 0; e < 2; ++e) {
        const f16v& a = acc[k >> 1];
        const int r = 8 * (k & 1) + 4 * e;
        red[(kq ^ k) * 384 + (k - 1) * 128 + 64 * e] = (f4){a[r], a[r + 1], a[r + 2], a[r + 3]};
      }
    bar();
    u2 o[2];
#pragma unroll
    for (int e = 0; e < 2; ++e) {
      const f4* rp = red + kq * 384 + 64 * e;
      const f4 y = ((f4){acc[0][4 * e], acc[0][4 * e + 1], acc[0][4 * e + 2], acc[0][4 * e + 3]} + rp[0] + rp[128] + rp[256]) * gb[e] + xr[e];
      o[e] = (u2){pk(y[0], y[1]), pk(y[2], y[3])};
    }
    const auto r0 = __builtin_amdgcn_permlane32_swap(o[0].x, o[1].x, false, false);
    const auto r1 = __builtin_amdgcn_permlane32_swap(o[0].y, o[1].y, false, false);
    *(u4*)(out + (64 * lt + 32 * wl + (lane & 31)) * D + 64 * dt + 16 * kq + 8 * hh) = (u4){r0[0], r1[0], r0[1], r1[1]};
  }
}

extern "C" void kernel_launch(void* const* d_in, const int* in_sizes, int n_in, void* d_out, int out_size, void* d_ws, size_t ws_size,
                              hipStream_t stream) {
  const u16 *wg = (const u16*)d_in[8], *wu = (const u16*)d_in[9], *wd = (const u16*)d_in[10];
  char* ws = (char*)d_ws;
  (void)hipFuncSetAttribute((const void*)k1_conv, hipFuncAttributeMaxDynamicSharedMemorySize, K1_LDS);
  (void)hipFuncSetAttribute((const void*)k3_mlp, hipFuncAttributeMaxDynamicSharedMemorySize, K3_LDS);
  k1_conv<<<256, 512, K1_LDS, stream>>>((const u16*)d_in[0], (const u16*)d_in[1], (const u16*)d_in[2], (const u16*)d_in[3], (const u16*)d_in[4], (const u16*)d_in[5],
                                        (const u16*)d_in[6], (const u16*)d_in[11], (const u16*)d_in[12], wg, wu, wd, ws);
  k3_mlp<<<256, 768, K3_LDS, stream>>>(wg, wu, wd, ws, (u16*)d_out);
}
```
